```python
import torch

def _softmax(x, axis=-1):
    return torch.softmax(x, dim=axis)
def _gelu(x):
    return torch.nn.functional.gelu(x, approximate="tanh")
def _silu(x):
    return torch.nn.functional.silu(x)
def _sigmoid(x):
    return torch.sigmoid(x)
def _layernorm(x, axis=-1, eps=1e-5):
    m = x.mean(dim=axis, keepdim=True)
    v = x.var(dim=axis, keepdim=True, unbiased=False)
    return (x - m) / torch.sqrt(v + eps)
def _scatter_add(tgt, idx, src, axis=0):
    return tgt.clone().index_add_(axis, idx, src)
def _segment_sum(src, seg, n):
    out = torch.zeros((n,) + src.shape[1:], dtype=src.dtype)
    return out.index_add_(0, seg, src)
def _pad(x, pw):
    flat = tuple(v for pair in reversed(pw) for v in pair)
    return torch.nn.functional.pad(x, flat)

kernel_name = 'recurrent_scan__ssm_conv_h3__glu_gated_swiglu__prompt'

def setup_inputs():

    _w = (lambda a: (lambda t: t.to(torch.bfloat16) if t.is_floating_point() else t)(torch.as_tensor(a)))
    def _A_setup():
        B, S, D = 1, 2048, 448
        ks = 4
        return {
            "q": _w((lambda *s: torch.randn(*s, dtype=torch.bfloat16) * 0.05)(B, S, D)),
            "k": _w((lambda *s: torch.randn(*s, dtype=torch.bfloat16) * 0.8)(B, S, D)),
            "v": _w((lambda *s: torch.randn(*s, dtype=torch.bfloat16) * 0.8)(B, S, D)),
            "shift_k": _w((lambda *s: torch.randn(*s, dtype=torch.bfloat16) * 0.8)(D, ks) * 0.5),
            "ssm_k": _w((lambda *s: torch.randn(*s, dtype=torch.bfloat16) * 0.8)(D, S) * (1.0 / S ** 0.5)),
            "ssm_D": _w((lambda *s: torch.randn(*s, dtype=torch.bfloat16) * 0.8)(D)),
        }
    _A_in = _A_setup()

    def _B_setup():
        B, S, D, F = 1, 2048, 448, 1792
        return {
            "x": _w((lambda *s: torch.randn(*s, dtype=torch.bfloat16) * 0.05)(B, S, D)),
            "w1": _w((lambda *s: torch.randn(*s, dtype=torch.bfloat16) * 0.2)(D, F) * (2.0 / D) ** 0.5),
            "w3": _w((lambda *s: torch.randn(*s, dtype=torch.bfloat16) * 0.2)(D, F) * (2.0 / D) ** 0.5),
            "w2": _w((lambda *s: torch.randn(*s, dtype=torch.bfloat16) * 0.2)(F, D) * (2.0 / F) ** 0.5),
        }
    _B_in = _B_setup()

    return {
        "A_q": _A_in["q"],
        "A_k": _A_in["k"],
        "A_v": _A_in["v"],
        "A_shift_k": _A_in["shift_k"],
        "A_ssm_k": _A_in["ssm_k"],
        "A_ssm_D": _A_in["ssm_D"],
        "B_x": _B_in["x"],
        "B_w1": _B_in["w1"],
        "B_w3": _B_in["w3"],
        "B_w2": _B_in["w2"],
    }

def reference(A_q, A_k, A_v, A_shift_k, A_ssm_k, A_ssm_D, B_x, B_w1, B_w3, B_w2):
    def _A_ref(q, k, v, shift_k, ssm_k, ssm_D):
        B, S, D = q.shape
        ks = shift_k.shape[-1]
        k_pad = _pad(k, ((0, 0), (ks - 1, 0), (0, 0)))
        k_shift = torch.zeros_like(k)
        for t in range(ks):
            k_shift = k_shift + k_pad[:, t:t + S, :] * shift_k[:, ks - 1 - t][None, None, :]
        kv = k_shift * v
        L = 2 * S
        kv_pad = _pad(kv, ((0, 0), (0, S), (0, 0)))
        kf_pad = _pad(ssm_k, ((0, 0), (0, S)))
        kv_f = (lambda x, axes=(-2,-1): torch.fft.rfft2(x.float(), dim=axes))(kv_pad[:, None, :, :], axes=(1, 2))
        kf_f = (lambda x, axes=(-2,-1): torch.fft.rfft2(x.float(), dim=axes))(kf_pad[:, None, :], axes=(1, 2))
        kf_f = (lambda x, *d: x.permute(*d))(kf_f, 1, 2, 0)
        y_f = kv_f * kf_f[None, :, :, :]
        y = (lambda x, s, axes=(-2,-1): torch.fft.irfft2(x, s=s, dim=axes).to(torch.bfloat16))(y_f, (1, L), axes=(1, 2))[:, 0, :S, :]
        y = y + kv * ssm_D[None, None, :]
        return q * y

    def _B_ref(x, w1, w3, w2):
        gate = torch.einsum("bsd,df->bsf", x, w1)
        up = torch.einsum("bsd,df->bsf", x, w3)
        h = _silu(gate) * up
        out = torch.einsum("bsf,fd->bsd", h, w2)
        return out

    x = A_q
    xn = _layernorm(x, axis=-1)
    s = _A_ref(xn, A_k, A_v, A_shift_k, A_ssm_k, A_ssm_D)
    g = _silu(_B_ref(xn, B_w1, B_w3, B_w2))
    return x + s * g
```

```cpp
#include <hip/hip_runtime.h>
#include <hip/hip_bf16.h>
#include <stdint.h>

namespace naive {
constexpr int S = 2048, D = 448, F = 1792;
typedef unsigned short u16;
__device__ __forceinline__ float bf2f(u16 v) { return __uint_as_float(((unsigned)v) << 16); }
__device__ __forceinline__ u16 f2bf(float f) { __hip_bfloat16 b = __float2bfloat16(f); return *(u16*)&b; }
__device__ __forceinline__ float rbf(float f) { return bf2f(f2bf(f)); }
__device__ __forceinline__ float silu_f(float x) { return x / (1.0f + __expf(-x)); }

__global__ void k_ln(const u16* x, u16* xn) {
  int row = blockIdx.x; int lane = threadIdx.x;
  float s = 0.f;
  for (int i = lane; i < D; i += 64) s += bf2f(x[row * D + i]);
  for (int o = 32; o; o >>= 1) s += __shfl_xor(s, o);
  float m = s / D;
  float v = 0.f;
  for (int i = lane; i < D; i += 64) { float d = bf2f(x[row * D + i]) - m; v += d * d; }
  for (int o = 32; o; o >>= 1) v += __shfl_xor(v, o);
  v /= D;
  float mb = rbf(m), vb = rbf(v);
  float sd = rbf(sqrtf(rbf(vb + 1e-5f)));
  for (int i = lane; i < D; i += 64) xn[row * D + i] = f2bf(rbf(bf2f(x[row * D + i]) - mb) / sd);
}
__global__ void k_kv(const u16* k, const u16* v, const u16* sk, u16* kv) {
  int idx = blockIdx.x * 256 + threadIdx.x; if (idx >= S * D) return;
  int t = idx / D, d = idx % D;
  float acc = 0.f;
  for (int j = 0; j < 4; ++j) {
    int tt = t + j - 3;
    float kk = tt >= 0 ? bf2f(k[tt * D + d]) : 0.f;
    acc = rbf(acc + rbf(kk * bf2f(sk[d * 4 + 3 - j])));
  }
  kv[idx] = f2bf(acc * bf2f(v[idx]));
}
__global__ void k_conv(const u16* kv, const u16* ssmk, const u16* ssmD, u16* y) {
  int idx = blockIdx.x * 256 + threadIdx.x; if (idx >= S * D) return;
  int t = idx / D, d = idx % D;
  float acc = 0.f;
  for (int s = 0; s <= t; ++s) acc += bf2f(kv[s * D + d]) * bf2f(ssmk[d * S + t - s]);
  y[idx] = f2bf(rbf(acc) + rbf(bf2f(kv[idx]) * bf2f(ssmD[d])));
}
__global__ void k_g1(const u16* xn, const u16* w1, const u16* w3, u16* h) {
  int idx = blockIdx.x * 256 + threadIdx.x; if (idx >= S * F) return;
  int t = idx / F, f = idx % F;
  float a = 0.f, b = 0.f;
  for (int k = 0; k < D; ++k) { float xv = bf2f(xn[t * D + k]); a += xv * bf2f(w1[k * F + f]); b += xv * bf2f(w3[k * F + f]); }
  a = rbf(a); b = rbf(b);
  h[idx] = f2bf(rbf(silu_f(a)) * b);
}
__global__ void k_g2(const u16* h, const u16* w2, const u16* x, const u16* xn, const u16* y, u16* out) {
  int idx = blockIdx.x * 256 + threadIdx.x; if (idx >= S * D) return;
  int t = idx / D, d = idx % D;
  float a = 0.f;
  for (int k = 0; k < F; ++k) a += bf2f(h[t * F + k]) * bf2f(w2[k * D + d]);
  a = rbf(a);
  float g = rbf(silu_f(a));
  float s = rbf(bf2f(xn[idx]) * bf2f(y[idx]));
  out[idx] = f2bf(bf2f(x[idx]) + rbf(s * g));
}
inline void run(void* const* d_in, void* d_out, void* d_ws, hipStream_t stream) {
  const u16* q = (const u16*)d_in[0]; const u16* k = (const u16*)d_in[1]; const u16* v = (const u16*)d_in[2];
  const u16* sk = (const u16*)d_in[3]; const u16* ssmk = (const u16*)d_in[4]; const u16* ssmD = (const u16*)d_in[5];
  const u16* w1 = (const u16*)d_in[7]; const u16* w3 = (const u16*)d_in[8]; const u16* w2 = (const u16*)d_in[9];
  u16* xn = (u16*)d_ws; u16* kv = xn + S * D; u16* y = kv + S * D; u16* h = y + S * D;
  k_ln<<<S, 64, 0, stream>>>(q, xn);
  k_kv<<<(S * D + 255) / 256, 256, 0, stream>>>(k, v, sk, kv);
  k_conv<<<(S * D + 255) / 256, 256, 0, stream>>>(kv, ssmk, ssmD, y);
  k_g1<<<(S * F + 255) / 256, 256, 0, stream>>>(xn, w1, w3, h);
  k_g2<<<(S * D + 255) / 256, 256, 0, stream>>>(h, w2, q, xn, y, (u16*)d_out);
}
}
extern "C" void kernel_launch(void* const* d_in, const int* in_sizes, int n_in,
                              void* d_out, int out_size, void* d_ws, size_t ws_size,
                              hipStream_t stream) {
  naive::run(d_in, d_out, d_ws, stream);
}
```

```cpp
#include <hip/hip_runtime.h>
#include <hip/hip_bf16.h>
#include <stdint.h>

namespace {
typedef unsigned short u16;
typedef unsigned int u32;
typedef __attribute__((ext_vector_type(4))) short s16x4;
typedef __attribute__((ext_vector_type(8))) short s16x8;
typedef __attribute__((ext_vector_type(8))) __bf16 bf16x8;
typedef __attribute__((ext_vector_type(2))) __bf16 bf16x2;
typedef __attribute__((ext_vector_type(4))) float f32x4;
typedef __attribute__((ext_vector_type(2))) float f32x2;
typedef __attribute__((ext_vector_type(4))) u32 u32x4;
typedef __attribute__((ext_vector_type(2))) u32 u32x2;
#define LDSP(T) __attribute__((address_space(3))) T*
#define DEV __device__ __forceinline__

constexpr int S = 2048, D = 448, F = 1792;
constexpr size_t WS_H = 0;
constexpr size_t WS_W2T = WS_H + 7340032;
constexpr size_t WS_UT = WS_W2T + 1605632;
constexpr size_t WS_YT = WS_UT + 1835008;
constexpr size_t WS_STAT = WS_YT + 1835008;
constexpr size_t WS_CSUM = WS_STAT + 16384;

DEV float bflo(u32 v) { return __uint_as_float(v << 16); }
DEV float bfhi(u32 v) { return __uint_as_float(v & 0xffff0000u); }
DEV u32 pk2(float lo, float hi) { bf16x2 r = {(__bf16)lo, (__bf16)hi}; return __builtin_bit_cast(u32, r); }
DEV float rbf(float f) { return bflo(pk2(f, f)); }
DEV f32x4 mfma16(s16x8 a, s16x8 b, f32x4 c) {
  return __builtin_amdgcn_mfma_f32_16x16x32_bf16(__builtin_bit_cast(bf16x8, a), __builtin_bit_cast(bf16x8, b), c, 0, 0, 0);
}
DEV s16x8 lds_tr2(u32 a0, u32 a1) {
  s16x4 lo = __builtin_amdgcn_ds_read_tr16_b64_v4i16((LDSP(s16x4))a0);
  s16x4 hi = __builtin_amdgcn_ds_read_tr16_b64_v4i16((LDSP(s16x4))a1);
  return __builtin_shufflevector(lo, hi, 0, 1, 2, 3, 4, 5, 6, 7);
}
DEV s16x8 lds_b128(u32 a) { return *(LDSP(s16x8))a; }
DEV void lds_st128(u32 a, u32x4 v) { *(LDSP(u32x4))a = v; }
DEV void glds16(u32 voff, const void* sbase, u32 ldst) {
  asm volatile("s_mov_b32 m0, %2\n\ts_nop 0\n\tglobal_load_lds_dwordx4 %0, %1" ::"v"(voff), "s"(sbase), "s"(ldst) : "memory");
}
#define WAIT_VM(n) asm volatile("s_waitcnt vmcnt(" #n ")" ::: "memory")
#define WAIT_LGKM0() asm volatile("s_waitcnt lgkmcnt(0)" ::: "memory")
#define BAR() __builtin_amdgcn_s_barrier()
DEV float silu_f(float x) { return x * __builtin_amdgcn_rcpf(1.0f + __builtin_amdgcn_exp2f(-1.44269504f * x)); }

DEV void prep_ut(int u, const char* kp, const char* vp, const char* skp, char* ws) {
  const int tid = threadIdx.x;
  if (tid < 448) {
    const int tg = tid / 56, cg = tid % 56;
    const int t0 = 64 * u + 8 * tg;
    u32x4 kr[11], vr[8], sw[4];
#pragma unroll
    for (int i = 0; i < 11; ++i) {
      int t = t0 - 3 + i;
      int tc = t < 0 ? 0 : t;
      u32x4 z = *(const u32x4*)(kp + (size_t)tc * (D * 2) + cg * 16);
      if (i < 3 && t < 0) z = u32x4{0, 0, 0, 0};
      kr[i] = z;
    }
#pragma unroll
    for (int i = 0; i < 8; ++i) vr[i] = *(const u32x4*)(vp + (size_t)(t0 + i) * (D * 2) + cg * 16);
#pragma unroll
    for (int i = 0; i < 4; ++i) sw[i] = *(const u32x4*)(skp + cg * 64 + i * 16);
#pragma unroll
    for (int c = 0; c < 8; ++c) {
      const u32 wa = sw[c >> 1][(c & 1) * 2], wb = sw[c >> 1][(c & 1) * 2 + 1];
      const float w0 = bflo(wa), w1 = bfhi(wa), w2 = bflo(wb), w3 = bfhi(wb);
      float kf[11], o[8];
#pragma unroll
      for (int i = 0; i < 11; ++i) kf[i] = (c & 1) ? bfhi(kr[i][c >> 1]) : bflo(kr[i][c >> 1]);
#pragma unroll
      for (int i = 0; i < 8; ++i) {
        u32 p01 = pk2(kf[i] * w3, kf[i + 1] * w2), p23 = pk2(kf[i + 2] * w1, kf[i + 3] * w0);
        float a = rbf(bflo(p01) + bfhi(p01));
        a = rbf(a + bflo(p23));
        a = rbf(a + bfhi(p23));
        o[i] = a * ((c & 1) ? bfhi(vr[i][c >> 1]) : bflo(vr[i][c >> 1]));
      }
      u32x4 r = {pk2(o[7], o[6]), pk2(o[5], o[4]), pk2(o[3], o[2]), pk2(o[1], o[0])};
      lds_st128((u32)((cg * 8 + c) * 128 + (((7 - tg) ^ (cg & 7)) << 4)), r);
    }
  }
  __syncthreads();
  char* dst = ws + WS_UT + (size_t)(1984 - 64 * u) * 2;
#pragma unroll
  for (int i = 0; i < 7; ++i) {
    int n = tid + 512 * i, d = n >> 3, c = n & 7;
    u32x4 r = *(LDSP(u32x4))(u32)(d * 128 + ((c ^ ((d >> 3) & 7)) << 4));
    *(u32x4*)(dst + (size_t)d * (S * 2) + c * 16) = r;
  }
}
DEV void prep_stats(int u, const char* xp, char* ws) {
  const int tid = threadIdx.x, row = 64 * u + (tid >> 3), c = tid & 7;
  u32x4 v[7];
#pragma unroll
  for (int i = 0; i < 7; ++i) v[i] = *(const u32x4*)(xp + (size_t)row * (D * 2) + (c + 8 * i) * 16);
  float s = 0.f;
#pragma unroll
  for (int i = 0; i < 7; ++i)
#pragma unroll
    for (int j = 0; j < 4; ++j) s += bflo(v[i][j]) + bfhi(v[i][j]);
  s += __shfl_xor(s, 1); s += __shfl_xor(s, 2); s += __shfl_xor(s, 4);
  const float m = s * (1.0f / D);
  float q = 0.f;
#pragma unroll
  for (int i = 0; i < 7; ++i)
#pragma unroll
    for (int j = 0; j < 4; ++j) { float a = bflo(v[i][j]) - m, b = bfhi(v[i][j]) - m; q += a * a + b * b; }
  q += __shfl_xor(q, 1); q += __shfl_xor(q, 2); q += __shfl_xor(q, 4);
  const float sd = rbf(sqrtf(rbf(rbf(q * (1.0f / D)) + 1e-5f)));
  if (c == 0) *(f32x2*)(ws + WS_STAT + row * 8) = f32x2{rbf(m), 1.0f / sd};
}
DEV void prep_csum(int cu, const char* w1p, const char* w3p, char* ws) {
  const int tid = threadIdx.x, kg = tid >> 4, c = tid & 15;
  const int mat = cu / 14, ct = cu % 14;
  const char* w = (mat ? w3p : w1p) + ct * 256 + c * 16;
  u32x4 v[14];
#pragma unroll
  for (int i = 0; i < 14; ++i) v[i] = *(const u32x4*)(w + (size_t)(kg + 32 * i) * (F * 2));
  float a[8] = {};
#pragma unroll
  for (int i = 0; i < 14; ++i)
#pragma unroll
    for (int j = 0; j < 4; ++j) { a[2 * j] += bflo(v[i][j]); a[2 * j + 1] += bfhi(v[i][j]); }
  *(LDSP(f32x4))(u32)(kg * 512 + c * 32) = f32x4{a[0], a[1], a[2], a[3]};
  *(LDSP(f32x4))(u32)(kg * 512 + c * 32 + 16) = f32x4{a[4], a[5], a[6], a[7]};
  __syncthreads();
  if (tid < 128) {
    float s = 0.f;
#pragma unroll
    for (int i = 0; i < 32; ++i) s += *(LDSP(float))(u32)(i * 512 + tid * 4);
    *(float*)(ws + WS_CSUM + (size_t)(mat * F + ct * 128 + tid) * 4) = s;
  }
}
DEV void prep_w2t(int ks, const char* w2p, char* ws) {
  const int tid = threadIdx.x, lane = tid & 63, wid = __builtin_amdgcn_readfirstlane(tid >> 6);
  const char* src = w2p + (size_t)ks * 32 * (D * 2);
#pragma unroll
  for (int i = 0; i < 4; ++i) {
    int n = tid + 512 * i;
    if (n < 1792) lds_st128((u32)(n * 16), *(const u32x4*)(src + n * 16));
  }
  __syncthreads();
  if (wid < 7) {
    const int g = lane >> 4, q = (lane >> 2) & 3, p = lane & 3;
#pragma unroll
    for (int a = 0; a < 4; ++a) {
      u32 ad = (u32)((4 * g + q) * 896 + (64 * wid + 16 * a + 4 * p) * 2);
      s16x8 fr = lds_tr2(ad, ad + 16 * 896);
      *(s16x8*)(ws + WS_W2T + (size_t)((wid * 56 + ks) * 4 + a) * 1024 + lane * 16) = fr;
    }
  }
}
__global__ __launch_bounds__(512) void k_prep(const char* x, const char* k, const char* v, const char* sk, const char* w1,
                                              const char* w3, const char* w2, char* ws) {
  const int b = blockIdx.x;
  if (b < 32) prep_ut(b, k, v, sk, ws);
  else if (b < 64) prep_stats(b - 32, x, ws);
  else if (b < 92) prep_csum(b - 64, w1, w3, ws);
  else prep_w2t(b - 92, w2, ws);
}

constexpr u32 CV = 49152, CV_HSTR = 4160, CV_U = CV + 8 * CV_HSTR, CV_USTR = 4608;
DEV u32 shr16(u32 hi, u32 lo) { return __builtin_amdgcn_alignbit(hi, lo, 16); }
DEV void conv_store_copies(u32 base, u32x4 c, u32x2 n) {
  lds_st128(base, c);
  lds_st128(base + CV_HSTR, u32x4{shr16(c[1], c[0]), shr16(c[2], c[1]), shr16(c[3], c[2]), shr16(n[0], c[3])});
  lds_st128(base + 2 * CV_HSTR, u32x4{c[1], c[2], c[3], n[0]});
  lds_st128(base + 3 * CV_HSTR, u32x4{shr16(c[2], c[1]), shr16(c[3], c[2]), shr16(n[0], c[3]), shr16(n[1], n[0])});
}
DEV s16x8 lds_2xb64(u32 a) {
  u32x2 lo, hi;
  lo = *(volatile LDSP(u32x2))a;
  hi = *(volatile LDSP(u32x2))(a + 8);
  return __builtin_bit_cast(s16x8, u32x4{lo[0], lo[1], hi[0], hi[1]});
}
template <int VAR>
DEV void conv_compute(int lane, int ch, int d, const char* ssmD, char* ws) {
  constexpr int NP = VAR == 0 ? 8 : 6;
  constexpr int T[4] = {VAR == 0 ? 7 : 5, VAR == 0 ? 6 : 4, VAR == 0 ? 1 : 3, VAR == 0 ? 0 : 2};
  const int m = lane & 15, g = lane >> 4;
  const u32 abase = CV + (u32)(ch * 4 + ((m + 1) & 3)) * CV_HSTR + (u32)(((m + 1) & ~3) + 8 * g) * 2;
  const u32 bbase = CV_U + (u32)ch * CV_USTR + (u32)(2032 - 1792 - 16 * m + 8 * g) * 2;
  f32x4 acc[4] = {};
#pragma unroll 2
  for (int q = 0; q < 8; ++q) {
    s16x8 A[NP], B[NP];
#pragma unroll
    for (int p = 0; p < NP; ++p) A[p] = lds_2xb64(abase + q * 64 + p * 512);
#pragma unroll
    for (int p = 0; p < NP; ++p) B[p] = lds_b128(bbase + q * 64 + (7 - p) * 512);
#pragma unroll
    for (int i = 0; i < 4; ++i)
#pragma unroll
      for (int p = 0; p <= T[i]; ++p) acc[i] = mfma16(A[p], B[T[i] - p], acc[i]);
  }
  const float Dd = bflo(*(const u16*)(ssmD + d * 2));
#pragma unroll
  for (int i = 0; i < 4; ++i) {
    const int t = 256 * T[i] + 16 * m + 4 * g;
    u32x2 uu = *(LDSP(u32x2))(CV_U + (u32)ch * CV_USTR + (u32)(2044 - t) * 2);
    float y0 = rbf(acc[i][0]) + rbf(bfhi(uu[1]) * Dd), y1 = rbf(acc[i][1]) + rbf(bflo(uu[1]) * Dd);
    float y2 = rbf(acc[i][2]) + rbf(bfhi(uu[0]) * Dd), y3 = rbf(acc[i][3]) + rbf(bflo(uu[0]) * Dd);
    *(u32x2*)(ws + WS_YT + (size_t)d * (S * 2) + t * 2) = u32x2{pk2(y0, y1), pk2(y2, y3)};
  }
}

__global__ __launch_bounds__(512, 2) void k_g1(const char* x, const char* w1, const char* w3, const char* ssmk,
                                               const char* ssmD, char* ws) {
  const int b = blockIdx.x, xcd = b & 7, r = b >> 3;
  const int panel = (xcd >> 1) * 4 + r / 7, ft = (xcd & 1) * 7 + r % 7;
  const int rot = (r % 7 + 2 * (r / 7)) % 7;
  const int tid = threadIdx.x, lane = tid & 63, wid = __builtin_amdgcn_readfirstlane(tid >> 6);
  const int g = lane >> 4, l15 = lane & 15;

  const u32 voffX = (u32)((lane >> 3) * (D * 2) + (((lane & 7) ^ g) << 4));
  const u32 voffW = (u32)(g * (F * 2) + (((l15 >> 1) ^ g) << 5) + ((lane & 1) << 4));
  const char* xs = x + (size_t)(panel * 128 + 16 * wid) * (D * 2);
  const char* wsrc = ((wid & 1) ? w3 : w1) + (size_t)(16 * (wid >> 1)) * (F * 2) + ft * 256;
  const u32 ldX = (u32)wid * 2048, ldW = 16384 + (u32)(wid & 1) * 16384 + (u32)(wid >> 1) * 4096;
  auto issue = [&](int c) {
    int kc = c + rot; kc = kc >= 7 ? kc - 7 : kc;
    const u32 slot = (u32)(c % 3) * 49152;
    const char* xb = xs + kc * 128;
    const char* wb = wsrc + (size_t)kc * 64 * (F * 2);
    glds16(voffX, xb, slot + ldX);
    glds16(voffW, wb, slot + ldW);
    glds16(voffW, wb + 4 * (F * 2), slot + ldW + 1024);
    glds16(voffX ^ 64, xb + 8 * (D * 2), slot + ldX + 1024);
    glds16(voffW ^ 128, wb + 8 * (F * 2), slot + ldW + 2048);
    glds16(voffW ^ 128, wb + 12 * (F * 2), slot + ldW + 3072);
  };
  issue(0); issue(1); issue(2);

  const int th = wid & 1, fq = wid >> 1;
  const int q = l15 >> 2, p = lane & 3;
  const u32 aA = 16384 + (u32)((8 * g + q) * 256 + (((2 * fq) ^ (q | ((g & 1) << 2))) << 5) + p * 8);
  const u32 aB = (u32)((64 * th + l15) * 128 + ((g ^ (l15 >> 1)) << 4));
  f32x4 acc[2][2][4] = {};
  s16x8 fa[2][4], fb[2][4];
  auto rd = [&](int set, int c, int ks2) {
    const u32 slot = (u32)(c % 3) * 49152;
#pragma unroll
    for (int j = 0; j < 4; ++j) fb[set][j] = lds_b128(slot + (aB ^ (u32)(ks2 * 64)) + j * 2048);
#pragma unroll
    for (int mi = 0; mi < 4; ++mi) {
      u32 a = slot + (aA ^ (u32)((mi & 1) * 32)) + (mi >> 1) * 16384 + ks2 * 8192;
      fa[set][mi] = lds_tr2(a, a + 1024);
    }
  };
  auto mm = [&](int set) {
#pragma unroll
    for (int mi = 0; mi < 4; ++mi)
#pragma unroll
      for (int j = 0; j < 4; ++j) acc[mi >> 1][mi & 1][j] = mfma16(fa[set][mi], fb[set][j], acc[mi >> 1][mi & 1][j]);
  };
  auto inter = [&]() {
#pragma unroll
    for (int i = 0; i < 12; ++i) { __builtin_amdgcn_sched_group_barrier(0x008, 1, 0); __builtin_amdgcn_sched_group_barrier(0x100, 1, 0); }
    __builtin_amdgcn_sched_group_barrier(0x008, 4, 0);
    __builtin_amdgcn_sched_barrier(0);
  };
  f32x2 st[4]; f32x4 cs[4];
  u32x4 hc[2], uc[2]; u32x2 hn[2];
  const int d0 = 2 * (panel * 14 + ft);
#pragma unroll
  for (int c = 0; c < 7; ++c) {
    if (c == 0) WAIT_VM(12); else if (c < 5) WAIT_VM(6); else if (c == 5) WAIT_VM(0);
    if (c < 6) { WAIT_LGKM0(); BAR(); }
    if (c >= 1 && c + 2 <= 6) issue(c + 2);
    if (c == 5) {
#pragma unroll
      for (int j = 0; j < 4; ++j) st[j] = *(const f32x2*)(ws + WS_STAT + (size_t)(panel * 128 + 64 * th + 16 * j + l15) * 8);
#pragma unroll
      for (int mi = 0; mi < 4; ++mi) cs[mi] = *(const f32x4*)(ws + WS_CSUM + (size_t)((mi >> 1) * F + ft * 128 + 32 * fq + 16 * (mi & 1) + 4 * g) * 4);
      if (wid < 4) {
#pragma unroll
        for (int ch = 0; ch < 2; ++ch) {
          const char* hp = ssmk + (size_t)(d0 + ch) * (S * 2) + tid * 16;
          hc[ch] = *(const u32x4*)hp;
          hn[ch] = *(const u32x2*)(hp + (tid < 255 ? 16 : 0));
          uc[ch] = *(const u32x4*)(ws + WS_UT + (size_t)(d0 + ch) * (S * 2) + tid * 16);
        }
      }
    }
    __builtin_amdgcn_sched_barrier(0);
    rd(0, c, 0);
    if (c > 0) { mm(1); inter(); }
    rd(1, c, 1);
    mm(0); inter();
  }
  mm(1);
  __builtin_amdgcn_sched_barrier(0);

  if (wid < 4) {
#pragma unroll
    for (int ch = 0; ch < 2; ++ch) {
      const u32 hb = CV + (u32)ch * 4 * CV_HSTR;
      conv_store_copies(hb + (u32)(tid + 2) * 16, hc[ch], hn[ch]);
      lds_st128(CV_U + (u32)ch * CV_USTR + (u32)tid * 16, uc[ch]);
      if (tid < 32) lds_st128(CV_U + (u32)ch * CV_USTR + 4096 + (u32)tid * 16, u32x4{0, 0, 0, 0});
      if (tid == 0) {
        conv_store_copies(hb, u32x4{0, 0, 0, 0}, u32x2{0, 0});
        conv_store_copies(hb + 16, u32x4{0, 0, 0, 0}, u32x2{hc[ch][0], hc[ch][1]});
      }
    }
  }
  {
    char* hdst = ws + WS_H + (size_t)(((2 * panel + th) * 56 + 4 * ft + fq) * 4) * 1024 + lane * 16;
#pragma unroll
    for (int j = 0; j < 4; ++j) {
      const float nm = -st[j][0], rs = st[j][1];
      const float c1 = -1.44269504f * rs, irs2 = __builtin_amdgcn_rcpf(rs * rs);
      float hv[8];
#pragma unroll
      for (int i = 0; i < 2; ++i)
#pragma unroll
        for (int e = 0; e < 4; ++e) {
          float a = __builtin_fmaf(nm, cs[i][e], acc[0][i][j][e]);
          float bb = __builtin_fmaf(nm, cs[2 + i][e], acc[1][i][j][e]);
          float ex = __builtin_amdgcn_exp2f(a * c1);
          float rr = __builtin_amdgcn_rcpf(__builtin_fmaf(ex, irs2, irs2));
          hv[i * 4 + e] = a * bb * rr;
        }
      *(u32x4*)(hdst + j * 1024) = u32x4{pk2(hv[0], hv[1]), pk2(hv[2], hv[3]), pk2(hv[4], hv[5]), pk2(hv[6], hv[7])};
    }
  }
  WAIT_LGKM0(); BAR();
  if (wid < 4) {
    const int ch = wid >> 1;
    if (wid & 1) conv_compute<1>(lane, ch, d0 + ch, ssmD, ws); else conv_compute<0>(lane, ch, d0 + ch, ssmD, ws);
  }
}

constexpr u32 E_Y = 65536, E_YSTR = 144, E_S = E_Y + 64 * E_YSTR, E_X = E_S + 8192;
__global__ __launch_bounds__(512, 2) void k_g2(const char* x, const char* ws, char* out) {
  const int b = blockIdx.x, xcd = b & 7, r = b >> 3;
  const int tau = xcd * 4 + r / 7, dl = r % 7;
  const int tid = threadIdx.x, lane = tid & 63, wid = __builtin_amdgcn_readfirstlane(tid >> 6);
  if (wid < 4) {
    const int rot = (2 * dl + 3 * (r / 7)) % 14;
    const char* hp = ws + WS_H + (size_t)(tau * 56 + 14 * wid) * 4096 + lane * 16;
    const char* wp = ws + WS_W2T + (size_t)(dl * 56 + 14 * wid) * 4096 + lane * 16;
    constexpr int DEPTH = 4;
    s16x8 fa[DEPTH][4], fb[DEPTH][4];
    f32x4 acc[4][4] = {};
    auto ld = [&](int i) {
      int ks = i + rot; ks = ks >= 14 ? ks - 14 : ks;
#pragma unroll
      for (int a = 0; a < 4; ++a) {
        fa[i % DEPTH][a] = *(const s16x8*)(wp + ks * 4096 + a * 1024);
        fb[i % DEPTH][a] = *(const s16x8*)(hp + ks * 4096 + a * 1024);
      }
    };
#pragma unroll
    for (int i = 0; i < DEPTH; ++i) ld(i);
    __builtin_amdgcn_sched_barrier(0);
#pragma unroll
    for (int i = 0; i < 14; ++i) {
#pragma unroll
      for (int a = 0; a < 4; ++a)
#pragma unroll
        for (int j = 0; j < 4; ++j) acc[a][j] = mfma16(fa[i % DEPTH][a], fb[i % DEPTH][j], acc[a][j]);
      __builtin_amdgcn_sched_barrier(0);
      if (i + DEPTH < 14) ld(i + DEPTH);
      __builtin_amdgcn_sched_barrier(0);
    }
    const int g = lane >> 4, l15 = lane & 15;
#pragma unroll
    for (int j = 0; j < 4; ++j)
#pragma unroll
      for (int a = 0; a < 4; ++a)
        *(LDSP(f32x4))(u32)(wid * 16384 + (16 * j + l15) * 256 + (((4 * a + g) ^ (l15 & 7)) << 4)) = acc[a][j];
  } else {
    const int ht = tid - 256;
    u32x4 yv[2], xv[2]; f32x2 st[2];
#pragma unroll
    for (int i = 0; i < 2; ++i) {
      const int n = ht + 256 * i, row = n >> 3, c = n & 7;
      yv[i] = *(const u32x4*)(ws + WS_YT + (size_t)(64 * dl + row) * (S * 2) + (64 * tau + 8 * c) * 2);
      xv[i] = *(const u32x4*)(x + (size_t)(64 * tau + row) * (D * 2) + (64 * dl + 8 * c) * 2);
      st[i] = *(const f32x2*)(ws + WS_STAT + (size_t)(64 * tau + row) * 8);
    }
#pragma unroll
    for (int i = 0; i < 2; ++i) {
      const int n = ht + 256 * i, row = n >> 3, c = n & 7;
      lds_st128(E_Y + (u32)(row * E_YSTR + c * 16), yv[i]);
      lds_st128(E_X + (u32)(n * 16), xv[i]);
      u32x4 xn;
#pragma unroll
      for (int e = 0; e < 4; ++e) {
        u32 dd = pk2(bflo(xv[i][e]) - st[i][0], bfhi(xv[i][e]) - st[i][0]);
        xn[e] = pk2(bflo(dd) * st[i][1], bfhi(dd) * st[i][1]);
      }
      lds_st128(E_S + (u32)(n * 16), xn);
    }
  }
  __syncthreads();
  {
    const int tok = tid >> 3, dg = tid & 7;
    f32x4 s0 = {}, s1 = {};
#pragma unroll
    for (int w = 0; w < 4; ++w) {
      s0 += *(LDSP(f32x4))(u32)(w * 16384 + tok * 256 + (((2 * dg) ^ (tok & 7)) << 4));
      s1 += *(LDSP(f32x4))(u32)(w * 16384 + tok * 256 + (((2 * dg + 1) ^ (tok & 7)) << 4));
    }
    const u32x4 xv = *(LDSP(u32x4))(E_X + (u32)tid * 16), xn = *(LDSP(u32x4))(E_S + (u32)tid * 16);
    float o[8];
#pragma unroll
    for (int e = 0; e < 8; ++e) {
      const float y = bflo(*(LDSP(u16))(E_Y + (u32)((8 * dg + e) * E_YSTR + tok * 2)));
      const float f = rbf(e < 4 ? s0[e & 3] : s1[e & 3]);
      const float gt = rbf(silu_f(f));
      const float xe = (e & 1) ? bfhi(xv[e >> 1]) : bflo(xv[e >> 1]);
      const float ne = (e & 1) ? bfhi(xn[e >> 1]) : bflo(xn[e >> 1]);
      o[e] = xe + rbf(rbf(ne * y) * gt);
    }
    *(u32x4*)(out + (size_t)(64 * tau + tok) * (D * 2) + (64 * dl + 8 * dg) * 2) =
        u32x4{pk2(o[0], o[1]), pk2(o[2], o[3]), pk2(o[4], o[5]), pk2(o[6], o[7])};
  }
}
}

extern "C" void kernel_launch(void* const* d_in, const int* in_sizes, int n_in, void* d_out, int out_size, void* d_ws,
                              size_t ws_size, hipStream_t stream) {
  const char* x = (const char*)d_in[0]; const char* k = (const char*)d_in[1]; const char* v = (const char*)d_in[2];
  const char* sk = (const char*)d_in[3]; const char* ssmk = (const char*)d_in[4]; const char* ssmD = (const char*)d_in[5];
  const char* w1 = (const char*)d_in[7]; const char* w3 = (const char*)d_in[8]; const char* w2 = (const char*)d_in[9];
  char* ws = (char*)d_ws;
  (void)hipFuncSetAttribute((const void*)k_g1, hipFuncAttributeMaxDynamicSharedMemorySize, 147456);
  (void)hipFuncSetAttribute((const void*)k_g2, hipFuncAttributeMaxDynamicSharedMemorySize, 98304);
  k_prep<<<148, 512, 57344, stream>>>(x, k, v, sk, w1, w3, w2, ws);
  k_g1<<<224, 512, 147456, stream>>>(x, w1, w3, ssmk, ssmD, ws);
  k_g2<<<224, 512, 98304, stream>>>(x, ws, (char*)d_out);
}
```

```cpp
#include <hip/hip_runtime.h>
#include <hip/hip_bf16.h>
#include <stdint.h>
#pragma clang diagnostic ignored "-Wint-to-pointer-cast"

namespace {
typedef unsigned short u16;
typedef unsigned int u32;
typedef unsigned long long u64;
typedef __attribute__((ext_vector_type(4))) short s16x4;
typedef __attribute__((ext_vector_type(8))) short s16x8;
typedef __attribute__((ext_vector_type(8))) __bf16 bf16x8;
typedef __attribute__((ext_vector_type(2))) __bf16 bf16x2;
typedef __attribute__((ext_vector_type(4))) float f32x4;
typedef __attribute__((ext_vector_type(2))) float f32x2;
typedef __attribute__((ext_vector_type(4))) u32 u32x4;
typedef __attribute__((ext_vector_type(2))) u32 u32x2;
#define LDSP(T) __attribute__((address_space(3))) T*
#define GLBP(T) __attribute__((address_space(1))) T*
#define DEV __device__ __forceinline__

constexpr int S = 2048, D = 448, F = 1792;
constexpr u32 WS_H = 0;
constexpr u32 WS_UT = WS_H + 7340032;
constexpr u32 WS_YT = WS_UT + 1835008;
constexpr u32 WS_STAT = WS_YT + 1835008;
constexpr u32 WS_CSUM = WS_STAT + 16384;
constexpr u32 WS_E = WS_CSUM + 16384;
constexpr u32 WS_FUT = WS_E + 256;
constexpr u32 WS_FST = WS_FUT + 3584;
constexpr u32 WS_FY = WS_FST + 256;
constexpr u32 WS_FH = WS_FY + 7168;
constexpr u32 WS_END = WS_FH + 16384;
constexpr u32 SLOT = 49152;
constexpr u32 MISC = 3 * SLOT;
constexpr u32 L_FULL = MISC, L_FREE = MISC + 32, L_EPI = MISC + 64, L_CONV = MISC + 72, L_W2 = MISC + 80, L_H = MISC + 88,
              L_TAG = MISC + 96, L_ADONE = MISC + 112, L_STAT = MISC + 256, L_CSUM = MISC + 1280, L_USCR = MISC + 4096;
constexpr u32 CV = SLOT, CV_HSTR = 4160, CV_U = CV + 8 * CV_HSTR, CV_USTR = 4608;
constexpr u32 E_S = SLOT, E_X = SLOT + 4096;
constexpr int SPIN_CAP = 1 << 18;

DEV float bflo(u32 v) { return __uint_as_float(v << 16); }
DEV float bfhi(u32 v) { return __uint_as_float(v & 0xffff0000u); }
DEV u32 pk2(float lo, float hi) { bf16x2 r = {(__bf16)lo, (__bf16)hi}; return __builtin_bit_cast(u32, r); }
DEV float rbf(float f) { return bflo(pk2(f, f)); }
DEV f32x4 mfma16(s16x8 a, s16x8 b, f32x4 c) {
  return __builtin_amdgcn_mfma_f32_16x16x32_bf16(__builtin_bit_cast(bf16x8, a), __builtin_bit_cast(bf16x8, b), c, 0, 0, 0);
}
DEV u32 opq(u32 v) { asm volatile("" : "+v"(v)); return v; }
DEV s16x8 lds_tr2(u32 base, int o0, int o1) {
  s16x4 lo = __builtin_amdgcn_ds_read_tr16_b64_v4i16((LDSP(s16x4))((LDSP(char))base + o0));
  s16x4 hi = __builtin_amdgcn_ds_read_tr16_b64_v4i16((LDSP(s16x4))((LDSP(char))base + o1));
  return __builtin_shufflevector(lo, hi, 0, 1, 2, 3, 4, 5, 6, 7);
}
DEV s16x8 lds_b128(u32 base, int o) { return *(LDSP(s16x8))((LDSP(char))base + o); }
DEV void lds_st128(u32 a, u32x4 v) { *(LDSP(u32x4))a = v; }
DEV void glds16(u32 voff, const void* sbase, u32 ldst) {
  asm volatile("s_mov_b32 m0, %2\n\ts_nop 0\n\tglobal_load_lds_dwordx4 %0, %1" ::"v"(voff), "s"(sbase), "s"(ldst) : "memory");
}
#define WAIT_VM(n) asm volatile("s_waitcnt vmcnt(%0)" ::"i"(n) : "memory")
#define WAIT_LGKM0() asm volatile("s_waitcnt lgkmcnt(0)" ::: "memory")
#define BAR() __builtin_amdgcn_s_barrier()
#define SCHED0() __builtin_amdgcn_sched_barrier(0)
#define SGB(mask, n) __builtin_amdgcn_sched_group_barrier(mask, n, 0)
DEV float silu_f(float x) { return x * __builtin_amdgcn_rcpf(1.0f + __builtin_amdgcn_exp2f(-1.44269504f * x)); }

DEV void lflag_set(u32 addr, u32 val) {
  asm volatile("s_waitcnt lgkmcnt(0)\n\tds_write_b32 %0, %1" ::"v"(addr), "v"(val) : "memory");
}
DEV void lflag_wait(u32 addr, u32 mask, u32 val) {
  const u32 a = addr + 4 * (threadIdx.x & mask);
#pragma unroll 1
  for (int i = 0; i < SPIN_CAP; ++i) {
    u32 v;
    asm volatile("ds_read_b32 %0, %1\n\ts_waitcnt lgkmcnt(0)" : "=v"(v) : "v"(a) : "memory");
    if (__builtin_amdgcn_ballot_w64(v < val) == 0) break;
    __builtin_amdgcn_s_sleep(1);
  }
}
DEV u64 gload64(const char* p) { return __hip_atomic_load((GLBP(u64))p, __ATOMIC_RELAXED, __HIP_MEMORY_SCOPE_AGENT); }
DEV void gstore64(char* p, u64 v) { __hip_atomic_store((GLBP(u64))p, v, __ATOMIC_RELAXED, __HIP_MEMORY_SCOPE_AGENT); }
DEV void gflag_wait(const char* lane_ptr, u64 tag) {
#pragma unroll 1
  for (int i = 0; i < SPIN_CAP; ++i) {
    asm volatile("" ::: "memory");
    if (__builtin_amdgcn_ballot_w64(gload64(lane_ptr) != tag) == 0) break;
    __builtin_amdgcn_s_sleep(2);
  }
  asm volatile("" ::: "memory");
}
DEV void gflag_publish(char* p, u64 tag, int lane) {
  WAIT_VM(0);
  if (lane == 0) gstore64(p, tag);
}
DEV u64 make_tag(const char* ws) { return gload64(ws + WS_E) * 0x9E3779B97F4A7C15ull + 0xD1B54A32D192ED03ull; }
typedef __amdgpu_buffer_rsrc_t rsrc_t;
DEV rsrc_t ws_rsrc(char* ws) { return __builtin_amdgcn_make_buffer_rsrc(ws, 0, WS_END, 0x00020000); }
DEV void st128_dev(rsrc_t r, u32 off, u32x4 v) { __builtin_amdgcn_raw_buffer_store_b128(v, r, off, 0, 16); }
DEV void st64_dev(rsrc_t r, u32 off, u32x2 v) { __builtin_amdgcn_raw_buffer_store_b64(v, r, off, 0, 16); }
DEV void st32_dev(rsrc_t r, u32 off, u32 v) { __builtin_amdgcn_raw_buffer_store_b32(v, r, off, 0, 16); }
DEV u32x4 ld128_dev(rsrc_t r, u32 off) { return __builtin_amdgcn_raw_buffer_load_b128(r, off, 0, 16); }
DEV u32x2 ld64_dev(rsrc_t r, u32 off) { return __builtin_amdgcn_raw_buffer_load_b64(r, off, 0, 16); }

DEV u32 shr16(u32 hi, u32 lo) { return __builtin_amdgcn_alignbit(hi, lo, 16); }
DEV void conv_store_copies(u32 base, u32x4 c, u32x2 n) {
  lds_st128(base, c);
  lds_st128(base + CV_HSTR, u32x4{shr16(c[1], c[0]), shr16(c[2], c[1]), shr16(c[3], c[2]), shr16(n[0], c[3])});
  lds_st128(base + 2 * CV_HSTR, u32x4{c[1], c[2], c[3], n[0]});
  lds_st128(base + 3 * CV_HSTR, u32x4{shr16(c[2], c[1]), shr16(c[3], c[2]), shr16(n[0], c[3]), shr16(n[1], n[0])});
}

template <int VAR>
DEV void conv_compute(int lane, int ch, int d, float Dd, rsrc_t rs, char* hflag, u64 tag) {
  constexpr int NP = VAR == 0 ? 8 : 6;
  constexpr int T[4] = {VAR == 0 ? 7 : 5, VAR == 0 ? 6 : 4, VAR == 0 ? 1 : 3, VAR == 0 ? 0 : 2};
  const int m = lane & 15, g = lane >> 4, q4 = m >> 2, p4 = lane & 3;
  u32 aa = CV + (u32)(ch * 4 + ((q4 + 1) & 3)) * CV_HSTR + (u32)(((q4 + 1) & 4) + 8 * g + 4 * p4) * 2;
  u32 ba = CV_U + (u32)ch * CV_USTR + (u32)(240 - 16 * m + 8 * g) * 2;
  f32x4 acc[4] = {};
  s16x8 A[2][NP], B[2][NP];
  auto rd = [&](int set, int off) {
#pragma unroll
    for (int p = 0; p < NP; ++p) {
      A[set][p] = lds_tr2(aa, off + p * 512, off + p * 512 + 8);
      B[set][NP - 1 - p] = lds_b128(ba, off + (7 - (NP - 1 - p)) * 512);
    }
  };
  auto mm = [&](int set) {
#pragma unroll
    for (int p = 0; p < NP; ++p)
#pragma unroll
      for (int i = 0; i < 4; ++i)
        if (i < 2 && p <= T[i]) acc[i] = mfma16(A[set][p], B[set][T[i] - p], acc[i]);
#pragma unroll
    for (int i = 2; i < 4; ++i)
#pragma unroll
      for (int p = 0; p <= T[i]; ++p) acc[i] = mfma16(A[set][p], B[set][T[i] - p], acc[i]);
  };
  auto inter = [&]() {
#pragma unroll
    for (int i = 0; i < NP; ++i) { SGB(0x008, 1); SGB(0x100, 2); SGB(0x008, 1); SGB(0x100, 1); }
    SGB(0x008, 18 - 2 * NP);
    SCHED0();
  };
  aa = opq(aa); ba = opq(ba);
  rd(0, 0);
  SCHED0();
#pragma unroll 1
  for (int it = 0; it < 4; ++it) {
    rd(1, 64); mm(0); inter();
    rd(0, 128); mm(1); inter();
    aa = opq(aa + 128); ba = opq(ba + 128);
    if (it == 0) gflag_publish(hflag, tag, lane);
  }
#pragma unroll
  for (int i = 0; i < 4; ++i) {
    const int t = 256 * T[i] + 16 * m + 4 * g;
    u32x2 uu = *(LDSP(u32x2))(CV_U + (u32)ch * CV_USTR + (u32)(2044 - t) * 2);
    float y0 = rbf(acc[i][0]) + rbf(bfhi(uu[1]) * Dd), y1 = rbf(acc[i][1]) + rbf(bflo(uu[1]) * Dd);
    float y2 = rbf(acc[i][2]) + rbf(bfhi(uu[0]) * Dd), y3 = rbf(acc[i][3]) + rbf(bflo(uu[0]) * Dd);
    st64_dev(rs, WS_YT + (u32)(d * (S * 2) + t * 2), u32x2{pk2(y0, y1), pk2(y2, y3)});
  }
}

DEV int g2_ks(int j, int i) { const int e = 7 * j + (i < 7 ? i : i - 7); return 4 * (e >> 1) + (e & 1) + (i < 7 ? 0 : 2); }
DEV u32 g2_place(int j, int p) { return (p < 3 ? 2 * SLOT + (u32)p * 4096 : (u32)(p - 3) * 4096) + (u32)j * 12288; }

__global__ __launch_bounds__(768, 3) void fused_kernel(const char* x, const char* kp, const char* vp, const char* skp,
                                                       const char* ssmk, const char* ssmD, const char* w1, const char* w3,
                                                       const char* w2, char* ws, char* out) {
  const int b = blockIdx.x, xcd = b & 7, r = b >> 3;
  const int tid = threadIdx.x, lane = tid & 63, wid = __builtin_amdgcn_readfirstlane(tid >> 6);
  const int g = lane >> 4, l15 = lane & 15;
  const rsrc_t rs = ws_rsrc(ws);

  if (b >= 224) {
    if (wid >= 8) return;
    const int sp = b - 224;
    const u64 tag = make_tag(ws);
    const int row = 64 * sp + (tid >> 3), c8 = tid & 7;
    u32x4 xv[7], wv[14];
#pragma unroll
    for (int i = 0; i < 7; ++i) xv[i] = *(const u32x4*)(x + (size_t)row * (D * 2) + (c8 + 8 * i) * 16);
    const int cu = sp < 28 ? sp : 27, mat = cu / 14, ct = cu % 14, kg = tid >> 4, c16 = tid & 15;
    const char* wp = (mat ? w3 : w1) + ct * 256 + c16 * 16;
#pragma unroll
    for (int i = 0; i < 14; ++i) wv[i] = *(const u32x4*)(wp + (size_t)(kg + 32 * i) * (F * 2));
    float s = 0.f;
#pragma unroll
    for (int i = 0; i < 7; ++i)
#pragma unroll
      for (int j = 0; j < 4; ++j) s += bflo(xv[i][j]) + bfhi(xv[i][j]);
    s += __shfl_xor(s, 1); s += __shfl_xor(s, 2); s += __shfl_xor(s, 4);
    const float m = s * (1.0f / D);
    float q = 0.f;
#pragma unroll
    for (int i = 0; i < 7; ++i)
#pragma unroll
      for (int j = 0; j < 4; ++j) { float a = bflo(xv[i][j]) - m, bb = bfhi(xv[i][j]) - m; q += a * a + bb * bb; }
    q += __shfl_xor(q, 1); q += __shfl_xor(q, 2); q += __shfl_xor(q, 4);
    const float sd = rbf(sqrtf(rbf(rbf(q * (1.0f / D)) + 1e-5f)));
    if (c8 == 0) st64_dev(rs, WS_STAT + (u32)row * 8, u32x2{__float_as_uint(rbf(m)), __float_as_uint(1.0f / sd)});
    float a[8] = {};
#pragma unroll
    for (int i = 0; i < 14; ++i)
#pragma unroll
      for (int j = 0; j < 4; ++j) { a[2 * j] += bflo(wv[i][j]); a[2 * j + 1] += bfhi(wv[i][j]); }
    *(LDSP(f32x4))(u32)(kg * 512 + c16 * 32) = f32x4{a[0], a[1], a[2], a[3]};
    *(LDSP(f32x4))(u32)(kg * 512 + c16 * 32 + 16) = f32x4{a[4], a[5], a[6], a[7]};
    WAIT_LGKM0(); BAR();
    if (tid < 128 && sp < 28) {
      float cs = 0.f;
#pragma unroll
      for (int i = 0; i < 32; ++i) cs += *(LDSP(float))(u32)(i * 512 + tid * 4);
      st32_dev(rs, WS_CSUM + (u32)(mat * F + ct * 128 + tid) * 4, __float_as_uint(cs));
    }
    WAIT_VM(0); BAR();
    if (tid == 0) gstore64(ws + WS_FST + sp * 8, tag);
    if (sp == 31) {
      if (wid < 7) gflag_wait(ws + WS_FUT + tid * 8, tag); else gflag_wait(ws + WS_FST + (lane & 31) * 8, tag);
      BAR();
      if (tid == 0) gstore64(ws + WS_E, tag);
    }
    return;
  }

  const int panel = (xcd >> 1) * 4 + r / 7, ft = (xcd & 1) * 7 + r % 7;
  const int tau = xcd * 4 + r / 7, dl = r % 7;
  const int d0 = 2 * b;

  if (wid < 2) {
    __builtin_amdgcn_s_setprio(3);
    const int lane = (int)opq(threadIdx.x) & 63, g = lane >> 4, l15 = lane & 15;
    u32 vx[16], vw[16];
    {
      const u32 ox = (u32)((lane >> 3) * (D * 2) + (((lane & 7) ^ g) << 4));
      const u32 ow = (u32)(g * (F * 2) + (((l15 >> 1) ^ g) << 5) + ((lane & 1) << 4));
#pragma unroll
      for (int i = 0; i < 16; ++i) { vx[i] = (ox ^ (u32)((i & 1) * 64)) + i * 8 * (D * 2); vw[i] = (ow ^ (u32)(((i >> 1) & 1) * 128)) + i * 4 * (F * 2); }
    }
    if (wid == 1) { if (lane < 32) *(LDSP(u32))(MISC + lane * 4) = 0; WAIT_LGKM0(); BAR(); }
#pragma unroll 1
    for (int c = wid; c < 7; c += 2) {
      if (c >= 3) lflag_wait(L_FREE, 7, c - 2);
      const u32 slot = (u32)(c % 3) * SLOT;
      const char* xb = x + (size_t)(panel * 128) * (D * 2) + c * 128;
      const char* w1b = w1 + (size_t)c * 64 * (F * 2) + ft * 256;
      const char* w3b = w3 + (size_t)c * 64 * (F * 2) + ft * 256;
#pragma unroll
      for (int i = 0; i < 16; ++i) {
        glds16(vx[i], xb, slot + i * 1024);
        glds16(vw[i], w1b, slot + 16384 + i * 1024);
        glds16(vw[i], w3b, slot + 32768 + i * 1024);
      }
      if (c == 0) { BAR(); }
      WAIT_VM(0);
      lflag_set(L_FULL + wid * 4, c + 1);
    }
    {
      lflag_wait(L_FREE, 7, 7 - wid);
      const u32 o2 = (u32)((lane >> 3) * (D * 2) + ((((lane & 7) >> 1) ^ g) << 5) + ((lane & 1) << 4));
      const int i0 = wid ? 0 : 3;
#pragma unroll 1
      for (int n = 0; n < 12; ++n) {
        const int i = i0 + n / 4, j = n & 3;
        const char* src = w2 + (size_t)g2_ks(j, i) * 32 * (D * 2) + dl * 128;
        const u32 dst = g2_place(j, i);
#pragma unroll
        for (int pc = 0; pc < 4; ++pc) glds16(o2 + pc * 8 * (D * 2), src, dst + pc * 1024);
      }
      WAIT_VM(0);
      lflag_set(L_W2 + (1 - wid) * 4, 1);
    }
    return;
  }

  if (wid < 4) {
    const int u = wid - 2;
    const int lane = (int)opq(threadIdx.x) & 63;
    const int ti = b / 7, cj = b % 7;
    const int tg = lane >> 3, cg = lane & 7;
    const int t0 = 64 * ti + 8 * tg, ch0 = 64 * cj + 32 * u + 4 * cg;
    u32x2 kr[11], vr[8]; u32x4 sw[2];
#pragma unroll
    for (int i = 0; i < 11; ++i) {
      const int t = t0 - 3 + i;
      u32x2 z = *(const u32x2*)(kp + (size_t)(t < 0 ? 0 : t) * (D * 2) + ch0 * 2);
      if (i < 3 && t < 0) z = u32x2{0, 0};
      kr[i] = z;
    }
#pragma unroll
    for (int i = 0; i < 8; ++i) vr[i] = *(const u32x2*)(vp + (size_t)(t0 + i) * (D * 2) + ch0 * 2);
    sw[0] = *(const u32x4*)(skp + ch0 * 8); sw[1] = *(const u32x4*)(skp + ch0 * 8 + 16);
    const u64 tag = make_tag(ws);
    SCHED0();
    BAR();
    const u32 scr = L_USCR + (u32)u * 4096;
#pragma unroll
    for (int c = 0; c < 4; ++c) {
      const u32 wa = sw[c >> 1][(c & 1) * 2], wb = sw[c >> 1][(c & 1) * 2 + 1];
      const float w0 = bflo(wa), w1f = bfhi(wa), w2f = bflo(wb), w3f = bfhi(wb);
      float kf[11], o[8];
#pragma unroll
      for (int i = 0; i < 11; ++i) kf[i] = (c & 1) ? bfhi(kr[i][c >> 1]) : bflo(kr[i][c >> 1]);
#pragma unroll
      for (int i = 0; i < 8; ++i) {
        u32 p01 = pk2(kf[i] * w3f, kf[i + 1] * w2f), p23 = pk2(kf[i + 2] * w1f, kf[i + 3] * w0);
        float a = rbf(bflo(p01) + bfhi(p01));
        a = rbf(a + bflo(p23));
        a = rbf(a + bfhi(p23));
        o[i] = a * ((c & 1) ? bfhi(vr[i][c >> 1]) : bflo(vr[i][c >> 1]));
      }
      lds_st128(scr + (u32)((cg * 4 + c) * 128 + (((7 - tg) ^ cg) << 4)),
                u32x4{pk2(o[7], o[6]), pk2(o[5], o[4]), pk2(o[3], o[2]), pk2(o[1], o[0])});
    }
    WAIT_LGKM0();
#pragma unroll
    for (int i = 0; i < 4; ++i) {
      const int dd = 8 * i + tg;
      u32x4 v = *(LDSP(u32x4))(scr + (u32)(dd * 128 + ((cg ^ (dd >> 2)) << 4)));
      st128_dev(rs, WS_UT + (u32)((64 * cj + 32 * u + dd) * (S * 2) + (1984 - 64 * ti) * 2 + cg * 16), v);
    }
    gflag_publish(ws + WS_FUT + ((cj * 2 + u) * 32 + ti) * 8, tag, lane);
    const int dch = d0 + u;
    u32x4 hc[4]; u32x2 hn[4];
#pragma unroll
    for (int i = 0; i < 4; ++i) {
      const int n = lane + 64 * i;
      const char* hp = ssmk + (size_t)dch * (S * 2) + n * 16;
      hc[i] = *(const u32x4*)hp;
      hn[i] = *(const u32x2*)(hp + (n < 255 ? 16 : 0));
    }
    gflag_wait(ws + WS_FST + (lane & 31) * 8, tag);
    {
      const u32 src = u == 0 ? WS_STAT + (u32)(panel * 128) * 8 + lane * 16
                             : WS_CSUM + (u32)((lane >> 5) * F + ft * 128 + (lane & 31) * 4) * 4;
      u32x4 v = ld128_dev(rs, src);
      lds_st128((u == 0 ? L_STAT : L_CSUM) + lane * 16, v);
      if (u == 0 && lane == 0) *(LDSP(u64))L_TAG = tag;
      lflag_set(L_EPI + u * 4, 1);
    }
    gflag_wait(ws + WS_FUT + ((dch >> 5) * 32 + (lane & 31)) * 8, tag);
    u32x4 uc[4];
#pragma unroll
    for (int i = 0; i < 4; ++i) uc[i] = ld128_dev(rs, WS_UT + (u32)(dch * (S * 2) + (lane + 64 * i) * 16));
    lflag_wait(L_FREE, 7, 5);
    {
      const u32 hb = CV + (u32)u * 4 * CV_HSTR, ub = CV_U + (u32)u * CV_USTR;
#pragma unroll
      for (int i = 0; i < 4; ++i) conv_store_copies(hb + (u32)(lane + 64 * i + 2) * 16, hc[i], hn[i]);
      if (lane < 32) lds_st128(ub + 4096 + (u32)lane * 16, u32x4{0, 0, 0, 0});
      if (lane == 0) {
        conv_store_copies(hb, u32x4{0, 0, 0, 0}, u32x2{0, 0});
        conv_store_copies(hb + 16, u32x4{0, 0, 0, 0}, u32x2{hc[0][0], hc[0][1]});
      }
#pragma unroll
      for (int i = 0; i < 4; ++i) lds_st128(ub + (u32)(lane + 64 * i) * 16, uc[i]);
      lflag_set(L_CONV + u * 4, 1);
    }
    {
      const int e = lane < 28 ? lane : 27;
      gflag_wait(ws + WS_FH + (tau * 64 + 4 * (e >> 1) + (e & 1) + 2 * u) * 8, tag);
      lflag_set(L_H + u * 4, 1);
    }
    return;
  }

  const int cw = wid - 4, th = cw & 1, fq = cw >> 1;
  const int q4 = l15 >> 2, p4 = lane & 3;
  {
    const u32 aA = 16384 + (u32)((8 * g + q4) * 256 + (((2 * fq) ^ (q4 | ((g & 1) << 2))) << 5) + p4 * 8);
    const u32 aB = (u32)((64 * th + l15) * 128 + ((g ^ (l15 >> 1)) << 4));
    f32x4 acc[2][2][4] = {};
    s16x8 fa[2][4], fb[2][4];
    u32 pa0, pa1, pb0, pb1;
    auto setslot = [&](u32 slot) { pa0 = opq(aA + slot); pa1 = opq((aA ^ 32u) + slot); pb0 = opq(aB + slot); pb1 = opq((aB ^ 64u) + slot); };
    auto rd = [&](int set) {
#pragma unroll
      for (int j = 0; j < 4; ++j) fb[set][j] = lds_b128(set ? pb1 : pb0, j * 2048);
#pragma unroll
      for (int mi = 0; mi < 4; ++mi) {
        const int o = (mi >> 1) * 16384 + set * 8192;
        fa[set][mi] = lds_tr2((mi & 1) ? pa1 : pa0, o, o + 1024);
      }
    };
    auto mm = [&](int set) {
#pragma unroll
      for (int mi = 0; mi < 4; ++mi)
#pragma unroll
        for (int j = 0; j < 4; ++j) acc[mi >> 1][mi & 1][j] = mfma16(fa[set][mi], fb[set][j], acc[mi >> 1][mi & 1][j]);
    };
    auto inter = [&]() {
#pragma unroll
      for (int i = 0; i < 12; ++i) { SGB(0x008, 1); SGB(0x100, 1); }
      SGB(0x008, 4);
      SCHED0();
    };
    setslot(0);
#pragma unroll
    for (int i = 0; i < 16; ++i) asm volatile("" : "+v"(acc[i >> 3][(i >> 2) & 1][i & 3]));
    BAR();
    lflag_wait(L_FULL, 0, 1);
    rd(0);
    SCHED0();
    u32 slot = 0;
#pragma unroll 1
    for (int c = 0; c < 7; ++c) {
      rd(1); mm(0); inter();
      lflag_set(L_FREE + cw * 4, c + 1);
      slot = slot == 2 * SLOT ? 0 : slot + SLOT;
      setslot(slot);
      if (c < 6) lflag_wait(L_FULL + ((c + 1) & 1) * 4, 0, c + 2);
      SCHED0();
      rd(0); mm(1); inter();
    }
    lflag_wait(L_EPI, 1, 1);
    f32x4 cs[4];
#pragma unroll
    for (int mi = 0; mi < 4; ++mi) cs[mi] = *(LDSP(f32x4))(L_CSUM + (u32)((mi >> 1) * 128 + 32 * fq + 16 * (mi & 1) + 4 * g) * 4);
    const u32 hdst = WS_H + (u32)(((2 * panel + th) * 56 + 4 * ft + fq) * 4) * 1024 + lane * 16;
#pragma unroll
    for (int j = 0; j < 4; ++j) {
      const f32x2 st = *(LDSP(f32x2))(L_STAT + (u32)(64 * th + 16 * j + l15) * 8);
      const float nm = -st[0], rsd = st[1];
      const float c1 = -1.44269504f * rsd, irs2 = __builtin_amdgcn_rcpf(rsd * rsd);
      float hv[8];
#pragma unroll
      for (int i = 0; i < 2; ++i)
#pragma unroll
        for (int e = 0; e < 4; ++e) {
          float a = __builtin_fmaf(nm, cs[i][e], acc[0][i][j][e]);
          float bb = __builtin_fmaf(nm, cs[2 + i][e], acc[1][i][j][e]);
          float ex = __builtin_amdgcn_exp2f(a * c1);
          float rr = __builtin_amdgcn_rcpf(__builtin_fmaf(ex, irs2, irs2));
          hv[i * 4 + e] = a * bb * rr;
        }
      st128_dev(rs, hdst + j * 1024, u32x4{pk2(hv[0], hv[1]), pk2(hv[2], hv[3]), pk2(hv[4], hv[5]), pk2(hv[6], hv[7])});
    }
  }
  char* hflag = ws + WS_FH + ((2 * panel + th) * 64 + 4 * ft + fq) * 8;
  u32x4 xv, sv;
  if (cw < 4) {
    const int ht = (int)opq(threadIdx.x) & 255, lane = ht & 63, l15 = ht & 15;
    const u64 tag = *(volatile LDSP(u64))L_TAG;
    const int ch = cw >> 1, var = cw & 1;
    const float Dd = bflo(*(const u16*)(ssmD + (d0 + ch) * 2));
    lflag_wait(L_CONV + ch * 4, 0, 1);
    if (var) conv_compute<1>(lane, ch, d0 + ch, Dd, rs, hflag, tag); else conv_compute<0>(lane, ch, d0 + ch, Dd, rs, hflag, tag);
    gflag_publish(ws + WS_FY + (var * 448 + d0 + ch) * 8, tag, lane);
    u32x4 xr[2]; u32x2 st[2];
#pragma unroll
    for (int i = 0; i < 2; ++i) {
      const int n = ht + 256 * i, row = n >> 3, c = n & 7;
      xr[i] = *(const u32x4*)(x + (size_t)(64 * tau + row) * (D * 2) + (64 * dl + 8 * c) * 2);
      st[i] = ld64_dev(rs, WS_STAT + (u32)(64 * tau + row) * 8);
    }
    const int T = tau >> 2, yvar = (T >= 2 && T <= 5) ? 1 : 0;
    gflag_wait(ws + WS_FY + (yvar * 448 + 64 * dl + 16 * cw + l15) * 8, tag);
#pragma unroll
    for (int i = 0; i < 2; ++i) {
      const int row = 16 * cw + 8 * i + (lane >> 3), c = lane & 7;
      u32x4 yv = ld128_dev(rs, WS_YT + (u32)((64 * dl + row) * (S * 2) + (64 * tau + 8 * c) * 2));
      lds_st128(L_USCR + (u32)(row * 128 + ((c ^ (row >> 3)) << 4)), yv);
    }
    lflag_set(L_ADONE + cw * 4, 1);
    lflag_wait(L_ADONE, 3, 1);
#pragma unroll
    for (int i = 0; i < 2; ++i) {
      const int n = ht + 256 * i, tok = n >> 3, dg = n & 7;
      const float mb = __uint_as_float(st[i][0]), rsd = __uint_as_float(st[i][1]);
      u32x4 so;
#pragma unroll
      for (int e = 0; e < 4; ++e) {
        const u32 ya = L_USCR + (u32)((8 * dg + 2 * e) * 128 + (((tok >> 3) ^ dg) << 4) + (tok & 7) * 2);
        const float y0 = bflo(*(LDSP(u16))ya), y1 = bflo(*(LDSP(u16))(ya + 128));
        const u32 dd = pk2(bflo(xr[i][e]) - mb, bfhi(xr[i][e]) - mb);
        const u32 xn = pk2(bflo(dd) * rsd, bfhi(dd) * rsd);
        so[e] = pk2(bflo(xn) * y0, bfhi(xn) * y1);
      }
      if (i == 0) { xv = xr[0]; sv = so; }
      else { lds_st128(E_S + (u32)ht * 16, so); lds_st128(E_X + (u32)ht * 16, xr[1]); }
    }
  } else {
    const int j = cw - 4;
    gflag_publish(hflag, *(volatile LDSP(u64))L_TAG, lane);
    const u32 o2 = (u32)((lane >> 3) * (D * 2) + ((((lane & 7) >> 1) ^ g) << 5) + ((lane & 1) << 4));
    const u32 hl = lane * 16;
    const char* hbase = ws + WS_H + (size_t)tau * 56 * 4096;
    const char* wbase = w2 + dl * 128;
    const u32 ta = (u32)((4 * g + q4) * 128 + (((2 * g + (q4 >> 1)) & 3) << 5) + p4 * 8);
    s16x8 fa[2][4], fb[3][4];
    f32x4 acc[4][4] = {};
    auto ldh = [&](int i) {
      const char* p = hbase + (size_t)g2_ks(j, i) * 4096;
      asm volatile("global_load_dwordx4 %0, %4, %5 sc1\n\tglobal_load_dwordx4 %1, %4, %5 offset:1024 sc1\n\t"
                   "global_load_dwordx4 %2, %4, %5 offset:2048 sc1\n\tglobal_load_dwordx4 %3, %4, %5 offset:3072 sc1"
                   : "=&v"(fb[i % 3][0]), "=&v"(fb[i % 3][1]), "=&v"(fb[i % 3][2]), "=&v"(fb[i % 3][3])
                   : "v"(hl), "s"(p) : "memory");
    };
    u32 tb[4];
#pragma unroll
    for (int a = 0; a < 4; ++a) tb[a] = opq((ta ^ (u32)(a << 5)) + g2_place(j, 0));
    auto rda = [&](int i) {
#pragma unroll
      for (int a = 0; a < 4; ++a) {
        if (i > 0 && i % 3 == 0) tb[a] = opq(i % 6 ? tb[a] - 2 * SLOT : tb[a] + 2 * SLOT);
        fa[i & 1][a] = lds_tr2(tb[a], (i % 3) * 4096, (i % 3) * 4096 + 2048);
      }
    };
    lflag_wait(L_H, 0, 1);
    ldh(0); ldh(1); ldh(2);
    lflag_wait(L_W2, 0, 1);
    rda(0);
    SCHED0();
#pragma unroll
    for (int i = 0; i < 14; ++i) {
      constexpr int cnt[14] = {8, 12, 16, 16, 16, 16, 16, 16, 16, 12, 8, 8, 4, 0};
      if (i == 2) lflag_wait(L_W2 + 4, 0, 1);
      asm volatile("s_waitcnt vmcnt(%4)" : "+v"(fb[i % 3][0]), "+v"(fb[i % 3][1]), "+v"(fb[i % 3][2]), "+v"(fb[i % 3][3]) : "i"(cnt[i]) : "memory");
      SCHED0();
      if (i < 13) rda(i + 1);
#pragma unroll
      for (int a = 0; a < 4; ++a)
#pragma unroll
        for (int t = 0; t < 4; ++t) acc[a][t] = mfma16(fa[i & 1][a], fb[i % 3][t], acc[a][t]);
      if (i < 13) {
#pragma unroll
        for (int n = 0; n < 8; ++n) { SGB(0x008, 1); SGB(0x100, 1); }
        SGB(0x008, 8);
      }
      SCHED0();
      if (i + 6 < 14) {
        const char* src = wbase + (size_t)g2_ks(j, i + 6) * 32 * (D * 2);
        const u32 dst = g2_place(j, i % 6);
#pragma unroll
        for (int pc = 0; pc < 4; ++pc) glds16(o2, src + pc * 8 * (D * 2), dst + pc * 1024);
      }
      if (i == 4) { lflag_wait(L_H + 4, 0, 1); }
      if (i + 3 < 14) ldh(i + 3);
      SCHED0();

    }
    const int ln = (int)opq(threadIdx.x), lt = ln & 15, lg = (ln >> 4) & 3;
#pragma unroll
    for (int t = 0; t < 4; ++t)
#pragma unroll
      for (int a = 0; a < 4; ++a)
        *(LDSP(f32x4))(g2_place(j, t < 2 ? 0 : 3) + (u32)((16 * (t & 1) + lt) * 256 + (((4 * a + lg) ^ (lt & 7)) << 4))) = acc[a][t];
  }
  WAIT_LGKM0();
  BAR();
  {
    const int ht = (int)opq(threadIdx.x) & 255;
    const int half = cw >> 2, tk = ht >> 3, dg = ht & 7;
    if (half) { sv = *(LDSP(u32x4))(E_S + (u32)ht * 16); xv = *(LDSP(u32x4))(E_X + (u32)ht * 16); }
    f32x4 s0 = {}, s1 = {};
#pragma unroll
    for (int w = 0; w < 4; ++w) {
      const u32 pb = (half ? 0u : 2 * SLOT) + (u32)w * 12288 + (u32)tk * 256;
      s0 += *(LDSP(f32x4))(pb + (u32)(((2 * dg) ^ (tk & 7)) << 4));
      s1 += *(LDSP(f32x4))(pb + (u32)(((2 * dg + 1) ^ (tk & 7)) << 4));
    }
    float o[8];
#pragma unroll
    for (int e = 0; e < 8; ++e) {
      const float f = rbf(e < 4 ? s0[e & 3] : s1[e & 3]);
      const float gt = rbf(silu_f(f));
      const float xe = (e & 1) ? bfhi(xv[e >> 1]) : bflo(xv[e >> 1]);
      const float se = (e & 1) ? bfhi(sv[e >> 1]) : bflo(sv[e >> 1]);
      o[e] = xe + rbf(se * gt);
    }
    *(u32x4*)(out + (size_t)(64 * tau + 32 * half + tk) * (D * 2) + (64 * dl + 8 * dg) * 2) =
        u32x4{pk2(o[0], o[1]), pk2(o[2], o[3]), pk2(o[4], o[5]), pk2(o[6], o[7])};
  }
}
}

extern "C" void kernel_launch(void* const* d_in, const int* in_sizes, int n_in, void* d_out, int out_size, void* d_ws,
                              size_t ws_size, hipStream_t stream) {
  (void)hipFuncSetAttribute((const void*)fused_kernel, hipFuncAttributeMaxDynamicSharedMemorySize, 163840);
  fused_kernel<<<256, 768, 163840, stream>>>((const char*)d_in[0], (const char*)d_in[1], (const char*)d_in[2], (const char*)d_in[3],
                                             (const char*)d_in[4], (const char*)d_in[5], (const char*)d_in[7], (const char*)d_in[8],
                                             (const char*)d_in[9], (char*)d_ws, (char*)d_out);
}
```

```cpp
#include <hip/hip_runtime.h>
#include <hip/hip_bf16.h>
#include <stdint.h>
#pragma clang diagnostic ignored "-Wint-to-pointer-cast"

#if defined(__HIP_DEVICE_COMPILE__) && __HIP_DEVICE_COMPILE__
#pragma clang attribute push(__attribute__((target("no-packed-fp32-ops"))), apply_to = function)
#endif
namespace {
typedef unsigned short u16;
typedef unsigned int u32;
typedef unsigned long long u64;
typedef __attribute__((ext_vector_type(4))) short s16x4;
typedef __attribute__((ext_vector_type(8))) short s16x8;
typedef __attribute__((ext_vector_type(8))) __bf16 bf16x8;
typedef __attribute__((ext_vector_type(2))) __bf16 bf16x2;
typedef __attribute__((ext_vector_type(16))) float f32x16;
typedef __attribute__((ext_vector_type(4))) float f32x4;
typedef __attribute__((ext_vector_type(2))) float f32x2;
typedef __attribute__((ext_vector_type(4))) u32 u32x4;
typedef __attribute__((ext_vector_type(2))) u32 u32x2;
#define LDSP(T) __attribute__((address_space(3))) T*
#define GLBP(T) __attribute__((address_space(1))) T*
#define DEV __device__ __forceinline__

constexpr int S = 2048, D = 448, F = 1792;
constexpr u32 WS_H = 0;
constexpr u32 WS_UT = WS_H + 7340032;
constexpr u32 WS_YT = WS_UT + 1835008;
constexpr u32 WS_STAT = WS_YT + 1835008;
constexpr u32 WS_CSUM = WS_STAT + 16384;
constexpr u32 WS_E = WS_CSUM + 16384;
constexpr u32 WS_FUT = WS_E + 128;
constexpr u32 WS_FST = WS_FUT + 448 * 128;
constexpr u32 WS_FY = WS_FST + 32 * 128;
constexpr u32 WS_FH = WS_FY + 896 * 128;
constexpr u32 WS_END = WS_FH + 1792 * 128;
constexpr int CODE_LINES = 224;
constexpr u32 SLOT = 49152;
constexpr u32 MISC = 3 * SLOT;
constexpr u32 L_FULL = MISC, L_EPI = MISC + 8, L_H = MISC + 16, L_FREE = MISC + 32, L_CONV = MISC + 64, L_FED = MISC + 80,
              L_TAG = MISC + 96, L_DD = MISC + 104, L_ADONE = MISC + 112, L_PROG = MISC + 128, L_STAT = MISC + 256,
              L_CSUM = MISC + 1280, L_USCR = MISC + 4096, L_DUMMY = MISC + 12288;
constexpr u32 CV = SLOT, CV_HSTR = 4160, CV_U = CV + 8 * CV_HSTR, CV_USTR = 4608;
constexpr u32 E_S = SLOT, E_X = SLOT + 4096;
constexpr int SPIN_CAP = 1 << 18;
#define ROT1 0
#define ROT2 0
#define TOUCH2_AT 0
#define PACE_L 63
#define PACE_S 8
#define FREE_SET dflag_set
#define W2PF 1
#define KVPF 0
#define KV_AT 1

__device__ const u32 seq_tab[160] __attribute__((aligned(128))) = {0, 1, 2, 3, 4, 5, 6, 7};
DEV float asf(u32 v) { return __builtin_bit_cast(float, v); }
DEV float bflo(u32 v) { return asf(v << 16); }
DEV float bfhi(u32 v) { return asf(v & 0xffff0000u); }
template <int CTRL> DEV float dpp(float v) { return asf(__builtin_amdgcn_update_dpp(0, __builtin_bit_cast(u32, v), CTRL, 0xf, 0xf, true)); }
DEV float sum8(float v) { v += dpp<0xB1>(v); v += dpp<0x4E>(v); return v + dpp<0x141>(v); }
DEV u32 pk2(float lo, float hi) {
  asm("" : "+v"(lo)); asm("" : "+v"(hi));
  bf16x2 r = {(__bf16)lo, (__bf16)hi};
  return __builtin_bit_cast(u32, r);
}
DEV float rbf(float f) { bf16x2 r = {(__bf16)f, (__bf16)f}; return bflo(__builtin_bit_cast(u32, r)); }
DEV f32x4 mfma16(s16x8 a, s16x8 b, f32x4 c) {
  return __builtin_amdgcn_mfma_f32_16x16x32_bf16(__builtin_bit_cast(bf16x8, a), __builtin_bit_cast(bf16x8, b), c, 0, 0, 0);
}
DEV f32x16 mfma32(s16x8 a, s16x8 b, f32x16 c) {
  return __builtin_amdgcn_mfma_f32_32x32x16_bf16(__builtin_bit_cast(bf16x8, a), __builtin_bit_cast(bf16x8, b), c, 0, 0, 0);
}
DEV u32 opq(u32 v) { asm volatile("" : "+v"(v)); return v; }
DEV s16x8 lds_tr2(u32 base, int o0, int o1) {
  s16x4 lo = __builtin_amdgcn_ds_read_tr16_b64_v4i16((LDSP(s16x4))((LDSP(char))base + o0));
  s16x4 hi = __builtin_amdgcn_ds_read_tr16_b64_v4i16((LDSP(s16x4))((LDSP(char))base + o1));
  return __builtin_shufflevector(lo, hi, 0, 1, 2, 3, 4, 5, 6, 7);
}
DEV s16x8 lds_b128(u32 base, int o) { return *(LDSP(s16x8))((LDSP(char))base + o); }
DEV void lds_st128(u32 a, u32x4 v) { *(LDSP(u32x4))a = v; }
DEV void glds16x4(u32 v0, u32 v1, u32 v2, u32 v3, const void* sbase, u32 ldst) {
  asm volatile("s_mov_b32 m0, %5\n\ts_nop 0\n\tglobal_load_lds_dwordx4 %0, %4\n\tglobal_load_lds_dwordx4 %1, %4 offset:1024\n\t"
               "global_load_lds_dwordx4 %2, %4 offset:2048\n\tglobal_load_lds_dwordx4 %3, %4 offset:3072"
               ::"v"(v0), "v"(v1), "v"(v2), "v"(v3), "s"(sbase), "s"(ldst) : "memory");
}
DEV void glds16(u32 voff, const void* sbase, u32 ldst) {
  asm volatile("s_mov_b32 m0, %2\n\ts_nop 0\n\tglobal_load_lds_dwordx4 %0, %1" ::"v"(voff), "s"(sbase), "s"(ldst) : "memory");
}
DEV void glds16x4_lin(u32 v, const void* sbase, u32 ldst) { glds16x4(v, v, v, v, sbase, ldst); }
DEV void glds16x4_dev(u32 v, const void* sbase, u32 ldst) {
  asm volatile("s_mov_b32 m0, %2\n\ts_nop 0\n\tglobal_load_lds_dwordx4 %0, %1 sc1\n\tglobal_load_lds_dwordx4 %0, %1 offset:1024 sc1\n\t"
               "global_load_lds_dwordx4 %0, %1 offset:2048 sc1\n\tglobal_load_lds_dwordx4 %0, %1 offset:3072 sc1"
               ::"v"(v), "s"(sbase), "s"(ldst) : "memory");
}
DEV void glds16_dev(u32 voff, const void* sbase, u32 ldst) {
  asm volatile("s_mov_b32 m0, %2\n\ts_nop 0\n\tglobal_load_lds_dwordx4 %0, %1 sc1" ::"v"(voff), "s"(sbase), "s"(ldst) : "memory");
}
DEV void dflag_set(u32 addr, u32 val) {
  asm volatile("s_waitcnt lgkmcnt(0)\n\ts_mov_b32 m0, %2\n\ts_mov_b64 exec, 1\n\tglobal_load_lds_dword %0, %1\n\ts_mov_b64 exec, -1"
               ::"v"(0), "s"(seq_tab + val), "s"(addr) : "memory");
}
#define WAIT_VM(n) asm volatile("s_waitcnt vmcnt(%0)" ::"i"(n) : "memory")
#define WAIT_LGKM0() asm volatile("s_waitcnt lgkmcnt(0)" ::: "memory")
#define BAR() __builtin_amdgcn_s_barrier()
#define SCHED0() __builtin_amdgcn_sched_barrier(0)
#define SGB(mask, n) __builtin_amdgcn_sched_group_barrier(mask, n, 0)
DEV float silu_f(float x) { return x * __builtin_amdgcn_rcpf(1.0f + __builtin_amdgcn_exp2f(-1.44269504f * x)); }

DEV void lflag_set(u32 addr, u32 val) {
  asm volatile("s_waitcnt lgkmcnt(0)\n\tds_write_b32 %0, %1" ::"v"(addr), "v"(val) : "memory");
}
DEV void lflag_wait(u32 addr, u32 mask, u32 val) {
  const u32 a = addr + 4 * (threadIdx.x & mask);
#pragma unroll 1
  for (int i = 0; i < SPIN_CAP; ++i) {
    u32 v;
    asm volatile("ds_read_b32 %0, %1\n\ts_waitcnt lgkmcnt(0)" : "=v"(v) : "v"(a) : "memory");
    if (__builtin_amdgcn_ballot_w64(v < val) == 0) break;
    __builtin_amdgcn_s_sleep(1);
  }
}
DEV u64 gload64(const char* p) { return __hip_atomic_load((GLBP(u64))p, __ATOMIC_RELAXED, __HIP_MEMORY_SCOPE_AGENT); }
DEV void gstore64(char* p, u64 v) { __hip_atomic_store((GLBP(u64))p, v, __ATOMIC_RELAXED, __HIP_MEMORY_SCOPE_AGENT); }
template <int SLEEP>
DEV void gflag_wait(const char* lane_ptr, u64 tag) {
#pragma unroll 1
  for (int i = 0; i < SPIN_CAP; ++i) {
    asm volatile("" ::: "memory");
    if (__builtin_amdgcn_ballot_w64(gload64(lane_ptr) != tag) == 0) break;
    __builtin_amdgcn_s_sleep(SLEEP);
  }
  asm volatile("" ::: "memory");
}
DEV void gflag_publish(char* p, u64 tag, int lane) {
  WAIT_VM(0);
  if (lane == 0) gstore64(p, tag);
}
DEV u64 tag_of(u64 seed) { return seed * 0x9E3779B97F4A7C15ull + 0xD1B54A32D192ED03ull; }
DEV u64 make_tag(const char* ws) { return tag_of(gload64(ws + WS_E)); }
typedef __amdgpu_buffer_rsrc_t rsrc_t;
DEV rsrc_t ws_rsrc(char* ws) { return __builtin_amdgcn_make_buffer_rsrc(ws, 0, WS_END, 0x00020000); }
DEV void st128_dev(rsrc_t r, u32 off, u32x4 v) { __builtin_amdgcn_raw_buffer_store_b128(v, r, off, 0, 16); }
DEV void st64_dev(rsrc_t r, u32 off, u32x2 v) { __builtin_amdgcn_raw_buffer_store_b64(v, r, off, 0, 16); }
DEV void st32_dev(rsrc_t r, u32 off, u32 v) { __builtin_amdgcn_raw_buffer_store_b32(v, r, off, 0, 16); }
DEV u32x4 ld128_dev(rsrc_t r, u32 off) { return __builtin_amdgcn_raw_buffer_load_b128(r, off, 0, 16); }
DEV u32x2 ld64_dev(rsrc_t r, u32 off) { return __builtin_amdgcn_raw_buffer_load_b64(r, off, 0, 16); }
DEV void touch(u32 voff, const void* sbase, u32 ldst) {
  asm volatile("s_mov_b32 m0, %2\n\ts_nop 0\n\tglobal_load_lds_dword %0, %1" ::"v"(voff), "s"(sbase), "s"(ldst) : "memory");
}

DEV void glds4_dev(u32 voff, const void* sbase, u32 ldst) {
  asm volatile("s_mov_b32 m0, %2\n\ts_nop 0\n\tglobal_load_lds_dword %0, %1 sc1" ::"v"(voff), "s"(sbase), "s"(ldst) : "memory");
}
DEV u32 shr16(u32 hi, u32 lo) { return __builtin_amdgcn_alignbit(hi, lo, 16); }

template <int VAR>
DEV void conv_compute(int lane, int ch, int d, rsrc_t rs, char* hflag, u64 tag, bool pub) {
  constexpr int NP = VAR == 0 ? 8 : 6;
  constexpr int T[4] = {VAR == 0 ? 7 : 5, VAR == 0 ? 6 : 4, VAR == 0 ? 1 : 3, VAR == 0 ? 0 : 2};
  const int m = lane & 15, g = lane >> 4, q4 = m >> 2, p4 = lane & 3;
  u32 aa = CV + (u32)(ch * 4 + ((q4 + 1) & 3)) * CV_HSTR + (u32)(((q4 + 1) & 4) + 8 * g + 4 * p4) * 2;
  u32 ba = CV_U + (u32)ch * CV_USTR + (u32)(240 - 16 * m + 8 * g) * 2;
  f32x4 acc[4] = {};
  s16x8 A[2][NP], B[2][NP];
  auto rd = [&](int set, int off) {
#pragma unroll
    for (int p = 0; p < NP; ++p) {
      A[set][p] = lds_tr2(aa, off + p * 512, off + p * 512 + 8);
      B[set][NP - 1 - p] = lds_b128(ba, off + (7 - (NP - 1 - p)) * 512);
    }
  };
  auto mm = [&](int set) {
#pragma unroll
    for (int p = 0; p < NP; ++p)
#pragma unroll
      for (int i = 0; i < 2; ++i)
        if (p <= T[i]) acc[i] = mfma16(A[set][p], B[set][T[i] - p], acc[i]);
#pragma unroll
    for (int i = 2; i < 4; ++i)
#pragma unroll
      for (int p = 0; p <= T[i]; ++p) acc[i] = mfma16(A[set][p], B[set][T[i] - p], acc[i]);
  };
  auto inter = [&]() {
#pragma unroll
    for (int i = 0; i < NP; ++i) { SGB(0x008, 1); SGB(0x100, 2); SGB(0x008, 1); SGB(0x100, 1); }
    SGB(0x008, 18 - 2 * NP);
    SCHED0();
  };
  aa = opq(aa); ba = opq(ba);
  rd(0, 0);
  {
    u32x4 f = __builtin_bit_cast(u32x4, A[0][0]);
#pragma unroll
    for (int i = 0; i < 4; ++i) { const int e = m + 8 * g + 2 * i - 15; f[i] &= e >= 0 ? ~0u : (e == -1 ? 0xffff0000u : 0u); }
    A[0][0] = __builtin_bit_cast(s16x8, f);
  }
  SCHED0();
#pragma unroll 1
  for (int it = 0; it < 4; ++it) {
    rd(1, 64); mm(0); inter();
    if (it == 0 && pub) gflag_publish(hflag, tag, lane);
    rd(0, 128); mm(1); inter();
    aa = opq(aa + 128); ba = opq(ba + 128);
  }
  const u32 dd = *(LDSP(u32))L_DD;
  const float Dd = ch ? bfhi(dd) : bflo(dd);
#pragma unroll
  for (int i = 0; i < 4; ++i) {
    const int t = 256 * T[i] + 16 * m + 4 * g;
    u32x2 uu = *(LDSP(u32x2))(CV_U + (u32)ch * CV_USTR + (u32)(2044 - t) * 2);
    float y0 = rbf(acc[i][0]) + rbf(bfhi(uu[1]) * Dd), y1 = rbf(acc[i][1]) + rbf(bflo(uu[1]) * Dd);
    float y2 = rbf(acc[i][2]) + rbf(bfhi(uu[0]) * Dd), y3 = rbf(acc[i][3]) + rbf(bflo(uu[0]) * Dd);
    st64_dev(rs, WS_YT + (u32)(d * (S * 2) + t * 2), u32x2{pk2(y0, y1), pk2(y2, y3)});
  }
}

DEV int rot7(int i, int rot) { const int v = i + rot; return v >= 7 ? v - 7 : v; }
DEV int g2_ks(int j, int i, int rot) { const int e = 7 * j + rot7(i < 7 ? i : i - 7, rot); return 4 * (e >> 1) + (e & 1) + (i < 7 ? 0 : 2); }
DEV u32 g2_place(int j, int p) { return (p < 3 ? 2 * SLOT + (u32)p * 4096 : (u32)(p - 3) * 4096) + (u32)j * 12288; }
DEV u32 w2_voff(int lane) { return (u32)((lane >> 3) * (D * 2) + (((lane & 7) ^ (((lane >> 4) & 1) << 2)) << 4)); }

DEV void share(int c0, int k, int panel, int ft, const char* base) {
  const int lane = (int)opq(threadIdx.x) & 63;
  const int tc = c0 + (lane >> 4) < 7 ? c0 + (lane >> 4) : 6;
  const int xp = (lane & 8) ? (ft < 2 ? 14 + ft : ft) : ft;
  const u32 tw = (u32)((64 * tc + 4 * panel + (lane & 3)) * (F * 2) + ft * 256 + ((lane >> 2) & 1) * 128);
  const u32 tx = (u32)((panel * 128 + 8 * xp + (lane & 7)) * (D * 2) + tc * 128);
  touch(k == 0 ? tx : tw, base, L_DUMMY);
}

__global__ __launch_bounds__(768, 3) void fused_kernel(const char* x, const char* kp, const char* vp, const char* skp,
                                                       const char* ssmk, const char* ssmD, const char* w1, const char* w3,
                                                       const char* w2, char* ws, char* out) {
  u64 pc0;
  asm volatile("s_getpc_b64 %0" : "=s"(pc0));
  const int b = blockIdx.x, xcd = b & 7, r = b >> 3;
  const int wid = __builtin_amdgcn_readfirstlane(threadIdx.x >> 6);
  const int panel = (xcd >> 1) * 4 + r / 7, ft = (xcd & 1) * 7 + r % 7;
  const int tau = xcd * 4 + r / 7, dl = r % 7;
  const int d0 = 2 * b;
  const int rot = ROT1 ? (r % 7 + 2 * (r / 7)) % 7 : 0, rot2 = ROT2 ? (r % 7 + 2 * (r / 7)) % 7 : 0;

  if (b < 224 && wid >= 2) {
    const char* sb = (wid & 3) == 0 ? x : ((wid & 3) == 1 ? w1 : w3);
    if (wid >= 4 && wid < 7) share(0, wid & 3, panel, ft, sb);
    if (wid == 7) {
      const int lane = (int)opq(threadIdx.x) & 63;
      if (lane < 5) touch((u32)lane * 128, seq_tab, L_DUMMY);
      if (false) touch((u32)(r * (CODE_LINES / 28) + lane) * 128, (const char*)pc0, L_DUMMY);
      *(LDSP(u32))(MISC + lane * 4) = 0;
      WAIT_LGKM0();
    }
    BAR();
    if (wid >= 8 && wid < 11) share(4, wid & 3, panel, ft, sb);
  }

  if (__builtin_expect(wid < 4 && b < 224, 1)) {
    const int lane = (int)opq(threadIdx.x) & 63;
    u64 tag = 0;
    if (__builtin_expect(wid < 2, 1)) {
      const int g = lane >> 4, l15 = lane & 15;
      u32 vx[8], vw[8];
      const u32 ox = (u32)((lane >> 3) * (D * 2) + (((lane & 7) ^ g) << 4));
      const u32 ow = (u32)(g * (F * 2) + (((l15 >> 2) ^ g) << 6) + ((lane & 3) << 4));
#pragma unroll
      for (int i = 0; i < 8; ++i) {
        vx[i] = (ox ^ (u32)((i & 1) * 64)) + i * 8 * (D * 2) - (i & 3) * 1024;
        vw[i] = ow + i * 4 * (F * 2) - (i & 3) * 1024;
      }
#pragma unroll 1
      for (int c = 0; c < 7; ++c) {
        if (c >= 3) BAR();
        const u32 slot = (u32)(c % 3) * SLOT + wid * 8192;
        const int kc = rot7(c, rot);
        const char* xb = x + (size_t)(panel * 128 + wid * 64) * (D * 2) + kc * 128;
        const char* w1b = w1 + (size_t)(kc * 64 + wid * 32) * (F * 2) + ft * 256;
        const char* w3b = w3 + (size_t)(kc * 64 + wid * 32) * (F * 2) + ft * 256;
#pragma unroll
        for (int i = 0; i < 8; i += 4) {
          WAIT_VM(PACE_L);
          glds16x4(vx[i], vx[i + 1], vx[i + 2], vx[i + 3], xb, slot + i * 1024);
          WAIT_VM(PACE_L);
          glds16x4(vw[i], vw[i + 1], vw[i + 2], vw[i + 3], w1b, slot + 16384 + i * 1024);
          WAIT_VM(PACE_L);
          glds16x4(vw[i], vw[i + 1], vw[i + 2], vw[i + 3], w3b, slot + 32768 + i * 1024);

        }
        if (c == 0) BAR();
        dflag_set(L_FULL + wid * 4, c + 1);
      }
      if (wid == 0) {
        tag = make_tag(ws);
        const int k = lane & 7;
        gflag_wait<8>(ws + WS_FST + (k < 2 ? 2 * panel + k : (k == 2 ? tau : (k & 1) * 14 + ft)) * 128, tag);
        glds16_dev(lane * 16, ws + WS_STAT + panel * 1024, L_STAT);
        glds16_dev(lane * 16, ws + WS_CSUM + ft * 1024, L_CSUM);
        if (lane < 2) glds4_dev(lane * 4, ws + WS_E, L_TAG);
        if (lane < 1) glds4_dev(0, ssmD + d0 * 2, L_DD);
        dflag_set(L_EPI, 1);
      }
    } else {
      __builtin_amdgcn_s_setprio(3);
      const rsrc_t rs = ws_rsrc(ws);
      const int u = wid - 2;
      const int ti = b / 7, cj = b % 7;
      const int tg = lane >> 3, cg = lane & 7;
      const int t0 = 64 * ti + 32 * u + 4 * tg, ch0 = 64 * cj + 8 * cg;
      u32x4 kr[7], vr[4], sw[4]; u32x2 o[8];
      u64 seed;
      auto ldkv = [&]() {
        const u32 ko = (u32)(t0 * (D * 2) + ch0 * 2);
#pragma unroll
        for (int i = 0; i < 7; ++i) kr[i] = *(const u32x4*)(kp + (i < 3 && t0 == 0 ? ko : ko + (u32)((i - 3) * (D * 2))));
#pragma unroll
        for (int i = 0; i < 4; ++i) vr[i] = *(const u32x4*)(vp + (ko + (u32)(i * (D * 2))));
        SCHED0();
      };
      auto piece = [&](int p) {
#pragma unroll
        for (int c = 2 * p; c < 2 * p + 2; ++c) {
          const u32 wa = sw[p][(c & 1) * 2], wb = sw[p][(c & 1) * 2 + 1];
          const float w0 = bflo(wa), w1f = bfhi(wa), w2f = bflo(wb), w3f = bfhi(wb);
          float kf[7], ks[4];
#pragma unroll
          for (int i = 0; i < 7; ++i) kf[i] = (c & 1) ? bfhi(kr[i][p]) : bflo(kr[i][p]);
#pragma unroll
          for (int i = 0; i < 4; ++i)
            ks[i] = __builtin_fmaf(kf[i + 3], w0, __builtin_fmaf(kf[i + 2], w1f, __builtin_fmaf(kf[i + 1], w2f, kf[i] * w3f)));
#pragma unroll
          for (int i = 0; i < 2; ++i) {
            const int ta = 3 - 2 * i, tb = 2 - 2 * i;
            const u32 kk = pk2(ks[ta], ks[tb]);
            o[c][i] = pk2(bflo(kk) * ((c & 1) ? bfhi(vr[ta][p]) : bflo(vr[ta][p])), bfhi(kk) * ((c & 1) ? bfhi(vr[tb][p]) : bflo(vr[tb][p])));
          }
          asm volatile("" : "+v"(o[c]));
        }
        SCHED0();
      };
#pragma unroll
      for (int i = 0; i < 4; ++i) sw[i] = *(const u32x4*)(skp + (u32)(ch0 * 8 + i * 16));
      seed = gload64(ws + WS_E + opq(0));
      SCHED0();
      if (KV_AT < 0) { ldkv(); }
#pragma unroll
      for (int s = 0; s <= 4; ++s) {
        if (s > 0) BAR();
        SCHED0();
        if (s == 4 && lane < 28) touch((u32)(((d0 + u) * 28 + lane) * 128), w2, L_DUMMY);
        if (s == 4 && lane >= 32) touch((u32)((d0 + u) * (S * 2) + (lane - 32) * 128), ssmk, L_DUMMY);
        if (s == KV_AT) { ldkv(); }
        if (s == KV_AT + 2) {
#pragma unroll
          for (int i = 0; i < 3; ++i) if (t0 == 0) kr[i] = u32x4{0, 0, 0, 0};
        }
        if (s >= KV_AT + 2 && s < 4) piece(s - KV_AT - 2);
      }
#pragma unroll
      for (int c = 2 - KV_AT; c < 4; ++c) piece(c);
      tag = tag_of(seed);
      const u32 scr = L_USCR + (u32)u * 4096;
#pragma unroll
      for (int c = 0; c < 8; ++c) *(LDSP(u32x2))(scr + (u32)((cg * 8 + c) * 64 + (((7 - tg) ^ (cg & 6)) << 3))) = o[c];
      WAIT_LGKM0();
#pragma unroll
      for (int i = 0; i < 4; ++i) {
        const int dd = 16 * i + (lane >> 2), c4 = lane & 3;
        u32x4 v = *(LDSP(u32x4))(scr + (u32)(dd * 64 + ((c4 ^ (dd >> 4)) << 4)));
        st128_dev(rs, WS_UT + (u32)((64 * cj + dd) * (S * 2) + (2016 - 64 * ti - 32 * u) * 2 + c4 * 16), v);
      }
      gflag_publish(ws + WS_FUT + (cj * 64 + 2 * ti + u) * 128, tag, lane);
    }
    const int j = wid;
    const u32 o2 = w2_voff(lane);
    const char* wbase = w2 + dl * 128;
    u32x4 pk[8][4];
    auto park = [&](int i0) {
#pragma unroll
    for (int i = i0; i < i0 + 4; ++i) {
      const char* src = wbase + (size_t)g2_ks(j, i + 6, rot2) * 32 * (D * 2);
      WAIT_VM(PACE_S);
#pragma unroll
      for (int pc = 0; pc < 4; ++pc) pk[i][pc] = *(const u32x4*)(src + (o2 + (u32)(pc * 8 * (D * 2))));
    }
    SCHED0();
    };
    park(0);
    if (wid == 2) {
        lflag_wait(L_FREE, 7, 5);
        if (lane < 32) {
          glds16(lane * 16, seq_tab + 32, CV_U + 4096);
          glds16(lane * 16, seq_tab + 32, CV_U + CV_USTR + 4096);
        }
        gflag_wait<8>(ws + WS_FUT + ((d0 >> 6) * 64 + lane) * 128, tag);
        glds16x4_dev(lane * 16, ws + WS_UT + d0 * (S * 2), CV_U);
        glds16x4_dev(lane * 16, ws + WS_UT + (d0 + 1) * (S * 2), CV_U + CV_USTR);
        dflag_set(L_CONV + 8, 1);
        dflag_set(L_CONV + 12, 1);
      }
    if (wid < 2) {
      lflag_wait(L_FREE, 7, 5);
      const u32 vl = lane * 16;
#pragma unroll 1
      for (int rr = 0; rr < 4; ++rr) {
        const char* src = ssmk + (size_t)(d0 + wid) * (S * 2) + (rr ? (rr - 4) * 2 : 0);
        const u32 dst = CV + (u32)(wid * 4 + rr) * CV_HSTR + (rr ? 24 : 32);
        if (__builtin_expect(d0 + wid == 0 && rr > 0, 0)) {
          glds16x4(lane ? vl : 16u, vl, vl, vl, src, dst);
          WAIT_VM(0);
          const u32x4 c = *(const u32x4*)ssmk;
          const u32 e0 = rr == 3 ? c[0] << 16 : 0u;
          const u32 e1 = rr == 1 ? c[0] << 16 : (rr == 2 ? c[0] : shr16(c[1], c[0]));
          const u32 e2 = rr == 1 ? shr16(c[1], c[0]) : (rr == 2 ? c[1] : shr16(c[2], c[1]));
          const u32 e3 = rr == 1 ? shr16(c[2], c[1]) : (rr == 2 ? c[2] : shr16(c[3], c[2]));
          *(LDSP(u32x2))dst = u32x2{e0, e1};
          *(LDSP(u32x2))(dst + 8) = u32x2{e2, e3};
        } else {
          glds16x4_lin(vl, src, dst);
        }
      }
      dflag_set(L_CONV + wid * 4, 1);
    }
    park(4);
#pragma unroll 1
    for (int i = 0; i < 6; ++i) {
      if (i == 0) lflag_wait(L_FREE, 7, 6);
      if (i == 3) { dflag_set(L_FED + j * 4, 3); lflag_wait(L_FREE, 7, 7); }
      const char* src = wbase + (size_t)g2_ks(j, i, rot2) * 32 * (D * 2);
      WAIT_VM(PACE_S);
      glds16x4(o2, o2 + 8 * (D * 2) - 1024, o2 + 16 * (D * 2) - 2048, o2 + 24 * (D * 2) - 3072, src, g2_place(j, i));
    }
    dflag_set(L_FED + j * 4, 6);
    if (wid == 1) {
      lflag_wait(L_EPI, 0, 1);
      tag = tag_of(*(volatile LDSP(u64))L_TAG);
      const int e = lane < 28 ? lane : 27;
      gflag_wait<4>(ws + WS_FH + (tau * 56 + 4 * (e >> 1) + (e & 1)) * 128, tag);
      dflag_set(L_H, 1);
      gflag_wait<4>(ws + WS_FH + (tau * 56 + 4 * (e >> 1) + (e & 1) + 2) * 128, tag);
      dflag_set(L_H + 4, 1);
    }
    WAIT_VM(0);
    const u32 wl = opq(lane * 16 + j * 12288);
#pragma unroll
    for (int i = 0; i < 8; i += 2) {
      lflag_wait(L_PROG + j * 4, 0, i + 2);
#pragma unroll
      for (int n = 0; n < 8; ++n) lds_st128(wl + g2_place(0, (i + (n >> 2)) % 6) + (n & 3) * 1024, pk[i + (n >> 2)][n & 3]);
      lflag_set(L_FED + j * 4, i + 8);
    }
    return;
  }
  const rsrc_t rs = ws_rsrc(ws);

  if (b >= 224) {
    if (wid >= 8 && b != 255) return;
    const int tid = (int)opq(threadIdx.x), lane = tid & 63;
    const int sp = b - 224;
    const u64 seed = gload64(ws + WS_E + opq(0));
    if (KVPF && wid < 7) {
      touch((u32)(sp * 448 + tid) * 128, kp, L_DUMMY);
      touch((u32)(sp * 448 + tid) * 128, vp, L_DUMMY);
    }
    if (KVPF) BAR();
    if (wid < 8) {
      const int row = 64 * sp + (tid >> 3), c8 = tid & 7;
      const int cu = sp < 28 ? sp : 27, mat = cu / 14, ct = cu % 14, kg = tid >> 4, c16 = tid & 15;
      const char* wp = (mat ? w3 : w1) + ct * 256;
      const u32 wo = (u32)(kg * (F * 2) + c16 * 16), xo = (u32)(row * (D * 2) + c8 * 16);
      u32x4 xv[7], wv[14];
#pragma unroll
      for (int i = 0; i < 7; ++i) xv[i] = *(const u32x4*)(x + (xo + i * 128));
#pragma unroll
      for (int i = 0; i < 14; ++i) wv[i] = *(const u32x4*)(wp + (wo + i * 32 * (F * 2)));
      SCHED0();
      float s = 0.f;
#pragma unroll
      for (int i = 0; i < 7; ++i)
#pragma unroll
        for (int j = 0; j < 4; ++j) s += bflo(xv[i][j]) + bfhi(xv[i][j]);
      const float m = sum8(s) * (1.0f / D);
      float q = 0.f;
#pragma unroll
      for (int i = 0; i < 7; ++i)
#pragma unroll
        for (int j = 0; j < 4; ++j) {
          const float aa = bflo(xv[i][j]) - m, bb = bfhi(xv[i][j]) - m;
          q = __builtin_fmaf(aa, aa, __builtin_fmaf(bb, bb, q));
        }
      const float sd = rbf(__builtin_amdgcn_sqrtf(rbf(rbf(sum8(q) * (1.0f / D)) + 1e-5f)));
      if (c8 == 0) *(LDSP(f32x2))(u32)(16384 + (tid >> 3) * 8) = f32x2{rbf(m), __builtin_amdgcn_rcpf(sd)};
      SCHED0();
      float a[8] = {};
#pragma unroll
      for (int i = 0; i < 14; ++i)
#pragma unroll
        for (int j = 0; j < 4; ++j) { a[2 * j] += bflo(wv[i][j]); a[2 * j + 1] += bfhi(wv[i][j]); }
      f32x4 t;
#pragma unroll
      for (int j = 0; j < 4; ++j) {
        auto p = __builtin_amdgcn_permlane16_swap(__builtin_bit_cast(u32, a[j]), __builtin_bit_cast(u32, a[j + 4]), false, false);
        t[j] = asf(p[0]) + asf(p[1]);
      }
      *(LDSP(f32x4))(u32)((kg >> 1) * 512 + c16 * 32 + (kg & 1) * 16) = t;
    }
    WAIT_LGKM0(); BAR();
    const u64 tag = tag_of(seed);
    if (wid == 0) {
      const int cu = sp < 28 ? sp : 27;
      f32x2 cs = {};
#pragma unroll
      for (int i = 0; i < 16; ++i) cs += *(LDSP(f32x2))(u32)(i * 512 + lane * 8);
      st64_dev(rs, WS_STAT + (u32)(64 * sp + lane) * 8, *(LDSP(u32x2))(u32)(16384 + lane * 8));
      if (sp < 28) st64_dev(rs, WS_CSUM + (u32)((cu % 14) * 1024 + (cu / 14) * 512 + lane * 8), __builtin_bit_cast(u32x2, cs));
      gflag_publish(ws + WS_FST + sp * 128, tag, lane);
    }
    WAIT_VM(0); BAR();
    if (W2PF && wid > 0 && wid < 8) {
#pragma unroll 1
      for (int i = 0; i < 7; ++i) touch((u32)((((sp >> 3) * 7 + i) * 7 + wid - 1) * 64 + lane) * 128, w2, L_DUMMY);
      WAIT_VM(0);
    }
    if (b == 255) {
      gflag_wait<64>(ws + (tid < 480 ? WS_FUT + tid * 128 : WS_FH + ((((tid < 704 ? tid - 480 : 223) / 14) * 2) * 56 + ((tid < 704 ? tid - 480 : 223) % 14) * 4) * 128), tag);
      BAR();
      if (tid == 0) gstore64(ws + WS_E, tag);
    }
    return;
  }

  const int cw = wid - 4, th = cw & 1, fq = cw >> 1;
  {
    const int lane = (int)opq(threadIdx.x) & 63, g = lane >> 4, hh = lane >> 5, l31 = lane & 31;
    const int q4 = (lane >> 2) & 3, p4 = lane & 3;
    const u32 aA = 16384 + (u32)((8 * hh + q4) * 256 + ((fq ^ q4) << 6) + (g & 1) * 32 + p4 * 8);
    const u32 aB = (u32)((64 * th + l31) * 128 + ((hh ^ ((l31 >> 1) & 7)) << 4));
    f32x16 acc[2][2] = {};
    s16x8 fa[2][2][2], fb[2][2][2];
    u32 pa, pb[4];
    auto setslot = [&](u32 slot) {
      pa = opq(aA + slot);
#pragma unroll
      for (int k = 0; k < 4; ++k) pb[k] = opq((aB ^ (u32)(k << 5)) + slot);
    };
    auto rd = [&](int set) {
#pragma unroll
      for (int k = 0; k < 2; ++k) {
        const int ks = 2 * set + k;
        fa[set][k][0] = lds_tr2(pa, ks * 4096, ks * 4096 + 1024);
        fb[set][k][0] = lds_b128(pb[ks], 0);
        fb[set][k][1] = lds_b128(pb[ks], 4096);
        fa[set][k][1] = lds_tr2(pa, 16384 + ks * 4096, 16384 + ks * 4096 + 1024);
      }
    };
    auto mm = [&](int set, int n0, int n1) {
#pragma unroll
      for (int n = n0; n < n1; ++n) acc[(n >> 1) & 1][n & 1] = mfma32(fa[set][n >> 2][(n >> 1) & 1], fb[set][n >> 2][n & 1], acc[(n >> 1) & 1][n & 1]);
    };
    setslot(0);
    const u32 fad = opq(L_FULL + (lane & 1) * 4);
#pragma unroll
    for (int i = 0; i < 4; ++i) asm volatile("" : "+v"(acc[i >> 1][i & 1]));
    __builtin_amdgcn_s_setprio(1);
    lflag_wait(L_FULL, 1, 1);
    rd(0);
    SCHED0();
    u32 slot = 0;
#pragma unroll 1
    for (int c = 0; c < 7; ++c) {
      const u32 fl = *(volatile LDSP(u32))fad;
      rd(1); mm(0, 0, 5);
      SGB(0x008, 1); SGB(0x100, 4); SGB(0x008, 1); SGB(0x100, 3); SGB(0x008, 1); SGB(0x100, 3); SGB(0x008, 1); SGB(0x100, 3); SGB(0x008, 1);
      SCHED0();
      if (c < 6 && __builtin_amdgcn_ballot_w64(fl < (u32)(c + 2))) lflag_wait(L_FULL, 1, c + 2);
      SCHED0();
      slot = slot == 2 * SLOT ? 0 : slot + SLOT;
      mm(0, 5, 6);
      SCHED0();
      setslot(slot);
      SCHED0();
      mm(0, 6, 8);
      SCHED0();
      if (c < 4) { WAIT_LGKM0(); BAR(); } else FREE_SET(L_FREE + cw * 4, c + 1);
      SCHED0();
      rd(0); mm(1, 0, 8);
#pragma unroll
      for (int i = 0; i < 4; ++i) { SGB(0x008, 1); SGB(0x100, 3); }
      SGB(0x008, 4);
      SCHED0();
    }
    lflag_wait(L_EPI, 0, 1);
    const u32 hdst = WS_H + (u32)((2 * panel + th) * 56 + 4 * ft + fq) * 4096 + lane * 16;
    float nm[2], c1[2], irs2[2];
#pragma unroll
    for (int j = 0; j < 2; ++j) {
      const f32x2 st = *(LDSP(f32x2))(L_STAT + (u32)(64 * th + 32 * j + l31) * 8);
      nm[j] = -st[0]; c1[j] = -1.44269504f * st[1]; irs2[j] = __builtin_amdgcn_rcpf(st[1] * st[1]);
    }
#pragma unroll
    for (int s = 0; s < 2; ++s) {
      f32x4 cs[2][2];
#pragma unroll
      for (int m = 0; m < 2; ++m)
#pragma unroll
        for (int G = 0; G < 2; ++G) cs[m][G] = *(LDSP(f32x4))(L_CSUM + (u32)(m * 128 + 32 * fq + 16 * s + 8 * G + 4 * hh) * 4);
#pragma unroll
      for (int j = 0; j < 2; ++j) {
        float hv[8];
#pragma unroll
        for (int e = 0; e < 8; ++e) {
          float a = __builtin_fmaf(nm[j], cs[0][e >> 2][e & 3], acc[0][j][8 * s + e]);
          float bb = __builtin_fmaf(nm[j], cs[1][e >> 2][e & 3], acc[1][j][8 * s + e]);
          float ex = __builtin_amdgcn_exp2f(a * c1[j]);
          float rr = __builtin_amdgcn_rcpf(__builtin_fmaf(ex, irs2[j], irs2[j]));
          hv[e] = a * bb * rr;
        }
        st128_dev(rs, hdst + (s * 2 + j) * 1024, u32x4{pk2(hv[0], hv[1]), pk2(hv[2], hv[3]), pk2(hv[4], hv[5]), pk2(hv[6], hv[7])});
      }
    }
  }
  char* hflag = ws + WS_FH + ((2 * panel + th) * 56 + 4 * ft + fq) * 128;
  u32x4 xv, sv;
  if (cw < 4) {
    const int ht = (int)opq(threadIdx.x) & 255, lane = ht & 63, l15 = ht & 15;
    const u64 tag = tag_of(*(volatile LDSP(u64))L_TAG);
    const int ch = cw >> 1, var = cw & 1;
    const bool ready = __builtin_amdgcn_ballot_w64(*(volatile LDSP(u32))(L_CONV + (lane & 3) * 4) == 0) == 0;
    if (!ready) { gflag_publish(hflag, tag, lane); lflag_wait(L_CONV, 3, 1); }
    asm volatile("" ::: "memory");
    if (var) conv_compute<1>(lane, ch, d0 + ch, rs, hflag, tag, ready); else conv_compute<0>(lane, ch, d0 + ch, rs, hflag, tag, ready);
    gflag_publish(ws + WS_FY + (var * 448 + d0 + ch) * 128, tag, lane);
    u32x4 xr[2]; u32x2 st[2];
#pragma unroll
    for (int i = 0; i < 2; ++i) {
      const int n = ht + 256 * i, row = n >> 3, c = n & 7;
      xr[i] = *(const u32x4*)(x + (size_t)(64 * tau + row) * (D * 2) + (64 * dl + 8 * c) * 2);
      st[i] = ld64_dev(rs, WS_STAT + (u32)(64 * tau + row) * 8);
    }
    const int T = tau >> 2, yvar = (T >= 2 && T <= 5) ? 1 : 0;
    gflag_wait<4>(ws + WS_FY + (yvar * 448 + 64 * dl + 16 * cw + l15) * 128, tag);
#pragma unroll
    for (int i = 0; i < 2; ++i)
      glds16_dev((u32)((lane >> 3) * (S * 2) + (((lane & 7) ^ (2 * cw + i)) << 4)),
                 ws + WS_YT + (64 * dl + 16 * cw + 8 * i) * (S * 2) + 128 * tau, L_USCR + (u32)(2 * cw + i) * 1024);
    dflag_set(L_ADONE + cw * 4, 1);
    lflag_wait(L_ADONE, 3, 1);
#pragma unroll
    for (int i = 0; i < 2; ++i) {
      const int n = ht + 256 * i, tok = n >> 3, dg = n & 7;
      const float mb = asf(st[i][0]), rsd = asf(st[i][1]);
      u32x4 so;
#pragma unroll
      for (int e = 0; e < 4; ++e) {
        const u32 ya = L_USCR + (u32)((8 * dg + 2 * e) * 128 + (((tok >> 3) ^ dg) << 4) + (tok & 7) * 2);
        const float y0 = bflo(*(LDSP(u16))ya), y1 = bflo(*(LDSP(u16))(ya + 128));
        const u32 dd = pk2(bflo(xr[i][e]) - mb, bfhi(xr[i][e]) - mb);
        const u32 xn = pk2(bflo(dd) * rsd, bfhi(dd) * rsd);
        so[e] = pk2(bflo(xn) * y0, bfhi(xn) * y1);
      }
      if (i == 0) { xv = xr[0]; sv = so; }
      else { lds_st128(E_S + (u32)ht * 16, so); lds_st128(E_X + (u32)ht * 16, xr[1]); }
    }
  } else {
    const int j = cw - 4;
    const int lane = (int)opq(threadIdx.x) & 63;
    gflag_publish(hflag, tag_of(*(volatile LDSP(u64))L_TAG), lane);
    const u32 hl = lane * 16;
    const char* hbase = ws + WS_H + (size_t)tau * 56 * 4096;
    const int q4 = (lane >> 2) & 3;
    const u32 ta = (u32)((4 * (lane >> 5) + q4) * 128 + ((q4 >> 1) << 6) + ((lane >> 4) & 1) * 32 + (lane & 3) * 8);
    s16x8 fa[2][4], fb[3][4];
    f32x16 acc[2][2] = {};
    auto ldh = [&](int i) {
      const char* p = hbase + (size_t)g2_ks(j, i, rot2) * 4096;
      asm volatile("global_load_dwordx4 %0, %4, %5 sc1\n\tglobal_load_dwordx4 %1, %4, %5 offset:1024 sc1\n\t"
                   "global_load_dwordx4 %2, %4, %5 offset:2048 sc1\n\tglobal_load_dwordx4 %3, %4, %5 offset:3072 sc1"
                   : "=&v"(fb[i % 3][0]), "=&v"(fb[i % 3][1]), "=&v"(fb[i % 3][2]), "=&v"(fb[i % 3][3])
                   : "v"(hl), "s"(p) : "memory");
    };
    u32 tb[2];
#pragma unroll
    for (int a = 0; a < 2; ++a) tb[a] = opq((ta ^ (u32)(a << 6)) + g2_place(j, 0));
    auto rda = [&](int i) {
#pragma unroll
      for (int a = 0; a < 2; ++a)
        if (i > 0 && i % 3 == 0) tb[a] = opq(i % 6 ? tb[a] - 2 * SLOT : tb[a] + 2 * SLOT);
#pragma unroll
      for (int n = 0; n < 4; ++n) {
        const int o = (i % 3) * 4096 + (n >> 1) * 2048;
        fa[i & 1][n] = lds_tr2(tb[n & 1], o, o + 1024);
      }
    };
    const u32 fed = opq(L_FED + j * 4), prog = opq(L_PROG + j * 4);
    lflag_wait(L_H, 0, 1);
    ldh(0); ldh(1); ldh(2);
    lflag_wait(L_FED + j * 4, 0, 3);
    rda(0);
    SCHED0();
    u32 fl = 0;
#pragma unroll
    for (int i = 0; i < 14; ++i) {
      if (i == 2 || (i >= 5 && i < 13 && (i & 1))) {
        if (__builtin_amdgcn_readfirstlane(fl) < i + 2) lflag_wait(L_FED + j * 4, 0, i + 2);
      }
      asm volatile("s_waitcnt vmcnt(%4)" : "+v"(fb[i % 3][0]), "+v"(fb[i % 3][1]), "+v"(fb[i % 3][2]), "+v"(fb[i % 3][3])
                   : "i"(i < 12 ? 8 : (13 - i) * 4) : "memory");
      SCHED0();
      if (i < 13) rda(i + 1);
      if (i == 1 || (i >= 4 && i < 12 && !(i & 1))) fl = *(volatile LDSP(u32))fed;
#pragma unroll
      for (int s = 0; s < 2; ++s)
#pragma unroll
        for (int a = 0; a < 2; ++a)
#pragma unroll
          for (int t = 0; t < 2; ++t) acc[a][t] = mfma32(fa[i & 1][2 * s + a], fb[i % 3][2 * s + t], acc[a][t]);
      if (i < 13) {
#pragma unroll
        for (int n = 0; n < 5; ++n) { SGB(0x008, 1); SGB(0x100, 2); }
        SGB(0x008, 3);
      }
      SCHED0();
      if (i < 8 && (i & 1)) *(volatile LDSP(u32))prog = i + 1;
      if (i == 4) { lflag_wait(L_H + 4, 0, 1); }
      if (i + 3 < 14) ldh(i + 3);
      SCHED0();

    }
    const int ln = (int)opq(threadIdx.x), lt = ln & 31, lh = (ln >> 5) & 1;
#pragma unroll
    for (int t = 0; t < 2; ++t)
#pragma unroll
      for (int a = 0; a < 2; ++a)
#pragma unroll
        for (int G = 0; G < 4; ++G)
          *(LDSP(f32x4))(g2_place(j, t ? 3 : 0) + (u32)(lt * 256 + (((8 * a + 2 * G + lh) ^ (lt & 7)) << 4))) =
              f32x4{acc[a][t][4 * G], acc[a][t][4 * G + 1], acc[a][t][4 * G + 2], acc[a][t][4 * G + 3]};
  }
  WAIT_LGKM0();
  BAR();
  {
    const int ht = (int)opq(threadIdx.x) & 255;
    const int half = cw >> 2, tk = ht >> 3, dg = ht & 7;
    if (half) { sv = *(LDSP(u32x4))(E_S + (u32)ht * 16); xv = *(LDSP(u32x4))(E_X + (u32)ht * 16); }
    f32x4 s0 = {}, s1 = {};
#pragma unroll
    for (int w = 0; w < 4; ++w) {
      const u32 pb = (half ? 0u : 2 * SLOT) + (u32)w * 12288 + (u32)tk * 256;
      s0 += *(LDSP(f32x4))(pb + (u32)(((2 * dg) ^ (tk & 7)) << 4));
      s1 += *(LDSP(f32x4))(pb + (u32)(((2 * dg + 1) ^ (tk & 7)) << 4));
    }
    float o[8];
#pragma unroll
    for (int e = 0; e < 8; ++e) {
      const float f = rbf(e < 4 ? s0[e & 3] : s1[e & 3]);
      const float gt = rbf(silu_f(f));
      const float xe = (e & 1) ? bfhi(xv[e >> 1]) : bflo(xv[e >> 1]);
      const float se = (e & 1) ? bfhi(sv[e >> 1]) : bflo(sv[e >> 1]);
      o[e] = xe + rbf(se * gt);
    }
    *(u32x4*)(out + (size_t)(64 * tau + 32 * half + tk) * (D * 2) + (64 * dl + 8 * dg) * 2) =
        u32x4{pk2(o[0], o[1]), pk2(o[2], o[3]), pk2(o[4], o[5]), pk2(o[6], o[7])};
  }
}
}
#if defined(__HIP_DEVICE_COMPILE__) && __HIP_DEVICE_COMPILE__
#pragma clang attribute pop
#endif

extern "C" void kernel_launch(void* const* d_in, const int* in_sizes, int n_in, void* d_out, int out_size, void* d_ws,
                              size_t ws_size, hipStream_t stream) {
  (void)hipFuncSetAttribute((const void*)fused_kernel, hipFuncAttributeMaxDynamicSharedMemorySize, 163840);
  fused_kernel<<<256, 768, 163840, stream>>>((const char*)d_in[0], (const char*)d_in[1], (const char*)d_in[2], (const char*)d_in[3],
                                             (const char*)d_in[4], (const char*)d_in[5], (const char*)d_in[7], (const char*)d_in[8],
                                             (const char*)d_in[9], (char*)d_ws, (char*)d_out);
}
```

```cpp
#include <hip/hip_runtime.h>
#include <hip/hip_bf16.h>
#include <stdint.h>
#pragma clang diagnostic ignored "-Wint-to-pointer-cast"

#if defined(__HIP_DEVICE_COMPILE__) && __HIP_DEVICE_COMPILE__
#pragma clang attribute push(__attribute__((target("no-packed-fp32-ops"))), apply_to = function)
#endif
namespace {
typedef unsigned short u16;
typedef unsigned int u32;
typedef unsigned long long u64;
typedef __attribute__((ext_vector_type(4))) short s16x4;
typedef __attribute__((ext_vector_type(8))) short s16x8;
typedef __attribute__((ext_vector_type(8))) __bf16 bf16x8;
typedef __attribute__((ext_vector_type(2))) __bf16 bf16x2;
typedef __attribute__((ext_vector_type(16))) float f32x16;
typedef __attribute__((ext_vector_type(4))) float f32x4;
typedef __attribute__((ext_vector_type(2))) float f32x2;
typedef __attribute__((ext_vector_type(4))) u32 u32x4;
typedef __attribute__((ext_vector_type(2))) u32 u32x2;
#define LDSP(T) __attribute__((address_space(3))) T*
#define GLBP(T) __attribute__((address_space(1))) T*
#define DEV __device__ __forceinline__

constexpr int S = 2048, D = 448, F = 1792;
constexpr u32 WS_H = 0;
constexpr u32 WS_UT = WS_H + 7340032;
constexpr u32 WS_YT = WS_UT + 1835008;
constexpr u32 WS_STAT = WS_YT + 1835008;
constexpr u32 WS_CSUM = WS_STAT + 16384;
constexpr u32 WS_E = WS_CSUM + 16384;
constexpr u32 WS_FUT = WS_E + 128;
constexpr u32 WS_FST = WS_FUT + 448 * 128;
constexpr u32 WS_FY = WS_FST + 32 * 128;
constexpr u32 WS_FH = WS_FY + 896 * 128;
constexpr u32 WS_END = WS_FH + 1792 * 128;
constexpr int CODE_LINES = 224;
constexpr u32 SLOT = 49152;
constexpr u32 MISC = 3 * SLOT;
constexpr u32 L_FULL = MISC, L_EPI = MISC + 8, L_H = MISC + 16, L_FREE = MISC + 32, L_CONV = MISC + 64, L_FED = MISC + 80,
              L_TAG = MISC + 96, L_DD = MISC + 104, L_ADONE = MISC + 112, L_PROG = MISC + 128, L_XCH = MISC + 144, L_PUB = MISC + 160, L_STAT = MISC + 256,
              L_CSUM = MISC + 1280, L_USCR = MISC + 4096, L_DUMMY = MISC + 12288;
constexpr u32 CV = SLOT, CV_HSTR = 4160, CV_U = CV + 8 * CV_HSTR, CV_USTR = 4608;
constexpr u32 E_S = SLOT, E_X = SLOT + 4096;
constexpr int SPIN_CAP = 1 << 18;
#define ROT1 0
#define ROT2 0
#define TOUCH2_AT 0
#define PACE_L 63
#define PACE_S 63
#define FREE_SET dflag_set
#define W2PF 1
#define KVPF 0
#define SHARES 0
#define CODEPF 0
#define W2_HOLD 0
#define PUB2 0
#define KV_AT 1

__device__ const u32 seq_tab[160] __attribute__((aligned(128))) = {0, 1, 2, 3, 4, 5, 6, 7};
DEV float asf(u32 v) { return __builtin_bit_cast(float, v); }
DEV float bflo(u32 v) { return asf(v << 16); }
DEV float bfhi(u32 v) { return asf(v & 0xffff0000u); }
template <int CTRL> DEV float dpp(float v) { return asf(__builtin_amdgcn_update_dpp(0, __builtin_bit_cast(u32, v), CTRL, 0xf, 0xf, true)); }
DEV float sum8(float v) { v += dpp<0xB1>(v); v += dpp<0x4E>(v); return v + dpp<0x141>(v); }
DEV u32 pk2(float lo, float hi) {
  asm("" : "+v"(lo)); asm("" : "+v"(hi));
  bf16x2 r = {(__bf16)lo, (__bf16)hi};
  return __builtin_bit_cast(u32, r);
}
DEV float rbf(float f) { bf16x2 r = {(__bf16)f, (__bf16)f}; return bflo(__builtin_bit_cast(u32, r)); }
DEV f32x4 mfma16(s16x8 a, s16x8 b, f32x4 c) {
  return __builtin_amdgcn_mfma_f32_16x16x32_bf16(__builtin_bit_cast(bf16x8, a), __builtin_bit_cast(bf16x8, b), c, 0, 0, 0);
}
DEV f32x16 mfma32(s16x8 a, s16x8 b, f32x16 c) {
  return __builtin_amdgcn_mfma_f32_32x32x16_bf16(__builtin_bit_cast(bf16x8, a), __builtin_bit_cast(bf16x8, b), c, 0, 0, 0);
}
DEV u32 opq(u32 v) { asm volatile("" : "+v"(v)); return v; }
DEV s16x8 lds_tr2(u32 base, int o0, int o1) {
  s16x4 lo = __builtin_amdgcn_ds_read_tr16_b64_v4i16((LDSP(s16x4))((LDSP(char))base + o0));
  s16x4 hi = __builtin_amdgcn_ds_read_tr16_b64_v4i16((LDSP(s16x4))((LDSP(char))base + o1));
  return __builtin_shufflevector(lo, hi, 0, 1, 2, 3, 4, 5, 6, 7);
}
DEV s16x8 lds_b128(u32 base, int o) { return *(LDSP(s16x8))((LDSP(char))base + o); }
DEV void lds_st128(u32 a, u32x4 v) { *(LDSP(u32x4))a = v; }
DEV void glds16x4(u32 v0, u32 v1, u32 v2, u32 v3, const void* sbase, u32 ldst) {
  asm volatile("s_mov_b32 m0, %5\n\ts_nop 0\n\tglobal_load_lds_dwordx4 %0, %4\n\tglobal_load_lds_dwordx4 %1, %4 offset:1024\n\t"
               "global_load_lds_dwordx4 %2, %4 offset:2048\n\tglobal_load_lds_dwordx4 %3, %4 offset:3072"
               ::"v"(v0), "v"(v1), "v"(v2), "v"(v3), "s"(sbase), "s"(ldst) : "memory");
}
DEV void glds16(u32 voff, const void* sbase, u32 ldst) {
  asm volatile("s_mov_b32 m0, %2\n\ts_nop 0\n\tglobal_load_lds_dwordx4 %0, %1" ::"v"(voff), "s"(sbase), "s"(ldst) : "memory");
}
DEV void glds16x4_lin(u32 v, const void* sbase, u32 ldst) { glds16x4(v, v, v, v, sbase, ldst); }
DEV void glds16x4_dev(u32 v, const void* sbase, u32 ldst) {
  asm volatile("s_mov_b32 m0, %2\n\ts_nop 0\n\tglobal_load_lds_dwordx4 %0, %1 sc1\n\tglobal_load_lds_dwordx4 %0, %1 offset:1024 sc1\n\t"
               "global_load_lds_dwordx4 %0, %1 offset:2048 sc1\n\tglobal_load_lds_dwordx4 %0, %1 offset:3072 sc1"
               ::"v"(v), "s"(sbase), "s"(ldst) : "memory");
}
DEV void glds16_dev(u32 voff, const void* sbase, u32 ldst) {
  asm volatile("s_mov_b32 m0, %2\n\ts_nop 0\n\tglobal_load_lds_dwordx4 %0, %1 sc1" ::"v"(voff), "s"(sbase), "s"(ldst) : "memory");
}
DEV void dflag_set(u32 addr, u32 val) {
  asm volatile("s_waitcnt lgkmcnt(0)\n\ts_mov_b32 m0, %2\n\ts_mov_b64 exec, 1\n\tglobal_load_lds_dword %0, %1\n\ts_mov_b64 exec, -1"
               ::"v"(0), "s"(seq_tab + val), "s"(addr) : "memory");
}
#define WAIT_VM(n) asm volatile("s_waitcnt vmcnt(%0)" ::"i"(n) : "memory")
#define WAIT_LGKM0() asm volatile("s_waitcnt lgkmcnt(0)" ::: "memory")
#define BAR() __builtin_amdgcn_s_barrier()
#define SCHED0() __builtin_amdgcn_sched_barrier(0)
#define SGB(mask, n) __builtin_amdgcn_sched_group_barrier(mask, n, 0)
DEV float silu_f(float x) { return x * __builtin_amdgcn_rcpf(1.0f + __builtin_amdgcn_exp2f(-1.44269504f * x)); }

DEV void lflag_set(u32 addr, u32 val) {
  asm volatile("s_waitcnt lgkmcnt(0)\n\tds_write_b32 %0, %1" ::"v"(addr), "v"(val) : "memory");
}
DEV void lflag_wait(u32 addr, u32 mask, u32 val) {
  const u32 a = addr + 4 * (threadIdx.x & mask);
#pragma unroll 1
  for (int i = 0; i < SPIN_CAP; ++i) {
    u32 v;
    asm volatile("ds_read_b32 %0, %1\n\ts_waitcnt lgkmcnt(0)" : "=v"(v) : "v"(a) : "memory");
    if (__builtin_amdgcn_ballot_w64(v < val) == 0) break;
    __builtin_amdgcn_s_sleep(1);
  }
}
DEV u64 gload64(const char* p) { return __hip_atomic_load((GLBP(u64))p, __ATOMIC_RELAXED, __HIP_MEMORY_SCOPE_AGENT); }
DEV void gstore64(char* p, u64 v) { __hip_atomic_store((GLBP(u64))p, v, __ATOMIC_RELAXED, __HIP_MEMORY_SCOPE_AGENT); }
template <int SLEEP>
DEV void gflag_wait(const char* lane_ptr, u64 tag) {
#pragma unroll 1
  for (int i = 0; i < SPIN_CAP; ++i) {
    asm volatile("" ::: "memory");
    if (__builtin_amdgcn_ballot_w64(gload64(lane_ptr) != tag) == 0) break;
    __builtin_amdgcn_s_sleep(SLEEP);
  }
  asm volatile("" ::: "memory");
}
DEV void gflag_publish(char* p, u64 tag, int lane) {
  WAIT_VM(0);
  if (lane == 0) gstore64(p, tag);
}
DEV u64 tag_of(u64 seed) { return seed * 0x9E3779B97F4A7C15ull + 0xD1B54A32D192ED03ull; }
DEV u64 make_tag(const char* ws) { return tag_of(gload64(ws + WS_E)); }
typedef __amdgpu_buffer_rsrc_t rsrc_t;
DEV rsrc_t ws_rsrc(char* ws) { return __builtin_amdgcn_make_buffer_rsrc(ws, 0, WS_END, 0x00020000); }
DEV void st128_dev(rsrc_t r, u32 off, u32x4 v) { __builtin_amdgcn_raw_buffer_store_b128(v, r, off, 0, 16); }
DEV void st64_dev(rsrc_t r, u32 off, u32x2 v) { __builtin_amdgcn_raw_buffer_store_b64(v, r, off, 0, 16); }
DEV void st32_dev(rsrc_t r, u32 off, u32 v) { __builtin_amdgcn_raw_buffer_store_b32(v, r, off, 0, 16); }
DEV u32x4 ld128_dev(rsrc_t r, u32 off) { return __builtin_amdgcn_raw_buffer_load_b128(r, off, 0, 16); }
DEV u32x2 ld64_dev(rsrc_t r, u32 off) { return __builtin_amdgcn_raw_buffer_load_b64(r, off, 0, 16); }
DEV void touch(u32 voff, const void* sbase, u32 ldst) {
  asm volatile("s_mov_b32 m0, %2\n\ts_nop 0\n\tglobal_load_lds_dword %0, %1" ::"v"(voff), "s"(sbase), "s"(ldst) : "memory");
}

DEV void glds4_dev(u32 voff, const void* sbase, u32 ldst) {
  asm volatile("s_mov_b32 m0, %2\n\ts_nop 0\n\tglobal_load_lds_dword %0, %1 sc1" ::"v"(voff), "s"(sbase), "s"(ldst) : "memory");
}
DEV u32 shr16(u32 hi, u32 lo) { return __builtin_amdgcn_alignbit(hi, lo, 16); }

DEV u32 xbuf(int w) { return w < 2 ? L_USCR + (u32)w * 4096 : (w == 2 ? L_DUMMY : CV + 45056); }
DEV void conv_compute(int lane, int cw, int d, rsrc_t rs, char* hflag, u64 tag, bool pub) {
  const int ch = cw >> 1, var = cw & 1;
  const int m = lane & 15, g = lane >> 4, q4 = m >> 2, p4 = lane & 3;
  u32 aa = opq(CV + (u32)(ch * 4 + ((q4 + 1) & 3)) * CV_HSTR + (u32)(((q4 + 1) & 4) + 8 * g + 4 * p4) * 2 + var * 256);
  u32 ba = opq(CV_U + (u32)ch * CV_USTR + (u32)(240 - 16 * m + 8 * g) * 2 + var * 256);
  f32x4 acc[8] = {};
  s16x8 A[8], B[8];
#pragma unroll
  for (int p = 0; p < 8; ++p) { A[7 - p] = lds_tr2(aa, (7 - p) * 512, (7 - p) * 512 + 8); B[p] = lds_b128(ba, (7 - p) * 512); }
  if (var == 0) {
    const u32 a0 = CV + (u32)ch * 4 * CV_HSTR + (u32)(m + 1 + 8 * g) * 2;
#pragma unroll
    for (int j = 0; j < 8; ++j) { const short v = *(LDSP(short))(a0 + j * 2); A[0][j] = m + 8 * g + j >= 15 ? v : (short)0; }
  }
  SCHED0();
#pragma unroll 1
  for (int it = 0; it < 4; ++it) {
#pragma unroll
    for (int p = 7; p >= 0; --p) {
#pragma unroll
      for (int dd = 0; dd <= 7 - p; ++dd) {
        acc[p + dd] = mfma16(A[p], B[dd], acc[p + dd]);
        if (p == 0) { SCHED0(); B[dd] = lds_b128(ba, 64 + (7 - dd) * 512); SCHED0(); }
      }
      SCHED0();
      A[p] = lds_tr2(aa, 64 + p * 512, 64 + p * 512 + 8);
      SCHED0();
    }
    aa = opq(aa + 64); ba = opq(ba + 64);
    if (it == 0 && pub) gflag_publish(hflag, tag, lane);
  }
  f32x4 y[4];
#pragma unroll
  for (int i = 0; i < 4; ++i) {
    *(LDSP(f32x4))(xbuf(cw) + (u32)(i * 1024 + lane * 16)) = var ? acc[i] : acc[4 + i];
    y[i] = var ? acc[4 + i] : acc[i];
  }
  lflag_set(L_XCH + cw * 4, 1);
  const u32 dv = *(LDSP(u32))L_DD;
  const float Dd = ch ? bfhi(dv) : bflo(dv);
  u32x2 uu[4];
#pragma unroll
  for (int i = 0; i < 4; ++i) uu[i] = *(LDSP(u32x2))(CV_U + (u32)ch * CV_USTR + (u32)(2044 - (1024 * var + 256 * i + 16 * m + 4 * g)) * 2);
  lflag_wait(L_XCH + (cw ^ 1) * 4, 0, 1);
#pragma unroll
  for (int i = 0; i < 4; ++i) {
    const int t = 1024 * var + 256 * i + 16 * m + 4 * g;
    const f32x4 s = y[i] + *(LDSP(f32x4))(xbuf(cw ^ 1) + (u32)(i * 1024 + lane * 16));
    float y0 = rbf(s[0]) + rbf(bfhi(uu[i][1]) * Dd), y1 = rbf(s[1]) + rbf(bflo(uu[i][1]) * Dd);
    float y2 = rbf(s[2]) + rbf(bfhi(uu[i][0]) * Dd), y3 = rbf(s[3]) + rbf(bflo(uu[i][0]) * Dd);
    st64_dev(rs, WS_YT + (u32)(d * (S * 2) + t * 2), u32x2{pk2(y0, y1), pk2(y2, y3)});
  }
}

DEV int rot7(int i, int rot) { const int v = i + rot; return v >= 7 ? v - 7 : v; }
DEV int g2_ks(int j, int i, int rot) { const int e = 7 * j + rot7(i < 7 ? i : i - 7, rot); return 4 * (e >> 1) + (e & 1) + (i < 7 ? 0 : 2); }
DEV u32 g2_place(int j, int p) { return (p < 3 ? 2 * SLOT + (u32)p * 4096 : (u32)(p - 3) * 4096) + (u32)j * 12288; }
DEV u32 w2_voff(int lane) { return (u32)((lane >> 3) * (D * 2) + (((lane & 7) ^ (((lane >> 4) & 1) << 2)) << 4)); }

__global__ __launch_bounds__(768, 3) void fused_kernel(const char* x, const char* kp, const char* vp, const char* skp,
                                                       const char* ssmk, const char* ssmD, const char* w1, const char* w3,
                                                       const char* w2, char* ws, char* out) {
  const int b = blockIdx.x, xcd = b & 7, r = b >> 3;
  const int wid = __builtin_amdgcn_readfirstlane(threadIdx.x >> 6);
  const int panel = (xcd >> 1) * 4 + r / 7, ft = (xcd & 1) * 7 + r % 7;
  const int tau = xcd * 4 + r / 7, dl = r % 7;
  const int d0 = 2 * b;
  const int rot = ROT1 ? (r % 7 + 2 * (r / 7)) % 7 : 0, rot2 = ROT2 ? (r % 7 + 2 * (r / 7)) % 7 : 0;

  if (b < 224) {
    if (wid == 7) { *(LDSP(u32))(MISC + (threadIdx.x & 63) * 4) = 0; WAIT_LGKM0(); }
    BAR();
    if (wid == 7 && (threadIdx.x & 63) < 5) touch((threadIdx.x & 63) * 128, seq_tab, L_DUMMY);
  }

  if (__builtin_expect(wid < 4 && b < 224, 1)) {
    const int lane = (int)opq(threadIdx.x) & 63;
    u64 tag = 0;
    if (__builtin_expect(wid < 2, 1)) {
      const int g = lane >> 4, l15 = lane & 15;
      u32 vx[8], vw[8];
      const u32 ox = (u32)((lane >> 3) * (D * 2) + (((lane & 7) ^ g) << 4));
      const u32 ow = (u32)(g * (F * 2) + (((l15 >> 2) ^ g) << 6) + ((lane & 3) << 4));
#pragma unroll
      for (int i = 0; i < 8; ++i) {
        vx[i] = (ox ^ (u32)((i & 1) * 64)) + i * 8 * (D * 2) - (i & 3) * 1024;
        vw[i] = ow + i * 4 * (F * 2) - (i & 3) * 1024;
      }
#pragma unroll 1
      for (int c = 0; c < 7; ++c) {
        if (c >= 3) BAR();
        const u32 slot = (u32)(c % 3) * SLOT + wid * 8192;
        const int kc = rot7(c, rot);
        const char* xb = x + (size_t)(panel * 128 + wid * 64) * (D * 2) + kc * 128;
        const char* w1b = w1 + (size_t)(kc * 64 + wid * 32) * (F * 2) + ft * 256;
        const char* w3b = w3 + (size_t)(kc * 64 + wid * 32) * (F * 2) + ft * 256;
#pragma unroll
        for (int i = 0; i < 8; i += 4) {
          WAIT_VM(PACE_L);
          glds16x4(vx[i], vx[i + 1], vx[i + 2], vx[i + 3], xb, slot + i * 1024);
          WAIT_VM(PACE_L);
          glds16x4(vw[i], vw[i + 1], vw[i + 2], vw[i + 3], w1b, slot + 16384 + i * 1024);
          WAIT_VM(PACE_L);
          glds16x4(vw[i], vw[i + 1], vw[i + 2], vw[i + 3], w3b, slot + 32768 + i * 1024);

        }
        dflag_set(L_FULL + wid * 4, c + 1);
      }
    } else {
      __builtin_amdgcn_s_setprio(3);
      const rsrc_t rs = ws_rsrc(ws);
      const int u = wid - 2;
      const int ti = b / 7, cj = b % 7;
      const int tg = lane >> 3, cg = lane & 7;
      const int t0 = 64 * ti + 32 * u + 4 * tg, ch0 = 64 * cj + 8 * cg;
      u32x4 kr[7], vr[4], sw[4]; u32x2 o[8];
      u64 seed;
      auto ldkv = [&]() {
        const u32 ko = (u32)(t0 * (D * 2) + ch0 * 2);
#pragma unroll
        for (int i = 0; i < 7; ++i) kr[i] = *(const u32x4*)(kp + (i < 3 && t0 == 0 ? ko : ko + (u32)((i - 3) * (D * 2))));
#pragma unroll
        for (int i = 0; i < 4; ++i) vr[i] = *(const u32x4*)(vp + (ko + (u32)(i * (D * 2))));
        SCHED0();
      };
      auto piece = [&](int p) {
#pragma unroll
        for (int c = 2 * p; c < 2 * p + 2; ++c) {
          const u32 wa = sw[p][(c & 1) * 2], wb = sw[p][(c & 1) * 2 + 1];
          const float w0 = bflo(wa), w1f = bfhi(wa), w2f = bflo(wb), w3f = bfhi(wb);
          float kf[7], ks[4];
#pragma unroll
          for (int i = 0; i < 7; ++i) kf[i] = (c & 1) ? bfhi(kr[i][p]) : bflo(kr[i][p]);
#pragma unroll
          for (int i = 0; i < 4; ++i)
            ks[i] = __builtin_fmaf(kf[i + 3], w0, __builtin_fmaf(kf[i + 2], w1f, __builtin_fmaf(kf[i + 1], w2f, kf[i] * w3f)));
#pragma unroll
          for (int i = 0; i < 2; ++i) {
            const int ta = 3 - 2 * i, tb = 2 - 2 * i;
            const u32 kk = pk2(ks[ta], ks[tb]);
            o[c][i] = pk2(bflo(kk) * ((c & 1) ? bfhi(vr[ta][p]) : bflo(vr[ta][p])), bfhi(kk) * ((c & 1) ? bfhi(vr[tb][p]) : bflo(vr[tb][p])));
          }
          asm volatile("" : "+v"(o[c]));
        }
        SCHED0();
      };
      ldkv();
#pragma unroll
      for (int i = 0; i < 4; ++i) sw[i] = *(const u32x4*)(skp + (u32)(ch0 * 8 + i * 16));
      seed = gload64(ws + WS_E + opq(0));
      SCHED0();
#pragma unroll
      for (int i = 0; i < 3; ++i) if (t0 == 0) kr[i] = u32x4{0, 0, 0, 0};
#pragma unroll
      for (int c = 0; c < 4; ++c) piece(c);
      tag = tag_of(seed);
      const u32 scr = L_USCR + (u32)u * 4096;
#pragma unroll
      for (int c = 0; c < 8; ++c) *(LDSP(u32x2))(scr + (u32)((cg * 8 + c) * 64 + (((7 - tg) ^ (cg & 6)) << 3))) = o[c];
      SCHED0();
      BAR();
      WAIT_LGKM0();
#pragma unroll
      for (int i = 0; i < 4; ++i) {
        const int dd = 16 * i + (lane >> 2), c4 = lane & 3;
        u32x4 v = *(LDSP(u32x4))(scr + (u32)(dd * 64 + ((c4 ^ (dd >> 4)) << 4)));
        st128_dev(rs, WS_UT + (u32)((64 * cj + dd) * (S * 2) + (2016 - 64 * ti - 32 * u) * 2 + c4 * 16), v);
      }
      SCHED0();
      BAR();
      gflag_publish(ws + WS_FUT + (cj * 64 + 2 * ti + u) * 128, tag, lane);
      BAR();
      BAR();
      const int k = lane & 7;
      const char* fp = ws + (u ? WS_FST + (k < 2 ? 2 * panel + k : (k == 2 ? tau : (k & 1) * 14 + ft)) * 128 : WS_FUT + ((d0 >> 6) * 64 + lane) * 128);
      gflag_wait<8>(fp, tag);
      if (u) {
        glds16_dev(lane * 16, ws + WS_STAT + panel * 1024, L_STAT);
        glds16_dev(lane * 16, ws + WS_CSUM + ft * 1024, L_CSUM);
        if (lane < 2) glds4_dev(lane * 4, ws + WS_E, L_TAG);
        if (lane < 1) glds4_dev(0, ssmD + d0 * 2, L_DD);
        dflag_set(L_EPI, 1);
      }
    }
    const int j = wid;
    const u32 o2 = w2_voff(lane);
    const char* wbase = w2 + dl * 128;
    u32x4 pk[8][4];
    auto park = [&](int i0) {
#pragma unroll
    for (int i = i0; i < i0 + 4; ++i) {
      const char* src = wbase + (size_t)g2_ks(j, i + 6, rot2) * 32 * (D * 2);
      WAIT_VM(PACE_S);
#pragma unroll
      for (int pc = 0; pc < 4; ++pc) pk[i][pc] = *(const u32x4*)(src + (o2 + (u32)(pc * 8 * (D * 2))));
    }
    SCHED0();
    };
    park(0);
    park(4);
    lflag_wait(L_FREE, 7, 7);
    if (wid == 2) {
        if (lane < 32) {
          glds16(lane * 16, seq_tab + 32, CV_U + 4096);
          glds16(lane * 16, seq_tab + 32, CV_U + CV_USTR + 4096);
        }
        glds16x4_dev(lane * 16, ws + WS_UT + d0 * (S * 2), CV_U);
        glds16x4_dev(lane * 16, ws + WS_UT + (d0 + 1) * (S * 2), CV_U + CV_USTR);
        dflag_set(L_CONV + 8, 1);
        dflag_set(L_CONV + 12, 1);
      }
    if (wid < 2) {
      const u32 vl = lane * 16;
#pragma unroll 1
      for (int rr = 0; rr < 4; ++rr) {
        const char* src = ssmk + (size_t)(d0 + wid) * (S * 2) + (rr ? (rr - 4) * 2 : 0);
        const u32 dst = CV + (u32)(wid * 4 + rr) * CV_HSTR + (rr ? 24 : 32);
        glds16x4(rr && !lane ? 16u : vl, vl, vl, vl, src, dst);
      }
      dflag_set(L_CONV + wid * 4, 1);
    }
#pragma unroll 1
    for (int i = 0; i < 6; ++i) {
      if (i == W2_HOLD) lflag_wait(L_PUB, 3, 1);
      if (i == 3) dflag_set(L_FED + j * 4, 3);
      const char* src = wbase + (size_t)g2_ks(j, i, rot2) * 32 * (D * 2);
      WAIT_VM(PACE_S);
      glds16x4(o2, o2 + 8 * (D * 2) - 1024, o2 + 16 * (D * 2) - 2048, o2 + 24 * (D * 2) - 3072, src, g2_place(j, i));
    }
    dflag_set(L_FED + j * 4, 6);
    if (wid >= 2) {
      lflag_wait(L_PUB, 3, PUB2 ? 2 : 1);
      const int e = lane < 28 ? lane : 27;
      gflag_wait<4>(ws + WS_FH + (tau * 56 + 4 * (e >> 1) + (e & 1) + 2 * (3 - wid)) * 128, tag);
      dflag_set(L_H + (3 - wid) * 4, 1);
    }
    WAIT_VM(0);
    const u32 wl = opq(lane * 16 + j * 12288);
#pragma unroll
    for (int i = 0; i < 8; i += 2) {
      lflag_wait(L_PROG + j * 4, 0, i + 2);
#pragma unroll
      for (int n = 0; n < 8; ++n) lds_st128(wl + g2_place(0, (i + (n >> 2)) % 6) + (n & 3) * 1024, pk[i + (n >> 2)][n & 3]);
      lflag_set(L_FED + j * 4, i + 8);
    }
    return;
  }
  const rsrc_t rs = ws_rsrc(ws);

  if (b >= 224) {
    if (wid >= 8 && b != 255) return;
    const int tid = (int)opq(threadIdx.x), lane = tid & 63;
    const int sp = b - 224;
    const u64 seed = gload64(ws + WS_E + opq(0));
    if (KVPF && wid < 7) {
      touch((u32)(sp * 448 + tid) * 128, kp, L_DUMMY);
      touch((u32)(sp * 448 + tid) * 128, vp, L_DUMMY);
    }
    if (KVPF) BAR();
    if (wid < 8) {
      const int row = 64 * sp + (tid >> 3), c8 = tid & 7;
      const int cu = sp < 28 ? sp : 27, mat = cu / 14, ct = cu % 14, kg = tid >> 4, c16 = tid & 15;
      const char* wp = (mat ? w3 : w1) + ct * 256;
      const u32 wo = (u32)(kg * (F * 2) + c16 * 16), xo = (u32)(row * (D * 2) + c8 * 16);
      u32x4 xv[7], wv[14];
#pragma unroll
      for (int i = 0; i < 7; ++i) xv[i] = *(const u32x4*)(x + (xo + i * 128));
#pragma unroll
      for (int i = 0; i < 14; ++i) wv[i] = *(const u32x4*)(wp + (wo + i * 32 * (F * 2)));
      SCHED0();
      float s = 0.f;
#pragma unroll
      for (int i = 0; i < 7; ++i)
#pragma unroll
        for (int j = 0; j < 4; ++j) s += bflo(xv[i][j]) + bfhi(xv[i][j]);
      const float m = sum8(s) * (1.0f / D);
      float q = 0.f;
#pragma unroll
      for (int i = 0; i < 7; ++i)
#pragma unroll
        for (int j = 0; j < 4; ++j) {
          const float aa = bflo(xv[i][j]) - m, bb = bfhi(xv[i][j]) - m;
          q = __builtin_fmaf(aa, aa, __builtin_fmaf(bb, bb, q));
        }
      const float sd = rbf(__builtin_amdgcn_sqrtf(rbf(rbf(sum8(q) * (1.0f / D)) + 1e-5f)));
      if (c8 == 0) *(LDSP(f32x2))(u32)(16384 + (tid >> 3) * 8) = f32x2{rbf(m), __builtin_amdgcn_rcpf(sd)};
      SCHED0();
      float a[8] = {};
#pragma unroll
      for (int i = 0; i < 14; ++i)
#pragma unroll
        for (int j = 0; j < 4; ++j) { a[2 * j] += bflo(wv[i][j]); a[2 * j + 1] += bfhi(wv[i][j]); }
      f32x4 t;
#pragma unroll
      for (int j = 0; j < 4; ++j) {
        auto p = __builtin_amdgcn_permlane16_swap(__builtin_bit_cast(u32, a[j]), __builtin_bit_cast(u32, a[j + 4]), false, false);
        t[j] = asf(p[0]) + asf(p[1]);
      }
      *(LDSP(f32x4))(u32)((kg >> 1) * 512 + c16 * 32 + (kg & 1) * 16) = t;
    }
    WAIT_LGKM0(); BAR();
    const u64 tag = tag_of(seed);
    if (wid == 0) {
      const int cu = sp < 28 ? sp : 27;
      f32x2 cs = {};
#pragma unroll
      for (int i = 0; i < 16; ++i) cs += *(LDSP(f32x2))(u32)(i * 512 + lane * 8);
      st64_dev(rs, WS_STAT + (u32)(64 * sp + lane) * 8, *(LDSP(u32x2))(u32)(16384 + lane * 8));
      if (sp < 28) st64_dev(rs, WS_CSUM + (u32)((cu % 14) * 1024 + (cu / 14) * 512 + lane * 8), __builtin_bit_cast(u32x2, cs));
      gflag_publish(ws + WS_FST + sp * 128, tag, lane);
    }
    WAIT_VM(0); BAR();
    if (W2PF && wid > 0 && wid < 8) {
#pragma unroll 1
      for (int i = 0; i < 7; ++i) touch((u32)((((sp >> 3) * 7 + i) * 7 + wid - 1) * 64 + lane) * 128, w2, L_DUMMY);
      WAIT_VM(0);
    }
    if (b == 255) {
      gflag_wait<64>(ws + (tid < 480 ? WS_FUT + tid * 128 : WS_FH + ((((tid < 704 ? tid - 480 : 223) / 14) * 2) * 56 + ((tid < 704 ? tid - 480 : 223) % 14) * 4) * 128), tag);
      BAR();
      if (tid == 0) gstore64(ws + WS_E, tag);
    }
    return;
  }

  const int cw = wid - 4, th = cw & 1, fq = cw >> 1;
  {
    const int lane = (int)opq(threadIdx.x) & 63, g = lane >> 4, hh = lane >> 5, l31 = lane & 31;
    const int q4 = (lane >> 2) & 3, p4 = lane & 3;
    const u32 aA = 16384 + (u32)((8 * hh + q4) * 256 + ((fq ^ q4) << 6) + (g & 1) * 32 + p4 * 8);
    const u32 aB = (u32)((64 * th + l31) * 128 + ((hh ^ ((l31 >> 1) & 7)) << 4));
    f32x16 acc[2][2] = {};
    s16x8 fa[2][2][2], fb[2][2][2];
    u32 pa, pb[4];
    auto setslot = [&](u32 slot) {
      pa = opq(aA + slot);
#pragma unroll
      for (int k = 0; k < 4; ++k) pb[k] = opq((aB ^ (u32)(k << 5)) + slot);
    };
    auto rd = [&](int set) {
#pragma unroll
      for (int k = 0; k < 2; ++k) {
        const int ks = 2 * set + k;
        fa[set][k][0] = lds_tr2(pa, ks * 4096, ks * 4096 + 1024);
        fb[set][k][0] = lds_b128(pb[ks], 0);
        fb[set][k][1] = lds_b128(pb[ks], 4096);
        fa[set][k][1] = lds_tr2(pa, 16384 + ks * 4096, 16384 + ks * 4096 + 1024);
      }
    };
    auto mm = [&](int set, int n0, int n1) {
#pragma unroll
      for (int n = n0; n < n1; ++n) acc[(n >> 1) & 1][n & 1] = mfma32(fa[set][n >> 2][(n >> 1) & 1], fb[set][n >> 2][n & 1], acc[(n >> 1) & 1][n & 1]);
    };
    setslot(0);
    const u32 fad = opq(L_FULL + (lane & 1) * 4);
#pragma unroll
    for (int i = 0; i < 4; ++i) asm volatile("" : "+v"(acc[i >> 1][i & 1]));
    __builtin_amdgcn_s_setprio(1);
    lflag_wait(L_FULL, 1, 1);
    rd(0);
    SCHED0();
    u32 slot = 0;
#pragma unroll 1
    for (int c = 0; c < 7; ++c) {
      const u32 fl = *(volatile LDSP(u32))fad;
      rd(1); mm(0, 0, 5);
      SGB(0x008, 1); SGB(0x100, 4); SGB(0x008, 1); SGB(0x100, 3); SGB(0x008, 1); SGB(0x100, 3); SGB(0x008, 1); SGB(0x100, 3); SGB(0x008, 1);
      SCHED0();
      if (c < 6 && __builtin_amdgcn_ballot_w64(fl < (u32)(c + 2))) lflag_wait(L_FULL, 1, c + 2);
      SCHED0();
      slot = slot == 2 * SLOT ? 0 : slot + SLOT;
      mm(0, 5, 6);
      SCHED0();
      setslot(slot);
      SCHED0();
      mm(0, 6, 8);
      SCHED0();
      if (c < 4) { WAIT_LGKM0(); BAR(); } else if (c == 6) lflag_set(L_FREE + cw * 4, 7);
      SCHED0();
      rd(0); mm(1, 0, 8);
#pragma unroll
      for (int i = 0; i < 4; ++i) { SGB(0x008, 1); SGB(0x100, 3); }
      SGB(0x008, 4);
      SCHED0();
    }
    lflag_wait(L_EPI, 0, 1);
    const u32 hdst = WS_H + (u32)((2 * panel + th) * 56 + 4 * ft + fq) * 4096 + lane * 16;
    float nm[2], c1[2], irs2[2];
#pragma unroll
    for (int j = 0; j < 2; ++j) {
      const f32x2 st = *(LDSP(f32x2))(L_STAT + (u32)(64 * th + 32 * j + l31) * 8);
      nm[j] = -st[0]; c1[j] = -1.44269504f * st[1]; irs2[j] = __builtin_amdgcn_rcpf(st[1] * st[1]);
    }
#pragma unroll
    for (int s = 0; s < 2; ++s) {
      f32x4 cs[2][2];
#pragma unroll
      for (int m = 0; m < 2; ++m)
#pragma unroll
        for (int G = 0; G < 2; ++G) cs[m][G] = *(LDSP(f32x4))(L_CSUM + (u32)(m * 128 + 32 * fq + 16 * s + 8 * G + 4 * hh) * 4);
#pragma unroll
      for (int j = 0; j < 2; ++j) {
        float hv[8];
#pragma unroll
        for (int e = 0; e < 8; ++e) {
          float a = __builtin_fmaf(nm[j], cs[0][e >> 2][e & 3], acc[0][j][8 * s + e]);
          float bb = __builtin_fmaf(nm[j], cs[1][e >> 2][e & 3], acc[1][j][8 * s + e]);
          float ex = __builtin_amdgcn_exp2f(a * c1[j]);
          float rr = __builtin_amdgcn_rcpf(__builtin_fmaf(ex, irs2[j], irs2[j]));
          hv[e] = a * bb * rr;
        }
        st128_dev(rs, hdst + (s * 2 + j) * 1024, u32x4{pk2(hv[0], hv[1]), pk2(hv[2], hv[3]), pk2(hv[4], hv[5]), pk2(hv[6], hv[7])});
      }
    }
  }
  char* hflag = ws + WS_FH + ((2 * panel + th) * 56 + 4 * ft + fq) * 128;
  u32x4 xv, sv;
  if (cw < 4) {
    const int ht = (int)opq(threadIdx.x) & 255, lane = ht & 63, l15 = ht & 15;
    const u64 tag = tag_of(*(volatile LDSP(u64))L_TAG);
    const int ch = cw >> 1, var = cw & 1;
    const bool ready = __builtin_amdgcn_ballot_w64(*(volatile LDSP(u32))(L_CONV + (lane & 3) * 4) == 0) == 0;
    if (!ready) { gflag_publish(hflag, tag, lane); lflag_wait(L_CONV, 3, 1); }
    asm volatile("" ::: "memory");
    conv_compute(lane, cw, d0 + ch, rs, hflag, tag, ready);
    gflag_publish(ws + WS_FY + (var * 448 + d0 + ch) * 128, tag, lane);
    u32x4 xr[2]; u32x2 st[2];
#pragma unroll
    for (int i = 0; i < 2; ++i) {
      const int n = ht + 256 * i, row = n >> 3, c = n & 7;
      xr[i] = *(const u32x4*)(x + (size_t)(64 * tau + row) * (D * 2) + (64 * dl + 8 * c) * 2);
      st[i] = ld64_dev(rs, WS_STAT + (u32)(64 * tau + row) * 8);
    }
    const int yvar = tau >> 4;
    gflag_wait<4>(ws + WS_FY + (yvar * 448 + 64 * dl + 16 * cw + l15) * 128, tag);
#pragma unroll
    for (int i = 0; i < 2; ++i)
      glds16_dev((u32)((lane >> 3) * (S * 2) + (((lane & 7) ^ (2 * cw + i)) << 4)),
                 ws + WS_YT + (64 * dl + 16 * cw + 8 * i) * (S * 2) + 128 * tau, xbuf(cw ^ 1) + (u32)i * 1024);
    dflag_set(L_ADONE + cw * 4, 1);
    lflag_wait(L_ADONE, 3, 1);
#pragma unroll
    for (int i = 0; i < 2; ++i) {
      const int n = ht + 256 * i, tok = n >> 3, dg = n & 7;
      const float mb = asf(st[i][0]), rsd = asf(st[i][1]);
      u32x4 so;
#pragma unroll
      for (int e = 0; e < 4; ++e) {
        const u32 ya = xbuf((dg >> 1) ^ 1) + (u32)((8 * (dg & 1) + 2 * e) * 128 + (((tok >> 3) ^ dg) << 4) + (tok & 7) * 2);
        const float y0 = bflo(*(LDSP(u16))ya), y1 = bflo(*(LDSP(u16))(ya + 128));
        const u32 dd = pk2(bflo(xr[i][e]) - mb, bfhi(xr[i][e]) - mb);
        const u32 xn = pk2(bflo(dd) * rsd, bfhi(dd) * rsd);
        so[e] = pk2(bflo(xn) * y0, bfhi(xn) * y1);
      }
      if (i == 0) { xv = xr[0]; sv = so; }
      else { lds_st128(E_S + (u32)ht * 16, so); lds_st128(E_X + (u32)ht * 16, xr[1]); }
    }
  } else {
    const int j = cw - 4;
    const int lane = (int)opq(threadIdx.x) & 63;
    lflag_set(L_PUB + j * 4, 1);
    gflag_publish(hflag, tag_of(*(volatile LDSP(u64))L_TAG), lane);
    if (PUB2) lflag_set(L_PUB + j * 4, 2);
    const u32 hl = lane * 16;
    const char* hbase = ws + WS_H + (size_t)tau * 56 * 4096;
    const int q4 = (lane >> 2) & 3;
    const u32 ta = (u32)((4 * (lane >> 5) + q4) * 128 + ((q4 >> 1) << 6) + ((lane >> 4) & 1) * 32 + (lane & 3) * 8);
    s16x8 fa[2][4], fb[3][4];
    f32x16 acc[2][2] = {};
    auto ldh = [&](int i) {
      const char* p = hbase + (size_t)g2_ks(j, i, rot2) * 4096;
      asm volatile("global_load_dwordx4 %0, %4, %5 sc1\n\tglobal_load_dwordx4 %1, %4, %5 offset:1024 sc1\n\t"
                   "global_load_dwordx4 %2, %4, %5 offset:2048 sc1\n\tglobal_load_dwordx4 %3, %4, %5 offset:3072 sc1"
                   : "=&v"(fb[i % 3][0]), "=&v"(fb[i % 3][1]), "=&v"(fb[i % 3][2]), "=&v"(fb[i % 3][3])
                   : "v"(hl), "s"(p) : "memory");
    };
    u32 tb[2];
#pragma unroll
    for (int a = 0; a < 2; ++a) tb[a] = opq((ta ^ (u32)(a << 6)) + g2_place(j, 0));
    auto rda = [&](int i) {
#pragma unroll
      for (int a = 0; a < 2; ++a)
        if (i > 0 && i % 3 == 0) tb[a] = opq(i % 6 ? tb[a] - 2 * SLOT : tb[a] + 2 * SLOT);
#pragma unroll
      for (int n = 0; n < 4; ++n) {
        const int o = (i % 3) * 4096 + (n >> 1) * 2048;
        fa[i & 1][n] = lds_tr2(tb[n & 1], o, o + 1024);
      }
    };
    const u32 fed = opq(L_FED + j * 4), prog = opq(L_PROG + j * 4);
    lflag_wait(L_H, 0, 1);
    ldh(0); ldh(1); ldh(2);
    lflag_wait(L_FED + j * 4, 0, 3);
    rda(0);
    SCHED0();
    u32 fl = 0;
#pragma unroll
    for (int i = 0; i < 14; ++i) {
      if (i == 2 || (i >= 5 && i < 13 && (i & 1))) {
        if (__builtin_amdgcn_readfirstlane(fl) < i + 2) lflag_wait(L_FED + j * 4, 0, i + 2);
      }
      asm volatile("s_waitcnt vmcnt(%4)" : "+v"(fb[i % 3][0]), "+v"(fb[i % 3][1]), "+v"(fb[i % 3][2]), "+v"(fb[i % 3][3])
                   : "i"(i < 12 ? 8 : (13 - i) * 4) : "memory");
      SCHED0();
      if (i < 13) rda(i + 1);
      if (i == 1 || (i >= 4 && i < 12 && !(i & 1))) fl = *(volatile LDSP(u32))fed;
#pragma unroll
      for (int s = 0; s < 2; ++s)
#pragma unroll
        for (int a = 0; a < 2; ++a)
#pragma unroll
          for (int t = 0; t < 2; ++t) acc[a][t] = mfma32(fa[i & 1][2 * s + a], fb[i % 3][2 * s + t], acc[a][t]);
      if (i < 13) {
#pragma unroll
        for (int n = 0; n < 5; ++n) { SGB(0x008, 1); SGB(0x100, 2); }
        SGB(0x008, 3);
      }
      SCHED0();
      if (i < 8 && (i & 1)) *(volatile LDSP(u32))prog = i + 1;
      if (i == 4) { lflag_wait(L_H + 4, 0, 1); }
      if (i + 3 < 14) ldh(i + 3);
      SCHED0();

    }
    const int ln = (int)opq(threadIdx.x), lt = ln & 31, lh = (ln >> 5) & 1;
#pragma unroll
    for (int t = 0; t < 2; ++t)
#pragma unroll
      for (int a = 0; a < 2; ++a)
#pragma unroll
        for (int G = 0; G < 4; ++G)
          *(LDSP(f32x4))(g2_place(j, t ? 3 : 0) + (u32)(lt * 256 + (((8 * a + 2 * G + lh) ^ (lt & 7)) << 4))) =
              f32x4{acc[a][t][4 * G], acc[a][t][4 * G + 1], acc[a][t][4 * G + 2], acc[a][t][4 * G + 3]};
  }
  WAIT_LGKM0();
  BAR();
  {
    const int ht = (int)opq(threadIdx.x) & 255;
    const int half = cw >> 2, tk = ht >> 3, dg = ht & 7;
    if (half) { sv = *(LDSP(u32x4))(E_S + (u32)ht * 16); xv = *(LDSP(u32x4))(E_X + (u32)ht * 16); }
    f32x4 s0 = {}, s1 = {};
#pragma unroll
    for (int w = 0; w < 4; ++w) {
      const u32 pb = (half ? 0u : 2 * SLOT) + (u32)w * 12288 + (u32)tk * 256;
      s0 += *(LDSP(f32x4))(pb + (u32)(((2 * dg) ^ (tk & 7)) << 4));
      s1 += *(LDSP(f32x4))(pb + (u32)(((2 * dg + 1) ^ (tk & 7)) << 4));
    }
    float o[8];
#pragma unroll
    for (int e = 0; e < 8; ++e) {
      const float f = rbf(e < 4 ? s0[e & 3] : s1[e & 3]);
      const float gt = rbf(silu_f(f));
      const float xe = (e & 1) ? bfhi(xv[e >> 1]) : bflo(xv[e >> 1]);
      const float se = (e & 1) ? bfhi(sv[e >> 1]) : bflo(sv[e >> 1]);
      o[e] = xe + rbf(se * gt);
    }
    *(u32x4*)(out + (size_t)(64 * tau + 32 * half + tk) * (D * 2) + (64 * dl + 8 * dg) * 2) =
        u32x4{pk2(o[0], o[1]), pk2(o[2], o[3]), pk2(o[4], o[5]), pk2(o[6], o[7])};
  }
}
}
#if defined(__HIP_DEVICE_COMPILE__) && __HIP_DEVICE_COMPILE__
#pragma clang attribute pop
#endif

extern "C" void kernel_launch(void* const* d_in, const int* in_sizes, int n_in, void* d_out, int out_size, void* d_ws,
                              size_t ws_size, hipStream_t stream) {
  (void)hipFuncSetAttribute((const void*)fused_kernel, hipFuncAttributeMaxDynamicSharedMemorySize, 163840);
  fused_kernel<<<256, 768, 163840, stream>>>((const char*)d_in[0], (const char*)d_in[1], (const char*)d_in[2], (const char*)d_in[3],
                                             (const char*)d_in[4], (const char*)d_in[5], (const char*)d_in[7], (const char*)d_in[8],
                                             (const char*)d_in[9], (char*)d_ws, (char*)d_out);
}
```

```cpp
#define PROBE_ID -1
#define PROBE_W 4
#include <hip/hip_runtime.h>
#include <hip/hip_bf16.h>
#include <stdint.h>
#define DIAG_DECL const unsigned long long pr_t0 = __builtin_amdgcn_s_memrealtime(); unsigned long long pr_t = pr_t0;
#define STAMP(i) do { if ((i) == PROBE_ID) pr_t = __builtin_amdgcn_s_memrealtime(); } while (0)
#define CYC_BEGIN
#define CYC_END
#define DIAG_FLUSH(w) do { if (blockIdx.x < 224 && wid == PROBE_W) { const unsigned long long e = PROBE_ID < 0 ? pr_t0 + 4000 : pr_t0 + 4000 + 4 * (pr_t - pr_t0); \
    for (int pi = 0; pi < 100000 && __builtin_amdgcn_s_memrealtime() < e; ++pi) __builtin_amdgcn_s_sleep(2); } } while (0)
#pragma clang diagnostic ignored "-Wint-to-pointer-cast"

#if defined(__HIP_DEVICE_COMPILE__) && __HIP_DEVICE_COMPILE__
#pragma clang attribute push(__attribute__((target("no-packed-fp32-ops"))), apply_to = function)
#endif
namespace {
typedef unsigned short u16;
typedef unsigned int u32;
typedef unsigned long long u64;
typedef __attribute__((ext_vector_type(4))) short s16x4;
typedef __attribute__((ext_vector_type(8))) short s16x8;
typedef __attribute__((ext_vector_type(8))) __bf16 bf16x8;
typedef __attribute__((ext_vector_type(2))) __bf16 bf16x2;
typedef __attribute__((ext_vector_type(16))) float f32x16;
typedef __attribute__((ext_vector_type(4))) float f32x4;
typedef __attribute__((ext_vector_type(2))) float f32x2;
typedef __attribute__((ext_vector_type(4))) u32 u32x4;
typedef __attribute__((ext_vector_type(2))) u32 u32x2;
#define LDSP(T) __attribute__((address_space(3))) T*
#define GLBP(T) __attribute__((address_space(1))) T*
#define DEV __device__ __forceinline__

constexpr int S = 2048, D = 448, F = 1792;
constexpr u32 WS_H = 0;
constexpr u32 WS_UT = WS_H + 7340032;
constexpr u32 WS_YT = WS_UT + 1835008;
constexpr u32 WS_STAT = WS_YT + 1835008;
constexpr u32 WS_CSUM = WS_STAT + 16384;
constexpr u32 WS_E = WS_CSUM + 16384;
constexpr u32 WS_FUT = WS_E + 128;
constexpr u32 WS_FST = WS_FUT + 448 * 128;
constexpr u32 WS_FY = WS_FST + 32 * 128;
constexpr u32 WS_FH = WS_FY + 896 * 128;
constexpr u32 WS_END = WS_FH + 1792 * 128;
constexpr int CODE_LINES = 224;
constexpr u32 SLOT = 49152;
constexpr u32 MISC = 3 * SLOT;
constexpr u32 L_FULL = MISC, L_EPI = MISC + 8, L_H = MISC + 16, L_FREE = MISC + 32, L_CONV = MISC + 64, L_FED = MISC + 80,
              L_TAG = MISC + 96, L_DD = MISC + 104, L_ADONE = MISC + 112, L_PROG = MISC + 128, L_XCH = MISC + 144, L_PUB = MISC + 160, L_STAT = MISC + 256,
              L_CSUM = MISC + 1280, L_USCR = MISC + 4096, L_DUMMY = MISC + 12288;
constexpr u32 CV = SLOT, CV_HSTR = 4160, CV_U = CV + 8 * CV_HSTR, CV_USTR = 4608;
constexpr u32 E_S = SLOT, E_X = SLOT + 4096;
constexpr int SPIN_CAP = 1 << 18;
#define ROT1 0
#define ROT2 0
#define TOUCH2_AT 0
#define PACE_L 63
#define PACE_S 63
#define FREE_SET dflag_set
#define W2PF 1
#define KVPF 0
#define SHARES 0
#define CODEPF 0
#define W2_HOLD 0
#define PUB2 0
#define KV_AT 1

__device__ const u32 seq_tab[160] __attribute__((aligned(128))) = {0, 1, 2, 3, 4, 5, 6, 7};
DEV float asf(u32 v) { return __builtin_bit_cast(float, v); }
DEV float bflo(u32 v) { return asf(v << 16); }
DEV float bfhi(u32 v) { return asf(v & 0xffff0000u); }
template <int CTRL> DEV float dpp(float v) { return asf(__builtin_amdgcn_update_dpp(0, __builtin_bit_cast(u32, v), CTRL, 0xf, 0xf, true)); }
DEV float sum8(float v) { v += dpp<0xB1>(v); v += dpp<0x4E>(v); return v + dpp<0x141>(v); }
DEV u32 pk2(float lo, float hi) {
  asm("" : "+v"(lo)); asm("" : "+v"(hi));
  bf16x2 r = {(__bf16)lo, (__bf16)hi};
  return __builtin_bit_cast(u32, r);
}
DEV float rbf(float f) { bf16x2 r = {(__bf16)f, (__bf16)f}; return bflo(__builtin_bit_cast(u32, r)); }
DEV f32x4 mfma16(s16x8 a, s16x8 b, f32x4 c) {
  return __builtin_amdgcn_mfma_f32_16x16x32_bf16(__builtin_bit_cast(bf16x8, a), __builtin_bit_cast(bf16x8, b), c, 0, 0, 0);
}
DEV f32x16 mfma32(s16x8 a, s16x8 b, f32x16 c) {
  return __builtin_amdgcn_mfma_f32_32x32x16_bf16(__builtin_bit_cast(bf16x8, a), __builtin_bit_cast(bf16x8, b), c, 0, 0, 0);
}
DEV u32 opq(u32 v) { asm volatile("" : "+v"(v)); return v; }
DEV s16x8 lds_tr2(u32 base, int o0, int o1) {
  s16x4 lo = __builtin_amdgcn_ds_read_tr16_b64_v4i16((LDSP(s16x4))((LDSP(char))base + o0));
  s16x4 hi = __builtin_amdgcn_ds_read_tr16_b64_v4i16((LDSP(s16x4))((LDSP(char))base + o1));
  return __builtin_shufflevector(lo, hi, 0, 1, 2, 3, 4, 5, 6, 7);
}
DEV s16x8 lds_b128(u32 base, int o) { return *(LDSP(s16x8))((LDSP(char))base + o); }
DEV void lds_st128(u32 a, u32x4 v) { *(LDSP(u32x4))a = v; }
DEV void glds16x4(u32 v0, u32 v1, u32 v2, u32 v3, const void* sbase, u32 ldst) {
  asm volatile("s_mov_b32 m0, %5\n\ts_nop 0\n\tglobal_load_lds_dwordx4 %0, %4\n\tglobal_load_lds_dwordx4 %1, %4 offset:1024\n\t"
               "global_load_lds_dwordx4 %2, %4 offset:2048\n\tglobal_load_lds_dwordx4 %3, %4 offset:3072"
               ::"v"(v0), "v"(v1), "v"(v2), "v"(v3), "s"(sbase), "s"(ldst) : "memory");
}
DEV void glds16(u32 voff, const void* sbase, u32 ldst) {
  asm volatile("s_mov_b32 m0, %2\n\ts_nop 0\n\tglobal_load_lds_dwordx4 %0, %1" ::"v"(voff), "s"(sbase), "s"(ldst) : "memory");
}
DEV void glds16x4_lin(u32 v, const void* sbase, u32 ldst) { glds16x4(v, v, v, v, sbase, ldst); }
DEV void glds16x4_dev(u32 v, const void* sbase, u32 ldst) {
  asm volatile("s_mov_b32 m0, %2\n\ts_nop 0\n\tglobal_load_lds_dwordx4 %0, %1 sc1\n\tglobal_load_lds_dwordx4 %0, %1 offset:1024 sc1\n\t"
               "global_load_lds_dwordx4 %0, %1 offset:2048 sc1\n\tglobal_load_lds_dwordx4 %0, %1 offset:3072 sc1"
               ::"v"(v), "s"(sbase), "s"(ldst) : "memory");
}
DEV void glds16_dev(u32 voff, const void* sbase, u32 ldst) {
  asm volatile("s_mov_b32 m0, %2\n\ts_nop 0\n\tglobal_load_lds_dwordx4 %0, %1 sc1" ::"v"(voff), "s"(sbase), "s"(ldst) : "memory");
}
DEV void dflag_set(u32 addr, u32 val) {
  asm volatile("s_waitcnt lgkmcnt(0)\n\ts_mov_b32 m0, %2\n\ts_mov_b64 exec, 1\n\tglobal_load_lds_dword %0, %1\n\ts_mov_b64 exec, -1"
               ::"v"(0), "s"(seq_tab + val), "s"(addr) : "memory");
}
#define WAIT_VM(n) asm volatile("s_waitcnt vmcnt(%0)" ::"i"(n) : "memory")
#define WAIT_LGKM0() asm volatile("s_waitcnt lgkmcnt(0)" ::: "memory")
#define BAR() __builtin_amdgcn_s_barrier()
#define SCHED0() __builtin_amdgcn_sched_barrier(0)
#define SGB(mask, n) __builtin_amdgcn_sched_group_barrier(mask, n, 0)
DEV float silu_f(float x) { return x * __builtin_amdgcn_rcpf(1.0f + __builtin_amdgcn_exp2f(-1.44269504f * x)); }

DEV void lflag_set(u32 addr, u32 val) {
  asm volatile("s_waitcnt lgkmcnt(0)\n\tds_write_b32 %0, %1" ::"v"(addr), "v"(val) : "memory");
}
DEV void lflag_wait(u32 addr, u32 mask, u32 val) {
  const u32 a = addr + 4 * (threadIdx.x & mask);
#pragma unroll 1
  for (int i = 0; i < SPIN_CAP; ++i) {
    u32 v;
    asm volatile("ds_read_b32 %0, %1\n\ts_waitcnt lgkmcnt(0)" : "=v"(v) : "v"(a) : "memory");
    if (__builtin_amdgcn_ballot_w64(v < val) == 0) break;
    __builtin_amdgcn_s_sleep(1);
  }
}
DEV u64 gload64(const char* p) { return __hip_atomic_load((GLBP(u64))p, __ATOMIC_RELAXED, __HIP_MEMORY_SCOPE_AGENT); }
DEV void gstore64(char* p, u64 v) { __hip_atomic_store((GLBP(u64))p, v, __ATOMIC_RELAXED, __HIP_MEMORY_SCOPE_AGENT); }
template <int SLEEP>
DEV void gflag_wait(const char* lane_ptr, u64 tag) {
#pragma unroll 1
  for (int i = 0; i < SPIN_CAP; ++i) {
    asm volatile("" ::: "memory");
    if (__builtin_amdgcn_ballot_w64(gload64(lane_ptr) != tag) == 0) break;
    __builtin_amdgcn_s_sleep(SLEEP);
  }
  asm volatile("" ::: "memory");
}
DEV void gflag_publish(char* p, u64 tag, int lane) {
  WAIT_VM(0);
  if (lane == 0) gstore64(p, tag);
}
DEV u64 tag_of(u64 seed) { return seed * 0x9E3779B97F4A7C15ull + 0xD1B54A32D192ED03ull; }
DEV u64 make_tag(const char* ws) { return tag_of(gload64(ws + WS_E)); }
typedef __amdgpu_buffer_rsrc_t rsrc_t;
DEV rsrc_t ws_rsrc(char* ws) { return __builtin_amdgcn_make_buffer_rsrc(ws, 0, WS_END, 0x00020000); }
DEV void st128_dev(rsrc_t r, u32 off, u32x4 v) { __builtin_amdgcn_raw_buffer_store_b128(v, r, off, 0, 16); }
DEV void st64_dev(rsrc_t r, u32 off, u32x2 v) { __builtin_amdgcn_raw_buffer_store_b64(v, r, off, 0, 16); }
DEV void st32_dev(rsrc_t r, u32 off, u32 v) { __builtin_amdgcn_raw_buffer_store_b32(v, r, off, 0, 16); }
DEV u32x4 ld128_dev(rsrc_t r, u32 off) { return __builtin_amdgcn_raw_buffer_load_b128(r, off, 0, 16); }
DEV u32x2 ld64_dev(rsrc_t r, u32 off) { return __builtin_amdgcn_raw_buffer_load_b64(r, off, 0, 16); }
DEV void touch(u32 voff, const void* sbase, u32 ldst) {
  asm volatile("s_mov_b32 m0, %2\n\ts_nop 0\n\tglobal_load_lds_dword %0, %1" ::"v"(voff), "s"(sbase), "s"(ldst) : "memory");
}

DEV void glds4_dev(u32 voff, const void* sbase, u32 ldst) {
  asm volatile("s_mov_b32 m0, %2\n\ts_nop 0\n\tglobal_load_lds_dword %0, %1 sc1" ::"v"(voff), "s"(sbase), "s"(ldst) : "memory");
}
DEV u32 shr16(u32 hi, u32 lo) { return __builtin_amdgcn_alignbit(hi, lo, 16); }

DEV u32 xbuf(int w) { return w < 2 ? L_USCR + (u32)w * 4096 : (w == 2 ? L_DUMMY : CV + 45056); }
DEV void conv_compute(int lane, int cw, int d, rsrc_t rs, char* hflag, u64 tag, bool pub) {
  const int ch = cw >> 1, var = cw & 1;
  const int m = lane & 15, g = lane >> 4, q4 = m >> 2, p4 = lane & 3;
  u32 aa = opq(CV + (u32)(ch * 4 + ((q4 + 1) & 3)) * CV_HSTR + (u32)(((q4 + 1) & 4) + 8 * g + 4 * p4) * 2 + var * 256);
  u32 ba = opq(CV_U + (u32)ch * CV_USTR + (u32)(240 - 16 * m + 8 * g) * 2 + var * 256);
  f32x4 acc[8] = {};
  s16x8 A[8], B[8];
#pragma unroll
  for (int p = 0; p < 8; ++p) { A[7 - p] = lds_tr2(aa, (7 - p) * 512, (7 - p) * 512 + 8); B[p] = lds_b128(ba, (7 - p) * 512); }
  if (var == 0) {
    const u32 a0 = CV + (u32)ch * 4 * CV_HSTR + (u32)(m + 1 + 8 * g) * 2;
#pragma unroll
    for (int j = 0; j < 8; ++j) { const short v = *(LDSP(short))(a0 + j * 2); A[0][j] = m + 8 * g + j >= 15 ? v : (short)0; }
  }
  SCHED0();
#pragma unroll 1
  for (int it = 0; it < 4; ++it) {
#pragma unroll
    for (int p = 7; p >= 0; --p) {
#pragma unroll
      for (int dd = 0; dd <= 7 - p; ++dd) {
        acc[p + dd] = mfma16(A[p], B[dd], acc[p + dd]);
        if (p == 0) { SCHED0(); B[dd] = lds_b128(ba, 64 + (7 - dd) * 512); SCHED0(); }
      }
      SCHED0();
      A[p] = lds_tr2(aa, 64 + p * 512, 64 + p * 512 + 8);
      SCHED0();
    }
    aa = opq(aa + 64); ba = opq(ba + 64);
    if (it == 0 && pub) gflag_publish(hflag, tag, lane);
  }
  f32x4 y[4];
#pragma unroll
  for (int i = 0; i < 4; ++i) {
    *(LDSP(f32x4))(xbuf(cw) + (u32)(i * 1024 + lane * 16)) = var ? acc[i] : acc[4 + i];
    y[i] = var ? acc[4 + i] : acc[i];
  }
  lflag_set(L_XCH + cw * 4, 1);
  const u32 dv = *(LDSP(u32))L_DD;
  const float Dd = ch ? bfhi(dv) : bflo(dv);
  u32x2 uu[4];
#pragma unroll
  for (int i = 0; i < 4; ++i) uu[i] = *(LDSP(u32x2))(CV_U + (u32)ch * CV_USTR + (u32)(2044 - (1024 * var + 256 * i + 16 * m + 4 * g)) * 2);
  lflag_wait(L_XCH + (cw ^ 1) * 4, 0, 1);
#pragma unroll
  for (int i = 0; i < 4; ++i) {
    const int t = 1024 * var + 256 * i + 16 * m + 4 * g;
    const f32x4 s = y[i] + *(LDSP(f32x4))(xbuf(cw ^ 1) + (u32)(i * 1024 + lane * 16));
    float y0 = rbf(s[0]) + rbf(bfhi(uu[i][1]) * Dd), y1 = rbf(s[1]) + rbf(bflo(uu[i][1]) * Dd);
    float y2 = rbf(s[2]) + rbf(bfhi(uu[i][0]) * Dd), y3 = rbf(s[3]) + rbf(bflo(uu[i][0]) * Dd);
    st64_dev(rs, WS_YT + (u32)(d * (S * 2) + t * 2), u32x2{pk2(y0, y1), pk2(y2, y3)});
  }
}

DEV int rot7(int i, int rot) { const int v = i + rot; return v >= 7 ? v - 7 : v; }
DEV int g2_ks(int j, int i, int rot) { const int e = 7 * j + rot7(i < 7 ? i : i - 7, rot); return 4 * (e >> 1) + (e & 1) + (i < 7 ? 0 : 2); }
DEV u32 g2_place(int j, int p) { return (p < 3 ? 2 * SLOT + (u32)p * 4096 : (u32)(p - 3) * 4096) + (u32)j * 12288; }
DEV u32 w2_voff(int lane) { return (u32)((lane >> 3) * (D * 2) + (((lane & 7) ^ (((lane >> 4) & 1) << 2)) << 4)); }

__global__ __launch_bounds__(768, 3) void fused_kernel(const char* x, const char* kp, const char* vp, const char* skp,
                                                       const char* ssmk, const char* ssmD, const char* w1, const char* w3,
                                                       const char* w2, char* ws, char* out) {
  DIAG_DECL STAMP(0);
  const int b = blockIdx.x, xcd = b & 7, r = b >> 3;
  const int wid = __builtin_amdgcn_readfirstlane(threadIdx.x >> 6);
  const int panel = (xcd >> 1) * 4 + r / 7, ft = (xcd & 1) * 7 + r % 7;
  const int tau = xcd * 4 + r / 7, dl = r % 7;
  const int d0 = 2 * b;
  const int rot = ROT1 ? (r % 7 + 2 * (r / 7)) % 7 : 0, rot2 = ROT2 ? (r % 7 + 2 * (r / 7)) % 7 : 0;

  if (b < 224) {
    if (wid == 7) { *(LDSP(u32))(MISC + (threadIdx.x & 63) * 4) = 0; WAIT_LGKM0(); }
    BAR();
    if (wid == 7 && (threadIdx.x & 63) < 5) touch((threadIdx.x & 63) * 128, seq_tab, L_DUMMY);
  }

  if (__builtin_expect(wid < 4 && b < 224, 1)) {
    const int lane = (int)opq(threadIdx.x) & 63;
    u64 tag = 0;
    if (__builtin_expect(wid < 2, 1)) {
      const int g = lane >> 4, l15 = lane & 15;
      u32 vx[8], vw[8];
      const u32 ox = (u32)((lane >> 3) * (D * 2) + (((lane & 7) ^ g) << 4));
      const u32 ow = (u32)(g * (F * 2) + (((l15 >> 2) ^ g) << 6) + ((lane & 3) << 4));
#pragma unroll
      for (int i = 0; i < 8; ++i) {
        vx[i] = (ox ^ (u32)((i & 1) * 64)) + i * 8 * (D * 2) - (i & 3) * 1024;
        vw[i] = ow + i * 4 * (F * 2) - (i & 3) * 1024;
      }
#pragma unroll 1
      for (int c = 0; c < 7; ++c) {
        if (c >= 3) BAR();
        const u32 slot = (u32)(c % 3) * SLOT + wid * 8192;
        const int kc = rot7(c, rot);
        const char* xb = x + (size_t)(panel * 128 + wid * 64) * (D * 2) + kc * 128;
        const char* w1b = w1 + (size_t)(kc * 64 + wid * 32) * (F * 2) + ft * 256;
        const char* w3b = w3 + (size_t)(kc * 64 + wid * 32) * (F * 2) + ft * 256;
#pragma unroll
        for (int i = 0; i < 8; i += 4) {
          WAIT_VM(PACE_L);
          glds16x4(vx[i], vx[i + 1], vx[i + 2], vx[i + 3], xb, slot + i * 1024);
          WAIT_VM(PACE_L);
          glds16x4(vw[i], vw[i + 1], vw[i + 2], vw[i + 3], w1b, slot + 16384 + i * 1024);
          WAIT_VM(PACE_L);
          glds16x4(vw[i], vw[i + 1], vw[i + 2], vw[i + 3], w3b, slot + 32768 + i * 1024);

        }
        dflag_set(L_FULL + wid * 4, c + 1);
        if (c == 0) STAMP(1); else if (c == 2) STAMP(2); else if (c == 4) STAMP(3); else if (c == 6) STAMP(4);
      }
    } else {
      __builtin_amdgcn_s_setprio(3);
      const rsrc_t rs = ws_rsrc(ws);
      const int u = wid - 2;
      const int ti = b / 7, cj = b % 7;
      const int tg = lane >> 3, cg = lane & 7;
      const int t0 = 64 * ti + 32 * u + 4 * tg, ch0 = 64 * cj + 8 * cg;
      u32x4 kr[7], vr[4], sw[4]; u32x2 o[8];
      u64 seed;
      auto ldkv = [&]() {
        const u32 ko = (u32)(t0 * (D * 2) + ch0 * 2);
#pragma unroll
        for (int i = 0; i < 7; ++i) kr[i] = *(const u32x4*)(kp + (i < 3 && t0 == 0 ? ko : ko + (u32)((i - 3) * (D * 2))));
#pragma unroll
        for (int i = 0; i < 4; ++i) vr[i] = *(const u32x4*)(vp + (ko + (u32)(i * (D * 2))));
        SCHED0();
      };
      auto piece = [&](int p) {
#pragma unroll
        for (int c = 2 * p; c < 2 * p + 2; ++c) {
          const u32 wa = sw[p][(c & 1) * 2], wb = sw[p][(c & 1) * 2 + 1];
          const float w0 = bflo(wa), w1f = bfhi(wa), w2f = bflo(wb), w3f = bfhi(wb);
          float kf[7], ks[4];
#pragma unroll
          for (int i = 0; i < 7; ++i) kf[i] = (c & 1) ? bfhi(kr[i][p]) : bflo(kr[i][p]);
#pragma unroll
          for (int i = 0; i < 4; ++i)
            ks[i] = __builtin_fmaf(kf[i + 3], w0, __builtin_fmaf(kf[i + 2], w1f, __builtin_fmaf(kf[i + 1], w2f, kf[i] * w3f)));
#pragma unroll
          for (int i = 0; i < 2; ++i) {
            const int ta = 3 - 2 * i, tb = 2 - 2 * i;
            const u32 kk = pk2(ks[ta], ks[tb]);
            o[c][i] = pk2(bflo(kk) * ((c & 1) ? bfhi(vr[ta][p]) : bflo(vr[ta][p])), bfhi(kk) * ((c & 1) ? bfhi(vr[tb][p]) : bflo(vr[tb][p])));
          }
          asm volatile("" : "+v"(o[c]));
        }
        SCHED0();
      };
      ldkv();
#pragma unroll
      for (int i = 0; i < 4; ++i) sw[i] = *(const u32x4*)(skp + (u32)(ch0 * 8 + i * 16));
      seed = gload64(ws + WS_E + opq(0));
      SCHED0();
      STAMP(1);
#pragma unroll
      for (int i = 0; i < 3; ++i) if (t0 == 0) kr[i] = u32x4{0, 0, 0, 0};
#pragma unroll
      for (int c = 0; c < 4; ++c) piece(c);
      tag = tag_of(seed);
      const u32 scr = L_USCR + (u32)u * 4096;
#pragma unroll
      for (int c = 0; c < 8; ++c) *(LDSP(u32x2))(scr + (u32)((cg * 8 + c) * 64 + (((7 - tg) ^ (cg & 6)) << 3))) = o[c];
      SCHED0();
      BAR();
      WAIT_LGKM0();
      STAMP(3);
#pragma unroll
      for (int i = 0; i < 4; ++i) {
        const int dd = 16 * i + (lane >> 2), c4 = lane & 3;
        u32x4 v = *(LDSP(u32x4))(scr + (u32)(dd * 64 + ((c4 ^ (dd >> 4)) << 4)));
        st128_dev(rs, WS_UT + (u32)((64 * cj + dd) * (S * 2) + (2016 - 64 * ti - 32 * u) * 2 + c4 * 16), v);
      }
      SCHED0();
      STAMP(4);
      BAR();
      gflag_publish(ws + WS_FUT + (cj * 64 + 2 * ti + u) * 128, tag, lane);
      STAMP(5);
      BAR();
      BAR();
      const int k = lane & 7;
      const char* fp = ws + (u ? WS_FST + (k < 2 ? 2 * panel + k : (k == 2 ? tau : (k & 1) * 14 + ft)) * 128 : WS_FUT + ((d0 >> 6) * 64 + lane) * 128);
      gflag_wait<8>(fp, tag);
      if (u) {
        glds16_dev(lane * 16, ws + WS_STAT + panel * 1024, L_STAT);
        glds16_dev(lane * 16, ws + WS_CSUM + ft * 1024, L_CSUM);
        if (lane < 2) glds4_dev(lane * 4, ws + WS_E, L_TAG);
        if (lane < 1) glds4_dev(0, ssmD + d0 * 2, L_DD);
        dflag_set(L_EPI, 1);
      }
      STAMP(6);
    }
    const int j = wid;
    const u32 o2 = w2_voff(lane);
    const char* wbase = w2 + dl * 128;
    u32x4 pk[8][4];
    auto park = [&](int i0) {
#pragma unroll
    for (int i = i0; i < i0 + 4; ++i) {
      const char* src = wbase + (size_t)g2_ks(j, i + 6, rot2) * 32 * (D * 2);
      WAIT_VM(PACE_S);
#pragma unroll
      for (int pc = 0; pc < 4; ++pc) pk[i][pc] = *(const u32x4*)(src + (o2 + (u32)(pc * 8 * (D * 2))));
    }
    SCHED0();
    STAMP(8);
    };
    park(0);
    park(4);
    lflag_wait(L_FREE, 7, 7);
    STAMP(13);
    if (wid == 2) {
        if (lane < 32) {
          glds16(lane * 16, seq_tab + 32, CV_U + 4096);
          glds16(lane * 16, seq_tab + 32, CV_U + CV_USTR + 4096);
        }
        glds16x4_dev(lane * 16, ws + WS_UT + d0 * (S * 2), CV_U);
        glds16x4_dev(lane * 16, ws + WS_UT + (d0 + 1) * (S * 2), CV_U + CV_USTR);
        dflag_set(L_CONV + 8, 1);
        dflag_set(L_CONV + 12, 1);
        STAMP(7);
      }
    if (wid < 2) {
      const u32 vl = lane * 16;
#pragma unroll 1
      for (int rr = 0; rr < 4; ++rr) {
        const char* src = ssmk + (size_t)(d0 + wid) * (S * 2) + (rr ? (rr - 4) * 2 : 0);
        const u32 dst = CV + (u32)(wid * 4 + rr) * CV_HSTR + (rr ? 24 : 32);
        glds16x4(rr && !lane ? 16u : vl, vl, vl, vl, src, dst);
      }
      dflag_set(L_CONV + wid * 4, 1);
      STAMP(9);
    }
#pragma unroll 1
    for (int i = 0; i < 6; ++i) {
      if (i == W2_HOLD) lflag_wait(L_PUB, 3, 1);
      if (i == 3) dflag_set(L_FED + j * 4, 3);
      const char* src = wbase + (size_t)g2_ks(j, i, rot2) * 32 * (D * 2);
      WAIT_VM(PACE_S);
      glds16x4(o2, o2 + 8 * (D * 2) - 1024, o2 + 16 * (D * 2) - 2048, o2 + 24 * (D * 2) - 3072, src, g2_place(j, i));
    }
    dflag_set(L_FED + j * 4, 6);
    STAMP(10);
    if (wid >= 2) {
      lflag_wait(L_PUB, 3, PUB2 ? 2 : 1);
      const int e = lane < 28 ? lane : 27;
      gflag_wait<4>(ws + WS_FH + (tau * 56 + 4 * (e >> 1) + (e & 1) + 2 * (3 - wid)) * 128, tag);
      dflag_set(L_H + (3 - wid) * 4, 1);
    }
    WAIT_VM(0);
    STAMP(11);
    const u32 wl = opq(lane * 16 + j * 12288);
#pragma unroll
    for (int i = 0; i < 8; i += 2) {
      lflag_wait(L_PROG + j * 4, 0, i + 2);
#pragma unroll
      for (int n = 0; n < 8; ++n) lds_st128(wl + g2_place(0, (i + (n >> 2)) % 6) + (n & 3) * 1024, pk[i + (n >> 2)][n & 3]);
      lflag_set(L_FED + j * 4, i + 8);
    }
    STAMP(12);
    DIAG_FLUSH(ws);
    return;
  }
  const rsrc_t rs = ws_rsrc(ws);

  if (b >= 224) {
    if (wid >= 8 && b != 255) return;
    const int tid = (int)opq(threadIdx.x), lane = tid & 63;
    const int sp = b - 224;
    const u64 seed = gload64(ws + WS_E + opq(0));
    if (KVPF && wid < 7) {
      touch((u32)(sp * 448 + tid) * 128, kp, L_DUMMY);
      touch((u32)(sp * 448 + tid) * 128, vp, L_DUMMY);
    }
    if (KVPF) BAR();
    if (wid < 8) {
      const int row = 64 * sp + (tid >> 3), c8 = tid & 7;
      const int cu = sp < 28 ? sp : 27, mat = cu / 14, ct = cu % 14, kg = tid >> 4, c16 = tid & 15;
      const char* wp = (mat ? w3 : w1) + ct * 256;
      const u32 wo = (u32)(kg * (F * 2) + c16 * 16), xo = (u32)(row * (D * 2) + c8 * 16);
      u32x4 xv[7], wv[14];
#pragma unroll
      for (int i = 0; i < 7; ++i) xv[i] = *(const u32x4*)(x + (xo + i * 128));
#pragma unroll
      for (int i = 0; i < 14; ++i) wv[i] = *(const u32x4*)(wp + (wo + i * 32 * (F * 2)));
      SCHED0();
      float s = 0.f;
#pragma unroll
      for (int i = 0; i < 7; ++i)
#pragma unroll
        for (int j = 0; j < 4; ++j) s += bflo(xv[i][j]) + bfhi(xv[i][j]);
      const float m = sum8(s) * (1.0f / D);
      float q = 0.f;
#pragma unroll
      for (int i = 0; i < 7; ++i)
#pragma unroll
        for (int j = 0; j < 4; ++j) {
          const float aa = bflo(xv[i][j]) - m, bb = bfhi(xv[i][j]) - m;
          q = __builtin_fmaf(aa, aa, __builtin_fmaf(bb, bb, q));
        }
      const float sd = rbf(__builtin_amdgcn_sqrtf(rbf(rbf(sum8(q) * (1.0f / D)) + 1e-5f)));
      if (c8 == 0) *(LDSP(f32x2))(u32)(16384 + (tid >> 3) * 8) = f32x2{rbf(m), __builtin_amdgcn_rcpf(sd)};
      SCHED0();
      float a[8] = {};
#pragma unroll
      for (int i = 0; i < 14; ++i)
#pragma unroll
        for (int j = 0; j < 4; ++j) { a[2 * j] += bflo(wv[i][j]); a[2 * j + 1] += bfhi(wv[i][j]); }
      f32x4 t;
#pragma unroll
      for (int j = 0; j < 4; ++j) {
        auto p = __builtin_amdgcn_permlane16_swap(__builtin_bit_cast(u32, a[j]), __builtin_bit_cast(u32, a[j + 4]), false, false);
        t[j] = asf(p[0]) + asf(p[1]);
      }
      *(LDSP(f32x4))(u32)((kg >> 1) * 512 + c16 * 32 + (kg & 1) * 16) = t;
      STAMP(1);
    }
    WAIT_LGKM0(); BAR();
    const u64 tag = tag_of(seed);
    if (wid == 0) {
      const int cu = sp < 28 ? sp : 27;
      f32x2 cs = {};
#pragma unroll
      for (int i = 0; i < 16; ++i) cs += *(LDSP(f32x2))(u32)(i * 512 + lane * 8);
      st64_dev(rs, WS_STAT + (u32)(64 * sp + lane) * 8, *(LDSP(u32x2))(u32)(16384 + lane * 8));
      if (sp < 28) st64_dev(rs, WS_CSUM + (u32)((cu % 14) * 1024 + (cu / 14) * 512 + lane * 8), __builtin_bit_cast(u32x2, cs));
      gflag_publish(ws + WS_FST + sp * 128, tag, lane);
    }
    STAMP(2);
    WAIT_VM(0); BAR();
    if (W2PF && wid > 0 && wid < 8) {
#pragma unroll 1
      for (int i = 0; i < 7; ++i) touch((u32)((((sp >> 3) * 7 + i) * 7 + wid - 1) * 64 + lane) * 128, w2, L_DUMMY);
      WAIT_VM(0);
    }
    STAMP(4);
    if (b == 255) {
      gflag_wait<64>(ws + (tid < 480 ? WS_FUT + tid * 128 : WS_FH + ((((tid < 704 ? tid - 480 : 223) / 14) * 2) * 56 + ((tid < 704 ? tid - 480 : 223) % 14) * 4) * 128), tag);
      BAR();
      if (tid == 0) gstore64(ws + WS_E, tag);
    }
    STAMP(3);
    DIAG_FLUSH(ws);
    return;
  }

  const int cw = wid - 4, th = cw & 1, fq = cw >> 1;
  {
    const int lane = (int)opq(threadIdx.x) & 63, g = lane >> 4, hh = lane >> 5, l31 = lane & 31;
    const int q4 = (lane >> 2) & 3, p4 = lane & 3;
    const u32 aA = 16384 + (u32)((8 * hh + q4) * 256 + ((fq ^ q4) << 6) + (g & 1) * 32 + p4 * 8);
    const u32 aB = (u32)((64 * th + l31) * 128 + ((hh ^ ((l31 >> 1) & 7)) << 4));
    f32x16 acc[2][2] = {};
    s16x8 fa[2][2][2], fb[2][2][2];
    u32 pa, pb[4];
    auto setslot = [&](u32 slot) {
      pa = opq(aA + slot);
#pragma unroll
      for (int k = 0; k < 4; ++k) pb[k] = opq((aB ^ (u32)(k << 5)) + slot);
    };
    auto rd = [&](int set) {
#pragma unroll
      for (int k = 0; k < 2; ++k) {
        const int ks = 2 * set + k;
        fa[set][k][0] = lds_tr2(pa, ks * 4096, ks * 4096 + 1024);
        fb[set][k][0] = lds_b128(pb[ks], 0);
        fb[set][k][1] = lds_b128(pb[ks], 4096);
        fa[set][k][1] = lds_tr2(pa, 16384 + ks * 4096, 16384 + ks * 4096 + 1024);
      }
    };
    auto mm = [&](int set, int n0, int n1) {
#pragma unroll
      for (int n = n0; n < n1; ++n) acc[(n >> 1) & 1][n & 1] = mfma32(fa[set][n >> 2][(n >> 1) & 1], fb[set][n >> 2][n & 1], acc[(n >> 1) & 1][n & 1]);
    };
    setslot(0);
    const u32 fad = opq(L_FULL + (lane & 1) * 4);
#pragma unroll
    for (int i = 0; i < 4; ++i) asm volatile("" : "+v"(acc[i >> 1][i & 1]));
    __builtin_amdgcn_s_setprio(1);
    lflag_wait(L_FULL, 1, 1);
    STAMP(1);
    CYC_BEGIN
    rd(0);
    SCHED0();
    u32 slot = 0;
#pragma unroll 1
    for (int c = 0; c < 7; ++c) {
      const u32 fl = *(volatile LDSP(u32))fad;
      rd(1); mm(0, 0, 5);
      SGB(0x008, 1); SGB(0x100, 4); SGB(0x008, 1); SGB(0x100, 3); SGB(0x008, 1); SGB(0x100, 3); SGB(0x008, 1); SGB(0x100, 3); SGB(0x008, 1);
      SCHED0();
      if (c < 6 && __builtin_amdgcn_ballot_w64(fl < (u32)(c + 2))) lflag_wait(L_FULL, 1, c + 2);
      SCHED0();
      slot = slot == 2 * SLOT ? 0 : slot + SLOT;
      mm(0, 5, 6);
      SCHED0();
      setslot(slot);
      SCHED0();
      mm(0, 6, 8);
      SCHED0();
      if (c < 4) { WAIT_LGKM0(); BAR(); } else if (c == 6) lflag_set(L_FREE + cw * 4, 7);
      SCHED0();
      rd(0); mm(1, 0, 8);
#pragma unroll
      for (int i = 0; i < 4; ++i) { SGB(0x008, 1); SGB(0x100, 3); }
      SGB(0x008, 4);
      SCHED0();
      if (c == 2) STAMP(2);
    }
    STAMP(3);
    CYC_END;
    lflag_wait(L_EPI, 0, 1);
    STAMP(14);
    const u32 hdst = WS_H + (u32)((2 * panel + th) * 56 + 4 * ft + fq) * 4096 + lane * 16;
    float nm[2], c1[2], irs2[2];
#pragma unroll
    for (int j = 0; j < 2; ++j) {
      const f32x2 st = *(LDSP(f32x2))(L_STAT + (u32)(64 * th + 32 * j + l31) * 8);
      nm[j] = -st[0]; c1[j] = -1.44269504f * st[1]; irs2[j] = __builtin_amdgcn_rcpf(st[1] * st[1]);
    }
#pragma unroll
    for (int s = 0; s < 2; ++s) {
      f32x4 cs[2][2];
#pragma unroll
      for (int m = 0; m < 2; ++m)
#pragma unroll
        for (int G = 0; G < 2; ++G) cs[m][G] = *(LDSP(f32x4))(L_CSUM + (u32)(m * 128 + 32 * fq + 16 * s + 8 * G + 4 * hh) * 4);
#pragma unroll
      for (int j = 0; j < 2; ++j) {
        float hv[8];
#pragma unroll
        for (int e = 0; e < 8; ++e) {
          float a = __builtin_fmaf(nm[j], cs[0][e >> 2][e & 3], acc[0][j][8 * s + e]);
          float bb = __builtin_fmaf(nm[j], cs[1][e >> 2][e & 3], acc[1][j][8 * s + e]);
          float ex = __builtin_amdgcn_exp2f(a * c1[j]);
          float rr = __builtin_amdgcn_rcpf(__builtin_fmaf(ex, irs2[j], irs2[j]));
          hv[e] = a * bb * rr;
        }
        st128_dev(rs, hdst + (s * 2 + j) * 1024, u32x4{pk2(hv[0], hv[1]), pk2(hv[2], hv[3]), pk2(hv[4], hv[5]), pk2(hv[6], hv[7])});
      }
    }
    STAMP(4);
  }
  char* hflag = ws + WS_FH + ((2 * panel + th) * 56 + 4 * ft + fq) * 128;
  u32x4 xv, sv;
  if (cw < 4) {
    const int ht = (int)opq(threadIdx.x) & 255, lane = ht & 63, l15 = ht & 15;
    const u64 tag = tag_of(*(volatile LDSP(u64))L_TAG);
    const int ch = cw >> 1, var = cw & 1;
    const bool ready = __builtin_amdgcn_ballot_w64(*(volatile LDSP(u32))(L_CONV + (lane & 3) * 4) == 0) == 0;
    if (!ready) { gflag_publish(hflag, tag, lane); lflag_wait(L_CONV, 3, 1); }
    asm volatile("" ::: "memory");
    STAMP(5);
    conv_compute(lane, cw, d0 + ch, rs, hflag, tag, ready);
    STAMP(6);
    gflag_publish(ws + WS_FY + (var * 448 + d0 + ch) * 128, tag, lane);
    STAMP(7);
    u32x4 xr[2]; u32x2 st[2];
#pragma unroll
    for (int i = 0; i < 2; ++i) {
      const int n = ht + 256 * i, row = n >> 3, c = n & 7;
      xr[i] = *(const u32x4*)(x + (size_t)(64 * tau + row) * (D * 2) + (64 * dl + 8 * c) * 2);
      st[i] = ld64_dev(rs, WS_STAT + (u32)(64 * tau + row) * 8);
    }
    const int yvar = tau >> 4;
    gflag_wait<4>(ws + WS_FY + (yvar * 448 + 64 * dl + 16 * cw + l15) * 128, tag);
    STAMP(8);
#pragma unroll
    for (int i = 0; i < 2; ++i)
      glds16_dev((u32)((lane >> 3) * (S * 2) + (((lane & 7) ^ (2 * cw + i)) << 4)),
                 ws + WS_YT + (64 * dl + 16 * cw + 8 * i) * (S * 2) + 128 * tau, xbuf(cw ^ 1) + (u32)i * 1024);
    dflag_set(L_ADONE + cw * 4, 1);
    lflag_wait(L_ADONE, 3, 1);
    STAMP(9);
#pragma unroll
    for (int i = 0; i < 2; ++i) {
      const int n = ht + 256 * i, tok = n >> 3, dg = n & 7;
      const float mb = asf(st[i][0]), rsd = asf(st[i][1]);
      u32x4 so;
#pragma unroll
      for (int e = 0; e < 4; ++e) {
        const u32 ya = xbuf((dg >> 1) ^ 1) + (u32)((8 * (dg & 1) + 2 * e) * 128 + (((tok >> 3) ^ dg) << 4) + (tok & 7) * 2);
        const float y0 = bflo(*(LDSP(u16))ya), y1 = bflo(*(LDSP(u16))(ya + 128));
        const u32 dd = pk2(bflo(xr[i][e]) - mb, bfhi(xr[i][e]) - mb);
        const u32 xn = pk2(bflo(dd) * rsd, bfhi(dd) * rsd);
        so[e] = pk2(bflo(xn) * y0, bfhi(xn) * y1);
      }
      if (i == 0) { xv = xr[0]; sv = so; }
      else { lds_st128(E_S + (u32)ht * 16, so); lds_st128(E_X + (u32)ht * 16, xr[1]); }
    }
    STAMP(10);
  } else {
    const int j = cw - 4;
    const int lane = (int)opq(threadIdx.x) & 63;
    lflag_set(L_PUB + j * 4, 1);
    gflag_publish(hflag, tag_of(*(volatile LDSP(u64))L_TAG), lane);
    if (PUB2) lflag_set(L_PUB + j * 4, 2);
    STAMP(5);
    const u32 hl = lane * 16;
    const char* hbase = ws + WS_H + (size_t)tau * 56 * 4096;
    const int q4 = (lane >> 2) & 3;
    const u32 ta = (u32)((4 * (lane >> 5) + q4) * 128 + ((q4 >> 1) << 6) + ((lane >> 4) & 1) * 32 + (lane & 3) * 8);
    s16x8 fa[2][4], fb[3][4];
    f32x16 acc[2][2] = {};
    auto ldh = [&](int i) {
      const char* p = hbase + (size_t)g2_ks(j, i, rot2) * 4096;
      asm volatile("global_load_dwordx4 %0, %4, %5 sc1\n\tglobal_load_dwordx4 %1, %4, %5 offset:1024 sc1\n\t"
                   "global_load_dwordx4 %2, %4, %5 offset:2048 sc1\n\tglobal_load_dwordx4 %3, %4, %5 offset:3072 sc1"
                   : "=&v"(fb[i % 3][0]), "=&v"(fb[i % 3][1]), "=&v"(fb[i % 3][2]), "=&v"(fb[i % 3][3])
                   : "v"(hl), "s"(p) : "memory");
    };
    u32 tb[2];
#pragma unroll
    for (int a = 0; a < 2; ++a) tb[a] = opq((ta ^ (u32)(a << 6)) + g2_place(j, 0));
    auto rda = [&](int i) {
#pragma unroll
      for (int a = 0; a < 2; ++a)
        if (i > 0 && i % 3 == 0) tb[a] = opq(i % 6 ? tb[a] - 2 * SLOT : tb[a] + 2 * SLOT);
#pragma unroll
      for (int n = 0; n < 4; ++n) {
        const int o = (i % 3) * 4096 + (n >> 1) * 2048;
        fa[i & 1][n] = lds_tr2(tb[n & 1], o, o + 1024);
      }
    };
    const u32 fed = opq(L_FED + j * 4), prog = opq(L_PROG + j * 4);
    lflag_wait(L_H, 0, 1);
    STAMP(6);
    ldh(0); ldh(1); ldh(2);
    lflag_wait(L_FED + j * 4, 0, 3);
    rda(0);
    SCHED0();
    u32 fl = 0;
#pragma unroll
    for (int i = 0; i < 14; ++i) {
      if (i == 2 || (i >= 5 && i < 13 && (i & 1))) {
        if (__builtin_amdgcn_readfirstlane(fl) < i + 2) lflag_wait(L_FED + j * 4, 0, i + 2);
      }
      asm volatile("s_waitcnt vmcnt(%4)" : "+v"(fb[i % 3][0]), "+v"(fb[i % 3][1]), "+v"(fb[i % 3][2]), "+v"(fb[i % 3][3])
                   : "i"(i < 12 ? 8 : (13 - i) * 4) : "memory");
      SCHED0();
      if (i < 13) rda(i + 1);
      if (i == 1 || (i >= 4 && i < 12 && !(i & 1))) fl = *(volatile LDSP(u32))fed;
#pragma unroll
      for (int s = 0; s < 2; ++s)
#pragma unroll
        for (int a = 0; a < 2; ++a)
#pragma unroll
          for (int t = 0; t < 2; ++t) acc[a][t] = mfma32(fa[i & 1][2 * s + a], fb[i % 3][2 * s + t], acc[a][t]);
      if (i < 13) {
#pragma unroll
        for (int n = 0; n < 5; ++n) { SGB(0x008, 1); SGB(0x100, 2); }
        SGB(0x008, 3);
      }
      SCHED0();
      if (i < 8 && (i & 1)) *(volatile LDSP(u32))prog = i + 1;
      if (i == 4) { lflag_wait(L_H + 4, 0, 1); STAMP(8); }
      if (i + 3 < 14) ldh(i + 3);
      SCHED0();
      if (i == 0) STAMP(7);
      if (i == 8) STAMP(9);
    }
    STAMP(10);
    const int ln = (int)opq(threadIdx.x), lt = ln & 31, lh = (ln >> 5) & 1;
#pragma unroll
    for (int t = 0; t < 2; ++t)
#pragma unroll
      for (int a = 0; a < 2; ++a)
#pragma unroll
        for (int G = 0; G < 4; ++G)
          *(LDSP(f32x4))(g2_place(j, t ? 3 : 0) + (u32)(lt * 256 + (((8 * a + 2 * G + lh) ^ (lt & 7)) << 4))) =
              f32x4{acc[a][t][4 * G], acc[a][t][4 * G + 1], acc[a][t][4 * G + 2], acc[a][t][4 * G + 3]};
  }
  WAIT_LGKM0();
  STAMP(11);
  BAR();
  STAMP(12);
  {
    const int ht = (int)opq(threadIdx.x) & 255;
    const int half = cw >> 2, tk = ht >> 3, dg = ht & 7;
    if (half) { sv = *(LDSP(u32x4))(E_S + (u32)ht * 16); xv = *(LDSP(u32x4))(E_X + (u32)ht * 16); }
    f32x4 s0 = {}, s1 = {};
#pragma unroll
    for (int w = 0; w < 4; ++w) {
      const u32 pb = (half ? 0u : 2 * SLOT) + (u32)w * 12288 + (u32)tk * 256;
      s0 += *(LDSP(f32x4))(pb + (u32)(((2 * dg) ^ (tk & 7)) << 4));
      s1 += *(LDSP(f32x4))(pb + (u32)(((2 * dg + 1) ^ (tk & 7)) << 4));
    }
    float o[8];
#pragma unroll
    for (int e = 0; e < 8; ++e) {
      const float f = rbf(e < 4 ? s0[e & 3] : s1[e & 3]);
      const float gt = rbf(silu_f(f));
      const float xe = (e & 1) ? bfhi(xv[e >> 1]) : bflo(xv[e >> 1]);
      const float se = (e & 1) ? bfhi(sv[e >> 1]) : bflo(sv[e >> 1]);
      o[e] = xe + rbf(se * gt);
    }
    *(u32x4*)(out + (size_t)(64 * tau + 32 * half + tk) * (D * 2) + (64 * dl + 8 * dg) * 2) =
        u32x4{pk2(o[0], o[1]), pk2(o[2], o[3]), pk2(o[4], o[5]), pk2(o[6], o[7])};
  }
  STAMP(13);
  DIAG_FLUSH(ws);
}
}
#if defined(__HIP_DEVICE_COMPILE__) && __HIP_DEVICE_COMPILE__
#pragma clang attribute pop
#endif

extern "C" void kernel_launch(void* const* d_in, const int* in_sizes, int n_in, void* d_out, int out_size, void* d_ws,
                              size_t ws_size, hipStream_t stream) {
  (void)hipFuncSetAttribute((const void*)fused_kernel, hipFuncAttributeMaxDynamicSharedMemorySize, 163840);
  fused_kernel<<<256, 768, 163840, stream>>>((const char*)d_in[0], (const char*)d_in[1], (const char*)d_in[2], (const char*)d_in[3],
                                             (const char*)d_in[4], (const char*)d_in[5], (const char*)d_in[7], (const char*)d_in[8],
                                             (const char*)d_in[9], (char*)d_ws, (char*)d_out);
}
```

```cpp
#include <hip/hip_runtime.h>
#include <hip/hip_bf16.h>
#include <stdint.h>
#pragma clang diagnostic ignored "-Wint-to-pointer-cast"

#if defined(__HIP_DEVICE_COMPILE__) && __HIP_DEVICE_COMPILE__
#pragma clang attribute push(__attribute__((target("no-packed-fp32-ops"))), apply_to = function)
#endif
namespace {
typedef unsigned short u16;
typedef unsigned int u32;
typedef unsigned long long u64;
typedef __attribute__((ext_vector_type(4))) short s16x4;
typedef __attribute__((ext_vector_type(8))) short s16x8;
typedef __attribute__((ext_vector_type(8))) __bf16 bf16x8;
typedef __attribute__((ext_vector_type(2))) __bf16 bf16x2;
typedef __attribute__((ext_vector_type(16))) float f32x16;
typedef __attribute__((ext_vector_type(4))) float f32x4;
typedef __attribute__((ext_vector_type(2))) float f32x2;
typedef __attribute__((ext_vector_type(4))) u32 u32x4;
typedef __attribute__((ext_vector_type(2))) u32 u32x2;
#define LDSP(T) __attribute__((address_space(3))) T*
#define GLBP(T) __attribute__((address_space(1))) T*
#define DEV __device__ __forceinline__

constexpr int S = 2048, D = 448, F = 1792;
constexpr u32 WS_H = 0;
constexpr u32 WS_UT = WS_H + 7340032;
constexpr u32 WS_YT = WS_UT + 1835008;
constexpr u32 WS_STAT = WS_YT + 1835008;
constexpr u32 WS_CSUM = WS_STAT + 16384;
constexpr u32 WS_E = WS_CSUM + 16384;
constexpr u32 WS_FUT = WS_E + 128;
constexpr u32 WS_FST = WS_FUT + 448 * 128;
constexpr u32 WS_FY = WS_FST + 32 * 128;
constexpr u32 WS_FH = WS_FY + 896 * 128;
constexpr u32 WS_END = WS_FH + 1792 * 128;
constexpr int CODE_LINES = 224;
constexpr u32 SLOT = 49152;
constexpr u32 MISC = 3 * SLOT;
constexpr u32 L_FULL = MISC, L_EPI = MISC + 8, L_H = MISC + 16, L_FREE = MISC + 32, L_CONV = MISC + 64, L_FED = MISC + 80,
              L_TAG = MISC + 96, L_DD = MISC + 104, L_ADONE = MISC + 112, L_PROG = MISC + 128, L_XCH = MISC + 144, L_PUB = MISC + 160, L_STAT = MISC + 256,
              L_CSUM = MISC + 1280, L_USCR = MISC + 4096, L_DUMMY = MISC + 12288;
constexpr u32 CV = SLOT, CV_HSTR = 4160, CV_U = CV + 8 * CV_HSTR, CV_USTR = 4608;
constexpr u32 E_S = SLOT, E_X = SLOT + 4096;
constexpr int SPIN_CAP = 1 << 18;
#define ROT1 0
#define ROT2 0
#define TOUCH2_AT 0
#define PACE_L 63
#define PACE_S 63
#define FREE_SET dflag_set
#define W2PF 1
#define KVPF 0
#define SHARES 0
#define CODEPF 0
#define W2_HOLD 0
#define PUB2 0
#define KV_AT 1

__device__ const u32 seq_tab[160] __attribute__((aligned(128))) = {0, 1, 2, 3, 4, 5, 6, 7};
DEV float asf(u32 v) { return __builtin_bit_cast(float, v); }
DEV float bflo(u32 v) { return asf(v << 16); }
DEV float bfhi(u32 v) { return asf(v & 0xffff0000u); }
template <int CTRL> DEV float dpp(float v) { return asf(__builtin_amdgcn_update_dpp(0, __builtin_bit_cast(u32, v), CTRL, 0xf, 0xf, true)); }
DEV float sum8(float v) { v += dpp<0xB1>(v); v += dpp<0x4E>(v); return v + dpp<0x141>(v); }
DEV u32 pk2(float lo, float hi) {
  asm("" : "+v"(lo)); asm("" : "+v"(hi));
  bf16x2 r = {(__bf16)lo, (__bf16)hi};
  return __builtin_bit_cast(u32, r);
}
DEV float rbf(float f) { bf16x2 r = {(__bf16)f, (__bf16)f}; return bflo(__builtin_bit_cast(u32, r)); }
DEV f32x4 mfma16(s16x8 a, s16x8 b, f32x4 c) {
  return __builtin_amdgcn_mfma_f32_16x16x32_bf16(__builtin_bit_cast(bf16x8, a), __builtin_bit_cast(bf16x8, b), c, 0, 0, 0);
}
DEV f32x16 mfma32(s16x8 a, s16x8 b, f32x16 c) {
  return __builtin_amdgcn_mfma_f32_32x32x16_bf16(__builtin_bit_cast(bf16x8, a), __builtin_bit_cast(bf16x8, b), c, 0, 0, 0);
}
DEV u32 opq(u32 v) { asm volatile("" : "+v"(v)); return v; }
DEV s16x8 lds_tr2(u32 base, int o0, int o1) {
  s16x4 lo = __builtin_amdgcn_ds_read_tr16_b64_v4i16((LDSP(s16x4))((LDSP(char))base + o0));
  s16x4 hi = __builtin_amdgcn_ds_read_tr16_b64_v4i16((LDSP(s16x4))((LDSP(char))base + o1));
  return __builtin_shufflevector(lo, hi, 0, 1, 2, 3, 4, 5, 6, 7);
}
DEV s16x8 lds_b128(u32 base, int o) { return *(LDSP(s16x8))((LDSP(char))base + o); }
DEV void lds_st128(u32 a, u32x4 v) { *(LDSP(u32x4))a = v; }
DEV void glds16x4(u32 v0, u32 v1, u32 v2, u32 v3, const void* sbase, u32 ldst) {
  asm volatile("s_mov_b32 m0, %5\n\ts_nop 0\n\tglobal_load_lds_dwordx4 %0, %4\n\tglobal_load_lds_dwordx4 %1, %4 offset:1024\n\t"
               "global_load_lds_dwordx4 %2, %4 offset:2048\n\tglobal_load_lds_dwordx4 %3, %4 offset:3072"
               ::"v"(v0), "v"(v1), "v"(v2), "v"(v3), "s"(sbase), "s"(ldst) : "memory");
}
DEV void glds16(u32 voff, const void* sbase, u32 ldst) {
  asm volatile("s_mov_b32 m0, %2\n\ts_nop 0\n\tglobal_load_lds_dwordx4 %0, %1" ::"v"(voff), "s"(sbase), "s"(ldst) : "memory");
}
DEV void glds16x4_lin(u32 v, const void* sbase, u32 ldst) { glds16x4(v, v, v, v, sbase, ldst); }
DEV void glds16x4_dev(u32 v, const void* sbase, u32 ldst) {
  asm volatile("s_mov_b32 m0, %2\n\ts_nop 0\n\tglobal_load_lds_dwordx4 %0, %1 sc1\n\tglobal_load_lds_dwordx4 %0, %1 offset:1024 sc1\n\t"
               "global_load_lds_dwordx4 %0, %1 offset:2048 sc1\n\tglobal_load_lds_dwordx4 %0, %1 offset:3072 sc1"
               ::"v"(v), "s"(sbase), "s"(ldst) : "memory");
}
DEV void glds16_dev(u32 voff, const void* sbase, u32 ldst) {
  asm volatile("s_mov_b32 m0, %2\n\ts_nop 0\n\tglobal_load_lds_dwordx4 %0, %1 sc1" ::"v"(voff), "s"(sbase), "s"(ldst) : "memory");
}
DEV void dflag_set(u32 addr, u32 val) {
  asm volatile("s_waitcnt lgkmcnt(0)\n\ts_mov_b32 m0, %2\n\ts_mov_b64 exec, 1\n\tglobal_load_lds_dword %0, %1\n\ts_mov_b64 exec, -1"
               ::"v"(0), "s"(seq_tab + val), "s"(addr) : "memory");
}
#define WAIT_VM(n) asm volatile("s_waitcnt vmcnt(%0)" ::"i"(n) : "memory")
#define WAIT_LGKM0() asm volatile("s_waitcnt lgkmcnt(0)" ::: "memory")
#define BAR() __builtin_amdgcn_s_barrier()
#define SCHED0() __builtin_amdgcn_sched_barrier(0)
#define SGB(mask, n) __builtin_amdgcn_sched_group_barrier(mask, n, 0)
DEV float silu_f(float x) { return x * __builtin_amdgcn_rcpf(1.0f + __builtin_amdgcn_exp2f(-1.44269504f * x)); }

DEV void lflag_set(u32 addr, u32 val) {
  asm volatile("s_waitcnt lgkmcnt(0)\n\tds_write_b32 %0, %1" ::"v"(addr), "v"(val) : "memory");
}
DEV void lflag_wait(u32 addr, u32 mask, u32 val) {
  const u32 a = addr + 4 * (threadIdx.x & mask);
#pragma unroll 1
  for (int i = 0; i < SPIN_CAP; ++i) {
    u32 v;
    asm volatile("ds_read_b32 %0, %1\n\ts_waitcnt lgkmcnt(0)" : "=v"(v) : "v"(a) : "memory");
    if (__builtin_amdgcn_ballot_w64(v < val) == 0) break;
    __builtin_amdgcn_s_sleep(1);
  }
}
DEV u64 gload64(const char* p) { return __hip_atomic_load((GLBP(u64))p, __ATOMIC_RELAXED, __HIP_MEMORY_SCOPE_AGENT); }
DEV void gstore64(char* p, u64 v) { __hip_atomic_store((GLBP(u64))p, v, __ATOMIC_RELAXED, __HIP_MEMORY_SCOPE_AGENT); }
template <int SLEEP>
DEV void gflag_wait(const char* lane_ptr, u64 tag) {
#pragma unroll 1
  for (int i = 0; i < SPIN_CAP; ++i) {
    asm volatile("" ::: "memory");
    if (__builtin_amdgcn_ballot_w64(gload64(lane_ptr) != tag) == 0) break;
    __builtin_amdgcn_s_sleep(SLEEP);
  }
  asm volatile("" ::: "memory");
}
DEV void gflag_publish(char* p, u64 tag, int lane) {
  WAIT_VM(0);
  if (lane == 0) gstore64(p, tag);
}
DEV u64 tag_of(u64 seed) { return seed * 0x9E3779B97F4A7C15ull + 0xD1B54A32D192ED03ull; }
DEV u64 make_tag(const char* ws) { return tag_of(gload64(ws + WS_E)); }
typedef __amdgpu_buffer_rsrc_t rsrc_t;
DEV rsrc_t ws_rsrc(char* ws) { return __builtin_amdgcn_make_buffer_rsrc(ws, 0, WS_END, 0x00020000); }
DEV void st128_dev(rsrc_t r, u32 off, u32x4 v) { __builtin_amdgcn_raw_buffer_store_b128(v, r, off, 0, 16); }
DEV void st64_dev(rsrc_t r, u32 off, u32x2 v) { __builtin_amdgcn_raw_buffer_store_b64(v, r, off, 0, 16); }
DEV void st32_dev(rsrc_t r, u32 off, u32 v) { __builtin_amdgcn_raw_buffer_store_b32(v, r, off, 0, 16); }
DEV u32x4 ld128_dev(rsrc_t r, u32 off) { return __builtin_amdgcn_raw_buffer_load_b128(r, off, 0, 16); }
DEV u32x2 ld64_dev(rsrc_t r, u32 off) { return __builtin_amdgcn_raw_buffer_load_b64(r, off, 0, 16); }
DEV void touch(u32 voff, const void* sbase, u32 ldst) {
  asm volatile("s_mov_b32 m0, %2\n\ts_nop 0\n\tglobal_load_lds_dword %0, %1" ::"v"(voff), "s"(sbase), "s"(ldst) : "memory");
}

DEV void glds4_dev(u32 voff, const void* sbase, u32 ldst) {
  asm volatile("s_mov_b32 m0, %2\n\ts_nop 0\n\tglobal_load_lds_dword %0, %1 sc1" ::"v"(voff), "s"(sbase), "s"(ldst) : "memory");
}
DEV u32 shr16(u32 hi, u32 lo) { return __builtin_amdgcn_alignbit(hi, lo, 16); }

DEV u32 xbuf(int w) { return w < 2 ? L_USCR + (u32)w * 4096 : (w == 2 ? L_DUMMY : CV + 45056); }
DEV void conv_compute(int lane, int cw, int d, rsrc_t rs, char* hflag, u64 tag, bool pub) {
  const int ch = cw >> 1, var = cw & 1;
  const int m = lane & 15, g = lane >> 4, q4 = m >> 2, p4 = lane & 3;
  u32 aa = opq(CV + (u32)(ch * 4 + ((q4 + 1) & 3)) * CV_HSTR + (u32)(((q4 + 1) & 4) + 8 * g + 4 * p4) * 2 + var * 256);
  u32 ba = opq(CV_U + (u32)ch * CV_USTR + (u32)(240 - 16 * m + 8 * g) * 2 + var * 256);
  f32x4 acc[8] = {};
  s16x8 A[8], B[8];
#pragma unroll
  for (int p = 0; p < 8; ++p) { A[7 - p] = lds_tr2(aa, (7 - p) * 512, (7 - p) * 512 + 8); B[p] = lds_b128(ba, (7 - p) * 512); }
  if (var == 0) {
    const u32 a0 = CV + (u32)ch * 4 * CV_HSTR + (u32)(m + 1 + 8 * g) * 2;
#pragma unroll
    for (int j = 0; j < 8; ++j) { const short v = *(LDSP(short))(a0 + j * 2); A[0][j] = m + 8 * g + j >= 15 ? v : (short)0; }
  }
  SCHED0();
#pragma unroll 1
  for (int it = 0; it < 4; ++it) {
#pragma unroll
    for (int p = 7; p >= 0; --p) {
#pragma unroll
      for (int dd = 0; dd <= 7 - p; ++dd) {
        acc[p + dd] = mfma16(A[p], B[dd], acc[p + dd]);
        if (p == 0) { SCHED0(); B[dd] = lds_b128(ba, 64 + (7 - dd) * 512); SCHED0(); }
      }
      SCHED0();
      A[p] = lds_tr2(aa, 64 + p * 512, 64 + p * 512 + 8);
      SCHED0();
    }
    aa = opq(aa + 64); ba = opq(ba + 64);
    if (it == 0 && pub) gflag_publish(hflag, tag, lane);
  }
  f32x4 y[4];
#pragma unroll
  for (int i = 0; i < 4; ++i) {
    *(LDSP(f32x4))(xbuf(cw) + (u32)(i * 1024 + lane * 16)) = var ? acc[i] : acc[4 + i];
    y[i] = var ? acc[4 + i] : acc[i];
  }
  lflag_set(L_XCH + cw * 4, 1);
  const u32 dv = *(LDSP(u32))L_DD;
  const float Dd = ch ? bfhi(dv) : bflo(dv);
  u32x2 uu[4];
#pragma unroll
  for (int i = 0; i < 4; ++i) uu[i] = *(LDSP(u32x2))(CV_U + (u32)ch * CV_USTR + (u32)(2044 - (1024 * var + 256 * i + 16 * m + 4 * g)) * 2);
  lflag_wait(L_XCH + (cw ^ 1) * 4, 0, 1);
#pragma unroll
  for (int i = 0; i < 4; ++i) {
    const int t = 1024 * var + 256 * i + 16 * m + 4 * g;
    const f32x4 s = y[i] + *(LDSP(f32x4))(xbuf(cw ^ 1) + (u32)(i * 1024 + lane * 16));
    float y0 = rbf(s[0]) + rbf(bfhi(uu[i][1]) * Dd), y1 = rbf(s[1]) + rbf(bflo(uu[i][1]) * Dd);
    float y2 = rbf(s[2]) + rbf(bfhi(uu[i][0]) * Dd), y3 = rbf(s[3]) + rbf(bflo(uu[i][0]) * Dd);
    st64_dev(rs, WS_YT + (u32)(d * (S * 2) + t * 2), u32x2{pk2(y0, y1), pk2(y2, y3)});
  }
}

DEV int rot7(int i, int rot) { const int v = i + rot; return v >= 7 ? v - 7 : v; }
DEV int g2_ks(int j, int i, int rot) { const int e = 7 * j + rot7(i < 7 ? i : i - 7, rot); return 4 * (e >> 1) + (e & 1) + (i < 7 ? 0 : 2); }
DEV u32 g2_place(int j, int p) { return (p < 3 ? 2 * SLOT + (u32)p * 4096 : (u32)(p - 3) * 4096) + (u32)j * 12288; }
DEV u32 w2_voff(int lane) { return (u32)((lane >> 3) * (D * 2) + (((lane & 7) ^ (((lane >> 4) & 1) << 2)) << 4)); }

__global__ __launch_bounds__(768, 3) void fused_kernel(const char* x, const char* kp, const char* vp, const char* skp,
                                                       const char* ssmk, const char* ssmD, const char* w1, const char* w3,
                                                       const char* w2, char* ws, char* out) {
  const int b = blockIdx.x, xcd = b & 7, r = b >> 3;
  const int wid = __builtin_amdgcn_readfirstlane(threadIdx.x >> 6);
  const int panel = (xcd >> 1) * 4 + r / 7, ft = (xcd & 1) * 7 + r % 7;
  const int tau = xcd * 4 + r / 7, dl = r % 7;
  const int d0 = 2 * b;
  const int rot = ROT1 ? (r % 7 + 2 * (r / 7)) % 7 : 0, rot2 = ROT2 ? (r % 7 + 2 * (r / 7)) % 7 : 0;

  if (b < 224) {
    if (wid == 7) { *(LDSP(u32))(MISC + (threadIdx.x & 63) * 4) = 0; WAIT_LGKM0(); }
    BAR();
    if (wid == 7 && (threadIdx.x & 63) < 5) touch((threadIdx.x & 63) * 128, seq_tab, L_DUMMY);
  }

  if (__builtin_expect(wid < 4 && b < 224, 1)) {
    const int lane = (int)opq(threadIdx.x) & 63;
    u64 tag = 0;
    if (__builtin_expect(wid < 2, 1)) {
      const int g = lane >> 4, l15 = lane & 15;
      u32 vx[8], vw[8];
      const u32 ox = (u32)((lane >> 3) * (D * 2) + (((lane & 7) ^ g) << 4));
      const u32 ow = (u32)(g * (F * 2) + (((l15 >> 2) ^ g) << 6) + ((lane & 3) << 4));
#pragma unroll
      for (int i = 0; i < 8; ++i) {
        vx[i] = (ox ^ (u32)((i & 1) * 64)) + i * 8 * (D * 2) - (i & 3) * 1024;
        vw[i] = ow + i * 4 * (F * 2) - (i & 3) * 1024;
      }
#pragma unroll 1
      for (int c = 0; c < 7; ++c) {
        if (c >= 3) BAR();
        const u32 slot = (u32)(c % 3) * SLOT + wid * 8192;
        const int kc = rot7(c, rot);
        const char* xb = x + (size_t)(panel * 128 + wid * 64) * (D * 2) + kc * 128;
        const char* w1b = w1 + (size_t)(kc * 64 + wid * 32) * (F * 2) + ft * 256;
        const char* w3b = w3 + (size_t)(kc * 64 + wid * 32) * (F * 2) + ft * 256;
#pragma unroll
        for (int i = 0; i < 8; i += 4) {
          WAIT_VM(PACE_L);
          glds16x4(vx[i], vx[i + 1], vx[i + 2], vx[i + 3], xb, slot + i * 1024);
          WAIT_VM(PACE_L);
          glds16x4(vw[i], vw[i + 1], vw[i + 2], vw[i + 3], w1b, slot + 16384 + i * 1024);
          WAIT_VM(PACE_L);
          glds16x4(vw[i], vw[i + 1], vw[i + 2], vw[i + 3], w3b, slot + 32768 + i * 1024);

        }
        dflag_set(L_FULL + wid * 4, c + 1);
      }
    } else {
      __builtin_amdgcn_s_setprio(3);
      const rsrc_t rs = ws_rsrc(ws);
      const int u = wid - 2;
      const int ti = b / 7, cj = b % 7;
      const int tg = lane >> 3, cg = lane & 7;
      const int t0 = 64 * ti + 32 * u + 4 * tg, ch0 = 64 * cj + 8 * cg;
      u32x4 kr[7], vr[4], sw[4]; u32x2 o[8];
      u64 seed;
      auto ldkv = [&]() {
        const u32 ko = (u32)(t0 * (D * 2) + ch0 * 2);
#pragma unroll
        for (int i = 0; i < 7; ++i) kr[i] = *(const u32x4*)(kp + (i < 3 && t0 == 0 ? ko : ko + (u32)((i - 3) * (D * 2))));
#pragma unroll
        for (int i = 0; i < 4; ++i) vr[i] = *(const u32x4*)(vp + (ko + (u32)(i * (D * 2))));
        SCHED0();
      };
      auto piece = [&](int p) {
#pragma unroll
        for (int c = 2 * p; c < 2 * p + 2; ++c) {
          const u32 wa = sw[p][(c & 1) * 2], wb = sw[p][(c & 1) * 2 + 1];
          const float w0 = bflo(wa), w1f = bfhi(wa), w2f = bflo(wb), w3f = bfhi(wb);
          float kf[7], ks[4];
#pragma unroll
          for (int i = 0; i < 7; ++i) kf[i] = (c & 1) ? bfhi(kr[i][p]) : bflo(kr[i][p]);
#pragma unroll
          for (int i = 0; i < 4; ++i)
            ks[i] = __builtin_fmaf(kf[i + 3], w0, __builtin_fmaf(kf[i + 2], w1f, __builtin_fmaf(kf[i + 1], w2f, kf[i] * w3f)));
#pragma unroll
          for (int i = 0; i < 2; ++i) {
            const int ta = 3 - 2 * i, tb = 2 - 2 * i;
            const u32 kk = pk2(ks[ta], ks[tb]);
            o[c][i] = pk2(bflo(kk) * ((c & 1) ? bfhi(vr[ta][p]) : bflo(vr[ta][p])), bfhi(kk) * ((c & 1) ? bfhi(vr[tb][p]) : bflo(vr[tb][p])));
          }
          asm volatile("" : "+v"(o[c]));
        }
        SCHED0();
      };
      ldkv();
#pragma unroll
      for (int i = 0; i < 4; ++i) sw[i] = *(const u32x4*)(skp + (u32)(ch0 * 8 + i * 16));
      seed = gload64(ws + WS_E + opq(0));
      SCHED0();
#pragma unroll
      for (int i = 0; i < 3; ++i) if (t0 == 0) kr[i] = u32x4{0, 0, 0, 0};
#pragma unroll
      for (int c = 0; c < 4; ++c) piece(c);
      tag = tag_of(seed);
      const u32 scr = L_USCR + (u32)u * 4096;
#pragma unroll
      for (int c = 0; c < 8; ++c) *(LDSP(u32x2))(scr + (u32)((cg * 8 + c) * 64 + (((7 - tg) ^ (cg & 6)) << 3))) = o[c];
      SCHED0();
      BAR();
      WAIT_LGKM0();
#pragma unroll
      for (int i = 0; i < 4; ++i) {
        const int dd = 16 * i + (lane >> 2), c4 = lane & 3;
        u32x4 v = *(LDSP(u32x4))(scr + (u32)(dd * 64 + ((c4 ^ (dd >> 4)) << 4)));
        st128_dev(rs, WS_UT + (u32)((64 * cj + dd) * (S * 2) + (2016 - 64 * ti - 32 * u) * 2 + c4 * 16), v);
      }
      SCHED0();
      BAR();
      gflag_publish(ws + WS_FUT + (cj * 64 + 2 * ti + u) * 128, tag, lane);
      BAR();
      BAR();
      const int k = lane & 7;
      const char* fp = ws + (u ? WS_FST + (k < 2 ? 2 * panel + k : (k == 2 ? tau : (k & 1) * 14 + ft)) * 128 : WS_FUT + ((d0 >> 6) * 64 + lane) * 128);
      gflag_wait<8>(fp, tag);
      if (u) {
        glds16_dev(lane * 16, ws + WS_STAT + panel * 1024, L_STAT);
        glds16_dev(lane * 16, ws + WS_CSUM + ft * 1024, L_CSUM);
        if (lane < 2) glds4_dev(lane * 4, ws + WS_E, L_TAG);
        if (lane < 1) glds4_dev(0, ssmD + d0 * 2, L_DD);
        dflag_set(L_EPI, 1);
      }
    }
    const int j = wid;
    const u32 o2 = w2_voff(lane);
    const char* wbase = w2 + dl * 128;
    u32x4 pk[8][4];
    auto park = [&](int i0) {
#pragma unroll
    for (int i = i0; i < i0 + 4; ++i) {
      const char* src = wbase + (size_t)g2_ks(j, i + 6, rot2) * 32 * (D * 2);
      WAIT_VM(PACE_S);
#pragma unroll
      for (int pc = 0; pc < 4; ++pc) pk[i][pc] = *(const u32x4*)(src + (o2 + (u32)(pc * 8 * (D * 2))));
    }
    SCHED0();
    };
    park(0);
    park(4);
    lflag_wait(L_FREE, 7, 7);
    if (wid == 2) {
        if (lane < 32) {
          glds16(lane * 16, seq_tab + 32, CV_U + 4096);
          glds16(lane * 16, seq_tab + 32, CV_U + CV_USTR + 4096);
        }
        glds16x4_dev(lane * 16, ws + WS_UT + d0 * (S * 2), CV_U);
        glds16x4_dev(lane * 16, ws + WS_UT + (d0 + 1) * (S * 2), CV_U + CV_USTR);
        dflag_set(L_CONV + 8, 1);
        dflag_set(L_CONV + 12, 1);
      }
    if (wid < 2) {
      const u32 vl = lane * 16;
#pragma unroll 1
      for (int rr = 0; rr < 4; ++rr) {
        const char* src = ssmk + (size_t)(d0 + wid) * (S * 2) + (rr ? (rr - 4) * 2 : 0);
        const u32 dst = CV + (u32)(wid * 4 + rr) * CV_HSTR + (rr ? 24 : 32);
        glds16x4(rr && !lane ? 16u : vl, vl, vl, vl, src, dst);
      }
      dflag_set(L_CONV + wid * 4, 1);
    }
#pragma unroll 1
    for (int i = 0; i < 6; ++i) {
      if (i == W2_HOLD) lflag_wait(L_PUB, 3, 1);
      if (i == 3) dflag_set(L_FED + j * 4, 3);
      const char* src = wbase + (size_t)g2_ks(j, i, rot2) * 32 * (D * 2);
      WAIT_VM(PACE_S);
      glds16x4(o2, o2 + 8 * (D * 2) - 1024, o2 + 16 * (D * 2) - 2048, o2 + 24 * (D * 2) - 3072, src, g2_place(j, i));
    }
    dflag_set(L_FED + j * 4, 6);
    if (wid >= 2) {
      lflag_wait(L_PUB, 3, PUB2 ? 2 : 1);
      const int e = lane < 28 ? lane : 27;
      gflag_wait<4>(ws + WS_FH + (tau * 56 + 4 * (e >> 1) + (e & 1) + 2 * (3 - wid)) * 128, tag);
      dflag_set(L_H + (3 - wid) * 4, 1);
    }
    WAIT_VM(0);
    const u32 wl = opq(lane * 16 + j * 12288);
#pragma unroll
    for (int i = 0; i < 8; i += 2) {
      lflag_wait(L_PROG + j * 4, 0, i + 2);
#pragma unroll
      for (int n = 0; n < 8; ++n) lds_st128(wl + g2_place(0, (i + (n >> 2)) % 6) + (n & 3) * 1024, pk[i + (n >> 2)][n & 3]);
      lflag_set(L_FED + j * 4, i + 8);
    }
    return;
  }
  const rsrc_t rs = ws_rsrc(ws);

  if (b >= 224) {
    if (wid >= 8 && b != 255) return;
    const int tid = (int)opq(threadIdx.x), lane = tid & 63;
    const int sp = b - 224;
    const u64 seed = gload64(ws + WS_E + opq(0));
    if (KVPF && wid < 7) {
      touch((u32)(sp * 448 + tid) * 128, kp, L_DUMMY);
      touch((u32)(sp * 448 + tid) * 128, vp, L_DUMMY);
    }
    if (KVPF) BAR();
    if (wid < 8) {
      const int row = 64 * sp + (tid >> 3), c8 = tid & 7;
      const int cu = sp < 28 ? sp : 27, mat = cu / 14, ct = cu % 14, kg = tid >> 4, c16 = tid & 15;
      const char* wp = (mat ? w3 : w1) + ct * 256;
      const u32 wo = (u32)(kg * (F * 2) + c16 * 16), xo = (u32)(row * (D * 2) + c8 * 16);
      u32x4 xv[7], wv[14];
#pragma unroll
      for (int i = 0; i < 7; ++i) xv[i] = *(const u32x4*)(x + (xo + i * 128));
#pragma unroll
      for (int i = 0; i < 14; ++i) wv[i] = *(const u32x4*)(wp + (wo + i * 32 * (F * 2)));
      SCHED0();
      float s = 0.f;
#pragma unroll
      for (int i = 0; i < 7; ++i)
#pragma unroll
        for (int j = 0; j < 4; ++j) s += bflo(xv[i][j]) + bfhi(xv[i][j]);
      const float m = sum8(s) * (1.0f / D);
      float q = 0.f;
#pragma unroll
      for (int i = 0; i < 7; ++i)
#pragma unroll
        for (int j = 0; j < 4; ++j) {
          const float aa = bflo(xv[i][j]) - m, bb = bfhi(xv[i][j]) - m;
          q = __builtin_fmaf(aa, aa, __builtin_fmaf(bb, bb, q));
        }
      const float sd = rbf(__builtin_amdgcn_sqrtf(rbf(rbf(sum8(q) * (1.0f / D)) + 1e-5f)));
      if (c8 == 0) *(LDSP(f32x2))(u32)(16384 + (tid >> 3) * 8) = f32x2{rbf(m), __builtin_amdgcn_rcpf(sd)};
      SCHED0();
      float a[8] = {};
#pragma unroll
      for (int i = 0; i < 14; ++i)
#pragma unroll
        for (int j = 0; j < 4; ++j) { a[2 * j] += bflo(wv[i][j]); a[2 * j + 1] += bfhi(wv[i][j]); }
      f32x4 t;
#pragma unroll
      for (int j = 0; j < 4; ++j) {
        auto p = __builtin_amdgcn_permlane16_swap(__builtin_bit_cast(u32, a[j]), __builtin_bit_cast(u32, a[j + 4]), false, false);
        t[j] = asf(p[0]) + asf(p[1]);
      }
      *(LDSP(f32x4))(u32)((kg >> 1) * 512 + c16 * 32 + (kg & 1) * 16) = t;
    }
    WAIT_LGKM0(); BAR();
    const u64 tag = tag_of(seed);
    if (wid == 0) {
      const int cu = sp < 28 ? sp : 27;
      f32x2 cs = {};
#pragma unroll
      for (int i = 0; i < 16; ++i) cs += *(LDSP(f32x2))(u32)(i * 512 + lane * 8);
      st64_dev(rs, WS_STAT + (u32)(64 * sp + lane) * 8, *(LDSP(u32x2))(u32)(16384 + lane * 8));
      if (sp < 28) st64_dev(rs, WS_CSUM + (u32)((cu % 14) * 1024 + (cu / 14) * 512 + lane * 8), __builtin_bit_cast(u32x2, cs));
      gflag_publish(ws + WS_FST + sp * 128, tag, lane);
    }
    WAIT_VM(0); BAR();
    if (W2PF && wid > 0 && wid < 8) {
#pragma unroll 1
      for (int i = 0; i < 7; ++i) touch((u32)((((sp >> 3) * 7 + i) * 7 + wid - 1) * 64 + lane) * 128, w2, L_DUMMY);
      WAIT_VM(0);
    }
    if (b == 255) {
      gflag_wait<64>(ws + (tid < 480 ? WS_FUT + tid * 128 : WS_FH + ((((tid < 704 ? tid - 480 : 223) / 14) * 2) * 56 + ((tid < 704 ? tid - 480 : 223) % 14) * 4) * 128), tag);
      BAR();
      if (tid == 0) gstore64(ws + WS_E, tag);
    }
    return;
  }

  const int cw = wid - 4, th = cw & 1, fq = cw >> 1;
  {
    const int lane = (int)opq(threadIdx.x) & 63, g = lane >> 4, hh = lane >> 5, l31 = lane & 31;
    const int q4 = (lane >> 2) & 3, p4 = lane & 3;
    const u32 aA = 16384 + (u32)((8 * hh + q4) * 256 + ((fq ^ q4) << 6) + (g & 1) * 32 + p4 * 8);
    const u32 aB = (u32)((64 * th + l31) * 128 + ((hh ^ ((l31 >> 1) & 7)) << 4));
    f32x16 acc[2][2] = {};
    s16x8 fa[2][2][2], fb[2][2][2];
    u32 pa, pb[4];
    auto setslot = [&](u32 slot) {
      pa = opq(aA + slot);
#pragma unroll
      for (int k = 0; k < 4; ++k) pb[k] = opq((aB ^ (u32)(k << 5)) + slot);
    };
    auto rd = [&](int set) {
#pragma unroll
      for (int k = 0; k < 2; ++k) {
        const int ks = 2 * set + k;
        fa[set][k][0] = lds_tr2(pa, ks * 4096, ks * 4096 + 1024);
        fb[set][k][0] = lds_b128(pb[ks], 0);
        fb[set][k][1] = lds_b128(pb[ks], 4096);
        fa[set][k][1] = lds_tr2(pa, 16384 + ks * 4096, 16384 + ks * 4096 + 1024);
      }
    };
    auto mm = [&](int set, int n0, int n1) {
#pragma unroll
      for (int n = n0; n < n1; ++n) acc[(n >> 1) & 1][n & 1] = mfma32(fa[set][n >> 2][(n >> 1) & 1], fb[set][n >> 2][n & 1], acc[(n >> 1) & 1][n & 1]);
    };
    setslot(0);
    const u32 fad = opq(L_FULL + (lane & 1) * 4);
#pragma unroll
    for (int i = 0; i < 4; ++i) asm volatile("" : "+v"(acc[i >> 1][i & 1]));
    __builtin_amdgcn_s_setprio(1);
    lflag_wait(L_FULL, 1, 1);
    rd(0);
    SCHED0();
    u32 slot = 0;
#pragma unroll 1
    for (int c = 0; c < 7; ++c) {
      const u32 fl = *(volatile LDSP(u32))fad;
      rd(1); mm(0, 0, 5);
      SGB(0x008, 1); SGB(0x100, 4); SGB(0x008, 1); SGB(0x100, 3); SGB(0x008, 1); SGB(0x100, 3); SGB(0x008, 1); SGB(0x100, 3); SGB(0x008, 1);
      SCHED0();
      if (c < 6 && __builtin_amdgcn_ballot_w64(fl < (u32)(c + 2))) lflag_wait(L_FULL, 1, c + 2);
      SCHED0();
      slot = slot == 2 * SLOT ? 0 : slot + SLOT;
      mm(0, 5, 6);
      SCHED0();
      setslot(slot);
      SCHED0();
      mm(0, 6, 8);
      SCHED0();
      if (c < 4) { WAIT_LGKM0(); BAR(); } else if (c == 6) lflag_set(L_FREE + cw * 4, 7);
      SCHED0();
      rd(0); mm(1, 0, 8);
#pragma unroll
      for (int i = 0; i < 4; ++i) { SGB(0x008, 1); SGB(0x100, 3); }
      SGB(0x008, 4);
      SCHED0();
    }
    lflag_wait(L_EPI, 0, 1);
    const u32 hdst = WS_H + (u32)((2 * panel + th) * 56 + 4 * ft + fq) * 4096 + lane * 16;
    float nm[2], c1[2], irs2[2];
#pragma unroll
    for (int j = 0; j < 2; ++j) {
      const f32x2 st = *(LDSP(f32x2))(L_STAT + (u32)(64 * th + 32 * j + l31) * 8);
      nm[j] = -st[0]; c1[j] = -1.44269504f * st[1]; irs2[j] = __builtin_amdgcn_rcpf(st[1] * st[1]);
    }
#pragma unroll
    for (int s = 0; s < 2; ++s) {
      f32x4 cs[2][2];
#pragma unroll
      for (int m = 0; m < 2; ++m)
#pragma unroll
        for (int G = 0; G < 2; ++G) cs[m][G] = *(LDSP(f32x4))(L_CSUM + (u32)(m * 128 + 32 * fq + 16 * s + 8 * G + 4 * hh) * 4);
#pragma unroll
      for (int j = 0; j < 2; ++j) {
        float hv[8];
#pragma unroll
        for (int e = 0; e < 8; ++e) {
          float a = __builtin_fmaf(nm[j], cs[0][e >> 2][e & 3], acc[0][j][8 * s + e]);
          float bb = __builtin_fmaf(nm[j], cs[1][e >> 2][e & 3], acc[1][j][8 * s + e]);
          float ex = __builtin_amdgcn_exp2f(a * c1[j]);
          float rr = __builtin_amdgcn_rcpf(__builtin_fmaf(ex, irs2[j], irs2[j]));
          hv[e] = a * bb * rr;
        }
        st128_dev(rs, hdst + (s * 2 + j) * 1024, u32x4{pk2(hv[0], hv[1]), pk2(hv[2], hv[3]), pk2(hv[4], hv[5]), pk2(hv[6], hv[7])});
      }
    }
  }
  char* hflag = ws + WS_FH + ((2 * panel + th) * 56 + 4 * ft + fq) * 128;
  u32x4 xv, sv;
  if (cw < 4) {
    const int ht = (int)opq(threadIdx.x) & 255, lane = ht & 63, l15 = ht & 15;
    const u64 tag = tag_of(*(volatile LDSP(u64))L_TAG);
    const int ch = cw >> 1, var = cw & 1;
    const bool ready = __builtin_amdgcn_ballot_w64(*(volatile LDSP(u32))(L_CONV + (lane & 3) * 4) == 0) == 0;
    if (!ready) { gflag_publish(hflag, tag, lane); lflag_wait(L_CONV, 3, 1); }
    asm volatile("" ::: "memory");
    conv_compute(lane, cw, d0 + ch, rs, hflag, tag, ready);
    gflag_publish(ws + WS_FY + (var * 448 + d0 + ch) * 128, tag, lane);
    u32x4 xr[2]; u32x2 st[2];
#pragma unroll
    for (int i = 0; i < 2; ++i) {
      const int n = ht + 256 * i, row = n >> 3, c = n & 7;
      xr[i] = *(const u32x4*)(x + (size_t)(64 * tau + row) * (D * 2) + (64 * dl + 8 * c) * 2);
      st[i] = ld64_dev(rs, WS_STAT + (u32)(64 * tau + row) * 8);
    }
    const int yvar = tau >> 4;
    gflag_wait<4>(ws + WS_FY + (yvar * 448 + 64 * dl + 16 * cw + l15) * 128, tag);
#pragma unroll
    for (int i = 0; i < 2; ++i)
      glds16_dev((u32)((lane >> 3) * (S * 2) + (((lane & 7) ^ (2 * cw + i)) << 4)),
                 ws + WS_YT + (64 * dl + 16 * cw + 8 * i) * (S * 2) + 128 * tau, xbuf(cw ^ 1) + (u32)i * 1024);
    dflag_set(L_ADONE + cw * 4, 1);
    lflag_wait(L_ADONE, 3, 1);
#pragma unroll
    for (int i = 0; i < 2; ++i) {
      const int n = ht + 256 * i, tok = n >> 3, dg = n & 7;
      const float mb = asf(st[i][0]), rsd = asf(st[i][1]);
      u32x4 so;
#pragma unroll
      for (int e = 0; e < 4; ++e) {
        const u32 ya = xbuf((dg >> 1) ^ 1) + (u32)((8 * (dg & 1) + 2 * e) * 128 + (((tok >> 3) ^ dg) << 4) + (tok & 7) * 2);
        const float y0 = bflo(*(LDSP(u16))ya), y1 = bflo(*(LDSP(u16))(ya + 128));
        const u32 dd = pk2(bflo(xr[i][e]) - mb, bfhi(xr[i][e]) - mb);
        const u32 xn = pk2(bflo(dd) * rsd, bfhi(dd) * rsd);
        so[e] = pk2(bflo(xn) * y0, bfhi(xn) * y1);
      }
      if (i == 0) { xv = xr[0]; sv = so; }
      else { lds_st128(E_S + (u32)ht * 16, so); lds_st128(E_X + (u32)ht * 16, xr[1]); }
    }
  } else {
    const int j = cw - 4;
    const int lane = (int)opq(threadIdx.x) & 63;
    lflag_set(L_PUB + j * 4, 1);
    gflag_publish(hflag, tag_of(*(volatile LDSP(u64))L_TAG), lane);
    if (PUB2) lflag_set(L_PUB + j * 4, 2);
    const u32 hl = lane * 16;
    const char* hbase = ws + WS_H + (size_t)tau * 56 * 4096;
    const int q4 = (lane >> 2) & 3;
    const u32 ta = (u32)((4 * (lane >> 5) + q4) * 128 + ((q4 >> 1) << 6) + ((lane >> 4) & 1) * 32 + (lane & 3) * 8);
    s16x8 fa[2][4], fb[3][4];
    f32x16 acc[2][2] = {};
    auto ldh = [&](int i) {
      const char* p = hbase + (size_t)g2_ks(j, i, rot2) * 4096;
      asm volatile("global_load_dwordx4 %0, %4, %5 sc1\n\tglobal_load_dwordx4 %1, %4, %5 offset:1024 sc1\n\t"
                   "global_load_dwordx4 %2, %4, %5 offset:2048 sc1\n\tglobal_load_dwordx4 %3, %4, %5 offset:3072 sc1"
                   : "=&v"(fb[i % 3][0]), "=&v"(fb[i % 3][1]), "=&v"(fb[i % 3][2]), "=&v"(fb[i % 3][3])
                   : "v"(hl), "s"(p) : "memory");
    };
    u32 tb[2];
#pragma unroll
    for (int a = 0; a < 2; ++a) tb[a] = opq((ta ^ (u32)(a << 6)) + g2_place(j, 0));
    auto rda = [&](int i) {
#pragma unroll
      for (int a = 0; a < 2; ++a)
        if (i > 0 && i % 3 == 0) tb[a] = opq(i % 6 ? tb[a] - 2 * SLOT : tb[a] + 2 * SLOT);
#pragma unroll
      for (int n = 0; n < 4; ++n) {
        const int o = (i % 3) * 4096 + (n >> 1) * 2048;
        fa[i & 1][n] = lds_tr2(tb[n & 1], o, o + 1024);
      }
    };
    const u32 fed = opq(L_FED + j * 4), prog = opq(L_PROG + j * 4);
    lflag_wait(L_H, 0, 1);
    ldh(0); ldh(1); ldh(2);
    lflag_wait(L_FED + j * 4, 0, 3);
    rda(0);
    SCHED0();
    u32 fl = 0;
#pragma unroll
    for (int i = 0; i < 14; ++i) {
      if (i == 2 || (i >= 5 && i < 13 && (i & 1))) {
        if (__builtin_amdgcn_readfirstlane(fl) < i + 2) lflag_wait(L_FED + j * 4, 0, i + 2);
      }
      asm volatile("s_waitcnt vmcnt(%4)" : "+v"(fb[i % 3][0]), "+v"(fb[i % 3][1]), "+v"(fb[i % 3][2]), "+v"(fb[i % 3][3])
                   : "i"(i < 12 ? 8 : (13 - i) * 4) : "memory");
      SCHED0();
      if (i < 13) rda(i + 1);
      if (i == 1 || (i >= 4 && i < 12 && !(i & 1))) fl = *(volatile LDSP(u32))fed;
#pragma unroll
      for (int s = 0; s < 2; ++s)
#pragma unroll
        for (int a = 0; a < 2; ++a)
#pragma unroll
          for (int t = 0; t < 2; ++t) acc[a][t] = mfma32(fa[i & 1][2 * s + a], fb[i % 3][2 * s + t], acc[a][t]);
      if (i < 13) {
#pragma unroll
        for (int n = 0; n < 5; ++n) { SGB(0x008, 1); SGB(0x100, 2); }
        SGB(0x008, 3);
      }
      SCHED0();
      if (i < 8 && (i & 1)) *(volatile LDSP(u32))prog = i + 1;
      if (i == 4) { lflag_wait(L_H + 4, 0, 1); }
      if (i + 3 < 14) ldh(i + 3);
      SCHED0();

    }
    const int ln = (int)opq(threadIdx.x), lt = ln & 31, lh = (ln >> 5) & 1;
#pragma unroll
    for (int t = 0; t < 2; ++t)
#pragma unroll
      for (int a = 0; a < 2; ++a)
#pragma unroll
        for (int G = 0; G < 4; ++G)
          *(LDSP(f32x4))(g2_place(j, t ? 3 : 0) + (u32)(lt * 256 + (((8 * a + 2 * G + lh) ^ (lt & 7)) << 4))) =
              f32x4{acc[a][t][4 * G], acc[a][t][4 * G + 1], acc[a][t][4 * G + 2], acc[a][t][4 * G + 3]};
  }
  WAIT_LGKM0();
  BAR();
  {
    const int ht = (int)opq(threadIdx.x) & 255;
    const int half = cw >> 2, tk = ht >> 3, dg = ht & 7;
    if (half) { sv = *(LDSP(u32x4))(E_S + (u32)ht * 16); xv = *(LDSP(u32x4))(E_X + (u32)ht * 16); }
    f32x4 s0 = {}, s1 = {};
#pragma unroll
    for (int w = 0; w < 4; ++w) {
      const u32 pb = (half ? 0u : 2 * SLOT) + (u32)w * 12288 + (u32)tk * 256;
      s0 += *(LDSP(f32x4))(pb + (u32)(((2 * dg) ^ (tk & 7)) << 4));
      s1 += *(LDSP(f32x4))(pb + (u32)(((2 * dg + 1) ^ (tk & 7)) << 4));
    }
    float o[8];
#pragma unroll
    for (int e = 0; e < 8; ++e) {
      const float f = rbf(e < 4 ? s0[e & 3] : s1[e & 3]);
      const float gt = rbf(silu_f(f));
      const float xe = (e & 1) ? bfhi(xv[e >> 1]) : bflo(xv[e >> 1]);
      const float se = (e & 1) ? bfhi(sv[e >> 1]) : bflo(sv[e >> 1]);
      o[e] = xe + rbf(se * gt);
    }
    __builtin_amdgcn_raw_buffer_store_b128(u32x4{pk2(o[0], o[1]), pk2(o[2], o[3]), pk2(o[4], o[5]), pk2(o[6], o[7])},
        __builtin_amdgcn_make_buffer_rsrc(out, 0, S * D * 2, 0x00020000), (64 * tau + 32 * half + tk) * (D * 2) + (64 * dl + 8 * dg) * 2, 0, 16);
  }
}
}
#if defined(__HIP_DEVICE_COMPILE__) && __HIP_DEVICE_COMPILE__
#pragma clang attribute pop
#endif

extern "C" void kernel_launch(void* const* d_in, const int* in_sizes, int n_in, void* d_out, int out_size, void* d_ws,
                              size_t ws_size, hipStream_t stream) {
  (void)hipFuncSetAttribute((const void*)fused_kernel, hipFuncAttributeMaxDynamicSharedMemorySize, 163840);
  fused_kernel<<<256, 768, 163840, stream>>>((const char*)d_in[0], (const char*)d_in[1], (const char*)d_in[2], (const char*)d_in[3],
                                             (const char*)d_in[4], (const char*)d_in[5], (const char*)d_in[7], (const char*)d_in[8],
                                             (const char*)d_in[9], (char*)d_ws, (char*)d_out);
}
```

```cpp
#define PROBE_ID -1
#define PROBE_W 4
#include <hip/hip_runtime.h>
#include <hip/hip_bf16.h>
#include <stdint.h>
#define DIAG_DECL const unsigned long long pr_t0 = __builtin_amdgcn_s_memrealtime(); unsigned long long pr_t = pr_t0;
#define STAMP(i) do { if ((i) == PROBE_ID) pr_t = __builtin_amdgcn_s_memrealtime(); } while (0)
#define CYC_BEGIN
#define CYC_END
#define DIAG_FLUSH(w) do { if (blockIdx.x == 0 && wid == PROBE_W) { const unsigned long long e = PROBE_ID < 0 ? pr_t0 + 4000 : pr_t0 + 4000 + 4 * (pr_t - pr_t0); \
    for (int pi = 0; pi < 100000 && __builtin_amdgcn_s_memrealtime() < e; ++pi) __builtin_amdgcn_s_sleep(2); } } while (0)
#pragma clang diagnostic ignored "-Wint-to-pointer-cast"

#if defined(__HIP_DEVICE_COMPILE__) && __HIP_DEVICE_COMPILE__
#pragma clang attribute push(__attribute__((target("no-packed-fp32-ops"))), apply_to = function)
#endif
namespace {
typedef unsigned short u16;
typedef unsigned int u32;
typedef unsigned long long u64;
typedef __attribute__((ext_vector_type(4))) short s16x4;
typedef __attribute__((ext_vector_type(8))) short s16x8;
typedef __attribute__((ext_vector_type(8))) __bf16 bf16x8;
typedef __attribute__((ext_vector_type(2))) __bf16 bf16x2;
typedef __attribute__((ext_vector_type(16))) float f32x16;
typedef __attribute__((ext_vector_type(4))) float f32x4;
typedef __attribute__((ext_vector_type(2))) float f32x2;
typedef __attribute__((ext_vector_type(4))) u32 u32x4;
typedef __attribute__((ext_vector_type(2))) u32 u32x2;
#define LDSP(T) __attribute__((address_space(3))) T*
#define GLBP(T) __attribute__((address_space(1))) T*
#define DEV __device__ __forceinline__

constexpr int S = 2048, D = 448, F = 1792;
constexpr u32 WS_H = 0;
constexpr u32 WS_UT = WS_H + 7340032;
constexpr u32 WS_YT = WS_UT + 1835008;
constexpr u32 WS_STAT = WS_YT + 1835008;
constexpr u32 WS_CSUM = WS_STAT + 16384;
constexpr u32 WS_E = WS_CSUM + 16384;
constexpr u32 WS_FUT = WS_E + 128;
constexpr u32 WS_FST = WS_FUT + 448 * 128;
constexpr u32 WS_FY = WS_FST + 32 * 128;
constexpr u32 WS_FH = WS_FY + 896 * 128;
constexpr u32 WS_END = WS_FH + 1792 * 128;
constexpr int CODE_LINES = 224;
constexpr u32 SLOT = 49152;
constexpr u32 MISC = 3 * SLOT;
constexpr u32 L_FULL = MISC, L_EPI = MISC + 8, L_H = MISC + 16, L_FREE = MISC + 32, L_CONV = MISC + 64, L_FED = MISC + 80,
              L_TAG = MISC + 96, L_DD = MISC + 104, L_ADONE = MISC + 112, L_PROG = MISC + 128, L_XCH = MISC + 144, L_PUB = MISC + 160, L_STAT = MISC + 256,
              L_CSUM = MISC + 1280, L_USCR = MISC + 4096, L_DUMMY = MISC + 12288;
constexpr u32 CV = SLOT, CV_HSTR = 4160, CV_U = CV + 8 * CV_HSTR, CV_USTR = 4608;
constexpr u32 E_S = SLOT, E_X = SLOT + 4096;
constexpr int SPIN_CAP = 1 << 18;
#define ROT1 0
#define ROT2 0
#define TOUCH2_AT 0
#define PACE_L 63
#define PACE_S 63
#define FREE_SET dflag_set
#define W2PF 1
#define KVPF 0
#define SHARES 0
#define CODEPF 0
#define W2_HOLD 0
#define PUB2 0
#define KV_AT 1

__device__ const u32 seq_tab[160] __attribute__((aligned(128))) = {0, 1, 2, 3, 4, 5, 6, 7};
DEV float asf(u32 v) { return __builtin_bit_cast(float, v); }
DEV float bflo(u32 v) { return asf(v << 16); }
DEV float bfhi(u32 v) { return asf(v & 0xffff0000u); }
template <int CTRL> DEV float dpp(float v) { return asf(__builtin_amdgcn_update_dpp(0, __builtin_bit_cast(u32, v), CTRL, 0xf, 0xf, true)); }
DEV float sum8(float v) { v += dpp<0xB1>(v); v += dpp<0x4E>(v); return v + dpp<0x141>(v); }
DEV u32 pk2(float lo, float hi) {
  asm("" : "+v"(lo)); asm("" : "+v"(hi));
  bf16x2 r = {(__bf16)lo, (__bf16)hi};
  return __builtin_bit_cast(u32, r);
}
DEV float rbf(float f) { bf16x2 r = {(__bf16)f, (__bf16)f}; return bflo(__builtin_bit_cast(u32, r)); }
DEV f32x4 mfma16(s16x8 a, s16x8 b, f32x4 c) {
  return __builtin_amdgcn_mfma_f32_16x16x32_bf16(__builtin_bit_cast(bf16x8, a), __builtin_bit_cast(bf16x8, b), c, 0, 0, 0);
}
DEV f32x16 mfma32(s16x8 a, s16x8 b, f32x16 c) {
  return __builtin_amdgcn_mfma_f32_32x32x16_bf16(__builtin_bit_cast(bf16x8, a), __builtin_bit_cast(bf16x8, b), c, 0, 0, 0);
}
DEV u32 opq(u32 v) { asm volatile("" : "+v"(v)); return v; }
DEV s16x8 lds_tr2(u32 base, int o0, int o1) {
  s16x4 lo = __builtin_amdgcn_ds_read_tr16_b64_v4i16((LDSP(s16x4))((LDSP(char))base + o0));
  s16x4 hi = __builtin_amdgcn_ds_read_tr16_b64_v4i16((LDSP(s16x4))((LDSP(char))base + o1));
  return __builtin_shufflevector(lo, hi, 0, 1, 2, 3, 4, 5, 6, 7);
}
DEV s16x8 lds_b128(u32 base, int o) { return *(LDSP(s16x8))((LDSP(char))base + o); }
DEV void lds_st128(u32 a, u32x4 v) { *(LDSP(u32x4))a = v; }
DEV void glds16x4(u32 v0, u32 v1, u32 v2, u32 v3, const void* sbase, u32 ldst) {
  asm volatile("s_mov_b32 m0, %5\n\ts_nop 0\n\tglobal_load_lds_dwordx4 %0, %4\n\tglobal_load_lds_dwordx4 %1, %4 offset:1024\n\t"
               "global_load_lds_dwordx4 %2, %4 offset:2048\n\tglobal_load_lds_dwordx4 %3, %4 offset:3072"
               ::"v"(v0), "v"(v1), "v"(v2), "v"(v3), "s"(sbase), "s"(ldst) : "memory");
}
DEV void glds16(u32 voff, const void* sbase, u32 ldst) {
  asm volatile("s_mov_b32 m0, %2\n\ts_nop 0\n\tglobal_load_lds_dwordx4 %0, %1" ::"v"(voff), "s"(sbase), "s"(ldst) : "memory");
}
DEV void glds16x4_lin(u32 v, const void* sbase, u32 ldst) { glds16x4(v, v, v, v, sbase, ldst); }
DEV void glds16x4_dev(u32 v, const void* sbase, u32 ldst) {
  asm volatile("s_mov_b32 m0, %2\n\ts_nop 0\n\tglobal_load_lds_dwordx4 %0, %1 sc1\n\tglobal_load_lds_dwordx4 %0, %1 offset:1024 sc1\n\t"
               "global_load_lds_dwordx4 %0, %1 offset:2048 sc1\n\tglobal_load_lds_dwordx4 %0, %1 offset:3072 sc1"
               ::"v"(v), "s"(sbase), "s"(ldst) : "memory");
}
DEV void glds16_dev(u32 voff, const void* sbase, u32 ldst) {
  asm volatile("s_mov_b32 m0, %2\n\ts_nop 0\n\tglobal_load_lds_dwordx4 %0, %1 sc1" ::"v"(voff), "s"(sbase), "s"(ldst) : "memory");
}
DEV void dflag_set(u32 addr, u32 val) {
  asm volatile("s_waitcnt lgkmcnt(0)\n\ts_mov_b32 m0, %2\n\ts_mov_b64 exec, 1\n\tglobal_load_lds_dword %0, %1\n\ts_mov_b64 exec, -1"
               ::"v"(0), "s"(seq_tab + val), "s"(addr) : "memory");
}
#define WAIT_VM(n) asm volatile("s_waitcnt vmcnt(%0)" ::"i"(n) : "memory")
#define WAIT_LGKM0() asm volatile("s_waitcnt lgkmcnt(0)" ::: "memory")
#define BAR() __builtin_amdgcn_s_barrier()
#define SCHED0() __builtin_amdgcn_sched_barrier(0)
#define SGB(mask, n) __builtin_amdgcn_sched_group_barrier(mask, n, 0)
DEV float silu_f(float x) { return x * __builtin_amdgcn_rcpf(1.0f + __builtin_amdgcn_exp2f(-1.44269504f * x)); }

DEV void lflag_set(u32 addr, u32 val) {
  asm volatile("s_waitcnt lgkmcnt(0)\n\tds_write_b32 %0, %1" ::"v"(addr), "v"(val) : "memory");
}
DEV void lflag_wait(u32 addr, u32 mask, u32 val) {
  const u32 a = addr + 4 * (threadIdx.x & mask);
#pragma unroll 1
  for (int i = 0; i < SPIN_CAP; ++i) {
    u32 v;
    asm volatile("ds_read_b32 %0, %1\n\ts_waitcnt lgkmcnt(0)" : "=v"(v) : "v"(a) : "memory");
    if (__builtin_amdgcn_ballot_w64(v < val) == 0) break;
    __builtin_amdgcn_s_sleep(1);
  }
}
DEV u64 gload64(const char* p) { return __hip_atomic_load((GLBP(u64))p, __ATOMIC_RELAXED, __HIP_MEMORY_SCOPE_AGENT); }
DEV void gstore64(char* p, u64 v) { __hip_atomic_store((GLBP(u64))p, v, __ATOMIC_RELAXED, __HIP_MEMORY_SCOPE_AGENT); }
template <int SLEEP>
DEV void gflag_wait(const char* lane_ptr, u64 tag) {
#pragma unroll 1
  for (int i = 0; i < SPIN_CAP; ++i) {
    asm volatile("" ::: "memory");
    if (__builtin_amdgcn_ballot_w64(gload64(lane_ptr) != tag) == 0) break;
    __builtin_amdgcn_s_sleep(SLEEP);
  }
  asm volatile("" ::: "memory");
}
DEV void gflag_publish(char* p, u64 tag, int lane) {
  WAIT_VM(0);
  if (lane == 0) gstore64(p, tag);
}
DEV u64 tag_of(u64 seed) { return seed * 0x9E3779B97F4A7C15ull + 0xD1B54A32D192ED03ull; }
DEV u64 make_tag(const char* ws) { return tag_of(gload64(ws + WS_E)); }
typedef __amdgpu_buffer_rsrc_t rsrc_t;
DEV rsrc_t ws_rsrc(char* ws) { return __builtin_amdgcn_make_buffer_rsrc(ws, 0, WS_END, 0x00020000); }
DEV void st128_dev(rsrc_t r, u32 off, u32x4 v) { __builtin_amdgcn_raw_buffer_store_b128(v, r, off, 0, 16); }
DEV void st64_dev(rsrc_t r, u32 off, u32x2 v) { __builtin_amdgcn_raw_buffer_store_b64(v, r, off, 0, 16); }
DEV void st32_dev(rsrc_t r, u32 off, u32 v) { __builtin_amdgcn_raw_buffer_store_b32(v, r, off, 0, 16); }
DEV u32x4 ld128_dev(rsrc_t r, u32 off) { return __builtin_amdgcn_raw_buffer_load_b128(r, off, 0, 16); }
DEV u32x2 ld64_dev(rsrc_t r, u32 off) { return __builtin_amdgcn_raw_buffer_load_b64(r, off, 0, 16); }
DEV void touch(u32 voff, const void* sbase, u32 ldst) {
  asm volatile("s_mov_b32 m0, %2\n\ts_nop 0\n\tglobal_load_lds_dword %0, %1" ::"v"(voff), "s"(sbase), "s"(ldst) : "memory");
}

DEV void glds4_dev(u32 voff, const void* sbase, u32 ldst) {
  asm volatile("s_mov_b32 m0, %2\n\ts_nop 0\n\tglobal_load_lds_dword %0, %1 sc1" ::"v"(voff), "s"(sbase), "s"(ldst) : "memory");
}
DEV u32 shr16(u32 hi, u32 lo) { return __builtin_amdgcn_alignbit(hi, lo, 16); }

DEV u32 xbuf(int w) { return w < 2 ? L_USCR + (u32)w * 4096 : (w == 2 ? L_DUMMY : CV + 45056); }
DEV void conv_compute(int lane, int cw, int d, rsrc_t rs, char* hflag, u64 tag, bool pub) {
  const int ch = cw >> 1, var = cw & 1;
  const int m = lane & 15, g = lane >> 4, q4 = m >> 2, p4 = lane & 3;
  u32 aa = opq(CV + (u32)(ch * 4 + ((q4 + 1) & 3)) * CV_HSTR + (u32)(((q4 + 1) & 4) + 8 * g + 4 * p4) * 2 + var * 256);
  u32 ba = opq(CV_U + (u32)ch * CV_USTR + (u32)(240 - 16 * m + 8 * g) * 2 + var * 256);
  f32x4 acc[8] = {};
  s16x8 A[8], B[8];
#pragma unroll
  for (int p = 0; p < 8; ++p) { A[7 - p] = lds_tr2(aa, (7 - p) * 512, (7 - p) * 512 + 8); B[p] = lds_b128(ba, (7 - p) * 512); }
  if (var == 0) {
    const u32 a0 = CV + (u32)ch * 4 * CV_HSTR + (u32)(m + 1 + 8 * g) * 2;
#pragma unroll
    for (int j = 0; j < 8; ++j) { const short v = *(LDSP(short))(a0 + j * 2); A[0][j] = m + 8 * g + j >= 15 ? v : (short)0; }
  }
  SCHED0();
#pragma unroll 1
  for (int it = 0; it < 4; ++it) {
#pragma unroll
    for (int p = 7; p >= 0; --p) {
#pragma unroll
      for (int dd = 0; dd <= 7 - p; ++dd) {
        acc[p + dd] = mfma16(A[p], B[dd], acc[p + dd]);
        if (p == 0) { SCHED0(); B[dd] = lds_b128(ba, 64 + (7 - dd) * 512); SCHED0(); }
      }
      SCHED0();
      A[p] = lds_tr2(aa, 64 + p * 512, 64 + p * 512 + 8);
      SCHED0();
    }
    aa = opq(aa + 64); ba = opq(ba + 64);
    if (it == 0 && pub) gflag_publish(hflag, tag, lane);
  }
  f32x4 y[4];
#pragma unroll
  for (int i = 0; i < 4; ++i) {
    *(LDSP(f32x4))(xbuf(cw) + (u32)(i * 1024 + lane * 16)) = var ? acc[i] : acc[4 + i];
    y[i] = var ? acc[4 + i] : acc[i];
  }
  lflag_set(L_XCH + cw * 4, 1);
  const u32 dv = *(LDSP(u32))L_DD;
  const float Dd = ch ? bfhi(dv) : bflo(dv);
  u32x2 uu[4];
#pragma unroll
  for (int i = 0; i < 4; ++i) uu[i] = *(LDSP(u32x2))(CV_U + (u32)ch * CV_USTR + (u32)(2044 - (1024 * var + 256 * i + 16 * m + 4 * g)) * 2);
  lflag_wait(L_XCH + (cw ^ 1) * 4, 0, 1);
#pragma unroll
  for (int i = 0; i < 4; ++i) {
    const int t = 1024 * var + 256 * i + 16 * m + 4 * g;
    const f32x4 s = y[i] + *(LDSP(f32x4))(xbuf(cw ^ 1) + (u32)(i * 1024 + lane * 16));
    float y0 = rbf(s[0]) + rbf(bfhi(uu[i][1]) * Dd), y1 = rbf(s[1]) + rbf(bflo(uu[i][1]) * Dd);
    float y2 = rbf(s[2]) + rbf(bfhi(uu[i][0]) * Dd), y3 = rbf(s[3]) + rbf(bflo(uu[i][0]) * Dd);
    st64_dev(rs, WS_YT + (u32)(d * (S * 2) + t * 2), u32x2{pk2(y0, y1), pk2(y2, y3)});
  }
}

DEV int rot7(int i, int rot) { const int v = i + rot; return v >= 7 ? v - 7 : v; }
DEV int g2_ks(int j, int i, int rot) { const int e = 7 * j + rot7(i < 7 ? i : i - 7, rot); return 4 * (e >> 1) + (e & 1) + (i < 7 ? 0 : 2); }
DEV u32 g2_place(int j, int p) { return (p < 3 ? 2 * SLOT + (u32)p * 4096 : (u32)(p - 3) * 4096) + (u32)j * 12288; }
DEV u32 w2_voff(int lane) { return (u32)((lane >> 3) * (D * 2) + (((lane & 7) ^ (((lane >> 4) & 1) << 2)) << 4)); }

__global__ __launch_bounds__(768, 3) void fused_kernel(const char* x, const char* kp, const char* vp, const char* skp,
                                                       const char* ssmk, const char* ssmD, const char* w1, const char* w3,
                                                       const char* w2, char* ws, char* out) {
  DIAG_DECL STAMP(0);
  const int b = blockIdx.x, xcd = b & 7, r = b >> 3;
  const int wid = __builtin_amdgcn_readfirstlane(threadIdx.x >> 6);
  const int panel = (xcd >> 1) * 4 + r / 7, ft = (xcd & 1) * 7 + r % 7;
  const int tau = xcd * 4 + r / 7, dl = r % 7;
  const int d0 = 2 * b;
  const int rot = ROT1 ? (r % 7 + 2 * (r / 7)) % 7 : 0, rot2 = ROT2 ? (r % 7 + 2 * (r / 7)) % 7 : 0;

  if (b < 224) {
    if (wid == 7) { *(LDSP(u32))(MISC + (threadIdx.x & 63) * 4) = 0; WAIT_LGKM0(); }
    BAR();
    if (wid == 7 && (threadIdx.x & 63) < 5) touch((threadIdx.x & 63) * 128, seq_tab, L_DUMMY);
  }

  if (__builtin_expect(wid < 4 && b < 224, 1)) {
    const int lane = (int)opq(threadIdx.x) & 63;
    u64 tag = 0;
    if (__builtin_expect(wid < 2, 1)) {
      const int g = lane >> 4, l15 = lane & 15;
      u32 vx[8], vw[8];
      const u32 ox = (u32)((lane >> 3) * (D * 2) + (((lane & 7) ^ g) << 4));
      const u32 ow = (u32)(g * (F * 2) + (((l15 >> 2) ^ g) << 6) + ((lane & 3) << 4));
#pragma unroll
      for (int i = 0; i < 8; ++i) {
        vx[i] = (ox ^ (u32)((i & 1) * 64)) + i * 8 * (D * 2) - (i & 3) * 1024;
        vw[i] = ow + i * 4 * (F * 2) - (i & 3) * 1024;
      }
#pragma unroll 1
      for (int c = 0; c < 7; ++c) {
        if (c >= 3) BAR();
        const u32 slot = (u32)(c % 3) * SLOT + wid * 8192;
        const int kc = rot7(c, rot);
        const char* xb = x + (size_t)(panel * 128 + wid * 64) * (D * 2) + kc * 128;
        const char* w1b = w1 + (size_t)(kc * 64 + wid * 32) * (F * 2) + ft * 256;
        const char* w3b = w3 + (size_t)(kc * 64 + wid * 32) * (F * 2) + ft * 256;
#pragma unroll
        for (int i = 0; i < 8; i += 4) {
          WAIT_VM(PACE_L);
          glds16x4(vx[i], vx[i + 1], vx[i + 2], vx[i + 3], xb, slot + i * 1024);
          WAIT_VM(PACE_L);
          glds16x4(vw[i], vw[i + 1], vw[i + 2], vw[i + 3], w1b, slot + 16384 + i * 1024);
          WAIT_VM(PACE_L);
          glds16x4(vw[i], vw[i + 1], vw[i + 2], vw[i + 3], w3b, slot + 32768 + i * 1024);

        }
        dflag_set(L_FULL + wid * 4, c + 1);
        if (c == 0) STAMP(1); else if (c == 2) STAMP(2); else if (c == 4) STAMP(3); else if (c == 6) STAMP(4);
      }
    } else {
      __builtin_amdgcn_s_setprio(3);
      const rsrc_t rs = ws_rsrc(ws);
      const int u = wid - 2;
      const int ti = b / 7, cj = b % 7;
      const int tg = lane >> 3, cg = lane & 7;
      const int t0 = 64 * ti + 32 * u + 4 * tg, ch0 = 64 * cj + 8 * cg;
      u32x4 kr[7], vr[4], sw[4]; u32x2 o[8];
      u64 seed;
      auto ldkv = [&]() {
        const u32 ko = (u32)(t0 * (D * 2) + ch0 * 2);
#pragma unroll
        for (int i = 0; i < 7; ++i) kr[i] = *(const u32x4*)(kp + (i < 3 && t0 == 0 ? ko : ko + (u32)((i - 3) * (D * 2))));
#pragma unroll
        for (int i = 0; i < 4; ++i) vr[i] = *(const u32x4*)(vp + (ko + (u32)(i * (D * 2))));
        SCHED0();
      };
      auto piece = [&](int p) {
#pragma unroll
        for (int c = 2 * p; c < 2 * p + 2; ++c) {
          const u32 wa = sw[p][(c & 1) * 2], wb = sw[p][(c & 1) * 2 + 1];
          const float w0 = bflo(wa), w1f = bfhi(wa), w2f = bflo(wb), w3f = bfhi(wb);
          float kf[7], ks[4];
#pragma unroll
          for (int i = 0; i < 7; ++i) kf[i] = (c & 1) ? bfhi(kr[i][p]) : bflo(kr[i][p]);
#pragma unroll
          for (int i = 0; i < 4; ++i)
            ks[i] = __builtin_fmaf(kf[i + 3], w0, __builtin_fmaf(kf[i + 2], w1f, __builtin_fmaf(kf[i + 1], w2f, kf[i] * w3f)));
#pragma unroll
          for (int i = 0; i < 2; ++i) {
            const int ta = 3 - 2 * i, tb = 2 - 2 * i;
            const u32 kk = pk2(ks[ta], ks[tb]);
            o[c][i] = pk2(bflo(kk) * ((c & 1) ? bfhi(vr[ta][p]) : bflo(vr[ta][p])), bfhi(kk) * ((c & 1) ? bfhi(vr[tb][p]) : bflo(vr[tb][p])));
          }
          asm volatile("" : "+v"(o[c]));
        }
        SCHED0();
      };
      ldkv();
#pragma unroll
      for (int i = 0; i < 4; ++i) sw[i] = *(const u32x4*)(skp + (u32)(ch0 * 8 + i * 16));
      seed = gload64(ws + WS_E + opq(0));
      SCHED0();
      STAMP(1);
#pragma unroll
      for (int i = 0; i < 3; ++i) if (t0 == 0) kr[i] = u32x4{0, 0, 0, 0};
#pragma unroll
      for (int c = 0; c < 4; ++c) piece(c);
      tag = tag_of(seed);
      const u32 scr = L_USCR + (u32)u * 4096;
#pragma unroll
      for (int c = 0; c < 8; ++c) *(LDSP(u32x2))(scr + (u32)((cg * 8 + c) * 64 + (((7 - tg) ^ (cg & 6)) << 3))) = o[c];
      SCHED0();
      BAR();
      WAIT_LGKM0();
      STAMP(3);
#pragma unroll
      for (int i = 0; i < 4; ++i) {
        const int dd = 16 * i + (lane >> 2), c4 = lane & 3;
        u32x4 v = *(LDSP(u32x4))(scr + (u32)(dd * 64 + ((c4 ^ (dd >> 4)) << 4)));
        st128_dev(rs, WS_UT + (u32)((64 * cj + dd) * (S * 2) + (2016 - 64 * ti - 32 * u) * 2 + c4 * 16), v);
      }
      SCHED0();
      STAMP(4);
      BAR();
      gflag_publish(ws + WS_FUT + (cj * 64 + 2 * ti + u) * 128, tag, lane);
      STAMP(5);
      BAR();
      BAR();
      const int k = lane & 7;
      const char* fp = ws + (u ? WS_FST + (k < 2 ? 2 * panel + k : (k == 2 ? tau : (k & 1) * 14 + ft)) * 128 : WS_FUT + ((d0 >> 6) * 64 + lane) * 128);
      gflag_wait<8>(fp, tag);
      if (u) {
        glds16_dev(lane * 16, ws + WS_STAT + panel * 1024, L_STAT);
        glds16_dev(lane * 16, ws + WS_CSUM + ft * 1024, L_CSUM);
        if (lane < 2) glds4_dev(lane * 4, ws + WS_E, L_TAG);
        if (lane < 1) glds4_dev(0, ssmD + d0 * 2, L_DD);
        dflag_set(L_EPI, 1);
      }
      STAMP(6);
    }
    const int j = wid;
    const u32 o2 = w2_voff(lane);
    const char* wbase = w2 + dl * 128;
    u32x4 pk[8][4];
    auto park = [&](int i0) {
#pragma unroll
    for (int i = i0; i < i0 + 4; ++i) {
      const char* src = wbase + (size_t)g2_ks(j, i + 6, rot2) * 32 * (D * 2);
      WAIT_VM(PACE_S);
#pragma unroll
      for (int pc = 0; pc < 4; ++pc) pk[i][pc] = *(const u32x4*)(src + (o2 + (u32)(pc * 8 * (D * 2))));
    }
    SCHED0();
    STAMP(8);
    };
    park(0);
    park(4);
    lflag_wait(L_FREE, 7, 7);
    STAMP(13);
    if (wid == 2) {
        if (lane < 32) {
          glds16(lane * 16, seq_tab + 32, CV_U + 4096);
          glds16(lane * 16, seq_tab + 32, CV_U + CV_USTR + 4096);
        }
        glds16x4_dev(lane * 16, ws + WS_UT + d0 * (S * 2), CV_U);
        glds16x4_dev(lane * 16, ws + WS_UT + (d0 + 1) * (S * 2), CV_U + CV_USTR);
        dflag_set(L_CONV + 8, 1);
        dflag_set(L_CONV + 12, 1);
        STAMP(7);
      }
    if (wid < 2) {
      const u32 vl = lane * 16;
#pragma unroll 1
      for (int rr = 0; rr < 4; ++rr) {
        const char* src = ssmk + (size_t)(d0 + wid) * (S * 2) + (rr ? (rr - 4) * 2 : 0);
        const u32 dst = CV + (u32)(wid * 4 + rr) * CV_HSTR + (rr ? 24 : 32);
        glds16x4(rr && !lane ? 16u : vl, vl, vl, vl, src, dst);
      }
      dflag_set(L_CONV + wid * 4, 1);
      STAMP(9);
    }
#pragma unroll 1
    for (int i = 0; i < 6; ++i) {
      if (i == W2_HOLD) lflag_wait(L_PUB, 3, 1);
      if (i == 3) dflag_set(L_FED + j * 4, 3);
      const char* src = wbase + (size_t)g2_ks(j, i, rot2) * 32 * (D * 2);
      WAIT_VM(PACE_S);
      glds16x4(o2, o2 + 8 * (D * 2) - 1024, o2 + 16 * (D * 2) - 2048, o2 + 24 * (D * 2) - 3072, src, g2_place(j, i));
    }
    dflag_set(L_FED + j * 4, 6);
    STAMP(10);
    if (wid >= 2) {
      lflag_wait(L_PUB, 3, PUB2 ? 2 : 1);
      const int e = lane < 28 ? lane : 27;
      gflag_wait<4>(ws + WS_FH + (tau * 56 + 4 * (e >> 1) + (e & 1) + 2 * (3 - wid)) * 128, tag);
      dflag_set(L_H + (3 - wid) * 4, 1);
    }
    WAIT_VM(0);
    STAMP(11);
    const u32 wl = opq(lane * 16 + j * 12288);
#pragma unroll
    for (int i = 0; i < 8; i += 2) {
      lflag_wait(L_PROG + j * 4, 0, i + 2);
#pragma unroll
      for (int n = 0; n < 8; ++n) lds_st128(wl + g2_place(0, (i + (n >> 2)) % 6) + (n & 3) * 1024, pk[i + (n >> 2)][n & 3]);
      lflag_set(L_FED + j * 4, i + 8);
    }
    STAMP(12);
    DIAG_FLUSH(ws);
    return;
  }
  const rsrc_t rs = ws_rsrc(ws);

  if (b >= 224) {
    if (wid >= 8 && b != 255) return;
    const int tid = (int)opq(threadIdx.x), lane = tid & 63;
    const int sp = b - 224;
    const u64 seed = gload64(ws + WS_E + opq(0));
    if (KVPF && wid < 7) {
      touch((u32)(sp * 448 + tid) * 128, kp, L_DUMMY);
      touch((u32)(sp * 448 + tid) * 128, vp, L_DUMMY);
    }
    if (KVPF) BAR();
    if (wid < 8) {
      const int row = 64 * sp + (tid >> 3), c8 = tid & 7;
      const int cu = sp < 28 ? sp : 27, mat = cu / 14, ct = cu % 14, kg = tid >> 4, c16 = tid & 15;
      const char* wp = (mat ? w3 : w1) + ct * 256;
      const u32 wo = (u32)(kg * (F * 2) + c16 * 16), xo = (u32)(row * (D * 2) + c8 * 16);
      u32x4 xv[7], wv[14];
#pragma unroll
      for (int i = 0; i < 7; ++i) xv[i] = *(const u32x4*)(x + (xo + i * 128));
#pragma unroll
      for (int i = 0; i < 14; ++i) wv[i] = *(const u32x4*)(wp + (wo + i * 32 * (F * 2)));
      SCHED0();
      float s = 0.f;
#pragma unroll
      for (int i = 0; i < 7; ++i)
#pragma unroll
        for (int j = 0; j < 4; ++j) s += bflo(xv[i][j]) + bfhi(xv[i][j]);
      const float m = sum8(s) * (1.0f / D);
      float q = 0.f;
#pragma unroll
      for (int i = 0; i < 7; ++i)
#pragma unroll
        for (int j = 0; j < 4; ++j) {
          const float aa = bflo(xv[i][j]) - m, bb = bfhi(xv[i][j]) - m;
          q = __builtin_fmaf(aa, aa, __builtin_fmaf(bb, bb, q));
        }
      const float sd = rbf(__builtin_amdgcn_sqrtf(rbf(rbf(sum8(q) * (1.0f / D)) + 1e-5f)));
      if (c8 == 0) *(LDSP(f32x2))(u32)(16384 + (tid >> 3) * 8) = f32x2{rbf(m), __builtin_amdgcn_rcpf(sd)};
      SCHED0();
      float a[8] = {};
#pragma unroll
      for (int i = 0; i < 14; ++i)
#pragma unroll
        for (int j = 0; j < 4; ++j) { a[2 * j] += bflo(wv[i][j]); a[2 * j + 1] += bfhi(wv[i][j]); }
      f32x4 t;
#pragma unroll
      for (int j = 0; j < 4; ++j) {
        auto p = __builtin_amdgcn_permlane16_swap(__builtin_bit_cast(u32, a[j]), __builtin_bit_cast(u32, a[j + 4]), false, false);
        t[j] = asf(p[0]) + asf(p[1]);
      }
      *(LDSP(f32x4))(u32)((kg >> 1) * 512 + c16 * 32 + (kg & 1) * 16) = t;
      STAMP(1);
    }
    WAIT_LGKM0(); BAR();
    const u64 tag = tag_of(seed);
    if (wid == 0) {
      const int cu = sp < 28 ? sp : 27;
      f32x2 cs = {};
#pragma unroll
      for (int i = 0; i < 16; ++i) cs += *(LDSP(f32x2))(u32)(i * 512 + lane * 8);
      st64_dev(rs, WS_STAT + (u32)(64 * sp + lane) * 8, *(LDSP(u32x2))(u32)(16384 + lane * 8));
      if (sp < 28) st64_dev(rs, WS_CSUM + (u32)((cu % 14) * 1024 + (cu / 14) * 512 + lane * 8), __builtin_bit_cast(u32x2, cs));
      gflag_publish(ws + WS_FST + sp * 128, tag, lane);
    }
    STAMP(2);
    WAIT_VM(0); BAR();
    if (W2PF && wid > 0 && wid < 8) {
#pragma unroll 1
      for (int i = 0; i < 7; ++i) touch((u32)((((sp >> 3) * 7 + i) * 7 + wid - 1) * 64 + lane) * 128, w2, L_DUMMY);
      WAIT_VM(0);
    }
    STAMP(4);
    if (b == 255) {
      gflag_wait<64>(ws + (tid < 480 ? WS_FUT + tid * 128 : WS_FH + ((((tid < 704 ? tid - 480 : 223) / 14) * 2) * 56 + ((tid < 704 ? tid - 480 : 223) % 14) * 4) * 128), tag);
      BAR();
      if (tid == 0) gstore64(ws + WS_E, tag);
    }
    STAMP(3);
    DIAG_FLUSH(ws);
    return;
  }

  const int cw = wid - 4, th = cw & 1, fq = cw >> 1;
  {
    const int lane = (int)opq(threadIdx.x) & 63, g = lane >> 4, hh = lane >> 5, l31 = lane & 31;
    const int q4 = (lane >> 2) & 3, p4 = lane & 3;
    const u32 aA = 16384 + (u32)((8 * hh + q4) * 256 + ((fq ^ q4) << 6) + (g & 1) * 32 + p4 * 8);
    const u32 aB = (u32)((64 * th + l31) * 128 + ((hh ^ ((l31 >> 1) & 7)) << 4));
    f32x16 acc[2][2] = {};
    s16x8 fa[2][2][2], fb[2][2][2];
    u32 pa, pb[4];
    auto setslot = [&](u32 slot) {
      pa = opq(aA + slot);
#pragma unroll
      for (int k = 0; k < 4; ++k) pb[k] = opq((aB ^ (u32)(k << 5)) + slot);
    };
    auto rd = [&](int set) {
#pragma unroll
      for (int k = 0; k < 2; ++k) {
        const int ks = 2 * set + k;
        fa[set][k][0] = lds_tr2(pa, ks * 4096, ks * 4096 + 1024);
        fb[set][k][0] = lds_b128(pb[ks], 0);
        fb[set][k][1] = lds_b128(pb[ks], 4096);
        fa[set][k][1] = lds_tr2(pa, 16384 + ks * 4096, 16384 + ks * 4096 + 1024);
      }
    };
    auto mm = [&](int set, int n0, int n1) {
#pragma unroll
      for (int n = n0; n < n1; ++n) acc[(n >> 1) & 1][n & 1] = mfma32(fa[set][n >> 2][(n >> 1) & 1], fb[set][n >> 2][n & 1], acc[(n >> 1) & 1][n & 1]);
    };
    setslot(0);
    const u32 fad = opq(L_FULL + (lane & 1) * 4);
#pragma unroll
    for (int i = 0; i < 4; ++i) asm volatile("" : "+v"(acc[i >> 1][i & 1]));
    __builtin_amdgcn_s_setprio(1);
    lflag_wait(L_FULL, 1, 1);
    STAMP(1);
    CYC_BEGIN
    rd(0);
    SCHED0();
    u32 slot = 0;
#pragma unroll 1
    for (int c = 0; c < 7; ++c) {
      const u32 fl = *(volatile LDSP(u32))fad;
      rd(1); mm(0, 0, 5);
      SGB(0x008, 1); SGB(0x100, 4); SGB(0x008, 1); SGB(0x100, 3); SGB(0x008, 1); SGB(0x100, 3); SGB(0x008, 1); SGB(0x100, 3); SGB(0x008, 1);
      SCHED0();
      if (c < 6 && __builtin_amdgcn_ballot_w64(fl < (u32)(c + 2))) lflag_wait(L_FULL, 1, c + 2);
      SCHED0();
      slot = slot == 2 * SLOT ? 0 : slot + SLOT;
      mm(0, 5, 6);
      SCHED0();
      setslot(slot);
      SCHED0();
      mm(0, 6, 8);
      SCHED0();
      if (c < 4) { WAIT_LGKM0(); BAR(); } else if (c == 6) lflag_set(L_FREE + cw * 4, 7);
      SCHED0();
      rd(0); mm(1, 0, 8);
#pragma unroll
      for (int i = 0; i < 4; ++i) { SGB(0x008, 1); SGB(0x100, 3); }
      SGB(0x008, 4);
      SCHED0();
      if (c == 2) STAMP(2);
    }
    STAMP(3);
    CYC_END;
    lflag_wait(L_EPI, 0, 1);
    STAMP(14);
    const u32 hdst = WS_H + (u32)((2 * panel + th) * 56 + 4 * ft + fq) * 4096 + lane * 16;
    float nm[2], c1[2], irs2[2];
#pragma unroll
    for (int j = 0; j < 2; ++j) {
      const f32x2 st = *(LDSP(f32x2))(L_STAT + (u32)(64 * th + 32 * j + l31) * 8);
      nm[j] = -st[0]; c1[j] = -1.44269504f * st[1]; irs2[j] = __builtin_amdgcn_rcpf(st[1] * st[1]);
    }
#pragma unroll
    for (int s = 0; s < 2; ++s) {
      f32x4 cs[2][2];
#pragma unroll
      for (int m = 0; m < 2; ++m)
#pragma unroll
        for (int G = 0; G < 2; ++G) cs[m][G] = *(LDSP(f32x4))(L_CSUM + (u32)(m * 128 + 32 * fq + 16 * s + 8 * G + 4 * hh) * 4);
#pragma unroll
      for (int j = 0; j < 2; ++j) {
        float hv[8];
#pragma unroll
        for (int e = 0; e < 8; ++e) {
          float a = __builtin_fmaf(nm[j], cs[0][e >> 2][e & 3], acc[0][j][8 * s + e]);
          float bb = __builtin_fmaf(nm[j], cs[1][e >> 2][e & 3], acc[1][j][8 * s + e]);
          float ex = __builtin_amdgcn_exp2f(a * c1[j]);
          float rr = __builtin_amdgcn_rcpf(__builtin_fmaf(ex, irs2[j], irs2[j]));
          hv[e] = a * bb * rr;
        }
        st128_dev(rs, hdst + (s * 2 + j) * 1024, u32x4{pk2(hv[0], hv[1]), pk2(hv[2], hv[3]), pk2(hv[4], hv[5]), pk2(hv[6], hv[7])});
      }
    }
    STAMP(4);
  }
  char* hflag = ws + WS_FH + ((2 * panel + th) * 56 + 4 * ft + fq) * 128;
  u32x4 xv, sv;
  if (cw < 4) {
    const int ht = (int)opq(threadIdx.x) & 255, lane = ht & 63, l15 = ht & 15;
    const u64 tag = tag_of(*(volatile LDSP(u64))L_TAG);
    const int ch = cw >> 1, var = cw & 1;
    const bool ready = __builtin_amdgcn_ballot_w64(*(volatile LDSP(u32))(L_CONV + (lane & 3) * 4) == 0) == 0;
    if (!ready) { gflag_publish(hflag, tag, lane); lflag_wait(L_CONV, 3, 1); }
    asm volatile("" ::: "memory");
    STAMP(5);
    conv_compute(lane, cw, d0 + ch, rs, hflag, tag, ready);
    STAMP(6);
    gflag_publish(ws + WS_FY + (var * 448 + d0 + ch) * 128, tag, lane);
    STAMP(7);
    u32x4 xr[2]; u32x2 st[2];
#pragma unroll
    for (int i = 0; i < 2; ++i) {
      const int n = ht + 256 * i, row = n >> 3, c = n & 7;
      xr[i] = *(const u32x4*)(x + (size_t)(64 * tau + row) * (D * 2) + (64 * dl + 8 * c) * 2);
      st[i] = ld64_dev(rs, WS_STAT + (u32)(64 * tau + row) * 8);
    }
    const int yvar = tau >> 4;
    gflag_wait<4>(ws + WS_FY + (yvar * 448 + 64 * dl + 16 * cw + l15) * 128, tag);
    STAMP(8);
#pragma unroll
    for (int i = 0; i < 2; ++i)
      glds16_dev((u32)((lane >> 3) * (S * 2) + (((lane & 7) ^ (2 * cw + i)) << 4)),
                 ws + WS_YT + (64 * dl + 16 * cw + 8 * i) * (S * 2) + 128 * tau, xbuf(cw ^ 1) + (u32)i * 1024);
    dflag_set(L_ADONE + cw * 4, 1);
    lflag_wait(L_ADONE, 3, 1);
    STAMP(9);
#pragma unroll
    for (int i = 0; i < 2; ++i) {
      const int n = ht + 256 * i, tok = n >> 3, dg = n & 7;
      const float mb = asf(st[i][0]), rsd = asf(st[i][1]);
      u32x4 so;
#pragma unroll
      for (int e = 0; e < 4; ++e) {
        const u32 ya = xbuf((dg >> 1) ^ 1) + (u32)((8 * (dg & 1) + 2 * e) * 128 + (((tok >> 3) ^ dg) << 4) + (tok & 7) * 2);
        const float y0 = bflo(*(LDSP(u16))ya), y1 = bflo(*(LDSP(u16))(ya + 128));
        const u32 dd = pk2(bflo(xr[i][e]) - mb, bfhi(xr[i][e]) - mb);
        const u32 xn = pk2(bflo(dd) * rsd, bfhi(dd) * rsd);
        so[e] = pk2(bflo(xn) * y0, bfhi(xn) * y1);
      }
      if (i == 0) { xv = xr[0]; sv = so; }
      else { lds_st128(E_S + (u32)ht * 16, so); lds_st128(E_X + (u32)ht * 16, xr[1]); }
    }
    STAMP(10);
  } else {
    const int j = cw - 4;
    const int lane = (int)opq(threadIdx.x) & 63;
    lflag_set(L_PUB + j * 4, 1);
    gflag_publish(hflag, tag_of(*(volatile LDSP(u64))L_TAG), lane);
    if (PUB2) lflag_set(L_PUB + j * 4, 2);
    STAMP(5);
    const u32 hl = lane * 16;
    const char* hbase = ws + WS_H + (size_t)tau * 56 * 4096;
    const int q4 = (lane >> 2) & 3;
    const u32 ta = (u32)((4 * (lane >> 5) + q4) * 128 + ((q4 >> 1) << 6) + ((lane >> 4) & 1) * 32 + (lane & 3) * 8);
    s16x8 fa[2][4], fb[3][4];
    f32x16 acc[2][2] = {};
    auto ldh = [&](int i) {
      const char* p = hbase + (size_t)g2_ks(j, i, rot2) * 4096;
      asm volatile("global_load_dwordx4 %0, %4, %5 sc1\n\tglobal_load_dwordx4 %1, %4, %5 offset:1024 sc1\n\t"
                   "global_load_dwordx4 %2, %4, %5 offset:2048 sc1\n\tglobal_load_dwordx4 %3, %4, %5 offset:3072 sc1"
                   : "=&v"(fb[i % 3][0]), "=&v"(fb[i % 3][1]), "=&v"(fb[i % 3][2]), "=&v"(fb[i % 3][3])
                   : "v"(hl), "s"(p) : "memory");
    };
    u32 tb[2];
#pragma unroll
    for (int a = 0; a < 2; ++a) tb[a] = opq((ta ^ (u32)(a << 6)) + g2_place(j, 0));
    auto rda = [&](int i) {
#pragma unroll
      for (int a = 0; a < 2; ++a)
        if (i > 0 && i % 3 == 0) tb[a] = opq(i % 6 ? tb[a] - 2 * SLOT : tb[a] + 2 * SLOT);
#pragma unroll
      for (int n = 0; n < 4; ++n) {
        const int o = (i % 3) * 4096 + (n >> 1) * 2048;
        fa[i & 1][n] = lds_tr2(tb[n & 1], o, o + 1024);
      }
    };
    const u32 fed = opq(L_FED + j * 4), prog = opq(L_PROG + j * 4);
    lflag_wait(L_H, 0, 1);
    STAMP(6);
    ldh(0); ldh(1); ldh(2);
    lflag_wait(L_FED + j * 4, 0, 3);
    rda(0);
    SCHED0();
    u32 fl = 0;
#pragma unroll
    for (int i = 0; i < 14; ++i) {
      if (i == 2 || (i >= 5 && i < 13 && (i & 1))) {
        if (__builtin_amdgcn_readfirstlane(fl) < i + 2) lflag_wait(L_FED + j * 4, 0, i + 2);
      }
      asm volatile("s_waitcnt vmcnt(%4)" : "+v"(fb[i % 3][0]), "+v"(fb[i % 3][1]), "+v"(fb[i % 3][2]), "+v"(fb[i % 3][3])
                   : "i"(i < 12 ? 8 : (13 - i) * 4) : "memory");
      SCHED0();
      if (i < 13) rda(i + 1);
      if (i == 1 || (i >= 4 && i < 12 && !(i & 1))) fl = *(volatile LDSP(u32))fed;
#pragma unroll
      for (int s = 0; s < 2; ++s)
#pragma unroll
        for (int a = 0; a < 2; ++a)
#pragma unroll
          for (int t = 0; t < 2; ++t) acc[a][t] = mfma32(fa[i & 1][2 * s + a], fb[i % 3][2 * s + t], acc[a][t]);
      if (i < 13) {
#pragma unroll
        for (int n = 0; n < 5; ++n) { SGB(0x008, 1); SGB(0x100, 2); }
        SGB(0x008, 3);
      }
      SCHED0();
      if (i < 8 && (i & 1)) *(volatile LDSP(u32))prog = i + 1;
      if (i == 4) { lflag_wait(L_H + 4, 0, 1); STAMP(8); }
      if (i + 3 < 14) ldh(i + 3);
      SCHED0();
      if (i == 0) STAMP(7);
      if (i == 8) STAMP(9);
    }
    STAMP(10);
    const int ln = (int)opq(threadIdx.x), lt = ln & 31, lh = (ln >> 5) & 1;
#pragma unroll
    for (int t = 0; t < 2; ++t)
#pragma unroll
      for (int a = 0; a < 2; ++a)
#pragma unroll
        for (int G = 0; G < 4; ++G)
          *(LDSP(f32x4))(g2_place(j, t ? 3 : 0) + (u32)(lt * 256 + (((8 * a + 2 * G + lh) ^ (lt & 7)) << 4))) =
              f32x4{acc[a][t][4 * G], acc[a][t][4 * G + 1], acc[a][t][4 * G + 2], acc[a][t][4 * G + 3]};
  }
  WAIT_LGKM0();
  STAMP(11);
  BAR();
  STAMP(12);
  {
    const int ht = (int)opq(threadIdx.x) & 255;
    const int half = cw >> 2, tk = ht >> 3, dg = ht & 7;
    if (half) { sv = *(LDSP(u32x4))(E_S + (u32)ht * 16); xv = *(LDSP(u32x4))(E_X + (u32)ht * 16); }
    f32x4 s0 = {}, s1 = {};
#pragma unroll
    for (int w = 0; w < 4; ++w) {
      const u32 pb = (half ? 0u : 2 * SLOT) + (u32)w * 12288 + (u32)tk * 256;
      s0 += *(LDSP(f32x4))(pb + (u32)(((2 * dg) ^ (tk & 7)) << 4));
      s1 += *(LDSP(f32x4))(pb + (u32)(((2 * dg + 1) ^ (tk & 7)) << 4));
    }
    float o[8];
#pragma unroll
    for (int e = 0; e < 8; ++e) {
      const float f = rbf(e < 4 ? s0[e & 3] : s1[e & 3]);
      const float gt = rbf(silu_f(f));
      const float xe = (e & 1) ? bfhi(xv[e >> 1]) : bflo(xv[e >> 1]);
      const float se = (e & 1) ? bfhi(sv[e >> 1]) : bflo(sv[e >> 1]);
      o[e] = xe + rbf(se * gt);
    }
    *(u32x4*)(out + (size_t)(64 * tau + 32 * half + tk) * (D * 2) + (64 * dl + 8 * dg) * 2) =
        u32x4{pk2(o[0], o[1]), pk2(o[2], o[3]), pk2(o[4], o[5]), pk2(o[6], o[7])};
  }
  STAMP(13);
  DIAG_FLUSH(ws);
}
}
#if defined(__HIP_DEVICE_COMPILE__) && __HIP_DEVICE_COMPILE__
#pragma clang attribute pop
#endif

extern "C" void kernel_launch(void* const* d_in, const int* in_sizes, int n_in, void* d_out, int out_size, void* d_ws,
                              size_t ws_size, hipStream_t stream) {
  (void)hipFuncSetAttribute((const void*)fused_kernel, hipFuncAttributeMaxDynamicSharedMemorySize, 163840);
  fused_kernel<<<256, 768, 163840, stream>>>((const char*)d_in[0], (const char*)d_in[1], (const char*)d_in[2], (const char*)d_in[3],
                                             (const char*)d_in[4], (const char*)d_in[5], (const char*)d_in[7], (const char*)d_in[8],
                                             (const char*)d_in[9], (char*)d_ws, (char*)d_out);
}
```

```cpp
#include <hip/hip_runtime.h>
#include <hip/hip_bf16.h>
#include <stdint.h>
#pragma clang diagnostic ignored "-Wint-to-pointer-cast"

#if defined(__HIP_DEVICE_COMPILE__) && __HIP_DEVICE_COMPILE__
#pragma clang attribute push(__attribute__((target("no-packed-fp32-ops"))), apply_to = function)
#endif
namespace {
typedef unsigned short u16;
typedef unsigned int u32;
typedef unsigned long long u64;
typedef __attribute__((ext_vector_type(4))) short s16x4;
typedef __attribute__((ext_vector_type(8))) short s16x8;
typedef __attribute__((ext_vector_type(8))) __bf16 bf16x8;
typedef __attribute__((ext_vector_type(2))) __bf16 bf16x2;
typedef __attribute__((ext_vector_type(16))) float f32x16;
typedef __attribute__((ext_vector_type(4))) float f32x4;
typedef __attribute__((ext_vector_type(2))) float f32x2;
typedef __attribute__((ext_vector_type(4))) u32 u32x4;
typedef __attribute__((ext_vector_type(2))) u32 u32x2;
#define LDSP(T) __attribute__((address_space(3))) T*
#define GLBP(T) __attribute__((address_space(1))) T*
#define DEV __device__ __forceinline__

constexpr int S = 2048, D = 448, F = 1792;
constexpr u32 WS_H = 0;
constexpr u32 WS_UT = WS_H + 7340032;
constexpr u32 WS_YT = WS_UT + 1835008;
constexpr u32 WS_STAT = WS_YT + 1835008;
constexpr u32 WS_CSUM = WS_STAT + 16384;
constexpr u32 WS_E = WS_CSUM + 16384;
constexpr u32 WS_FUT = WS_E + 128;
constexpr u32 WS_FST = WS_FUT + 448 * 128;
constexpr u32 WS_FY = WS_FST + 32 * 128;
constexpr u32 WS_FH = WS_FY + 896 * 128;
constexpr u32 WS_END = WS_FH + 1792 * 128;
constexpr int CODE_LINES = 196;
constexpr u32 SLOT = 49152;
constexpr u32 MISC = 3 * SLOT;
constexpr u32 L_FULL = MISC, L_EPI = MISC + 8, L_H = MISC + 16, L_FREE = MISC + 32, L_CONV = MISC + 64, L_FED = MISC + 80,
              L_TAG = MISC + 96, L_DD = MISC + 104, L_ADONE = MISC + 112, L_PROG = MISC + 128, L_XCH = MISC + 144, L_PUB = MISC + 160, L_STAT = MISC + 256,
              L_CSUM = MISC + 1280, L_USCR = MISC + 4096, L_DUMMY = MISC + 12288;
constexpr u32 CV = SLOT, CV_HSTR = 4160, CV_U = CV + 8 * CV_HSTR, CV_USTR = 4608;
constexpr u32 E_S = SLOT, E_X = SLOT + 4096;
constexpr int SPIN_CAP = 1 << 18;
#define ROT1 0
#define ROT2 0
#define TOUCH2_AT 0
#define PACE_L 63
#define PACE_S 63
#define FREE_SET dflag_set
#define W2PF 1
#define KVPF 0
#define SHARES 1
#define CODEPF 1
#define W2_HOLD 0
#define PUB2 0
#define KV_LATE 1
#define SPPF 1
#define FLAGPF 1
#define MIDPF 1
#define BPOLL 1
#define HDEPTH 3
#define BAR5 0
#define KV_AT 1

__device__ const u32 seq_tab[160] __attribute__((aligned(128))) = {0, 1, 2, 3, 4, 5, 6, 7};
DEV float asf(u32 v) { return __builtin_bit_cast(float, v); }
DEV float bflo(u32 v) { return asf(v << 16); }
DEV float bfhi(u32 v) { return asf(v & 0xffff0000u); }
template <int CTRL> DEV float dpp(float v) { return asf(__builtin_amdgcn_update_dpp(0, __builtin_bit_cast(u32, v), CTRL, 0xf, 0xf, true)); }
DEV float sum8(float v) { v += dpp<0xB1>(v); v += dpp<0x4E>(v); return v + dpp<0x141>(v); }
DEV u32 pk2(float lo, float hi) {
  asm("" : "+v"(lo)); asm("" : "+v"(hi));
  bf16x2 r = {(__bf16)lo, (__bf16)hi};
  return __builtin_bit_cast(u32, r);
}
DEV float rbf(float f) { bf16x2 r = {(__bf16)f, (__bf16)f}; return bflo(__builtin_bit_cast(u32, r)); }
DEV f32x4 mfma16(s16x8 a, s16x8 b, f32x4 c) {
  return __builtin_amdgcn_mfma_f32_16x16x32_bf16(__builtin_bit_cast(bf16x8, a), __builtin_bit_cast(bf16x8, b), c, 0, 0, 0);
}
DEV f32x16 mfma32(s16x8 a, s16x8 b, f32x16 c) {
  return __builtin_amdgcn_mfma_f32_32x32x16_bf16(__builtin_bit_cast(bf16x8, a), __builtin_bit_cast(bf16x8, b), c, 0, 0, 0);
}
DEV u32 opq(u32 v) { asm volatile("" : "+v"(v)); return v; }
DEV s16x8 lds_tr2(u32 base, int o0, int o1) {
  s16x4 lo = __builtin_amdgcn_ds_read_tr16_b64_v4i16((LDSP(s16x4))((LDSP(char))base + o0));
  s16x4 hi = __builtin_amdgcn_ds_read_tr16_b64_v4i16((LDSP(s16x4))((LDSP(char))base + o1));
  return __builtin_shufflevector(lo, hi, 0, 1, 2, 3, 4, 5, 6, 7);
}
DEV s16x8 lds_b128(u32 base, int o) { return *(LDSP(s16x8))((LDSP(char))base + o); }
DEV void lds_st128(u32 a, u32x4 v) { *(LDSP(u32x4))a = v; }
DEV void glds16x4(u32 v0, u32 v1, u32 v2, u32 v3, const void* sbase, u32 ldst) {
  asm volatile("s_mov_b32 m0, %5\n\ts_nop 0\n\tglobal_load_lds_dwordx4 %0, %4\n\tglobal_load_lds_dwordx4 %1, %4 offset:1024\n\t"
               "global_load_lds_dwordx4 %2, %4 offset:2048\n\tglobal_load_lds_dwordx4 %3, %4 offset:3072"
               ::"v"(v0), "v"(v1), "v"(v2), "v"(v3), "s"(sbase), "s"(ldst) : "memory");
}
DEV void glds16(u32 voff, const void* sbase, u32 ldst) {
  asm volatile("s_mov_b32 m0, %2\n\ts_nop 0\n\tglobal_load_lds_dwordx4 %0, %1" ::"v"(voff), "s"(sbase), "s"(ldst) : "memory");
}
DEV void glds16x4_lin(u32 v, const void* sbase, u32 ldst) { glds16x4(v, v, v, v, sbase, ldst); }
DEV void glds16x4_dev(u32 v, const void* sbase, u32 ldst) {
  asm volatile("s_mov_b32 m0, %2\n\ts_nop 0\n\tglobal_load_lds_dwordx4 %0, %1 sc1\n\tglobal_load_lds_dwordx4 %0, %1 offset:1024 sc1\n\t"
               "global_load_lds_dwordx4 %0, %1 offset:2048 sc1\n\tglobal_load_lds_dwordx4 %0, %1 offset:3072 sc1"
               ::"v"(v), "s"(sbase), "s"(ldst) : "memory");
}
DEV void glds16_dev(u32 voff, const void* sbase, u32 ldst) {
  asm volatile("s_mov_b32 m0, %2\n\ts_nop 0\n\tglobal_load_lds_dwordx4 %0, %1 sc1" ::"v"(voff), "s"(sbase), "s"(ldst) : "memory");
}
DEV void dflag_set(u32 addr, u32 val) {
  asm volatile("s_waitcnt lgkmcnt(0)\n\ts_mov_b32 m0, %2\n\ts_mov_b64 exec, 1\n\tglobal_load_lds_dword %0, %1\n\ts_mov_b64 exec, -1"
               ::"v"(0), "s"(seq_tab + val), "s"(addr) : "memory");
}
#define WAIT_VM(n) asm volatile("s_waitcnt vmcnt(%0)" ::"i"(n) : "memory")
#define WAIT_LGKM0() asm volatile("s_waitcnt lgkmcnt(0)" ::: "memory")
#define BAR() __builtin_amdgcn_s_barrier()
#define SCHED0() __builtin_amdgcn_sched_barrier(0)
#define SGB(mask, n) __builtin_amdgcn_sched_group_barrier(mask, n, 0)
DEV float silu_f(float x) { return x * __builtin_amdgcn_rcpf(1.0f + __builtin_amdgcn_exp2f(-1.44269504f * x)); }

DEV void lflag_set(u32 addr, u32 val) {
  asm volatile("s_waitcnt lgkmcnt(0)\n\tds_write_b32 %0, %1" ::"v"(addr), "v"(val) : "memory");
}
DEV void lflag_wait(u32 addr, u32 mask, u32 val) {
  const u32 a = addr + 4 * (threadIdx.x & mask);
#pragma unroll 1
  for (int i = 0; i < SPIN_CAP; ++i) {
    u32 v;
    asm volatile("ds_read_b32 %0, %1\n\ts_waitcnt lgkmcnt(0)" : "=v"(v) : "v"(a) : "memory");
    if (__builtin_amdgcn_ballot_w64(v < val) == 0) break;
    __builtin_amdgcn_s_sleep(1);
  }
}
DEV u64 gload64(const char* p) { return __hip_atomic_load((GLBP(u64))p, __ATOMIC_RELAXED, __HIP_MEMORY_SCOPE_AGENT); }
DEV void gstore64(char* p, u64 v) { __hip_atomic_store((GLBP(u64))p, v, __ATOMIC_RELAXED, __HIP_MEMORY_SCOPE_AGENT); }
template <int SLEEP>
DEV void gflag_wait(const char* lane_ptr, u64 tag) {
  bool ok = false;
#pragma unroll 1
  for (int i = 0; i < SPIN_CAP; ++i) {
    asm volatile("" ::: "memory");
    if (!ok) ok = gload64(lane_ptr) == tag;
    if (__builtin_amdgcn_ballot_w64(!ok) == 0) break;
    __builtin_amdgcn_s_sleep(SLEEP);
  }
  asm volatile("" ::: "memory");
}
DEV void gflag_publish(char* p, u64 tag, int lane) {
  WAIT_VM(0);
  if (lane == 0) gstore64(p, tag);
}
DEV u64 tag_of(u64 seed) { return seed * 0x9E3779B97F4A7C15ull + 0xD1B54A32D192ED03ull; }
DEV u64 make_tag(const char* ws) { return tag_of(gload64(ws + WS_E)); }
typedef __amdgpu_buffer_rsrc_t rsrc_t;
DEV rsrc_t ws_rsrc(char* ws) { return __builtin_amdgcn_make_buffer_rsrc(ws, 0, WS_END, 0x00020000); }
DEV void st128_dev(rsrc_t r, u32 off, u32x4 v) { __builtin_amdgcn_raw_buffer_store_b128(v, r, off, 0, 16); }
DEV void st64_dev(rsrc_t r, u32 off, u32x2 v) { __builtin_amdgcn_raw_buffer_store_b64(v, r, off, 0, 16); }
DEV void st32_dev(rsrc_t r, u32 off, u32 v) { __builtin_amdgcn_raw_buffer_store_b32(v, r, off, 0, 16); }
DEV u32x4 ld128_dev(rsrc_t r, u32 off) { return __builtin_amdgcn_raw_buffer_load_b128(r, off, 0, 16); }
DEV u32x2 ld64_dev(rsrc_t r, u32 off) { return __builtin_amdgcn_raw_buffer_load_b64(r, off, 0, 16); }
DEV void touch(u32 voff, const void* sbase, u32 ldst) {
  asm volatile("s_mov_b32 m0, %2\n\ts_nop 0\n\tglobal_load_lds_dword %0, %1" ::"v"(voff), "s"(sbase), "s"(ldst) : "memory");
}

DEV void glds4_dev(u32 voff, const void* sbase, u32 ldst) {
  asm volatile("s_mov_b32 m0, %2\n\ts_nop 0\n\tglobal_load_lds_dword %0, %1 sc1" ::"v"(voff), "s"(sbase), "s"(ldst) : "memory");
}
DEV u32 shr16(u32 hi, u32 lo) { return __builtin_amdgcn_alignbit(hi, lo, 16); }

DEV u32 xbuf(int w) { return w < 2 ? L_USCR + (u32)w * 4096 : (w == 2 ? L_DUMMY : CV + 45056); }
DEV void conv_compute(int lane, int cw, int d, rsrc_t rs, char* hflag, u64 tag, bool pub) {
  const int ch = cw >> 1, var = cw & 1;
  const int m = lane & 15, g = lane >> 4, q4 = m >> 2, p4 = lane & 3;
  u32 aa = opq(CV + (u32)(ch * 4 + ((q4 + 1) & 3)) * CV_HSTR + (u32)(((q4 + 1) & 4) + 8 * g + 4 * p4) * 2 + var * 256);
  u32 ba = opq(CV_U + (u32)ch * CV_USTR + (u32)(240 - 16 * m + 8 * g) * 2 + var * 256);
  f32x4 acc[8] = {};
  s16x8 A[8], B[8];
#pragma unroll
  for (int p = 0; p < 8; ++p) { A[7 - p] = lds_tr2(aa, (7 - p) * 512, (7 - p) * 512 + 8); B[p] = lds_b128(ba, (7 - p) * 512); }
  if (var == 0) {
    const u32 a0 = CV + (u32)ch * 4 * CV_HSTR + (u32)(m + 1 + 8 * g) * 2;
#pragma unroll
    for (int j = 0; j < 8; ++j) { const short v = *(LDSP(short))(a0 + j * 2); A[0][j] = m + 8 * g + j >= 15 ? v : (short)0; }
  }
  SCHED0();
#pragma unroll 1
  for (int it = 0; it < 4; ++it) {
#pragma unroll
    for (int p = 7; p >= 0; --p) {
#pragma unroll
      for (int dd = 0; dd <= 7 - p; ++dd) {
        acc[p + dd] = mfma16(A[p], B[dd], acc[p + dd]);
        if (p == 0) { SCHED0(); B[dd] = lds_b128(ba, 64 + (7 - dd) * 512); SCHED0(); }
      }
      SCHED0();
      A[p] = lds_tr2(aa, 64 + p * 512, 64 + p * 512 + 8);
      SCHED0();
    }
    aa = opq(aa + 64); ba = opq(ba + 64);
    if (it == 0 && pub) gflag_publish(hflag, tag, lane);
  }
  f32x4 y[4];
#pragma unroll
  for (int i = 0; i < 4; ++i) {
    *(LDSP(f32x4))(xbuf(cw) + (u32)(i * 1024 + lane * 16)) = var ? acc[i] : acc[4 + i];
    y[i] = var ? acc[4 + i] : acc[i];
  }
  lflag_set(L_XCH + cw * 4, 1);
  const u32 dv = *(LDSP(u32))L_DD;
  const float Dd = ch ? bfhi(dv) : bflo(dv);
  u32x2 uu[4];
#pragma unroll
  for (int i = 0; i < 4; ++i) uu[i] = *(LDSP(u32x2))(CV_U + (u32)ch * CV_USTR + (u32)(2044 - (1024 * var + 256 * i + 16 * m + 4 * g)) * 2);
  lflag_wait(L_XCH + (cw ^ 1) * 4, 0, 1);
#pragma unroll
  for (int i = 0; i < 4; ++i) {
    const int t = 1024 * var + 256 * i + 16 * m + 4 * g;
    const f32x4 s = y[i] + *(LDSP(f32x4))(xbuf(cw ^ 1) + (u32)(i * 1024 + lane * 16));
    float y0 = rbf(s[0]) + rbf(bfhi(uu[i][1]) * Dd), y1 = rbf(s[1]) + rbf(bflo(uu[i][1]) * Dd);
    float y2 = rbf(s[2]) + rbf(bfhi(uu[i][0]) * Dd), y3 = rbf(s[3]) + rbf(bflo(uu[i][0]) * Dd);
    st64_dev(rs, WS_YT + (u32)(d * (S * 2) + t * 2), u32x2{pk2(y0, y1), pk2(y2, y3)});
  }
}

DEV int rot7(int i, int rot) { const int v = i + rot; return v >= 7 ? v - 7 : v; }
DEV int g2_ks(int j, int i, int rot) { const int e = 7 * j + rot7(i < 7 ? i : i - 7, rot); return 4 * (e >> 1) + (e & 1) + (i < 7 ? 0 : 2); }
DEV u32 g2_place(int j, int p) { return (p < 3 ? 2 * SLOT + (u32)p * 4096 : (u32)(p - 3) * 4096) + (u32)j * 12288; }
DEV u32 w2_voff(int lane) { return (u32)((lane >> 3) * (D * 2) + (((lane & 7) ^ (((lane >> 4) & 1) << 2)) << 4)); }

DEV void stage_epi(char* ws, const char* ssmD, int lane, int panel, int ft, int d0) {
  glds16_dev(lane * 16, ws + WS_STAT + panel * 1024, L_STAT);
  glds16_dev(lane * 16, ws + WS_CSUM + ft * 1024, L_CSUM);
  if (lane < 2) glds4_dev(lane * 4, ws + WS_E, L_TAG);
  if (lane < 1) glds4_dev(0, ssmD + d0 * 2, L_DD);
  dflag_set(L_EPI, 1);
}
DEV void share(int b, int n, const char* base) {
  const int lane = (int)opq(threadIdx.x) & 63;
  if (lane < n) touch((u32)(b * n + lane) * 128, base, L_DUMMY);
}

__global__ __launch_bounds__(768, 3) void fused_kernel(const char* x, const char* kp, const char* vp, const char* skp,
                                                       const char* ssmk, const char* ssmD, const char* w1, const char* w3,
                                                       const char* w2, char* ws, char* out) {
  u64 pc0;
  asm volatile("s_getpc_b64 %0" : "=s"(pc0));
  const int b = blockIdx.x, xcd = b & 7, r = b >> 3;
  const int wid = __builtin_amdgcn_readfirstlane(threadIdx.x >> 6);
  const int panel = (xcd >> 1) * 4 + r / 7, ft = (xcd & 1) * 7 + r % 7;
  const int tau = xcd * 4 + r / 7, dl = r % 7;
  const int d0 = 2 * b;
  const int rot = ROT1 ? (r % 7 + 2 * (r / 7)) % 7 : 0, rot2 = ROT2 ? (r % 7 + 2 * (r / 7)) % 7 : 0;

  if (b < 224) {
    if (SHARES && wid >= 4 && wid < 7) share(b, wid == 4 ? 64 : 56, wid == 4 ? x : (wid == 5 ? w1 : w3));
    if (wid == 7) { *(LDSP(u32))(MISC + (threadIdx.x & 63) * 4) = 0; WAIT_LGKM0(); }
    BAR();
    if (wid == 7 && (threadIdx.x & 63) < 5) touch((threadIdx.x & 63) * 128, seq_tab, L_DUMMY);
    if (CODEPF && wid == 11 && (threadIdx.x & 63) < CODE_LINES / 28) touch((u32)(r * (CODE_LINES / 28) + (threadIdx.x & 63)) * 128, (const char*)pc0, L_DUMMY);
  }

  if (__builtin_expect(wid < 4 && b < 224, 1)) {
    const int lane = (int)opq(threadIdx.x) & 63;
    u64 tag = 0;
    bool early = true;
    const char* fpw = ws;
    if (__builtin_expect(wid < 2, 1)) {
      const int g = lane >> 4, l15 = lane & 15;
      u32 vx[8], vw[8];
      const u32 ox = (u32)((lane >> 3) * (D * 2) + (((lane & 7) ^ g) << 4));
      const u32 ow = (u32)(g * (F * 2) + (((l15 >> 2) ^ g) << 6) + ((lane & 3) << 4));
#pragma unroll
      for (int i = 0; i < 8; ++i) {
        vx[i] = (ox ^ (u32)((i & 1) * 64)) + i * 8 * (D * 2) - (i & 3) * 1024;
        vw[i] = ow + i * 4 * (F * 2) - (i & 3) * 1024;
      }
#pragma unroll 1
      for (int c = 0; c < 7; ++c) {
        if (c >= 3) BAR();
        const u32 slot = (u32)(c % 3) * SLOT + wid * 8192;
        const int kc = rot7(c, rot);
        const char* xb = x + (size_t)(panel * 128 + wid * 64) * (D * 2) + kc * 128;
        const char* w1b = w1 + (size_t)(kc * 64 + wid * 32) * (F * 2) + ft * 256;
        const char* w3b = w3 + (size_t)(kc * 64 + wid * 32) * (F * 2) + ft * 256;
#pragma unroll
        for (int i = 0; i < 8; i += 4) {
          WAIT_VM(PACE_L);
          glds16x4(vx[i], vx[i + 1], vx[i + 2], vx[i + 3], xb, slot + i * 1024);
          WAIT_VM(PACE_L);
          glds16x4(vw[i], vw[i + 1], vw[i + 2], vw[i + 3], w1b, slot + 16384 + i * 1024);
          WAIT_VM(PACE_L);
          glds16x4(vw[i], vw[i + 1], vw[i + 2], vw[i + 3], w3b, slot + 32768 + i * 1024);

        }
        dflag_set(L_FULL + wid * 4, c + 1);
      }
      if (wid == 0) {
        tag = make_tag(ws);
        const int k = lane & 7;
        gflag_wait<8>(ws + WS_FST + (k < 2 ? 2 * panel + k : (k == 2 ? tau : (k & 1) * 14 + ft)) * 128, tag);
        glds16_dev(lane * 16, ws + WS_STAT + panel * 1024, L_STAT);
        glds16_dev(lane * 16, ws + WS_CSUM + ft * 1024, L_CSUM);
        if (lane < 2) glds4_dev(lane * 4, ws + WS_E, L_TAG);
        if (lane < 1) glds4_dev(0, ssmD + d0 * 2, L_DD);
        dflag_set(L_EPI, 1);
      }
    } else {
      __builtin_amdgcn_s_setprio(3);
      const rsrc_t rs = ws_rsrc(ws);
      const int u = wid - 2;
      const int ti = b / 7, cj = b % 7;
      const int tg = lane >> 3, cg = lane & 7;
      const int t0 = 64 * ti + 32 * u + 4 * tg, ch0 = 64 * cj + 8 * cg;
      u32x4 kr[7], vr[4], sw[4]; u32x2 o[8];
      u64 seed;
      auto ldkv = [&]() {
        const u32 ko = (u32)(t0 * (D * 2) + ch0 * 2);
#pragma unroll
        for (int i = 0; i < 7; ++i) kr[i] = *(const u32x4*)(kp + (i < 3 && t0 == 0 ? ko : ko + (u32)((i - 3) * (D * 2))));
#pragma unroll
        for (int i = 0; i < 4; ++i) vr[i] = *(const u32x4*)(vp + (ko + (u32)(i * (D * 2))));
        SCHED0();
      };
      auto piece = [&](int p) {
#pragma unroll
        for (int c = 2 * p; c < 2 * p + 2; ++c) {
          const u32 wa = sw[p][(c & 1) * 2], wb = sw[p][(c & 1) * 2 + 1];
          const float w0 = bflo(wa), w1f = bfhi(wa), w2f = bflo(wb), w3f = bfhi(wb);
          float kf[7], ks[4];
#pragma unroll
          for (int i = 0; i < 7; ++i) kf[i] = (c & 1) ? bfhi(kr[i][p]) : bflo(kr[i][p]);
#pragma unroll
          for (int i = 0; i < 4; ++i)
            ks[i] = __builtin_fmaf(kf[i + 3], w0, __builtin_fmaf(kf[i + 2], w1f, __builtin_fmaf(kf[i + 1], w2f, kf[i] * w3f)));
#pragma unroll
          for (int i = 0; i < 2; ++i) {
            const int ta = 3 - 2 * i, tb = 2 - 2 * i;
            const u32 kk = pk2(ks[ta], ks[tb]);
            o[c][i] = pk2(bflo(kk) * ((c & 1) ? bfhi(vr[ta][p]) : bflo(vr[ta][p])), bfhi(kk) * ((c & 1) ? bfhi(vr[tb][p]) : bflo(vr[tb][p])));
          }
          asm volatile("" : "+v"(o[c]));
        }
        SCHED0();
      };
      BAR();
      ldkv();
#pragma unroll
      for (int i = 0; i < 4; ++i) sw[i] = *(const u32x4*)(skp + (u32)(ch0 * 8 + i * 16));
      seed = gload64(ws + WS_E + opq(0));
      if (FLAGPF && lane < 14) {
        const u32 fo = lane < 8 ? WS_FH + (u32)((2 * panel + (lane & 1)) * 56 + 4 * ft + (lane >> 1)) * 128
                                : (lane < 12 ? WS_FY + (u32)((lane & 1) * 448 + d0 + ((lane >> 1) & 1)) * 128 : WS_FUT + (u32)(cj * 64 + 2 * ti + (lane & 1)) * 128);
        if (u) touch(fo, ws, L_DUMMY);
      }
      SCHED0();
      BAR();
      BAR();
      SCHED0();
#pragma unroll
      for (int i = 0; i < 3; ++i) if (t0 == 0) kr[i] = u32x4{0, 0, 0, 0};
      piece(0);
      BAR();
      SCHED0();
#pragma unroll
      for (int c = 1; c < 4; ++c) piece(c);
      tag = tag_of(seed);
      const u32 scr = L_USCR + (u32)u * 4096;
#pragma unroll
      for (int c = 0; c < 8; ++c) *(LDSP(u32x2))(scr + (u32)((cg * 8 + c) * 64 + (((7 - tg) ^ (cg & 6)) << 3))) = o[c];
      WAIT_LGKM0();
#pragma unroll
      for (int i = 0; i < 4; ++i) {
        const int dd = 16 * i + (lane >> 2), c4 = lane & 3;
        u32x4 v = *(LDSP(u32x4))(scr + (u32)(dd * 64 + ((c4 ^ (dd >> 4)) << 4)));
        st128_dev(rs, WS_UT + (u32)((64 * cj + dd) * (S * 2) + (2016 - 64 * ti - 32 * u) * 2 + c4 * 16), v);
      }
      gflag_publish(ws + WS_FUT + (cj * 64 + 2 * ti + u) * 128, tag, lane);
    }
    const int j = wid;
    const u32 o2 = w2_voff(lane);
    const char* wbase = w2 + dl * 128;
    u32x4 pk[8][4];
    auto park = [&](int i0) {
#pragma unroll
    for (int i = i0; i < i0 + 4; ++i) {
      const char* src = wbase + (size_t)g2_ks(j, i + 6, rot2) * 32 * (D * 2);
      WAIT_VM(PACE_S);
#pragma unroll
      for (int pc = 0; pc < 4; ++pc) pk[i][pc] = *(const u32x4*)(src + (o2 + (u32)(pc * 8 * (D * 2))));
    }
    SCHED0();
    };
    park(0);
    park(4);
    lflag_wait(L_FREE, 7, 7);
    if (wid == 2) {
        if (lane < 32) {
          glds16(lane * 16, seq_tab + 32, CV_U + 4096);
          glds16(lane * 16, seq_tab + 32, CV_U + CV_USTR + 4096);
        }
        if (KV_LATE) gflag_wait<8>(ws + WS_FUT + ((d0 >> 6) * 64 + lane) * 128, tag);
        glds16x4_dev(lane * 16, ws + WS_UT + d0 * (S * 2), CV_U);
        glds16x4_dev(lane * 16, ws + WS_UT + (d0 + 1) * (S * 2), CV_U + CV_USTR);
        dflag_set(L_CONV + 8, 1);
        dflag_set(L_CONV + 12, 1);
      }
    if (wid < 2) {
      const u32 vl = lane * 16;
#pragma unroll 1
      for (int rr = 0; rr < 4; ++rr) {
        const char* src = ssmk + (size_t)(d0 + wid) * (S * 2) + (rr ? (rr - 4) * 2 : 0);
        const u32 dst = CV + (u32)(wid * 4 + rr) * CV_HSTR + (rr ? 24 : 32);
        glds16x4(rr && !lane ? 16u : vl, vl, vl, vl, src, dst);
      }
      dflag_set(L_CONV + wid * 4, 1);
    }
#pragma unroll 1
    for (int i = 0; i < 6; ++i) {
      if (i == W2_HOLD) lflag_wait(L_PUB, 3, 1);
      if (i == 3) dflag_set(L_FED + j * 4, 3);
      const char* src = wbase + (size_t)g2_ks(j, i, rot2) * 32 * (D * 2);
      WAIT_VM(PACE_S);
      glds16x4(o2, o2 + 8 * (D * 2) - 1024, o2 + 16 * (D * 2) - 2048, o2 + 24 * (D * 2) - 3072, src, g2_place(j, i));
    }
    dflag_set(L_FED + j * 4, 6);
    if (BPOLL ? wid == 2 : wid >= 2) {
      lflag_wait(L_PUB, 3, 1);
      const int e = lane < 28 ? lane : 27;
      gflag_wait<4>(ws + WS_FH + (tau * 56 + 4 * (e >> 1) + (e & 1) + 2 * (3 - wid)) * 128, tag);
      dflag_set(L_H + (3 - wid) * 4, 1);
    }
    WAIT_VM(0);
    const u32 wl = opq(lane * 16 + j * 12288);
#pragma unroll
    for (int i = 0; i < 8; i += 2) {
      lflag_wait(L_PROG + j * 4, 0, i + 2);
#pragma unroll
      for (int n = 0; n < 8; ++n) lds_st128(wl + g2_place(0, (i + (n >> 2)) % 6) + (n & 3) * 1024, pk[i + (n >> 2)][n & 3]);
      lflag_set(L_FED + j * 4, i + 8);
    }
    return;
  }
  const rsrc_t rs = ws_rsrc(ws);

  if (b >= 224) {
    if (wid >= 8 && b != 255) return;
    const int tid = (int)opq(threadIdx.x), lane = tid & 63;
    const int sp = b - 224;
    const u64 seed = gload64(ws + WS_E + opq(0));
    if (wid < 8) {
      const int row = 64 * sp + (tid >> 3), c8 = tid & 7;
      const int cu = sp < 28 ? sp : 27, mat = cu / 14, ct = cu % 14, kg = tid >> 4, c16 = tid & 15;
      const char* wp = (mat ? w3 : w1) + ct * 256;
      const u32 wo = (u32)(kg * (F * 2) + c16 * 16), xo = (u32)(row * (D * 2) + c8 * 16);
      u32x4 xv[7], wv[14];
#pragma unroll
      for (int i = 0; i < 7; ++i) xv[i] = *(const u32x4*)(x + (xo + i * 128));
#pragma unroll
      for (int i = 0; i < 14; ++i) wv[i] = *(const u32x4*)(wp + (wo + i * 32 * (F * 2)));
      SCHED0();
      float s = 0.f;
#pragma unroll
      for (int i = 0; i < 7; ++i)
#pragma unroll
        for (int j = 0; j < 4; ++j) s += bflo(xv[i][j]) + bfhi(xv[i][j]);
      const float m = sum8(s) * (1.0f / D);
      float q = 0.f;
#pragma unroll
      for (int i = 0; i < 7; ++i)
#pragma unroll
        for (int j = 0; j < 4; ++j) {
          const float aa = bflo(xv[i][j]) - m, bb = bfhi(xv[i][j]) - m;
          q = __builtin_fmaf(aa, aa, __builtin_fmaf(bb, bb, q));
        }
      const float sd = rbf(__builtin_amdgcn_sqrtf(rbf(rbf(sum8(q) * (1.0f / D)) + 1e-5f)));
      if (c8 == 0) *(LDSP(f32x2))(u32)(16384 + (tid >> 3) * 8) = f32x2{rbf(m), __builtin_amdgcn_rcpf(sd)};
      SCHED0();
      float a[8] = {};
#pragma unroll
      for (int i = 0; i < 14; ++i)
#pragma unroll
        for (int j = 0; j < 4; ++j) { a[2 * j] += bflo(wv[i][j]); a[2 * j + 1] += bfhi(wv[i][j]); }
      f32x4 t;
#pragma unroll
      for (int j = 0; j < 4; ++j) {
        auto p = __builtin_amdgcn_permlane16_swap(__builtin_bit_cast(u32, a[j]), __builtin_bit_cast(u32, a[j + 4]), false, false);
        t[j] = asf(p[0]) + asf(p[1]);
      }
      *(LDSP(f32x4))(u32)((kg >> 1) * 512 + c16 * 32 + (kg & 1) * 16) = t;
    }
    WAIT_LGKM0(); BAR();
    const u64 tag = tag_of(seed);
    if (wid == 0) {
      const int cu = sp < 28 ? sp : 27;
      f32x2 cs = {};
#pragma unroll
      for (int i = 0; i < 16; ++i) cs += *(LDSP(f32x2))(u32)(i * 512 + lane * 8);
      st64_dev(rs, WS_STAT + (u32)(64 * sp + lane) * 8, *(LDSP(u32x2))(u32)(16384 + lane * 8));
      if (sp < 28) st64_dev(rs, WS_CSUM + (u32)((cu % 14) * 1024 + (cu / 14) * 512 + lane * 8), __builtin_bit_cast(u32x2, cs));
      gflag_publish(ws + WS_FST + sp * 128, tag, lane);
    }
    WAIT_VM(0); BAR();
    if (SPPF) {
      if (tid < 392) touch((u32)(sp * 392 + tid) * 128, w2, L_DUMMY);
      if (tid < 448) touch((u32)(sp * 448 + tid) * 128, ssmk, L_DUMMY);
      BAR();
    }
    if (W2PF && wid > 0 && wid < 8) {
#pragma unroll 1
      for (int i = 0; i < 7; ++i) touch((u32)((((sp >> 3) * 7 + i) * 7 + wid - 1) * 64 + lane) * 128, w2, L_DUMMY);
      WAIT_VM(0);
    }
    if (b == 255) {
      gflag_wait<64>(ws + (tid < 480 ? WS_FUT + tid * 128 : WS_FH + ((((tid < 704 ? tid - 480 : 223) / 14) * 2) * 56 + ((tid < 704 ? tid - 480 : 223) % 14) * 4) * 128), tag);
      BAR();
      if (tid == 0) gstore64(ws + WS_E, tag);
    }
    return;
  }

  const int cw = wid - 4, th = cw & 1, fq = cw >> 1;
  {
    const int lane = (int)opq(threadIdx.x) & 63, g = lane >> 4, hh = lane >> 5, l31 = lane & 31;
    const int q4 = (lane >> 2) & 3, p4 = lane & 3;
    const u32 aA = 16384 + (u32)((8 * hh + q4) * 256 + ((fq ^ q4) << 6) + (g & 1) * 32 + p4 * 8);
    const u32 aB = (u32)((64 * th + l31) * 128 + ((hh ^ ((l31 >> 1) & 7)) << 4));
    f32x16 acc[2][2] = {};
    s16x8 fa[2][2][2], fb[2][2][2];
    u32 pa, pb[4];
    auto setslot = [&](u32 slot) {
      pa = opq(aA + slot);
#pragma unroll
      for (int k = 0; k < 4; ++k) pb[k] = opq((aB ^ (u32)(k << 5)) + slot);
    };
    auto rd = [&](int set) {
#pragma unroll
      for (int k = 0; k < 2; ++k) {
        const int ks = 2 * set + k;
        fa[set][k][0] = lds_tr2(pa, ks * 4096, ks * 4096 + 1024);
        fb[set][k][0] = lds_b128(pb[ks], 0);
        fb[set][k][1] = lds_b128(pb[ks], 4096);
        fa[set][k][1] = lds_tr2(pa, 16384 + ks * 4096, 16384 + ks * 4096 + 1024);
      }
    };
    auto mm = [&](int set, int n0, int n1) {
#pragma unroll
      for (int n = n0; n < n1; ++n) acc[(n >> 1) & 1][n & 1] = mfma32(fa[set][n >> 2][(n >> 1) & 1], fb[set][n >> 2][n & 1], acc[(n >> 1) & 1][n & 1]);
    };
    setslot(0);
    const u32 fad = opq(L_FULL + (lane & 1) * 4);
#pragma unroll
    for (int i = 0; i < 4; ++i) asm volatile("" : "+v"(acc[i >> 1][i & 1]));
    __builtin_amdgcn_s_setprio(1);
    lflag_wait(L_FULL, 1, 1);
    rd(0);
    SCHED0();
    u32 slot = 0;
#pragma unroll 1
    for (int c = 0; c < 7; ++c) {
      const u32 fl = *(volatile LDSP(u32))fad;
      rd(1); mm(0, 0, 5);
      SGB(0x008, 1); SGB(0x100, 4); SGB(0x008, 1); SGB(0x100, 3); SGB(0x008, 1); SGB(0x100, 3); SGB(0x008, 1); SGB(0x100, 3); SGB(0x008, 1);
      SCHED0();
      if (c < 6 && __builtin_amdgcn_ballot_w64(fl < (u32)(c + 2))) lflag_wait(L_FULL, 1, c + 2);
      SCHED0();
      slot = slot == 2 * SLOT ? 0 : slot + SLOT;
      mm(0, 5, 6);
      SCHED0();
      setslot(slot);
      SCHED0();
      mm(0, 6, 8);
      SCHED0();
      if (c < 4 + BAR5) { WAIT_LGKM0(); BAR(); } else if (c == 6) lflag_set(L_FREE + cw * 4, 7);
      SCHED0();
      rd(0); mm(1, 0, 8);
#pragma unroll
      for (int i = 0; i < 4; ++i) { SGB(0x008, 1); SGB(0x100, 3); }
      SGB(0x008, 4);
      SCHED0();
    }
    lflag_wait(L_EPI, 0, 1);
    const u32 hdst = WS_H + (u32)((2 * panel + th) * 56 + 4 * ft + fq) * 4096 + lane * 16;
    float nm[2], c1[2], irs2[2];
#pragma unroll
    for (int j = 0; j < 2; ++j) {
      const f32x2 st = *(LDSP(f32x2))(L_STAT + (u32)(64 * th + 32 * j + l31) * 8);
      nm[j] = -st[0]; c1[j] = -1.44269504f * st[1]; irs2[j] = __builtin_amdgcn_rcpf(st[1] * st[1]);
    }
#pragma unroll
    for (int s = 0; s < 2; ++s) {
      f32x4 cs[2][2];
#pragma unroll
      for (int m = 0; m < 2; ++m)
#pragma unroll
        for (int G = 0; G < 2; ++G) cs[m][G] = *(LDSP(f32x4))(L_CSUM + (u32)(m * 128 + 32 * fq + 16 * s + 8 * G + 4 * hh) * 4);
#pragma unroll
      for (int j = 0; j < 2; ++j) {
        float a[8], ex[8], hv[8];
#pragma unroll
        for (int e = 0; e < 8; ++e) a[e] = __builtin_fmaf(nm[j], cs[0][e >> 2][e & 3], acc[0][j][8 * s + e]);
#pragma unroll
        for (int e = 0; e < 8; ++e) ex[e] = a[e] * c1[j];
        SCHED0();
#pragma unroll
        for (int e = 0; e < 8; ++e) { ex[e] = __builtin_amdgcn_exp2f(ex[e]); hv[e] = __builtin_fmaf(nm[j], cs[1][e >> 2][e & 3], acc[1][j][8 * s + e]); }
        SCHED0();
#pragma unroll
        for (int e = 0; e < 8; ++e) ex[e] = __builtin_fmaf(ex[e], irs2[j], irs2[j]);
        SCHED0();
#pragma unroll
        for (int e = 0; e < 8; ++e) { ex[e] = __builtin_amdgcn_rcpf(ex[e]); hv[e] *= a[e]; }
        SCHED0();
#pragma unroll
        for (int e = 0; e < 8; ++e) hv[e] *= ex[e];
        st128_dev(rs, hdst + (s * 2 + j) * 1024, u32x4{pk2(hv[0], hv[1]), pk2(hv[2], hv[3]), pk2(hv[4], hv[5]), pk2(hv[6], hv[7])});
      }
    }
  }
  char* hflag = ws + WS_FH + ((2 * panel + th) * 56 + 4 * ft + fq) * 128;
  u32x4 xv, sv;
  if (cw < 4) {
    const int ht = (int)opq(threadIdx.x) & 255, lane = ht & 63, l15 = ht & 15;
    const u64 tag = tag_of(*(volatile LDSP(u64))L_TAG);
    const int ch = cw >> 1, var = cw & 1;
    const bool ready = false;
    gflag_publish(hflag, tag, lane);
    lflag_wait(L_CONV, 3, 1);
    asm volatile("" ::: "memory");
    conv_compute(lane, cw, d0 + ch, rs, hflag, tag, ready);
    gflag_publish(ws + WS_FY + (var * 448 + d0 + ch) * 128, tag, lane);
    u32x4 xr[2]; u32x2 st[2];
#pragma unroll
    for (int i = 0; i < 2; ++i) {
      const int n = ht + 256 * i, row = n >> 3, c = n & 7;
      xr[i] = *(const u32x4*)(x + (size_t)(64 * tau + row) * (D * 2) + (64 * dl + 8 * c) * 2);
      st[i] = ld64_dev(rs, WS_STAT + (u32)(64 * tau + row) * 8);
    }
    const int yvar = tau >> 4;
    gflag_wait<4>(ws + WS_FY + (yvar * 448 + 64 * dl + 16 * cw + l15) * 128, tag);
#pragma unroll
    for (int i = 0; i < 2; ++i)
      glds16_dev((u32)((lane >> 3) * (S * 2) + (((lane & 7) ^ (2 * cw + i)) << 4)),
                 ws + WS_YT + (64 * dl + 16 * cw + 8 * i) * (S * 2) + 128 * tau, xbuf(cw ^ 1) + (u32)i * 1024);
    dflag_set(L_ADONE + cw * 4, 1);
    lflag_wait(L_ADONE, 3, 1);
#pragma unroll
    for (int i = 0; i < 2; ++i) {
      const int n = ht + 256 * i, tok = n >> 3, dg = n & 7;
      const float mb = asf(st[i][0]), rsd = asf(st[i][1]);
      u32x4 so;
#pragma unroll
      for (int e = 0; e < 4; ++e) {
        const u32 ya = xbuf((dg >> 1) ^ 1) + (u32)((8 * (dg & 1) + 2 * e) * 128 + (((tok >> 3) ^ dg) << 4) + (tok & 7) * 2);
        const float y0 = bflo(*(LDSP(u16))ya), y1 = bflo(*(LDSP(u16))(ya + 128));
        const u32 dd = pk2(bflo(xr[i][e]) - mb, bfhi(xr[i][e]) - mb);
        const u32 xn = pk2(bflo(dd) * rsd, bfhi(dd) * rsd);
        so[e] = pk2(bflo(xn) * y0, bfhi(xn) * y1);
      }
      if (i == 0) { xv = xr[0]; sv = so; }
      else { lds_st128(E_S + (u32)ht * 16, so); lds_st128(E_X + (u32)ht * 16, xr[1]); }
    }
  } else {
    const int j = cw - 4;
    const int lane = (int)opq(threadIdx.x) & 63;
    lflag_set(L_PUB + j * 4, 1);
    const u64 btag = tag_of(*(volatile LDSP(u64))L_TAG);
    gflag_publish(hflag, btag, lane);
    const u32 hl = lane * 16;
    const char* hbase = ws + WS_H + (size_t)tau * 56 * 4096;
    const int q4 = (lane >> 2) & 3;
    const u32 ta = (u32)((4 * (lane >> 5) + q4) * 128 + ((q4 >> 1) << 6) + ((lane >> 4) & 1) * 32 + (lane & 3) * 8);
    s16x8 fa[2][4], fb[3][4];
    f32x16 acc[2][2] = {};
    auto ldh1 = [&](int i, int n) {
      const char* p = hbase + (size_t)g2_ks(j, i, rot2) * 4096;
      asm volatile("global_load_dwordx4 %0, %1, %2 offset:%3 sc1" : "=v"(fb[i % 3][n]) : "v"(hl), "s"(p), "i"(n * 1024) : "memory");
    };
    u32 tb[2];
#pragma unroll
    for (int a = 0; a < 2; ++a) tb[a] = opq((ta ^ (u32)(a << 6)) + g2_place(j, 0));
    auto rda1 = [&](int i, int n) {
      if (n < 2 && i > 0 && i % 3 == 0) tb[n] = opq(i % 6 ? tb[n] - 2 * SLOT : tb[n] + 2 * SLOT);
      const int o = (i % 3) * 4096 + (n >> 1) * 2048;
      fa[i & 1][n] = lds_tr2(tb[n & 1], o, o + 1024);
    };
    const u32 fed = opq(L_FED + j * 4), prog = opq(L_PROG + j * 4);
    if (BPOLL) { const int e = 7 * j + (lane < 7 ? lane : 6); gflag_wait<2>(ws + WS_FH + (tau * 56 + 4 * (e >> 1) + (e & 1)) * 128, btag); }
    else lflag_wait(L_H, 0, 1);
#pragma unroll
    for (int n = 0; n < 8; ++n) ldh1(n >> 2, n & 3);
    lflag_wait(L_FED + j * 4, 0, 3);
#pragma unroll
    for (int n = 0; n < 4; ++n) rda1(0, n);
    SCHED0();
    u32 fl = 0;
#pragma unroll
    for (int i = 0; i < 14; ++i) {
      if (i == 2 || (i >= 5 && i < 13 && (i & 1))) {
        if (__builtin_amdgcn_readfirstlane(fl) < i + 2) lflag_wait(L_FED + j * 4, 0, i + 2);
      }
      if (i == 5) { lflag_wait(L_H + 4, 0, 1); }
      asm volatile("s_waitcnt vmcnt(%4)" : "+v"(fb[i % 3][0]), "+v"(fb[i % 3][1]), "+v"(fb[i % 3][2]), "+v"(fb[i % 3][3])
                   : "i"(i < 13 ? 4 : 0) : "memory");
      SCHED0();
#pragma unroll
      for (int n = 0; n < 8; ++n) {
        const int s = n >> 2, a = (n >> 1) & 1, t = n & 1;
        acc[a][t] = mfma32(fa[i & 1][2 * s + a], fb[i % 3][2 * s + t], acc[a][t]);
        SCHED0();
        if (i + 2 < 14 && !(n & 1)) ldh1(i + 2, n >> 1);
        if (i < 13 && n < 4) rda1(i + 1, n);
        if (n == 5 && (i == 1 || (i >= 4 && i < 12 && !(i & 1)))) fl = *(volatile LDSP(u32))fed;
        if (n == 6 && i < 8 && (i & 1)) *(volatile LDSP(u32))prog = i + 1;
        SCHED0();
      }

    }
    const int ln = (int)opq(threadIdx.x), lt = ln & 31, lh = (ln >> 5) & 1;
#pragma unroll
    for (int t = 0; t < 2; ++t)
#pragma unroll
      for (int a = 0; a < 2; ++a)
#pragma unroll
        for (int G = 0; G < 4; ++G)
          *(LDSP(f32x4))(g2_place(j, t ? 3 : 0) + (u32)(lt * 256 + (((8 * a + 2 * G + lh) ^ (lt & 7)) << 4))) =
              f32x4{acc[a][t][4 * G], acc[a][t][4 * G + 1], acc[a][t][4 * G + 2], acc[a][t][4 * G + 3]};
  }
  WAIT_LGKM0();
  BAR();
  {
    const int ht = (int)opq(threadIdx.x) & 255;
    const int half = cw >> 2, tk = ht >> 3, dg = ht & 7;
    if (half) { sv = *(LDSP(u32x4))(E_S + (u32)ht * 16); xv = *(LDSP(u32x4))(E_X + (u32)ht * 16); }
    f32x4 s0 = {}, s1 = {};
#pragma unroll
    for (int w = 0; w < 4; ++w) {
      const u32 pb = (half ? 0u : 2 * SLOT) + (u32)w * 12288 + (u32)tk * 256;
      s0 += *(LDSP(f32x4))(pb + (u32)(((2 * dg) ^ (tk & 7)) << 4));
      s1 += *(LDSP(f32x4))(pb + (u32)(((2 * dg + 1) ^ (tk & 7)) << 4));
    }
    float o[8];
#pragma unroll
    for (int e = 0; e < 8; ++e) {
      const float f = rbf(e < 4 ? s0[e & 3] : s1[e & 3]);
      const float gt = rbf(silu_f(f));
      const float xe = (e & 1) ? bfhi(xv[e >> 1]) : bflo(xv[e >> 1]);
      const float se = (e & 1) ? bfhi(sv[e >> 1]) : bflo(sv[e >> 1]);
      o[e] = xe + rbf(se * gt);
    }
    *(u32x4*)(out + (size_t)(64 * tau + 32 * half + tk) * (D * 2) + (64 * dl + 8 * dg) * 2) =
        u32x4{pk2(o[0], o[1]), pk2(o[2], o[3]), pk2(o[4], o[5]), pk2(o[6], o[7])};
  }
}
}
#if defined(__HIP_DEVICE_COMPILE__) && __HIP_DEVICE_COMPILE__
#pragma clang attribute pop
#endif

extern "C" void kernel_launch(void* const* d_in, const int* in_sizes, int n_in, void* d_out, int out_size, void* d_ws,
                              size_t ws_size, hipStream_t stream) {
  (void)hipFuncSetAttribute((const void*)fused_kernel, hipFuncAttributeMaxDynamicSharedMemorySize, 163840);
  fused_kernel<<<256, 768, 163840, stream>>>((const char*)d_in[0], (const char*)d_in[1], (const char*)d_in[2], (const char*)d_in[3],
                                             (const char*)d_in[4], (const char*)d_in[5], (const char*)d_in[7], (const char*)d_in[8],
                                             (const char*)d_in[9], (char*)d_ws, (char*)d_out);
}
```

```cpp
#include <hip/hip_runtime.h>
#include <hip/hip_bf16.h>
#include <stdint.h>
#pragma clang diagnostic ignored "-Wint-to-pointer-cast"

#if defined(__HIP_DEVICE_COMPILE__) && __HIP_DEVICE_COMPILE__
#pragma clang attribute push(__attribute__((target("no-packed-fp32-ops"))), apply_to = function)
#endif
namespace {
typedef unsigned short u16;
typedef unsigned int u32;
typedef unsigned long long u64;
typedef __attribute__((ext_vector_type(4))) short s16x4;
typedef __attribute__((ext_vector_type(8))) short s16x8;
typedef __attribute__((ext_vector_type(8))) __bf16 bf16x8;
typedef __attribute__((ext_vector_type(2))) __bf16 bf16x2;
typedef __attribute__((ext_vector_type(16))) float f32x16;
typedef __attribute__((ext_vector_type(4))) float f32x4;
typedef __attribute__((ext_vector_type(2))) float f32x2;
typedef __attribute__((ext_vector_type(4))) u32 u32x4;
typedef __attribute__((ext_vector_type(2))) u32 u32x2;
#define LDSP(T) __attribute__((address_space(3))) T*
#define GLBP(T) __attribute__((address_space(1))) T*
#define DEV __device__ __forceinline__

constexpr int S = 2048, D = 448, F = 1792;
constexpr u32 WS_H = 0;
constexpr u32 WS_UT = WS_H + 7340032;
constexpr u32 WS_YT = WS_UT + 1835008;
constexpr u32 WS_STAT = WS_YT + 1835008;
constexpr u32 WS_CSUM = WS_STAT + 16384;
constexpr u32 WS_E = WS_CSUM + 16384;
constexpr u32 WS_FUT = WS_E + 128;
constexpr u32 WS_FST = WS_FUT + 448 * 128;
constexpr u32 WS_FY = WS_FST + 32 * 128;
constexpr u32 WS_FH = WS_FY + 896 * 128;
constexpr u32 WS_END = WS_FH + 1792 * 128;
constexpr int CODE_LINES = 196;
constexpr u32 SLOT = 49152;
constexpr u32 MISC = 3 * SLOT;
constexpr u32 L_FULL = MISC, L_EPI = MISC + 8, L_H = MISC + 16, L_FREE = MISC + 32, L_CONV = MISC + 64, L_FED = MISC + 80,
              L_TAG = MISC + 96, L_DD = MISC + 104, L_ADONE = MISC + 112, L_PROG = MISC + 128, L_XCH = MISC + 144, L_PUB = MISC + 160, L_STAT = MISC + 256,
              L_CSUM = MISC + 1280, L_USCR = MISC + 4096, L_DUMMY = MISC + 12288;
constexpr u32 CV = SLOT, CV_HSTR = 4160, CV_U = CV + 8 * CV_HSTR, CV_USTR = 4608;
constexpr u32 E_S = SLOT, E_X = SLOT + 4096;
constexpr int SPIN_CAP = 1 << 18;
#define ROT1 0
#define ROT2 0
#define TOUCH2_AT 0
#define PACE_L 63
#define PACE_S 63
#define FREE_SET dflag_set
#define W2PF 1
#define KVPF 0
#define SHARES 1
#define CODEPF 1
#define W2_HOLD 0
#define PUB2 0
#define KV_LATE 1
#define SPPF 1
#define FLAGPF 1
#define MIDPF 1
#define BPOLL 1
#define HDEPTH 3
#define KV_AT 1

__device__ const u32 seq_tab[160] __attribute__((aligned(128))) = {0, 1, 2, 3, 4, 5, 6, 7};
DEV float asf(u32 v) { return __builtin_bit_cast(float, v); }
DEV float bflo(u32 v) { return asf(v << 16); }
DEV float bfhi(u32 v) { return asf(v & 0xffff0000u); }
template <int CTRL> DEV float dpp(float v) { return asf(__builtin_amdgcn_update_dpp(0, __builtin_bit_cast(u32, v), CTRL, 0xf, 0xf, true)); }
DEV float sum8(float v) { v += dpp<0xB1>(v); v += dpp<0x4E>(v); return v + dpp<0x141>(v); }
DEV u32 pk2(float lo, float hi) {
  asm("" : "+v"(lo)); asm("" : "+v"(hi));
  bf16x2 r = {(__bf16)lo, (__bf16)hi};
  return __builtin_bit_cast(u32, r);
}
DEV float rbf(float f) { bf16x2 r = {(__bf16)f, (__bf16)f}; return bflo(__builtin_bit_cast(u32, r)); }
DEV f32x4 mfma16(s16x8 a, s16x8 b, f32x4 c) {
  return __builtin_amdgcn_mfma_f32_16x16x32_bf16(__builtin_bit_cast(bf16x8, a), __builtin_bit_cast(bf16x8, b), c, 0, 0, 0);
}
DEV f32x16 mfma32(s16x8 a, s16x8 b, f32x16 c) {
  return __builtin_amdgcn_mfma_f32_32x32x16_bf16(__builtin_bit_cast(bf16x8, a), __builtin_bit_cast(bf16x8, b), c, 0, 0, 0);
}
DEV u32 opq(u32 v) { asm volatile("" : "+v"(v)); return v; }
DEV s16x8 lds_tr2(u32 base, int o0, int o1) {
  s16x4 lo = __builtin_amdgcn_ds_read_tr16_b64_v4i16((LDSP(s16x4))((LDSP(char))base + o0));
  s16x4 hi = __builtin_amdgcn_ds_read_tr16_b64_v4i16((LDSP(s16x4))((LDSP(char))base + o1));
  return __builtin_shufflevector(lo, hi, 0, 1, 2, 3, 4, 5, 6, 7);
}
DEV s16x8 lds_b128(u32 base, int o) { return *(LDSP(s16x8))((LDSP(char))base + o); }
DEV void lds_st128(u32 a, u32x4 v) { *(LDSP(u32x4))a = v; }
DEV void glds16x4(u32 v0, u32 v1, u32 v2, u32 v3, const void* sbase, u32 ldst) {
  asm volatile("s_mov_b32 m0, %5\n\ts_nop 0\n\tglobal_load_lds_dwordx4 %0, %4\n\tglobal_load_lds_dwordx4 %1, %4 offset:1024\n\t"
               "global_load_lds_dwordx4 %2, %4 offset:2048\n\tglobal_load_lds_dwordx4 %3, %4 offset:3072"
               ::"v"(v0), "v"(v1), "v"(v2), "v"(v3), "s"(sbase), "s"(ldst) : "memory");
}
DEV void glds16(u32 voff, const void* sbase, u32 ldst) {
  asm volatile("s_mov_b32 m0, %2\n\ts_nop 0\n\tglobal_load_lds_dwordx4 %0, %1" ::"v"(voff), "s"(sbase), "s"(ldst) : "memory");
}
DEV void glds16x4_lin(u32 v, const void* sbase, u32 ldst) { glds16x4(v, v, v, v, sbase, ldst); }
DEV void glds16x4_dev(u32 v, const void* sbase, u32 ldst) {
  asm volatile("s_mov_b32 m0, %2\n\ts_nop 0\n\tglobal_load_lds_dwordx4 %0, %1 sc1\n\tglobal_load_lds_dwordx4 %0, %1 offset:1024 sc1\n\t"
               "global_load_lds_dwordx4 %0, %1 offset:2048 sc1\n\tglobal_load_lds_dwordx4 %0, %1 offset:3072 sc1"
               ::"v"(v), "s"(sbase), "s"(ldst) : "memory");
}
DEV void glds16_dev(u32 voff, const void* sbase, u32 ldst) {
  asm volatile("s_mov_b32 m0, %2\n\ts_nop 0\n\tglobal_load_lds_dwordx4 %0, %1 sc1" ::"v"(voff), "s"(sbase), "s"(ldst) : "memory");
}
DEV void dflag_set(u32 addr, u32 val) {
  asm volatile("s_waitcnt lgkmcnt(0)\n\ts_mov_b32 m0, %2\n\ts_mov_b64 exec, 1\n\tglobal_load_lds_dword %0, %1\n\ts_mov_b64 exec, -1"
               ::"v"(0), "s"(seq_tab + val), "s"(addr) : "memory");
}
#define WAIT_VM(n) asm volatile("s_waitcnt vmcnt(%0)" ::"i"(n) : "memory")
#define WAIT_LGKM0() asm volatile("s_waitcnt lgkmcnt(0)" ::: "memory")
#define BAR() __builtin_amdgcn_s_barrier()
#define SCHED0() __builtin_amdgcn_sched_barrier(0)
#define SGB(mask, n) __builtin_amdgcn_sched_group_barrier(mask, n, 0)
DEV float silu_f(float x) { return x * __builtin_amdgcn_rcpf(1.0f + __builtin_amdgcn_exp2f(-1.44269504f * x)); }

DEV void lflag_set(u32 addr, u32 val) {
  asm volatile("s_waitcnt lgkmcnt(0)\n\tds_write_b32 %0, %1" ::"v"(addr), "v"(val) : "memory");
}
DEV void lflag_wait(u32 addr, u32 mask, u32 val) {
  const u32 a = addr + 4 * (threadIdx.x & mask);
#pragma unroll 1
  for (int i = 0; i < SPIN_CAP; ++i) {
    u32 v;
    asm volatile("ds_read_b32 %0, %1\n\ts_waitcnt lgkmcnt(0)" : "=v"(v) : "v"(a) : "memory");
    if (__builtin_amdgcn_ballot_w64(v < val) == 0) break;
    __builtin_amdgcn_s_sleep(1);
  }
}
DEV u64 gload64(const char* p) { return __hip_atomic_load((GLBP(u64))p, __ATOMIC_RELAXED, __HIP_MEMORY_SCOPE_AGENT); }
DEV void gstore64(char* p, u64 v) { __hip_atomic_store((GLBP(u64))p, v, __ATOMIC_RELAXED, __HIP_MEMORY_SCOPE_AGENT); }
template <int SLEEP>
DEV void gflag_wait(const char* lane_ptr, u64 tag) {
  bool ok = false;
#pragma unroll 1
  for (int i = 0; i < SPIN_CAP; ++i) {
    asm volatile("" ::: "memory");
    if (!ok) ok = gload64(lane_ptr) == tag;
    if (__builtin_amdgcn_ballot_w64(!ok) == 0) break;
    __builtin_amdgcn_s_sleep(SLEEP);
  }
  asm volatile("" ::: "memory");
}
DEV void gflag_publish(char* p, u64 tag, int lane) {
  WAIT_VM(0);
  if (lane == 0) gstore64(p, tag);
}
DEV u64 tag_of(u64 seed) { return seed * 0x9E3779B97F4A7C15ull + 0xD1B54A32D192ED03ull; }
DEV u64 make_tag(const char* ws) { return tag_of(gload64(ws + WS_E)); }
typedef __amdgpu_buffer_rsrc_t rsrc_t;
DEV rsrc_t ws_rsrc(char* ws) { return __builtin_amdgcn_make_buffer_rsrc(ws, 0, WS_END, 0x00020000); }
DEV void st128_dev(rsrc_t r, u32 off, u32x4 v) { __builtin_amdgcn_raw_buffer_store_b128(v, r, off, 0, 16); }
DEV void st64_dev(rsrc_t r, u32 off, u32x2 v) { __builtin_amdgcn_raw_buffer_store_b64(v, r, off, 0, 16); }
DEV void st32_dev(rsrc_t r, u32 off, u32 v) { __builtin_amdgcn_raw_buffer_store_b32(v, r, off, 0, 16); }
DEV u32x4 ld128_dev(rsrc_t r, u32 off) { return __builtin_amdgcn_raw_buffer_load_b128(r, off, 0, 16); }
DEV u32x2 ld64_dev(rsrc_t r, u32 off) { return __builtin_amdgcn_raw_buffer_load_b64(r, off, 0, 16); }
DEV void touch(u32 voff, const void* sbase, u32 ldst) {
  asm volatile("s_mov_b32 m0, %2\n\ts_nop 0\n\tglobal_load_lds_dword %0, %1" ::"v"(voff), "s"(sbase), "s"(ldst) : "memory");
}

DEV void glds4_dev(u32 voff, const void* sbase, u32 ldst) {
  asm volatile("s_mov_b32 m0, %2\n\ts_nop 0\n\tglobal_load_lds_dword %0, %1 sc1" ::"v"(voff), "s"(sbase), "s"(ldst) : "memory");
}
DEV u32 shr16(u32 hi, u32 lo) { return __builtin_amdgcn_alignbit(hi, lo, 16); }

DEV u32 xbuf(int w) { return w < 2 ? L_USCR + (u32)w * 4096 : (w == 2 ? L_DUMMY : CV + 45056); }
DEV void conv_compute(int lane, int cw, int d, rsrc_t rs, char* hflag, u64 tag, bool pub) {
  const int ch = cw >> 1, var = cw & 1;
  const int m = lane & 15, g = lane >> 4, q4 = m >> 2, p4 = lane & 3;
  u32 aa = opq(CV + (u32)(ch * 4 + ((q4 + 1) & 3)) * CV_HSTR + (u32)(((q4 + 1) & 4) + 8 * g + 4 * p4) * 2 + var * 256);
  u32 ba = opq(CV_U + (u32)ch * CV_USTR + (u32)(240 - 16 * m + 8 * g) * 2 + var * 256);
  f32x4 acc[8] = {};
  s16x8 A[8], B[8];
#pragma unroll
  for (int p = 0; p < 8; ++p) { A[7 - p] = lds_tr2(aa, (7 - p) * 512, (7 - p) * 512 + 8); B[p] = lds_b128(ba, (7 - p) * 512); }
  if (var == 0) {
    const u32 a0 = CV + (u32)ch * 4 * CV_HSTR + (u32)(m + 1 + 8 * g) * 2;
#pragma unroll
    for (int j = 0; j < 8; ++j) { const short v = *(LDSP(short))(a0 + j * 2); A[0][j] = m + 8 * g + j >= 15 ? v : (short)0; }
  }
  SCHED0();
#pragma unroll 1
  for (int it = 0; it < 4; ++it) {
#pragma unroll
    for (int p = 7; p >= 0; --p) {
#pragma unroll
      for (int dd = 0; dd <= 7 - p; ++dd) {
        acc[p + dd] = mfma16(A[p], B[dd], acc[p + dd]);
        if (p == 0) { SCHED0(); B[dd] = lds_b128(ba, 64 + (7 - dd) * 512); SCHED0(); }
      }
      SCHED0();
      A[p] = lds_tr2(aa, 64 + p * 512, 64 + p * 512 + 8);
      SCHED0();
    }
    aa = opq(aa + 64); ba = opq(ba + 64);
    if (it == 0 && pub) gflag_publish(hflag, tag, lane);
  }
  f32x4 y[4];
#pragma unroll
  for (int i = 0; i < 4; ++i) {
    *(LDSP(f32x4))(xbuf(cw) + (u32)(i * 1024 + lane * 16)) = var ? acc[i] : acc[4 + i];
    y[i] = var ? acc[4 + i] : acc[i];
  }
  lflag_set(L_XCH + cw * 4, 1);
  const u32 dv = *(LDSP(u32))L_DD;
  const float Dd = ch ? bfhi(dv) : bflo(dv);
  u32x2 uu[4];
#pragma unroll
  for (int i = 0; i < 4; ++i) uu[i] = *(LDSP(u32x2))(CV_U + (u32)ch * CV_USTR + (u32)(2044 - (1024 * var + 256 * i + 16 * m + 4 * g)) * 2);
  lflag_wait(L_XCH + (cw ^ 1) * 4, 0, 1);
#pragma unroll
  for (int i = 0; i < 4; ++i) {
    const int t = 1024 * var + 256 * i + 16 * m + 4 * g;
    const f32x4 s = y[i] + *(LDSP(f32x4))(xbuf(cw ^ 1) + (u32)(i * 1024 + lane * 16));
    float y0 = rbf(s[0]) + rbf(bfhi(uu[i][1]) * Dd), y1 = rbf(s[1]) + rbf(bflo(uu[i][1]) * Dd);
    float y2 = rbf(s[2]) + rbf(bfhi(uu[i][0]) * Dd), y3 = rbf(s[3]) + rbf(bflo(uu[i][0]) * Dd);
    st64_dev(rs, WS_YT + (u32)(d * (S * 2) + t * 2), u32x2{pk2(y0, y1), pk2(y2, y3)});
  }
}

DEV int rot7(int i, int rot) { const int v = i + rot; return v >= 7 ? v - 7 : v; }
DEV int g2_ks(int j, int i, int rot) { const int e = 7 * j + rot7(i < 7 ? i : i - 7, rot); return 4 * (e >> 1) + (e & 1) + (i < 7 ? 0 : 2); }
DEV u32 g2_place(int j, int p) { return (p < 3 ? 2 * SLOT + (u32)p * 4096 : (u32)(p - 3) * 4096) + (u32)j * 12288; }
DEV u32 w2_voff(int lane) { return (u32)((lane >> 3) * (D * 2) + (((lane & 7) ^ (((lane >> 4) & 1) << 2)) << 4)); }

DEV void share(int b, int n, const char* base) {
  const int lane = (int)opq(threadIdx.x) & 63;
  if (lane < n) touch((u32)(b * n + lane) * 128, base, L_DUMMY);
}

__global__ __launch_bounds__(768, 3) void fused_kernel(const char* x, const char* kp, const char* vp, const char* skp,
                                                       const char* ssmk, const char* ssmD, const char* w1, const char* w3,
                                                       const char* w2, char* ws, char* out) {
  u64 pc0;
  asm volatile("s_getpc_b64 %0" : "=s"(pc0));
  const int b = blockIdx.x, xcd = b & 7, r = b >> 3;
  const int wid = __builtin_amdgcn_readfirstlane(threadIdx.x >> 6);
  const int panel = (xcd >> 1) * 4 + r / 7, ft = (xcd & 1) * 7 + r % 7;
  const int tau = xcd * 4 + r / 7, dl = r % 7;
  const int d0 = 2 * b;
  const int rot = ROT1 ? (r % 7 + 2 * (r / 7)) % 7 : 0, rot2 = ROT2 ? (r % 7 + 2 * (r / 7)) % 7 : 0;

  if (b < 224) {
    if (SHARES && wid >= 4 && wid < 7) share(b, wid == 4 ? 64 : 56, wid == 4 ? x : (wid == 5 ? w1 : w3));
    if (wid == 7) { *(LDSP(u32))(MISC + (threadIdx.x & 63) * 4) = 0; WAIT_LGKM0(); }
    BAR();
    if (wid == 7 && (threadIdx.x & 63) < 5) touch((threadIdx.x & 63) * 128, seq_tab, L_DUMMY);
    if (CODEPF && wid == 11 && (threadIdx.x & 63) < CODE_LINES / 28) touch((u32)(r * (CODE_LINES / 28) + (threadIdx.x & 63)) * 128, (const char*)pc0, L_DUMMY);
  }

  if (__builtin_expect(wid < 4 && b < 224, 1)) {
    const int lane = (int)opq(threadIdx.x) & 63;
    u64 tag = 0;
    if (__builtin_expect(wid < 2, 1)) {
      const int g = lane >> 4, l15 = lane & 15;
      u32 vx[8], vw[8];
      const u32 ox = (u32)((lane >> 3) * (D * 2) + (((lane & 7) ^ g) << 4));
      const u32 ow = (u32)(g * (F * 2) + (((l15 >> 2) ^ g) << 6) + ((lane & 3) << 4));
#pragma unroll
      for (int i = 0; i < 8; ++i) {
        vx[i] = (ox ^ (u32)((i & 1) * 64)) + i * 8 * (D * 2) - (i & 3) * 1024;
        vw[i] = ow + i * 4 * (F * 2) - (i & 3) * 1024;
      }
#pragma unroll 1
      for (int c = 0; c < 7; ++c) {
        if (c >= 3) BAR();
        const u32 slot = (u32)(c % 3) * SLOT + wid * 8192;
        const int kc = rot7(c, rot);
        const char* xb = x + (size_t)(panel * 128 + wid * 64) * (D * 2) + kc * 128;
        const char* w1b = w1 + (size_t)(kc * 64 + wid * 32) * (F * 2) + ft * 256;
        const char* w3b = w3 + (size_t)(kc * 64 + wid * 32) * (F * 2) + ft * 256;
#pragma unroll
        for (int i = 0; i < 8; i += 4) {
          WAIT_VM(PACE_L);
          glds16x4(vx[i], vx[i + 1], vx[i + 2], vx[i + 3], xb, slot + i * 1024);
          WAIT_VM(PACE_L);
          glds16x4(vw[i], vw[i + 1], vw[i + 2], vw[i + 3], w1b, slot + 16384 + i * 1024);
          WAIT_VM(PACE_L);
          glds16x4(vw[i], vw[i + 1], vw[i + 2], vw[i + 3], w3b, slot + 32768 + i * 1024);

        }
        dflag_set(L_FULL + wid * 4, c + 1);
      }
      if (wid == 0) {
        tag = make_tag(ws);
        const int k = lane & 7;
        gflag_wait<8>(ws + WS_FST + (k < 2 ? 2 * panel + k : (k == 2 ? tau : (k & 1) * 14 + ft)) * 128, tag);
        glds16_dev(lane * 16, ws + WS_STAT + panel * 1024, L_STAT);
        glds16_dev(lane * 16, ws + WS_CSUM + ft * 1024, L_CSUM);
        if (lane < 2) glds4_dev(lane * 4, ws + WS_E, L_TAG);
        if (lane < 1) glds4_dev(0, ssmD + d0 * 2, L_DD);
        dflag_set(L_EPI, 1);
      }
    } else {
      __builtin_amdgcn_s_setprio(3);
      const rsrc_t rs = ws_rsrc(ws);
      const int u = wid - 2;
      const int ti = b / 7, cj = b % 7;
      const int tg = lane >> 3, cg = lane & 7;
      const int t0 = 64 * ti + 32 * u + 4 * tg, ch0 = 64 * cj + 8 * cg;
      u32x4 kr[7], vr[4], sw[4]; u32x2 o[8];
      u64 seed;
      auto ldkv = [&]() {
        const u32 ko = (u32)(t0 * (D * 2) + ch0 * 2);
#pragma unroll
        for (int i = 0; i < 7; ++i) kr[i] = *(const u32x4*)(kp + (i < 3 && t0 == 0 ? ko : ko + (u32)((i - 3) * (D * 2))));
#pragma unroll
        for (int i = 0; i < 4; ++i) vr[i] = *(const u32x4*)(vp + (ko + (u32)(i * (D * 2))));
        SCHED0();
      };
      auto piece = [&](int p) {
#pragma unroll
        for (int c = 2 * p; c < 2 * p + 2; ++c) {
          const u32 wa = sw[p][(c & 1) * 2], wb = sw[p][(c & 1) * 2 + 1];
          const float w0 = bflo(wa), w1f = bfhi(wa), w2f = bflo(wb), w3f = bfhi(wb);
          float kf[7], ks[4];
#pragma unroll
          for (int i = 0; i < 7; ++i) kf[i] = (c & 1) ? bfhi(kr[i][p]) : bflo(kr[i][p]);
#pragma unroll
          for (int i = 0; i < 4; ++i)
            ks[i] = __builtin_fmaf(kf[i + 3], w0, __builtin_fmaf(kf[i + 2], w1f, __builtin_fmaf(kf[i + 1], w2f, kf[i] * w3f)));
#pragma unroll
          for (int i = 0; i < 2; ++i) {
            const int ta = 3 - 2 * i, tb = 2 - 2 * i;
            const u32 kk = pk2(ks[ta], ks[tb]);
            o[c][i] = pk2(bflo(kk) * ((c & 1) ? bfhi(vr[ta][p]) : bflo(vr[ta][p])), bfhi(kk) * ((c & 1) ? bfhi(vr[tb][p]) : bflo(vr[tb][p])));
          }
          asm volatile("" : "+v"(o[c]));
        }
        SCHED0();
      };
      BAR();
      ldkv();
#pragma unroll
      for (int i = 0; i < 4; ++i) sw[i] = *(const u32x4*)(skp + (u32)(ch0 * 8 + i * 16));
      seed = gload64(ws + WS_E + opq(0));
      if (FLAGPF && lane < 14) {
        const u32 fo = lane < 8 ? WS_FH + (u32)((2 * panel + (lane & 1)) * 56 + 4 * ft + (lane >> 1)) * 128
                                : (lane < 12 ? WS_FY + (u32)((lane & 1) * 448 + d0 + ((lane >> 1) & 1)) * 128 : WS_FUT + (u32)(cj * 64 + 2 * ti + (lane & 1)) * 128);
        if (u) touch(fo, ws, L_DUMMY);
      }
      SCHED0();
      BAR();
      BAR();
      SCHED0();
#pragma unroll
      for (int i = 0; i < 3; ++i) if (t0 == 0) kr[i] = u32x4{0, 0, 0, 0};
      piece(0);
      BAR();
      SCHED0();
#pragma unroll
      for (int c = 1; c < 4; ++c) piece(c);
      tag = tag_of(seed);
      const u32 scr = L_USCR + (u32)u * 4096;
#pragma unroll
      for (int c = 0; c < 8; ++c) *(LDSP(u32x2))(scr + (u32)((cg * 8 + c) * 64 + (((7 - tg) ^ (cg & 6)) << 3))) = o[c];
      WAIT_LGKM0();
#pragma unroll
      for (int i = 0; i < 4; ++i) {
        const int dd = 16 * i + (lane >> 2), c4 = lane & 3;
        u32x4 v = *(LDSP(u32x4))(scr + (u32)(dd * 64 + ((c4 ^ (dd >> 4)) << 4)));
        st128_dev(rs, WS_UT + (u32)((64 * cj + dd) * (S * 2) + (2016 - 64 * ti - 32 * u) * 2 + c4 * 16), v);
      }
      gflag_publish(ws + WS_FUT + (cj * 64 + 2 * ti + u) * 128, tag, lane);
    }
    const int j = wid;
    const u32 o2 = w2_voff(lane);
    const char* wbase = w2 + dl * 128;
    u32x4 pk[8][4];
    auto park = [&](int i0) {
#pragma unroll
    for (int i = i0; i < i0 + 4; ++i) {
      const char* src = wbase + (size_t)g2_ks(j, i + 6, rot2) * 32 * (D * 2);
      WAIT_VM(PACE_S);
#pragma unroll
      for (int pc = 0; pc < 4; ++pc) pk[i][pc] = *(const u32x4*)(src + (o2 + (u32)(pc * 8 * (D * 2))));
    }
    SCHED0();
    };
    park(0);
    park(4);
    lflag_wait(L_FREE, 7, 7);
    if (wid == 2) {
        if (lane < 32) {
          glds16(lane * 16, seq_tab + 32, CV_U + 4096);
          glds16(lane * 16, seq_tab + 32, CV_U + CV_USTR + 4096);
        }
        if (KV_LATE) gflag_wait<8>(ws + WS_FUT + ((d0 >> 6) * 64 + lane) * 128, tag);
        glds16x4_dev(lane * 16, ws + WS_UT + d0 * (S * 2), CV_U);
        glds16x4_dev(lane * 16, ws + WS_UT + (d0 + 1) * (S * 2), CV_U + CV_USTR);
        dflag_set(L_CONV + 8, 1);
        dflag_set(L_CONV + 12, 1);
      }
    if (wid < 2) {
      const u32 vl = lane * 16;
#pragma unroll 1
      for (int rr = 0; rr < 4; ++rr) {
        const char* src = ssmk + (size_t)(d0 + wid) * (S * 2) + (rr ? (rr - 4) * 2 : 0);
        const u32 dst = CV + (u32)(wid * 4 + rr) * CV_HSTR + (rr ? 24 : 32);
        glds16x4(rr && !lane ? 16u : vl, vl, vl, vl, src, dst);
      }
      dflag_set(L_CONV + wid * 4, 1);
    }
#pragma unroll 1
    for (int i = 0; i < 6; ++i) {
      if (i == W2_HOLD) lflag_wait(L_PUB, 3, 1);
      if (i == 3) dflag_set(L_FED + j * 4, 3);
      const char* src = wbase + (size_t)g2_ks(j, i, rot2) * 32 * (D * 2);
      WAIT_VM(PACE_S);
      glds16x4(o2, o2 + 8 * (D * 2) - 1024, o2 + 16 * (D * 2) - 2048, o2 + 24 * (D * 2) - 3072, src, g2_place(j, i));
    }
    dflag_set(L_FED + j * 4, 6);
    if (BPOLL ? wid == 2 : wid >= 2) {
      lflag_wait(L_PUB, 3, 1);
      const int e = lane < 28 ? lane : 27;
      gflag_wait<4>(ws + WS_FH + (tau * 56 + 4 * (e >> 1) + (e & 1) + 2 * (3 - wid)) * 128, tag);
      dflag_set(L_H + (3 - wid) * 4, 1);
    }
    WAIT_VM(0);
    const u32 wl = opq(lane * 16 + j * 12288);
#pragma unroll
    for (int i = 0; i < 8; i += 2) {
      lflag_wait(L_PROG + j * 4, 0, i + 2);
#pragma unroll
      for (int n = 0; n < 8; ++n) lds_st128(wl + g2_place(0, (i + (n >> 2)) % 6) + (n & 3) * 1024, pk[i + (n >> 2)][n & 3]);
      lflag_set(L_FED + j * 4, i + 8);
    }
    return;
  }
  const rsrc_t rs = ws_rsrc(ws);

  if (b >= 224) {
    if (wid >= 8 && b != 255) return;
    const int tid = (int)opq(threadIdx.x), lane = tid & 63;
    const int sp = b - 224;
    const u64 seed = gload64(ws + WS_E + opq(0));
    if (wid < 8) {
      const int row = 64 * sp + (tid >> 3), c8 = tid & 7;
      const int cu = sp < 28 ? sp : 27, mat = cu / 14, ct = cu % 14, kg = tid >> 4, c16 = tid & 15;
      const char* wp = (mat ? w3 : w1) + ct * 256;
      const u32 wo = (u32)(kg * (F * 2) + c16 * 16), xo = (u32)(row * (D * 2) + c8 * 16);
      u32x4 xv[7], wv[14];
#pragma unroll
      for (int i = 0; i < 7; ++i) xv[i] = *(const u32x4*)(x + (xo + i * 128));
#pragma unroll
      for (int i = 0; i < 14; ++i) wv[i] = *(const u32x4*)(wp + (wo + i * 32 * (F * 2)));
      SCHED0();
      float s = 0.f;
#pragma unroll
      for (int i = 0; i < 7; ++i)
#pragma unroll
        for (int j = 0; j < 4; ++j) s += bflo(xv[i][j]) + bfhi(xv[i][j]);
      const float m = sum8(s) * (1.0f / D);
      float q = 0.f;
#pragma unroll
      for (int i = 0; i < 7; ++i)
#pragma unroll
        for (int j = 0; j < 4; ++j) {
          const float aa = bflo(xv[i][j]) - m, bb = bfhi(xv[i][j]) - m;
          q = __builtin_fmaf(aa, aa, __builtin_fmaf(bb, bb, q));
        }
      const float sd = rbf(__builtin_amdgcn_sqrtf(rbf(rbf(sum8(q) * (1.0f / D)) + 1e-5f)));
      if (c8 == 0) *(LDSP(f32x2))(u32)(16384 + (tid >> 3) * 8) = f32x2{rbf(m), __builtin_amdgcn_rcpf(sd)};
      SCHED0();
      float a[8] = {};
#pragma unroll
      for (int i = 0; i < 14; ++i)
#pragma unroll
        for (int j = 0; j < 4; ++j) { a[2 * j] += bflo(wv[i][j]); a[2 * j + 1] += bfhi(wv[i][j]); }
      f32x4 t;
#pragma unroll
      for (int j = 0; j < 4; ++j) {
        auto p = __builtin_amdgcn_permlane16_swap(__builtin_bit_cast(u32, a[j]), __builtin_bit_cast(u32, a[j + 4]), false, false);
        t[j] = asf(p[0]) + asf(p[1]);
      }
      *(LDSP(f32x4))(u32)((kg >> 1) * 512 + c16 * 32 + (kg & 1) * 16) = t;
    }
    WAIT_LGKM0(); BAR();
    const u64 tag = tag_of(seed);
    if (wid == 0) {
      const int cu = sp < 28 ? sp : 27;
      f32x2 cs = {};
#pragma unroll
      for (int i = 0; i < 16; ++i) cs += *(LDSP(f32x2))(u32)(i * 512 + lane * 8);
      st64_dev(rs, WS_STAT + (u32)(64 * sp + lane) * 8, *(LDSP(u32x2))(u32)(16384 + lane * 8));
      if (sp < 28) st64_dev(rs, WS_CSUM + (u32)((cu % 14) * 1024 + (cu / 14) * 512 + lane * 8), __builtin_bit_cast(u32x2, cs));
      gflag_publish(ws + WS_FST + sp * 128, tag, lane);
    }
    WAIT_VM(0); BAR();
    if (SPPF) {
      if (tid < 392) touch((u32)(sp * 392 + tid) * 128, w2, L_DUMMY);
      if (tid < 448) touch((u32)(sp * 448 + tid) * 128, ssmk, L_DUMMY);
      BAR();
    }
    if (W2PF && wid > 0 && wid < 8) {
#pragma unroll 1
      for (int i = 0; i < 7; ++i) touch((u32)((((sp >> 3) * 7 + i) * 7 + wid - 1) * 64 + lane) * 128, w2, L_DUMMY);
      WAIT_VM(0);
    }
    if (b == 255) {
      gflag_wait<64>(ws + (tid < 480 ? WS_FUT + tid * 128 : WS_FH + ((((tid < 704 ? tid - 480 : 223) / 14) * 2) * 56 + ((tid < 704 ? tid - 480 : 223) % 14) * 4) * 128), tag);
      BAR();
      if (tid == 0) gstore64(ws + WS_E, tag);
    }
    return;
  }

  const int cw = wid - 4, th = cw & 1, fq = cw >> 1;
  {
    const int lane = (int)opq(threadIdx.x) & 63, g = lane >> 4, hh = lane >> 5, l31 = lane & 31;
    const int q4 = (lane >> 2) & 3, p4 = lane & 3;
    const u32 aA = 16384 + (u32)((8 * hh + q4) * 256 + ((fq ^ q4) << 6) + (g & 1) * 32 + p4 * 8);
    const u32 aB = (u32)((64 * th + l31) * 128 + ((hh ^ ((l31 >> 1) & 7)) << 4));
    f32x16 acc[2][2] = {};
    s16x8 fa[2][2][2], fb[2][2][2];
    u32 pa, pb[4];
    auto setslot = [&](u32 slot) {
      pa = opq(aA + slot);
#pragma unroll
      for (int k = 0; k < 4; ++k) pb[k] = opq((aB ^ (u32)(k << 5)) + slot);
    };
    auto rd = [&](int set) {
#pragma unroll
      for (int k = 0; k < 2; ++k) {
        const int ks = 2 * set + k;
        fa[set][k][0] = lds_tr2(pa, ks * 4096, ks * 4096 + 1024);
        fb[set][k][0] = lds_b128(pb[ks], 0);
        fb[set][k][1] = lds_b128(pb[ks], 4096);
        fa[set][k][1] = lds_tr2(pa, 16384 + ks * 4096, 16384 + ks * 4096 + 1024);
      }
    };
    auto mm = [&](int set, int n0, int n1) {
#pragma unroll
      for (int n = n0; n < n1; ++n) acc[(n >> 1) & 1][n & 1] = mfma32(fa[set][n >> 2][(n >> 1) & 1], fb[set][n >> 2][n & 1], acc[(n >> 1) & 1][n & 1]);
    };
    setslot(0);
    const u32 fad = opq(L_FULL + (lane & 1) * 4);
#pragma unroll
    for (int i = 0; i < 4; ++i) asm volatile("" : "+v"(acc[i >> 1][i & 1]));
    __builtin_amdgcn_s_setprio(1);
    lflag_wait(L_FULL, 1, 1);
    rd(0);
    SCHED0();
    u32 slot = 0;
#pragma unroll 1
    for (int c = 0; c < 7; ++c) {
      const u32 fl = *(volatile LDSP(u32))fad;
      rd(1); mm(0, 0, 5);
      SGB(0x008, 1); SGB(0x100, 4); SGB(0x008, 1); SGB(0x100, 3); SGB(0x008, 1); SGB(0x100, 3); SGB(0x008, 1); SGB(0x100, 3); SGB(0x008, 1);
      SCHED0();
      if (c < 6 && __builtin_amdgcn_ballot_w64(fl < (u32)(c + 2))) lflag_wait(L_FULL, 1, c + 2);
      SCHED0();
      slot = slot == 2 * SLOT ? 0 : slot + SLOT;
      mm(0, 5, 6);
      SCHED0();
      setslot(slot);
      SCHED0();
      mm(0, 6, 8);
      SCHED0();
      if (c < 4) { WAIT_LGKM0(); BAR(); } else if (c == 6) lflag_set(L_FREE + cw * 4, 7);
      SCHED0();
      rd(0); mm(1, 0, 8);
#pragma unroll
      for (int i = 0; i < 4; ++i) { SGB(0x008, 1); SGB(0x100, 3); }
      SGB(0x008, 4);
      SCHED0();
    }
    lflag_wait(L_EPI, 0, 1);
    const u32 hdst = WS_H + (u32)((2 * panel + th) * 56 + 4 * ft + fq) * 4096 + lane * 16;
    float nm[2], c1[2], irs2[2];
#pragma unroll
    for (int j = 0; j < 2; ++j) {
      const f32x2 st = *(LDSP(f32x2))(L_STAT + (u32)(64 * th + 32 * j + l31) * 8);
      nm[j] = -st[0]; c1[j] = -1.44269504f * st[1]; irs2[j] = __builtin_amdgcn_rcpf(st[1] * st[1]);
    }
#pragma unroll
    for (int s = 0; s < 2; ++s) {
      f32x4 cs[2][2];
#pragma unroll
      for (int m = 0; m < 2; ++m)
#pragma unroll
        for (int G = 0; G < 2; ++G) cs[m][G] = *(LDSP(f32x4))(L_CSUM + (u32)(m * 128 + 32 * fq + 16 * s + 8 * G + 4 * hh) * 4);
#pragma unroll
      for (int j = 0; j < 2; ++j) {
        float a[8], ex[8], hv[8];
#pragma unroll
        for (int e = 0; e < 8; ++e) a[e] = __builtin_fmaf(nm[j], cs[0][e >> 2][e & 3], acc[0][j][8 * s + e]);
#pragma unroll
        for (int e = 0; e < 8; ++e) ex[e] = a[e] * c1[j];
        SCHED0();
#pragma unroll
        for (int e = 0; e < 8; ++e) { ex[e] = __builtin_amdgcn_exp2f(ex[e]); hv[e] = __builtin_fmaf(nm[j], cs[1][e >> 2][e & 3], acc[1][j][8 * s + e]); }
        SCHED0();
#pragma unroll
        for (int e = 0; e < 8; ++e) ex[e] = __builtin_fmaf(ex[e], irs2[j], irs2[j]);
        SCHED0();
#pragma unroll
        for (int e = 0; e < 8; ++e) { ex[e] = __builtin_amdgcn_rcpf(ex[e]); hv[e] *= a[e]; }
        SCHED0();
#pragma unroll
        for (int e = 0; e < 8; ++e) hv[e] *= ex[e];
        st128_dev(rs, hdst + (s * 2 + j) * 1024, u32x4{pk2(hv[0], hv[1]), pk2(hv[2], hv[3]), pk2(hv[4], hv[5]), pk2(hv[6], hv[7])});
      }
    }
  }
  char* hflag = ws + WS_FH + ((2 * panel + th) * 56 + 4 * ft + fq) * 128;
  u32x4 xv, sv;
  if (cw < 4) {
    const int ht = (int)opq(threadIdx.x) & 255, lane = ht & 63, l15 = ht & 15;
    const u64 tag = tag_of(*(volatile LDSP(u64))L_TAG);
    const int ch = cw >> 1, var = cw & 1;
    const bool ready = false;
    gflag_publish(hflag, tag, lane);
    lflag_wait(L_CONV, 3, 1);
    asm volatile("" ::: "memory");
    conv_compute(lane, cw, d0 + ch, rs, hflag, tag, ready);
    gflag_publish(ws + WS_FY + (var * 448 + d0 + ch) * 128, tag, lane);
    u32x4 xr[2]; u32x2 st[2];
#pragma unroll
    for (int i = 0; i < 2; ++i) {
      const int n = ht + 256 * i, row = n >> 3, c = n & 7;
      xr[i] = *(const u32x4*)(x + (size_t)(64 * tau + row) * (D * 2) + (64 * dl + 8 * c) * 2);
      st[i] = ld64_dev(rs, WS_STAT + (u32)(64 * tau + row) * 8);
    }
    const int yvar = tau >> 4;
    gflag_wait<4>(ws + WS_FY + (yvar * 448 + 64 * dl + 16 * cw + l15) * 128, tag);
#pragma unroll
    for (int i = 0; i < 2; ++i)
      glds16_dev((u32)((lane >> 3) * (S * 2) + (((lane & 7) ^ (2 * cw + i)) << 4)),
                 ws + WS_YT + (64 * dl + 16 * cw + 8 * i) * (S * 2) + 128 * tau, xbuf(cw ^ 1) + (u32)i * 1024);
    dflag_set(L_ADONE + cw * 4, 1);
    lflag_wait(L_ADONE, 3, 1);
#pragma unroll
    for (int i = 0; i < 2; ++i) {
      const int n = ht + 256 * i, tok = n >> 3, dg = n & 7;
      const float mb = asf(st[i][0]), rsd = asf(st[i][1]);
      u32x4 so;
#pragma unroll
      for (int e = 0; e < 4; ++e) {
        const u32 ya = xbuf((dg >> 1) ^ 1) + (u32)((8 * (dg & 1) + 2 * e) * 128 + (((tok >> 3) ^ dg) << 4) + (tok & 7) * 2);
        const float y0 = bflo(*(LDSP(u16))ya), y1 = bflo(*(LDSP(u16))(ya + 128));
        const u32 dd = pk2(bflo(xr[i][e]) - mb, bfhi(xr[i][e]) - mb);
        const u32 xn = pk2(bflo(dd) * rsd, bfhi(dd) * rsd);
        so[e] = pk2(bflo(xn) * y0, bfhi(xn) * y1);
      }
      if (i == 0) { xv = xr[0]; sv = so; }
      else { lds_st128(E_S + (u32)ht * 16, so); lds_st128(E_X + (u32)ht * 16, xr[1]); }
    }
  } else {
    const int j = cw - 4;
    const int lane = (int)opq(threadIdx.x) & 63;
    lflag_set(L_PUB + j * 4, 1);
    const u64 btag = tag_of(*(volatile LDSP(u64))L_TAG);
    gflag_publish(hflag, btag, lane);
    const u32 hl = lane * 16;
    const char* hbase = ws + WS_H + (size_t)tau * 56 * 4096;
    const int q4 = (lane >> 2) & 3;
    const u32 ta = (u32)((4 * (lane >> 5) + q4) * 128 + ((q4 >> 1) << 6) + ((lane >> 4) & 1) * 32 + (lane & 3) * 8);
    s16x8 fa[2][4], fb[HDEPTH][4];
    f32x16 acc[2][2] = {};
    auto ldh = [&](int i) {
      const char* p = hbase + (size_t)g2_ks(j, i, rot2) * 4096;
      asm volatile("global_load_dwordx4 %0, %4, %5 sc1\n\tglobal_load_dwordx4 %1, %4, %5 offset:1024 sc1\n\t"
                   "global_load_dwordx4 %2, %4, %5 offset:2048 sc1\n\tglobal_load_dwordx4 %3, %4, %5 offset:3072 sc1"
                   : "=&v"(fb[i % HDEPTH][0]), "=&v"(fb[i % HDEPTH][1]), "=&v"(fb[i % HDEPTH][2]), "=&v"(fb[i % HDEPTH][3])
                   : "v"(hl), "s"(p) : "memory");
    };
    u32 tb[2];
#pragma unroll
    for (int a = 0; a < 2; ++a) tb[a] = opq((ta ^ (u32)(a << 6)) + g2_place(j, 0));
    auto rda = [&](int i) {
#pragma unroll
      for (int a = 0; a < 2; ++a)
        if (i > 0 && i % 3 == 0) tb[a] = opq(i % 6 ? tb[a] - 2 * SLOT : tb[a] + 2 * SLOT);
#pragma unroll
      for (int n = 0; n < 4; ++n) {
        const int o = (i % 3) * 4096 + (n >> 1) * 2048;
        fa[i & 1][n] = lds_tr2(tb[n & 1], o, o + 1024);
      }
    };
    const u32 fed = opq(L_FED + j * 4), prog = opq(L_PROG + j * 4);
    if (BPOLL) { const int e = 7 * j + (lane < 7 ? lane : 6); gflag_wait<2>(ws + WS_FH + (tau * 56 + 4 * (e >> 1) + (e & 1)) * 128, btag); }
    else lflag_wait(L_H, 0, 1);
#pragma unroll
    for (int i = 0; i < HDEPTH; ++i) ldh(i);
    lflag_wait(L_FED + j * 4, 0, 3);
    rda(0);
    SCHED0();
    u32 fl = 0;
#pragma unroll
    for (int i = 0; i < 14; ++i) {
      if (i == 2 || (i >= 5 && i < 13 && (i & 1))) {
        if (__builtin_amdgcn_readfirstlane(fl) < i + 2) lflag_wait(L_FED + j * 4, 0, i + 2);
      }
      asm volatile("s_waitcnt vmcnt(%4)" : "+v"(fb[i % HDEPTH][0]), "+v"(fb[i % HDEPTH][1]), "+v"(fb[i % HDEPTH][2]), "+v"(fb[i % HDEPTH][3])
                   : "i"(i + HDEPTH <= 14 ? (HDEPTH - 1) * 4 : (13 - i) * 4) : "memory");
      SCHED0();
      if (i < 13) rda(i + 1);
      if (i == 1 || (i >= 4 && i < 12 && !(i & 1))) fl = *(volatile LDSP(u32))fed;
#pragma unroll
      for (int s = 0; s < 2; ++s)
#pragma unroll
        for (int a = 0; a < 2; ++a)
#pragma unroll
          for (int t = 0; t < 2; ++t) acc[a][t] = mfma32(fa[i & 1][2 * s + a], fb[i % HDEPTH][2 * s + t], acc[a][t]);
      if (i < 13) {
#pragma unroll
        for (int n = 0; n < 5; ++n) { SGB(0x008, 1); SGB(0x100, 2); }
        SGB(0x008, 3);
      }
      SCHED0();
      if (i < 8 && (i & 1)) *(volatile LDSP(u32))prog = i + 1;
      if (i == 7 - HDEPTH) { lflag_wait(L_H + 4, 0, 1); }
      if (i + HDEPTH < 14) ldh(i + HDEPTH);
      SCHED0();

    }
    const int ln = (int)opq(threadIdx.x), lt = ln & 31, lh = (ln >> 5) & 1;
#pragma unroll
    for (int t = 0; t < 2; ++t)
#pragma unroll
      for (int a = 0; a < 2; ++a)
#pragma unroll
        for (int G = 0; G < 4; ++G)
          *(LDSP(f32x4))(g2_place(j, t ? 3 : 0) + (u32)(lt * 256 + (((8 * a + 2 * G + lh) ^ (lt & 7)) << 4))) =
              f32x4{acc[a][t][4 * G], acc[a][t][4 * G + 1], acc[a][t][4 * G + 2], acc[a][t][4 * G + 3]};
  }
  WAIT_LGKM0();
  BAR();
  {
    const int ht = (int)opq(threadIdx.x) & 255;
    const int half = cw >> 2, tk = ht >> 3, dg = ht & 7;
    if (half) { sv = *(LDSP(u32x4))(E_S + (u32)ht * 16); xv = *(LDSP(u32x4))(E_X + (u32)ht * 16); }
    f32x4 s0 = {}, s1 = {};
#pragma unroll
    for (int w = 0; w < 4; ++w) {
      const u32 pb = (half ? 0u : 2 * SLOT) + (u32)w * 12288 + (u32)tk * 256;
      s0 += *(LDSP(f32x4))(pb + (u32)(((2 * dg) ^ (tk & 7)) << 4));
      s1 += *(LDSP(f32x4))(pb + (u32)(((2 * dg + 1) ^ (tk & 7)) << 4));
    }
    float o[8];
#pragma unroll
    for (int e = 0; e < 8; ++e) {
      const float f = rbf(e < 4 ? s0[e & 3] : s1[e & 3]);
      const float gt = rbf(silu_f(f));
      const float xe = (e & 1) ? bfhi(xv[e >> 1]) : bflo(xv[e >> 1]);
      const float se = (e & 1) ? bfhi(sv[e >> 1]) : bflo(sv[e >> 1]);
      o[e] = xe + rbf(se * gt);
    }
    *(u32x4*)(out + (size_t)(64 * tau + 32 * half + tk) * (D * 2) + (64 * dl + 8 * dg) * 2) =
        u32x4{pk2(o[0], o[1]), pk2(o[2], o[3]), pk2(o[4], o[5]), pk2(o[6], o[7])};
  }
}
}
#if defined(__HIP_DEVICE_COMPILE__) && __HIP_DEVICE_COMPILE__
#pragma clang attribute pop
#endif

extern "C" void kernel_launch(void* const* d_in, const int* in_sizes, int n_in, void* d_out, int out_size, void* d_ws,
                              size_t ws_size, hipStream_t stream) {
  (void)hipFuncSetAttribute((const void*)fused_kernel, hipFuncAttributeMaxDynamicSharedMemorySize, 163840);
  fused_kernel<<<256, 768, 163840, stream>>>((const char*)d_in[0], (const char*)d_in[1], (const char*)d_in[2], (const char*)d_in[3],
                                             (const char*)d_in[4], (const char*)d_in[5], (const char*)d_in[7], (const char*)d_in[8],
                                             (const char*)d_in[9], (char*)d_ws, (char*)d_out);
}
```

```cpp
#define KV_P2 1
#include <hip/hip_runtime.h>
#include <hip/hip_bf16.h>
#include <stdint.h>
#pragma clang diagnostic ignored "-Wint-to-pointer-cast"

#if defined(__HIP_DEVICE_COMPILE__) && __HIP_DEVICE_COMPILE__
#pragma clang attribute push(__attribute__((target("no-packed-fp32-ops"))), apply_to = function)
#endif
namespace {
typedef unsigned short u16;
typedef unsigned int u32;
typedef unsigned long long u64;
typedef __attribute__((ext_vector_type(4))) short s16x4;
typedef __attribute__((ext_vector_type(8))) short s16x8;
typedef __attribute__((ext_vector_type(8))) __bf16 bf16x8;
typedef __attribute__((ext_vector_type(2))) __bf16 bf16x2;
typedef __attribute__((ext_vector_type(16))) float f32x16;
typedef __attribute__((ext_vector_type(4))) float f32x4;
typedef __attribute__((ext_vector_type(2))) float f32x2;
typedef __attribute__((ext_vector_type(4))) u32 u32x4;
typedef __attribute__((ext_vector_type(2))) u32 u32x2;
#define LDSP(T) __attribute__((address_space(3))) T*
#define GLBP(T) __attribute__((address_space(1))) T*
#define DEV __device__ __forceinline__

constexpr int S = 2048, D = 448, F = 1792;
constexpr u32 WS_H = 0;
constexpr u32 WS_UT = WS_H + 7340032;
constexpr u32 WS_YT = WS_UT + 1835008;
constexpr u32 WS_STAT = WS_YT + 1835008;
constexpr u32 WS_CSUM = WS_STAT + 16384;
constexpr u32 WS_E = WS_CSUM + 16384;
constexpr u32 WS_FUT = WS_E + 128;
constexpr u32 WS_FST = WS_FUT + 448 * 128;
constexpr u32 WS_FY = WS_FST + 32 * 128;
constexpr u32 WS_FH = WS_FY + 896 * 128;
constexpr u32 WS_END = WS_FH + 1792 * 128;
constexpr int CODE_LINES = 196;
constexpr u32 SLOT = 49152;
constexpr u32 MISC = 3 * SLOT;
constexpr u32 L_FULL = MISC, L_EPI = MISC + 8, L_H = MISC + 16, L_FREE = MISC + 32, L_CONV = MISC + 64, L_FED = MISC + 80,
              L_TAG = MISC + 96, L_DD = MISC + 104, L_ADONE = MISC + 112, L_PROG = MISC + 128, L_XCH = MISC + 144, L_PUB = MISC + 160, L_STAT = MISC + 256,
              L_CSUM = MISC + 1280, L_USCR = MISC + 4096, L_DUMMY = MISC + 12288;
constexpr u32 CV = SLOT, CV_HSTR = 4160, CV_U = CV + 8 * CV_HSTR, CV_USTR = 4608;
constexpr u32 E_S = SLOT, E_X = SLOT + 4096;
constexpr int SPIN_CAP = 1 << 18;
#define ROT1 0
#define ROT2 0
#define TOUCH2_AT 0
#define PACE_L 63
#define PACE_S 63
#define FREE_SET dflag_set
#define W2PF 1
#define KVPF 0
#define SHARES 1
#define CODEPF 0
#define W2_HOLD 0
#define PUB2 0
#define KV_LATE 1
#define SPPF 1
#define FLAGPF 1
#define MIDPF 1
#define BPOLL 1
#define HDEPTH 3
#define LSLEEP 1
#define KV_AT 1

__device__ const u32 seq_tab[160] __attribute__((aligned(128))) = {0, 1, 2, 3, 4, 5, 6, 7};
DEV float asf(u32 v) { return __builtin_bit_cast(float, v); }
DEV float bflo(u32 v) { return asf(v << 16); }
DEV float bfhi(u32 v) { return asf(v & 0xffff0000u); }
template <int CTRL> DEV float dpp(float v) { return asf(__builtin_amdgcn_update_dpp(0, __builtin_bit_cast(u32, v), CTRL, 0xf, 0xf, true)); }
DEV float sum8(float v) { v += dpp<0xB1>(v); v += dpp<0x4E>(v); return v + dpp<0x141>(v); }
DEV u32 pk2(float lo, float hi) {
  asm("" : "+v"(lo)); asm("" : "+v"(hi));
  bf16x2 r = {(__bf16)lo, (__bf16)hi};
  return __builtin_bit_cast(u32, r);
}
DEV float rbf(float f) { bf16x2 r = {(__bf16)f, (__bf16)f}; return bflo(__builtin_bit_cast(u32, r)); }
DEV f32x4 mfma16(s16x8 a, s16x8 b, f32x4 c) {
  return __builtin_amdgcn_mfma_f32_16x16x32_bf16(__builtin_bit_cast(bf16x8, a), __builtin_bit_cast(bf16x8, b), c, 0, 0, 0);
}
DEV f32x16 mfma32(s16x8 a, s16x8 b, f32x16 c) {
  return __builtin_amdgcn_mfma_f32_32x32x16_bf16(__builtin_bit_cast(bf16x8, a), __builtin_bit_cast(bf16x8, b), c, 0, 0, 0);
}
DEV u32 opq(u32 v) { asm volatile("" : "+v"(v)); return v; }
DEV s16x8 lds_tr2(u32 base, int o0, int o1) {
  s16x4 lo = __builtin_amdgcn_ds_read_tr16_b64_v4i16((LDSP(s16x4))((LDSP(char))base + o0));
  s16x4 hi = __builtin_amdgcn_ds_read_tr16_b64_v4i16((LDSP(s16x4))((LDSP(char))base + o1));
  return __builtin_shufflevector(lo, hi, 0, 1, 2, 3, 4, 5, 6, 7);
}
DEV s16x8 lds_b128(u32 base, int o) { return *(LDSP(s16x8))((LDSP(char))base + o); }
DEV void lds_st128(u32 a, u32x4 v) { *(LDSP(u32x4))a = v; }
DEV void glds16x4(u32 v0, u32 v1, u32 v2, u32 v3, const void* sbase, u32 ldst) {
  asm volatile("s_mov_b32 m0, %5\n\ts_nop 0\n\tglobal_load_lds_dwordx4 %0, %4\n\tglobal_load_lds_dwordx4 %1, %4 offset:1024\n\t"
               "global_load_lds_dwordx4 %2, %4 offset:2048\n\tglobal_load_lds_dwordx4 %3, %4 offset:3072"
               ::"v"(v0), "v"(v1), "v"(v2), "v"(v3), "s"(sbase), "s"(ldst) : "memory");
}
DEV void glds16(u32 voff, const void* sbase, u32 ldst) {
  asm volatile("s_mov_b32 m0, %2\n\ts_nop 0\n\tglobal_load_lds_dwordx4 %0, %1" ::"v"(voff), "s"(sbase), "s"(ldst) : "memory");
}
DEV void glds16x4_lin(u32 v, const void* sbase, u32 ldst) { glds16x4(v, v, v, v, sbase, ldst); }
DEV void glds16x4_dev(u32 v, const void* sbase, u32 ldst) {
  asm volatile("s_mov_b32 m0, %2\n\ts_nop 0\n\tglobal_load_lds_dwordx4 %0, %1 sc1\n\tglobal_load_lds_dwordx4 %0, %1 offset:1024 sc1\n\t"
               "global_load_lds_dwordx4 %0, %1 offset:2048 sc1\n\tglobal_load_lds_dwordx4 %0, %1 offset:3072 sc1"
               ::"v"(v), "s"(sbase), "s"(ldst) : "memory");
}
DEV void glds16_dev(u32 voff, const void* sbase, u32 ldst) {
  asm volatile("s_mov_b32 m0, %2\n\ts_nop 0\n\tglobal_load_lds_dwordx4 %0, %1 sc1" ::"v"(voff), "s"(sbase), "s"(ldst) : "memory");
}
DEV void dflag_set(u32 addr, u32 val) {
  asm volatile("s_waitcnt lgkmcnt(0)\n\ts_mov_b32 m0, %2\n\ts_mov_b64 exec, 1\n\tglobal_load_lds_dword %0, %1\n\ts_mov_b64 exec, -1"
               ::"v"(0), "s"(seq_tab + val), "s"(addr) : "memory");
}
#define WAIT_VM(n) asm volatile("s_waitcnt vmcnt(%0)" ::"i"(n) : "memory")
#define WAIT_LGKM0() asm volatile("s_waitcnt lgkmcnt(0)" ::: "memory")
#define BAR() __builtin_amdgcn_s_barrier()
#define SCHED0() __builtin_amdgcn_sched_barrier(0)
#define SGB(mask, n) __builtin_amdgcn_sched_group_barrier(mask, n, 0)
DEV float silu_f(float x) { return x * __builtin_amdgcn_rcpf(1.0f + __builtin_amdgcn_exp2f(-1.44269504f * x)); }

DEV void lflag_set(u32 addr, u32 val) {
  asm volatile("s_waitcnt lgkmcnt(0)\n\tds_write_b32 %0, %1" ::"v"(addr), "v"(val) : "memory");
}
DEV void lflag_wait(u32 addr, u32 mask, u32 val) {
  const u32 a = addr + 4 * (threadIdx.x & mask);
#pragma unroll 1
  for (int i = 0; i < SPIN_CAP; ++i) {
    u32 v;
    asm volatile("ds_read_b32 %0, %1\n\ts_waitcnt lgkmcnt(0)" : "=v"(v) : "v"(a) : "memory");
    if (__builtin_amdgcn_ballot_w64(v < val) == 0) break;
    __builtin_amdgcn_s_sleep(LSLEEP);
  }
}
DEV u64 gload64(const char* p) { return __hip_atomic_load((GLBP(u64))p, __ATOMIC_RELAXED, __HIP_MEMORY_SCOPE_AGENT); }
DEV void gstore64(char* p, u64 v) { __hip_atomic_store((GLBP(u64))p, v, __ATOMIC_RELAXED, __HIP_MEMORY_SCOPE_AGENT); }
template <int SLEEP>
DEV void gflag_wait(const char* lane_ptr, u64 tag) {
  bool ok = false;
#pragma unroll 1
  for (int i = 0; i < SPIN_CAP; ++i) {
    asm volatile("" ::: "memory");
    if (!ok) ok = gload64(lane_ptr) == tag;
    if (__builtin_amdgcn_ballot_w64(!ok) == 0) break;
    __builtin_amdgcn_s_sleep(SLEEP);
  }
  asm volatile("" ::: "memory");
}
DEV void gflag_publish(char* p, u64 tag, int lane) {
  WAIT_VM(0);
  if (lane == 0) gstore64(p, tag);
}
DEV u64 tag_of(u64 seed) { return seed * 0x9E3779B97F4A7C15ull + 0xD1B54A32D192ED03ull; }
DEV u64 make_tag(const char* ws) { return tag_of(gload64(ws + WS_E)); }
typedef __amdgpu_buffer_rsrc_t rsrc_t;
DEV rsrc_t ws_rsrc(char* ws) { return __builtin_amdgcn_make_buffer_rsrc(ws, 0, WS_END, 0x00020000); }
DEV void st128_dev(rsrc_t r, u32 off, u32x4 v) { __builtin_amdgcn_raw_buffer_store_b128(v, r, off, 0, 16); }
DEV void st64_dev(rsrc_t r, u32 off, u32x2 v) { __builtin_amdgcn_raw_buffer_store_b64(v, r, off, 0, 16); }
DEV void st32_dev(rsrc_t r, u32 off, u32 v) { __builtin_amdgcn_raw_buffer_store_b32(v, r, off, 0, 16); }
DEV u32x4 ld128_dev(rsrc_t r, u32 off) { return __builtin_amdgcn_raw_buffer_load_b128(r, off, 0, 16); }
DEV u32x2 ld64_dev(rsrc_t r, u32 off) { return __builtin_amdgcn_raw_buffer_load_b64(r, off, 0, 16); }
DEV void touch(u32 voff, const void* sbase, u32 ldst) {
  asm volatile("s_mov_b32 m0, %2\n\ts_nop 0\n\tglobal_load_lds_dword %0, %1" ::"v"(voff), "s"(sbase), "s"(ldst) : "memory");
}

DEV void glds4_dev(u32 voff, const void* sbase, u32 ldst) {
  asm volatile("s_mov_b32 m0, %2\n\ts_nop 0\n\tglobal_load_lds_dword %0, %1 sc1" ::"v"(voff), "s"(sbase), "s"(ldst) : "memory");
}
DEV u32 shr16(u32 hi, u32 lo) { return __builtin_amdgcn_alignbit(hi, lo, 16); }

DEV u32 xbuf(int w) { return w < 2 ? L_USCR + (u32)w * 4096 : (w == 2 ? L_DUMMY : CV + 45056); }
DEV void conv_compute(int lane, int cw, int d, rsrc_t rs, char* hflag, u64 tag, bool pub) {
  const int ch = cw >> 1, var = cw & 1;
  const int m = lane & 15, g = lane >> 4, q4 = m >> 2, p4 = lane & 3;
  u32 aa = opq(CV + (u32)(ch * 4 + ((q4 + 1) & 3)) * CV_HSTR + (u32)(((q4 + 1) & 4) + 8 * g + 4 * p4) * 2 + var * 256);
  u32 ba = opq(CV_U + (u32)ch * CV_USTR + (u32)(240 - 16 * m + 8 * g) * 2 + var * 256);
  f32x4 acc[8] = {};
  s16x8 A[8], B[8];
#pragma unroll
  for (int p = 0; p < 8; ++p) { A[7 - p] = lds_tr2(aa, (7 - p) * 512, (7 - p) * 512 + 8); B[p] = lds_b128(ba, (7 - p) * 512); }
  if (var == 0) {
    const u32 a0 = CV + (u32)ch * 4 * CV_HSTR + (u32)(m + 1 + 8 * g) * 2;
#pragma unroll
    for (int j = 0; j < 8; ++j) { const short v = *(LDSP(short))(a0 + j * 2); A[0][j] = m + 8 * g + j >= 15 ? v : (short)0; }
  }
  SCHED0();
#pragma unroll 1
  for (int it = 0; it < 4; ++it) {
#pragma unroll
    for (int p = 7; p >= 0; --p) {
#pragma unroll
      for (int dd = 0; dd <= 7 - p; ++dd) {
        acc[p + dd] = mfma16(A[p], B[dd], acc[p + dd]);
        if (p == 0) { SCHED0(); B[dd] = lds_b128(ba, 64 + (7 - dd) * 512); SCHED0(); }
      }
      SCHED0();
      A[p] = lds_tr2(aa, 64 + p * 512, 64 + p * 512 + 8);
      SCHED0();
    }
    aa = opq(aa + 64); ba = opq(ba + 64);
    if (it == 0 && pub) gflag_publish(hflag, tag, lane);
  }
  f32x4 y[4];
#pragma unroll
  for (int i = 0; i < 4; ++i) {
    *(LDSP(f32x4))(xbuf(cw) + (u32)(i * 1024 + lane * 16)) = var ? acc[i] : acc[4 + i];
    y[i] = var ? acc[4 + i] : acc[i];
  }
  lflag_set(L_XCH + cw * 4, 1);
  const u32 dv = *(LDSP(u32))L_DD;
  const float Dd = ch ? bfhi(dv) : bflo(dv);
  u32x2 uu[4];
#pragma unroll
  for (int i = 0; i < 4; ++i) uu[i] = *(LDSP(u32x2))(CV_U + (u32)ch * CV_USTR + (u32)(2044 - (1024 * var + 256 * i + 16 * m + 4 * g)) * 2);
  lflag_wait(L_XCH + (cw ^ 1) * 4, 0, 1);
#pragma unroll
  for (int i = 0; i < 4; ++i) {
    const int t = 1024 * var + 256 * i + 16 * m + 4 * g;
    const f32x4 s = y[i] + *(LDSP(f32x4))(xbuf(cw ^ 1) + (u32)(i * 1024 + lane * 16));
    float y0 = rbf(s[0]) + rbf(bfhi(uu[i][1]) * Dd), y1 = rbf(s[1]) + rbf(bflo(uu[i][1]) * Dd);
    float y2 = rbf(s[2]) + rbf(bfhi(uu[i][0]) * Dd), y3 = rbf(s[3]) + rbf(bflo(uu[i][0]) * Dd);
    st64_dev(rs, WS_YT + (u32)(d * (S * 2) + t * 2), u32x2{pk2(y0, y1), pk2(y2, y3)});
  }
}

DEV int rot7(int i, int rot) { const int v = i + rot; return v >= 7 ? v - 7 : v; }
DEV int g2_ks(int j, int i, int rot) { const int e = 7 * j + rot7(i < 7 ? i : i - 7, rot); return 4 * (e >> 1) + (e & 1) + (i < 7 ? 0 : 2); }
DEV u32 g2_place(int j, int p) { return (p < 3 ? 2 * SLOT + (u32)p * 4096 : (u32)(p - 3) * 4096) + (u32)j * 12288; }
DEV u32 w2_voff(int lane) { return (u32)((lane >> 3) * (D * 2) + (((lane & 7) ^ (((lane >> 4) & 1) << 2)) << 4)); }

DEV void share(int b, int n, const char* base) {
  const int lane = (int)opq(threadIdx.x) & 63;
  if (lane < n) touch((u32)(b * n + lane) * 128, base, L_DUMMY);
}

__global__ __launch_bounds__(768, 3) void fused_kernel(const char* x, const char* kp, const char* vp, const char* skp,
                                                       const char* ssmk, const char* ssmD, const char* w1, const char* w3,
                                                       const char* w2, char* ws, char* out) {
  u64 pc0;
  asm volatile("s_getpc_b64 %0" : "=s"(pc0));
  const int b = blockIdx.x, xcd = b & 7, r = b >> 3;
  const int wid = __builtin_amdgcn_readfirstlane(threadIdx.x >> 6);
  const int panel = (xcd >> 1) * 4 + r / 7, ft = (xcd & 1) * 7 + r % 7;
  const int tau = xcd * 4 + r / 7, dl = r % 7;
  const int d0 = 2 * b;
  const int rot = ROT1 ? (r % 7 + 2 * (r / 7)) % 7 : 0, rot2 = ROT2 ? (r % 7 + 2 * (r / 7)) % 7 : 0;

  if (b < 224) {
    if (SHARES && wid >= 4 && wid < 7) share(b, wid == 4 ? 64 : 56, wid == 4 ? x : (wid == 5 ? w1 : w3));
    if (wid == 7) { *(LDSP(u32))(MISC + (threadIdx.x & 63) * 4) = 0; WAIT_LGKM0(); }
    BAR();
    if (wid == 7 && (threadIdx.x & 63) < 5) touch((threadIdx.x & 63) * 128, seq_tab, L_DUMMY);
    if (CODEPF && wid == 11 && (threadIdx.x & 63) < CODE_LINES / 28) touch((u32)(r * (CODE_LINES / 28) + (threadIdx.x & 63)) * 128, (const char*)pc0, L_DUMMY);
  }

  if (__builtin_expect(wid < 4 && b < 224, 1)) {
    const int lane = (int)opq(threadIdx.x) & 63;
    u64 tag = 0;
    if (__builtin_expect(wid < 2, 1)) {
      const int g = lane >> 4, l15 = lane & 15;
      u32 vx[8], vw[8];
      const u32 ox = (u32)((lane >> 3) * (D * 2) + (((lane & 7) ^ g) << 4));
      const u32 ow = (u32)(g * (F * 2) + (((l15 >> 2) ^ g) << 6) + ((lane & 3) << 4));
#pragma unroll
      for (int i = 0; i < 8; ++i) {
        vx[i] = (ox ^ (u32)((i & 1) * 64)) + i * 8 * (D * 2) - (i & 3) * 1024;
        vw[i] = ow + i * 4 * (F * 2) - (i & 3) * 1024;
      }
#pragma unroll 1
      for (int c = 0; c < 7; ++c) {
        if (c >= 3) BAR();
        const u32 slot = (u32)(c % 3) * SLOT + wid * 8192;
        const int kc = rot7(c, rot);
        const char* xb = x + (size_t)(panel * 128 + wid * 64) * (D * 2) + kc * 128;
        const char* w1b = w1 + (size_t)(kc * 64 + wid * 32) * (F * 2) + ft * 256;
        const char* w3b = w3 + (size_t)(kc * 64 + wid * 32) * (F * 2) + ft * 256;
#pragma unroll
        for (int i = 0; i < 8; i += 4) {
          WAIT_VM(PACE_L);
          glds16x4(vx[i], vx[i + 1], vx[i + 2], vx[i + 3], xb, slot + i * 1024);
          WAIT_VM(PACE_L);
          glds16x4(vw[i], vw[i + 1], vw[i + 2], vw[i + 3], w1b, slot + 16384 + i * 1024);
          WAIT_VM(PACE_L);
          glds16x4(vw[i], vw[i + 1], vw[i + 2], vw[i + 3], w3b, slot + 32768 + i * 1024);

        }
        dflag_set(L_FULL + wid * 4, c + 1);
      }
      if (wid == 0) {
        tag = make_tag(ws);
        const int k = lane & 7;
        gflag_wait<8>(ws + WS_FST + (k < 2 ? 2 * panel + k : (k == 2 ? tau : (k & 1) * 14 + ft)) * 128, tag);
        glds16_dev(lane * 16, ws + WS_STAT + panel * 1024, L_STAT);
        glds16_dev(lane * 16, ws + WS_CSUM + ft * 1024, L_CSUM);
        if (lane < 2) glds4_dev(lane * 4, ws + WS_E, L_TAG);
        if (lane < 1) glds4_dev(0, ssmD + d0 * 2, L_DD);
        dflag_set(L_EPI, 1);
      }
    } else {
      __builtin_amdgcn_s_setprio(3);
      const rsrc_t rs = ws_rsrc(ws);
      const int u = wid - 2;
      const int ti = b / 7, cj = b % 7;
      const int tg = lane >> 3, cg = lane & 7;
      const int t0 = 64 * ti + 32 * u + 4 * tg, ch0 = 64 * cj + 8 * cg;
      u32x4 kr[7], vr[4], sw[4]; u32x2 o[8];
      u64 seed;
      auto ldkv = [&]() {
        const u32 ko = (u32)(t0 * (D * 2) + ch0 * 2);
#pragma unroll
        for (int i = 0; i < 7; ++i) kr[i] = *(const u32x4*)(kp + (i < 3 && t0 == 0 ? ko : ko + (u32)((i - 3) * (D * 2))));
#pragma unroll
        for (int i = 0; i < 4; ++i) vr[i] = *(const u32x4*)(vp + (ko + (u32)(i * (D * 2))));
        SCHED0();
      };
      auto piece = [&](int p) {
#pragma unroll
        for (int c = 2 * p; c < 2 * p + 2; ++c) {
          const u32 wa = sw[p][(c & 1) * 2], wb = sw[p][(c & 1) * 2 + 1];
          const float w0 = bflo(wa), w1f = bfhi(wa), w2f = bflo(wb), w3f = bfhi(wb);
          float kf[7], ks[4];
#pragma unroll
          for (int i = 0; i < 7; ++i) kf[i] = (c & 1) ? bfhi(kr[i][p]) : bflo(kr[i][p]);
#pragma unroll
          for (int i = 0; i < 4; ++i)
            ks[i] = __builtin_fmaf(kf[i + 3], w0, __builtin_fmaf(kf[i + 2], w1f, __builtin_fmaf(kf[i + 1], w2f, kf[i] * w3f)));
#pragma unroll
          for (int i = 0; i < 2; ++i) {
            const int ta = 3 - 2 * i, tb = 2 - 2 * i;
            const u32 kk = pk2(ks[ta], ks[tb]);
            o[c][i] = pk2(bflo(kk) * ((c & 1) ? bfhi(vr[ta][p]) : bflo(vr[ta][p])), bfhi(kk) * ((c & 1) ? bfhi(vr[tb][p]) : bflo(vr[tb][p])));
          }
          asm volatile("" : "+v"(o[c]));
        }
        SCHED0();
      };
      BAR();
      ldkv();
#pragma unroll
      for (int i = 0; i < 4; ++i) sw[i] = *(const u32x4*)(skp + (u32)(ch0 * 8 + i * 16));
      seed = gload64(ws + WS_E + opq(0));
      if (FLAGPF && lane < 14) {
        const u32 fo = lane < 8 ? WS_FH + (u32)((2 * panel + (lane & 1)) * 56 + 4 * ft + (lane >> 1)) * 128
                                : (lane < 12 ? WS_FY + (u32)((lane & 1) * 448 + d0 + ((lane >> 1) & 1)) * 128 : WS_FUT + (u32)(cj * 64 + 2 * ti + (lane & 1)) * 128);
        if (u) touch(fo, ws, L_DUMMY);
      }
      SCHED0();
      BAR();
      SCHED0();
      if (KV_P2) {
#pragma unroll
        for (int i = 0; i < 3; ++i) if (t0 == 0) kr[i] = u32x4{0, 0, 0, 0};
        piece(0);
      }
      BAR();
      SCHED0();
      if (!KV_P2) {
#pragma unroll
        for (int i = 0; i < 3; ++i) if (t0 == 0) kr[i] = u32x4{0, 0, 0, 0};
      }
      piece(KV_P2);
      BAR();
      SCHED0();
#pragma unroll
      for (int c = 1 + KV_P2; c < 4; ++c) piece(c);
      tag = tag_of(seed);
      const u32 scr = L_USCR + (u32)u * 4096;
#pragma unroll
      for (int c = 0; c < 8; ++c) *(LDSP(u32x2))(scr + (u32)((cg * 8 + c) * 64 + (((7 - tg) ^ (cg & 6)) << 3))) = o[c];
      WAIT_LGKM0();
#pragma unroll
      for (int i = 0; i < 4; ++i) {
        const int dd = 16 * i + (lane >> 2), c4 = lane & 3;
        u32x4 v = *(LDSP(u32x4))(scr + (u32)(dd * 64 + ((c4 ^ (dd >> 4)) << 4)));
        st128_dev(rs, WS_UT + (u32)((64 * cj + dd) * (S * 2) + (2016 - 64 * ti - 32 * u) * 2 + c4 * 16), v);
      }
      gflag_publish(ws + WS_FUT + (cj * 64 + 2 * ti + u) * 128, tag, lane);
    }
    const int j = wid;
    const u32 o2 = w2_voff(lane);
    const char* wbase = w2 + dl * 128;
    u32x4 pk[8][4];
    auto park = [&](int i0) {
#pragma unroll
    for (int i = i0; i < i0 + 4; ++i) {
      const char* src = wbase + (size_t)g2_ks(j, i + 6, rot2) * 32 * (D * 2);
      WAIT_VM(PACE_S);
#pragma unroll
      for (int pc = 0; pc < 4; ++pc) pk[i][pc] = *(const u32x4*)(src + (o2 + (u32)(pc * 8 * (D * 2))));
    }
    SCHED0();
    };
    park(0);
    park(4);
    lflag_wait(L_FREE, 7, 7);
    if (wid == 2) {
        if (lane < 32) {
          glds16(lane * 16, seq_tab + 32, CV_U + 4096);
          glds16(lane * 16, seq_tab + 32, CV_U + CV_USTR + 4096);
        }
        if (KV_LATE) gflag_wait<8>(ws + WS_FUT + ((d0 >> 6) * 64 + lane) * 128, tag);
        glds16x4_dev(lane * 16, ws + WS_UT + d0 * (S * 2), CV_U);
        glds16x4_dev(lane * 16, ws + WS_UT + (d0 + 1) * (S * 2), CV_U + CV_USTR);
        dflag_set(L_CONV + 8, 1);
        dflag_set(L_CONV + 12, 1);
      }
    if (wid < 2) {
      const u32 vl = lane * 16;
#pragma unroll 1
      for (int rr = 0; rr < 4; ++rr) {
        const char* src = ssmk + (size_t)(d0 + wid) * (S * 2) + (rr ? (rr - 4) * 2 : 0);
        const u32 dst = CV + (u32)(wid * 4 + rr) * CV_HSTR + (rr ? 24 : 32);
        glds16x4(rr && !lane ? 16u : vl, vl, vl, vl, src, dst);
      }
      dflag_set(L_CONV + wid * 4, 1);
    }
#pragma unroll 1
    for (int i = 0; i < 6; ++i) {
      if (i == W2_HOLD) lflag_wait(L_PUB, 3, 1);
      if (i == 3) dflag_set(L_FED + j * 4, 3);
      const char* src = wbase + (size_t)g2_ks(j, i, rot2) * 32 * (D * 2);
      WAIT_VM(PACE_S);
      glds16x4(o2, o2 + 8 * (D * 2) - 1024, o2 + 16 * (D * 2) - 2048, o2 + 24 * (D * 2) - 3072, src, g2_place(j, i));
    }
    dflag_set(L_FED + j * 4, 6);
    if (BPOLL ? wid == 2 : wid >= 2) {
      lflag_wait(L_PUB, 3, 1);
      const int e = lane < 28 ? lane : 27;
      gflag_wait<4>(ws + WS_FH + (tau * 56 + 4 * (e >> 1) + (e & 1) + 2 * (3 - wid)) * 128, tag);
      dflag_set(L_H + (3 - wid) * 4, 1);
    }
    WAIT_VM(0);
    const u32 wl = opq(lane * 16 + j * 12288);
#pragma unroll
    for (int i = 0; i < 8; i += 2) {
      lflag_wait(L_PROG + j * 4, 0, i + 2);
#pragma unroll
      for (int n = 0; n < 8; ++n) lds_st128(wl + g2_place(0, (i + (n >> 2)) % 6) + (n & 3) * 1024, pk[i + (n >> 2)][n & 3]);
      lflag_set(L_FED + j * 4, i + 8);
    }
    return;
  }
  const rsrc_t rs = ws_rsrc(ws);

  if (b >= 224) {
    if (wid >= 8 && b != 255) return;
    const int tid = (int)opq(threadIdx.x), lane = tid & 63;
    const int sp = b - 224;
    const u64 seed = gload64(ws + WS_E + opq(0));
    if (wid < 8) {
      const int row = 64 * sp + (tid >> 3), c8 = tid & 7;
      const int cu = sp < 28 ? sp : 27, mat = cu / 14, ct = cu % 14, kg = tid >> 4, c16 = tid & 15;
      const char* wp = (mat ? w3 : w1) + ct * 256;
      const u32 wo = (u32)(kg * (F * 2) + c16 * 16), xo = (u32)(row * (D * 2) + c8 * 16);
      u32x4 xv[7], wv[14];
#pragma unroll
      for (int i = 0; i < 7; ++i) xv[i] = *(const u32x4*)(x + (xo + i * 128));
#pragma unroll
      for (int i = 0; i < 14; ++i) wv[i] = *(const u32x4*)(wp + (wo + i * 32 * (F * 2)));
      SCHED0();
      float s = 0.f;
#pragma unroll
      for (int i = 0; i < 7; ++i)
#pragma unroll
        for (int j = 0; j < 4; ++j) s += bflo(xv[i][j]) + bfhi(xv[i][j]);
      const float m = sum8(s) * (1.0f / D);
      float q = 0.f;
#pragma unroll
      for (int i = 0; i < 7; ++i)
#pragma unroll
        for (int j = 0; j < 4; ++j) {
          const float aa = bflo(xv[i][j]) - m, bb = bfhi(xv[i][j]) - m;
          q = __builtin_fmaf(aa, aa, __builtin_fmaf(bb, bb, q));
        }
      const float sd = rbf(__builtin_amdgcn_sqrtf(rbf(rbf(sum8(q) * (1.0f / D)) + 1e-5f)));
      if (c8 == 0) *(LDSP(f32x2))(u32)(16384 + (tid >> 3) * 8) = f32x2{rbf(m), __builtin_amdgcn_rcpf(sd)};
      SCHED0();
      float a[8] = {};
#pragma unroll
      for (int i = 0; i < 14; ++i)
#pragma unroll
        for (int j = 0; j < 4; ++j) { a[2 * j] += bflo(wv[i][j]); a[2 * j + 1] += bfhi(wv[i][j]); }
      f32x4 t;
#pragma unroll
      for (int j = 0; j < 4; ++j) {
        auto p = __builtin_amdgcn_permlane16_swap(__builtin_bit_cast(u32, a[j]), __builtin_bit_cast(u32, a[j + 4]), false, false);
        t[j] = asf(p[0]) + asf(p[1]);
      }
      *(LDSP(f32x4))(u32)((kg >> 1) * 512 + c16 * 32 + (kg & 1) * 16) = t;
    }
    WAIT_LGKM0(); BAR();
    const u64 tag = tag_of(seed);
    if (wid == 0) {
      const int cu = sp < 28 ? sp : 27;
      f32x2 cs = {};
#pragma unroll
      for (int i = 0; i < 16; ++i) cs += *(LDSP(f32x2))(u32)(i * 512 + lane * 8);
      st64_dev(rs, WS_STAT + (u32)(64 * sp + lane) * 8, *(LDSP(u32x2))(u32)(16384 + lane * 8));
      if (sp < 28) st64_dev(rs, WS_CSUM + (u32)((cu % 14) * 1024 + (cu / 14) * 512 + lane * 8), __builtin_bit_cast(u32x2, cs));
      gflag_publish(ws + WS_FST + sp * 128, tag, lane);
    }
    WAIT_VM(0); BAR();
    if (SPPF) {
      if (tid < 392) touch((u32)(sp * 392 + tid) * 128, w2, L_DUMMY);
      if (tid < 448) touch((u32)(sp * 448 + tid) * 128, ssmk, L_DUMMY);
      BAR();
    }
    if (W2PF && wid > 0 && wid < 8) {
#pragma unroll 1
      for (int i = 0; i < 7; ++i) touch((u32)((((sp >> 3) * 7 + i) * 7 + wid - 1) * 64 + lane) * 128, w2, L_DUMMY);
      WAIT_VM(0);
    }
    if (b == 255) {
      gflag_wait<64>(ws + (tid < 480 ? WS_FUT + tid * 128 : WS_FH + ((((tid < 704 ? tid - 480 : 223) / 14) * 2) * 56 + ((tid < 704 ? tid - 480 : 223) % 14) * 4) * 128), tag);
      BAR();
      if (tid == 0) gstore64(ws + WS_E, tag);
    }
    return;
  }

  const int cw = wid - 4, th = cw & 1, fq = cw >> 1;
  {
    const int lane = (int)opq(threadIdx.x) & 63, g = lane >> 4, hh = lane >> 5, l31 = lane & 31;
    const int q4 = (lane >> 2) & 3, p4 = lane & 3;
    const u32 aA = 16384 + (u32)((8 * hh + q4) * 256 + ((fq ^ q4) << 6) + (g & 1) * 32 + p4 * 8);
    const u32 aB = (u32)((64 * th + l31) * 128 + ((hh ^ ((l31 >> 1) & 7)) << 4));
    f32x16 acc[2][2] = {};
    s16x8 fa[2][2][2], fb[2][2][2];
    u32 pa, pb[4];
    auto setslot = [&](u32 slot) {
      pa = opq(aA + slot);
#pragma unroll
      for (int k = 0; k < 4; ++k) pb[k] = opq((aB ^ (u32)(k << 5)) + slot);
    };
    auto rd = [&](int set) {
#pragma unroll
      for (int k = 0; k < 2; ++k) {
        const int ks = 2 * set + k;
        fa[set][k][0] = lds_tr2(pa, ks * 4096, ks * 4096 + 1024);
        fb[set][k][0] = lds_b128(pb[ks], 0);
        fb[set][k][1] = lds_b128(pb[ks], 4096);
        fa[set][k][1] = lds_tr2(pa, 16384 + ks * 4096, 16384 + ks * 4096 + 1024);
      }
    };
    auto mm = [&](int set, int n0, int n1) {
#pragma unroll
      for (int n = n0; n < n1; ++n) acc[(n >> 1) & 1][n & 1] = mfma32(fa[set][n >> 2][(n >> 1) & 1], fb[set][n >> 2][n & 1], acc[(n >> 1) & 1][n & 1]);
    };
    setslot(0);
    const u32 fad = opq(L_FULL + (lane & 1) * 4);
#pragma unroll
    for (int i = 0; i < 4; ++i) asm volatile("" : "+v"(acc[i >> 1][i & 1]));
    __builtin_amdgcn_s_setprio(1);
    lflag_wait(L_FULL, 1, 1);
    rd(0);
    SCHED0();
    u32 slot = 0;
#pragma unroll 1
    for (int c = 0; c < 7; ++c) {
      const u32 fl = *(volatile LDSP(u32))fad;
      rd(1); mm(0, 0, 5);
      SGB(0x008, 1); SGB(0x100, 4); SGB(0x008, 1); SGB(0x100, 3); SGB(0x008, 1); SGB(0x100, 3); SGB(0x008, 1); SGB(0x100, 3); SGB(0x008, 1);
      SCHED0();
      if (c < 6 && __builtin_amdgcn_ballot_w64(fl < (u32)(c + 2))) lflag_wait(L_FULL, 1, c + 2);
      SCHED0();
      slot = slot == 2 * SLOT ? 0 : slot + SLOT;
      mm(0, 5, 6);
      SCHED0();
      setslot(slot);
      SCHED0();
      mm(0, 6, 8);
      SCHED0();
      if (c < 4) { WAIT_LGKM0(); BAR(); } else if (c == 6) lflag_set(L_FREE + cw * 4, 7);
      SCHED0();
      rd(0); mm(1, 0, 8);
#pragma unroll
      for (int i = 0; i < 4; ++i) { SGB(0x008, 1); SGB(0x100, 3); }
      SGB(0x008, 4);
      SCHED0();
    }
    lflag_wait(L_EPI, 0, 1);
    const u32 hdst = WS_H + (u32)((2 * panel + th) * 56 + 4 * ft + fq) * 4096 + lane * 16;
    float nm[2], c1[2], irs2[2];
#pragma unroll
    for (int j = 0; j < 2; ++j) {
      const f32x2 st = *(LDSP(f32x2))(L_STAT + (u32)(64 * th + 32 * j + l31) * 8);
      nm[j] = -st[0]; c1[j] = -1.44269504f * st[1]; irs2[j] = __builtin_amdgcn_rcpf(st[1] * st[1]);
    }
#pragma unroll
    for (int s = 0; s < 2; ++s) {
      f32x4 cs[2][2];
#pragma unroll
      for (int m = 0; m < 2; ++m)
#pragma unroll
        for (int G = 0; G < 2; ++G) cs[m][G] = *(LDSP(f32x4))(L_CSUM + (u32)(m * 128 + 32 * fq + 16 * s + 8 * G + 4 * hh) * 4);
#pragma unroll
      for (int j = 0; j < 2; ++j) {
        float a[8], ex[8], hv[8];
#pragma unroll
        for (int e = 0; e < 8; ++e) a[e] = __builtin_fmaf(nm[j], cs[0][e >> 2][e & 3], acc[0][j][8 * s + e]);
#pragma unroll
        for (int e = 0; e < 8; ++e) ex[e] = a[e] * c1[j];
        SCHED0();
#pragma unroll
        for (int e = 0; e < 8; ++e) { ex[e] = __builtin_amdgcn_exp2f(ex[e]); hv[e] = __builtin_fmaf(nm[j], cs[1][e >> 2][e & 3], acc[1][j][8 * s + e]); }
        SCHED0();
#pragma unroll
        for (int e = 0; e < 8; ++e) ex[e] = __builtin_fmaf(ex[e], irs2[j], irs2[j]);
        SCHED0();
#pragma unroll
        for (int e = 0; e < 8; ++e) { ex[e] = __builtin_amdgcn_rcpf(ex[e]); hv[e] *= a[e]; }
        SCHED0();
#pragma unroll
        for (int e = 0; e < 8; ++e) hv[e] *= ex[e];
        st128_dev(rs, hdst + (s * 2 + j) * 1024, u32x4{pk2(hv[0], hv[1]), pk2(hv[2], hv[3]), pk2(hv[4], hv[5]), pk2(hv[6], hv[7])});
      }
    }
  }
  char* hflag = ws + WS_FH + ((2 * panel + th) * 56 + 4 * ft + fq) * 128;
  u32x4 xv, sv;
  if (cw < 4) {
    const int ht = (int)opq(threadIdx.x) & 255, lane = ht & 63, l15 = ht & 15;
    const u64 tag = tag_of(*(volatile LDSP(u64))L_TAG);
    const int ch = cw >> 1, var = cw & 1;
    const bool ready = false;
    gflag_publish(hflag, tag, lane);
    lflag_wait(L_CONV, 3, 1);
    asm volatile("" ::: "memory");
    conv_compute(lane, cw, d0 + ch, rs, hflag, tag, ready);
    gflag_publish(ws + WS_FY + (var * 448 + d0 + ch) * 128, tag, lane);
    u32x4 xr[2]; u32x2 st[2];
#pragma unroll
    for (int i = 0; i < 2; ++i) {
      const int n = ht + 256 * i, row = n >> 3, c = n & 7;
      xr[i] = *(const u32x4*)(x + (size_t)(64 * tau + row) * (D * 2) + (64 * dl + 8 * c) * 2);
      st[i] = ld64_dev(rs, WS_STAT + (u32)(64 * tau + row) * 8);
    }
    const int yvar = tau >> 4;
    gflag_wait<4>(ws + WS_FY + (yvar * 448 + 64 * dl + 16 * cw + l15) * 128, tag);
#pragma unroll
    for (int i = 0; i < 2; ++i)
      glds16_dev((u32)((lane >> 3) * (S * 2) + (((lane & 7) ^ (2 * cw + i)) << 4)),
                 ws + WS_YT + (64 * dl + 16 * cw + 8 * i) * (S * 2) + 128 * tau, xbuf(cw ^ 1) + (u32)i * 1024);
    dflag_set(L_ADONE + cw * 4, 1);
    lflag_wait(L_ADONE, 3, 1);
#pragma unroll
    for (int i = 0; i < 2; ++i) {
      const int n = ht + 256 * i, tok = n >> 3, dg = n & 7;
      const float mb = asf(st[i][0]), rsd = asf(st[i][1]);
      u32x4 so;
#pragma unroll
      for (int e = 0; e < 4; ++e) {
        const u32 ya = xbuf((dg >> 1) ^ 1) + (u32)((8 * (dg & 1) + 2 * e) * 128 + (((tok >> 3) ^ dg) << 4) + (tok & 7) * 2);
        const float y0 = bflo(*(LDSP(u16))ya), y1 = bflo(*(LDSP(u16))(ya + 128));
        const u32 dd = pk2(bflo(xr[i][e]) - mb, bfhi(xr[i][e]) - mb);
        const u32 xn = pk2(bflo(dd) * rsd, bfhi(dd) * rsd);
        so[e] = pk2(bflo(xn) * y0, bfhi(xn) * y1);
      }
      if (i == 0) { xv = xr[0]; sv = so; }
      else { lds_st128(E_S + (u32)ht * 16, so); lds_st128(E_X + (u32)ht * 16, xr[1]); }
    }
  } else {
    const int j = cw - 4;
    const int lane = (int)opq(threadIdx.x) & 63;
    lflag_set(L_PUB + j * 4, 1);
    const u64 btag = tag_of(*(volatile LDSP(u64))L_TAG);
    gflag_publish(hflag, btag, lane);
    const u32 hl = lane * 16;
    const char* hbase = ws + WS_H + (size_t)tau * 56 * 4096;
    const int q4 = (lane >> 2) & 3;
    const u32 ta = (u32)((4 * (lane >> 5) + q4) * 128 + ((q4 >> 1) << 6) + ((lane >> 4) & 1) * 32 + (lane & 3) * 8);
    s16x8 fa[2][4], fb[HDEPTH][4];
    f32x16 acc[2][2] = {};
    auto ldh = [&](int i) {
      const char* p = hbase + (size_t)g2_ks(j, i, rot2) * 4096;
      asm volatile("global_load_dwordx4 %0, %4, %5 sc1\n\tglobal_load_dwordx4 %1, %4, %5 offset:1024 sc1\n\t"
                   "global_load_dwordx4 %2, %4, %5 offset:2048 sc1\n\tglobal_load_dwordx4 %3, %4, %5 offset:3072 sc1"
                   : "=&v"(fb[i % HDEPTH][0]), "=&v"(fb[i % HDEPTH][1]), "=&v"(fb[i % HDEPTH][2]), "=&v"(fb[i % HDEPTH][3])
                   : "v"(hl), "s"(p) : "memory");
    };
    u32 tb[2];
#pragma unroll
    for (int a = 0; a < 2; ++a) tb[a] = opq((ta ^ (u32)(a << 6)) + g2_place(j, 0));
    auto rda = [&](int i) {
#pragma unroll
      for (int a = 0; a < 2; ++a)
        if (i > 0 && i % 3 == 0) tb[a] = opq(i % 6 ? tb[a] - 2 * SLOT : tb[a] + 2 * SLOT);
#pragma unroll
      for (int n = 0; n < 4; ++n) {
        const int o = (i % 3) * 4096 + (n >> 1) * 2048;
        fa[i & 1][n] = lds_tr2(tb[n & 1], o, o + 1024);
      }
    };
    const u32 fed = opq(L_FED + j * 4), prog = opq(L_PROG + j * 4);
    if (BPOLL) { const int e = 7 * j + (lane < 7 ? lane : 6); gflag_wait<2>(ws + WS_FH + (tau * 56 + 4 * (e >> 1) + (e & 1)) * 128, btag); }
    else lflag_wait(L_H, 0, 1);
#pragma unroll
    for (int i = 0; i < HDEPTH; ++i) ldh(i);
    lflag_wait(L_FED + j * 4, 0, 3);
    rda(0);
    SCHED0();
    u32 fl = 0;
#pragma unroll
    for (int i = 0; i < 14; ++i) {
      if (i == 2 || (i >= 5 && i < 13 && (i & 1))) {
        if (__builtin_amdgcn_readfirstlane(fl) < i + 2) lflag_wait(L_FED + j * 4, 0, i + 2);
      }
      asm volatile("s_waitcnt vmcnt(%4)" : "+v"(fb[i % HDEPTH][0]), "+v"(fb[i % HDEPTH][1]), "+v"(fb[i % HDEPTH][2]), "+v"(fb[i % HDEPTH][3])
                   : "i"(i + HDEPTH <= 14 ? (HDEPTH - 1) * 4 : (13 - i) * 4) : "memory");
      SCHED0();
      if (i < 13) rda(i + 1);
      if (i == 1 || (i >= 4 && i < 12 && !(i & 1))) fl = *(volatile LDSP(u32))fed;
#pragma unroll
      for (int s = 0; s < 2; ++s)
#pragma unroll
        for (int a = 0; a < 2; ++a)
#pragma unroll
          for (int t = 0; t < 2; ++t) acc[a][t] = mfma32(fa[i & 1][2 * s + a], fb[i % HDEPTH][2 * s + t], acc[a][t]);
      if (i < 13) {
#pragma unroll
        for (int n = 0; n < 5; ++n) { SGB(0x008, 1); SGB(0x100, 2); }
        SGB(0x008, 3);
      }
      SCHED0();
      if (i < 8 && (i & 1)) *(volatile LDSP(u32))prog = i + 1;
      if (i == 7 - HDEPTH) { lflag_wait(L_H + 4, 0, 1); }
      if (i + HDEPTH < 14) ldh(i + HDEPTH);
      SCHED0();

    }
    const int ln = (int)opq(threadIdx.x), lt = ln & 31, lh = (ln >> 5) & 1;
#pragma unroll
    for (int t = 0; t < 2; ++t)
#pragma unroll
      for (int a = 0; a < 2; ++a)
#pragma unroll
        for (int G = 0; G < 4; ++G)
          *(LDSP(f32x4))(g2_place(j, t ? 3 : 0) + (u32)(lt * 256 + (((8 * a + 2 * G + lh) ^ (lt & 7)) << 4))) =
              f32x4{acc[a][t][4 * G], acc[a][t][4 * G + 1], acc[a][t][4 * G + 2], acc[a][t][4 * G + 3]};
  }
  WAIT_LGKM0();
  BAR();
  {
    const int ht = (int)opq(threadIdx.x) & 255;
    const int half = cw >> 2, tk = ht >> 3, dg = ht & 7;
    if (half) { sv = *(LDSP(u32x4))(E_S + (u32)ht * 16); xv = *(LDSP(u32x4))(E_X + (u32)ht * 16); }
    f32x4 s0 = {}, s1 = {};
#pragma unroll
    for (int w = 0; w < 4; ++w) {
      const u32 pb = (half ? 0u : 2 * SLOT) + (u32)w * 12288 + (u32)tk * 256;
      s0 += *(LDSP(f32x4))(pb + (u32)(((2 * dg) ^ (tk & 7)) << 4));
      s1 += *(LDSP(f32x4))(pb + (u32)(((2 * dg + 1) ^ (tk & 7)) << 4));
    }
    float o[8];
#pragma unroll
    for (int e = 0; e < 8; ++e) {
      const float f = rbf(e < 4 ? s0[e & 3] : s1[e & 3]);
      const float gt = rbf(silu_f(f));
      const float xe = (e & 1) ? bfhi(xv[e >> 1]) : bflo(xv[e >> 1]);
      const float se = (e & 1) ? bfhi(sv[e >> 1]) : bflo(sv[e >> 1]);
      o[e] = xe + rbf(se * gt);
    }
    __builtin_amdgcn_raw_buffer_store_b128(u32x4{pk2(o[0], o[1]), pk2(o[2], o[3]), pk2(o[4], o[5]), pk2(o[6], o[7])},
        __builtin_amdgcn_make_buffer_rsrc(out, 0, S * D * 2, 0x00020000), (64 * tau + 32 * half + tk) * (D * 2) + (64 * dl + 8 * dg) * 2, 0, 16);
  }
}
}
#if defined(__HIP_DEVICE_COMPILE__) && __HIP_DEVICE_COMPILE__
#pragma clang attribute pop
#endif

extern "C" void kernel_launch(void* const* d_in, const int* in_sizes, int n_in, void* d_out, int out_size, void* d_ws,
                              size_t ws_size, hipStream_t stream) {
  (void)hipFuncSetAttribute((const void*)fused_kernel, hipFuncAttributeMaxDynamicSharedMemorySize, 163840);
  fused_kernel<<<256, 768, 163840, stream>>>((const char*)d_in[0], (const char*)d_in[1], (const char*)d_in[2], (const char*)d_in[3],
                                             (const char*)d_in[4], (const char*)d_in[5], (const char*)d_in[7], (const char*)d_in[8],
                                             (const char*)d_in[9], (char*)d_ws, (char*)d_out);
}
```

```cpp
#define KV_P2 1
#define W2PF 0
#include <hip/hip_runtime.h>
#include <hip/hip_bf16.h>
#include <stdint.h>
#pragma clang diagnostic ignored "-Wint-to-pointer-cast"

#if defined(__HIP_DEVICE_COMPILE__) && __HIP_DEVICE_COMPILE__
#pragma clang attribute push(__attribute__((target("no-packed-fp32-ops"))), apply_to = function)
#endif
namespace {
typedef unsigned short u16;
typedef unsigned int u32;
typedef unsigned long long u64;
typedef __attribute__((ext_vector_type(4))) short s16x4;
typedef __attribute__((ext_vector_type(8))) short s16x8;
typedef __attribute__((ext_vector_type(8))) __bf16 bf16x8;
typedef __attribute__((ext_vector_type(2))) __bf16 bf16x2;
typedef __attribute__((ext_vector_type(16))) float f32x16;
typedef __attribute__((ext_vector_type(4))) float f32x4;
typedef __attribute__((ext_vector_type(2))) float f32x2;
typedef __attribute__((ext_vector_type(4))) u32 u32x4;
typedef __attribute__((ext_vector_type(2))) u32 u32x2;
#define LDSP(T) __attribute__((address_space(3))) T*
#define GLBP(T) __attribute__((address_space(1))) T*
#define DEV __device__ __forceinline__

constexpr int S = 2048, D = 448, F = 1792;
constexpr u32 WS_H = 0;
constexpr u32 WS_UT = WS_H + 7340032;
constexpr u32 WS_YT = WS_UT + 1835008;
constexpr u32 WS_STAT = WS_YT + 1835008;
constexpr u32 WS_CSUM = WS_STAT + 16384;
constexpr u32 WS_E = WS_CSUM + 16384;
constexpr u32 WS_FUT = WS_E + 128;
constexpr u32 WS_FST = WS_FUT + 448 * 128;
constexpr u32 WS_FY = WS_FST + 32 * 128;
constexpr u32 WS_FH = WS_FY + 896 * 128;
constexpr u32 WS_END = WS_FH + 1792 * 128;
constexpr int CODE_LINES = 196;
constexpr u32 SLOT = 49152;
constexpr u32 MISC = 3 * SLOT;
constexpr u32 L_FULL = MISC, L_EPI = MISC + 8, L_H = MISC + 16, L_FREE = MISC + 32, L_CONV = MISC + 64, L_FED = MISC + 80,
              L_TAG = MISC + 96, L_DD = MISC + 104, L_ADONE = MISC + 112, L_PROG = MISC + 128, L_XCH = MISC + 144, L_PUB = MISC + 160, L_STAT = MISC + 256,
              L_CSUM = MISC + 1280, L_USCR = MISC + 4096, L_DUMMY = MISC + 12288;
constexpr u32 CV = SLOT, CV_HSTR = 4160, CV_U = CV + 8 * CV_HSTR, CV_USTR = 4608;
constexpr u32 E_S = SLOT, E_X = SLOT + 4096;
constexpr int SPIN_CAP = 1 << 18;
#define ROT1 0
#define ROT2 0
#define TOUCH2_AT 0
#define PACE_L 63
#define PACE_S 63
#define FREE_SET dflag_set
#define KVPF 0
#define SHARES 1
#define CODEPF 0
#define W2_HOLD 0
#define PUB2 0
#define KV_LATE 1
#define SPPF 1
#define FLAGPF 1
#define MIDPF 1
#define BPOLL 1
#define HDEPTH 3
#define LSLEEP 1
#define UT_FIRST 0
#define KV_AT 1

__device__ const u32 seq_tab[160] __attribute__((aligned(128))) = {0, 1, 2, 3, 4, 5, 6, 7};
DEV float asf(u32 v) { return __builtin_bit_cast(float, v); }
DEV float bflo(u32 v) { return asf(v << 16); }
DEV float bfhi(u32 v) { return asf(v & 0xffff0000u); }
template <int CTRL> DEV float dpp(float v) { return asf(__builtin_amdgcn_update_dpp(0, __builtin_bit_cast(u32, v), CTRL, 0xf, 0xf, true)); }
DEV float sum8(float v) { v += dpp<0xB1>(v); v += dpp<0x4E>(v); return v + dpp<0x141>(v); }
DEV u32 pk2(float lo, float hi) {
  asm("" : "+v"(lo)); asm("" : "+v"(hi));
  bf16x2 r = {(__bf16)lo, (__bf16)hi};
  return __builtin_bit_cast(u32, r);
}
DEV float rbf(float f) { bf16x2 r = {(__bf16)f, (__bf16)f}; return bflo(__builtin_bit_cast(u32, r)); }
DEV f32x4 mfma16(s16x8 a, s16x8 b, f32x4 c) {
  return __builtin_amdgcn_mfma_f32_16x16x32_bf16(__builtin_bit_cast(bf16x8, a), __builtin_bit_cast(bf16x8, b), c, 0, 0, 0);
}
DEV f32x16 mfma32(s16x8 a, s16x8 b, f32x16 c) {
  return __builtin_amdgcn_mfma_f32_32x32x16_bf16(__builtin_bit_cast(bf16x8, a), __builtin_bit_cast(bf16x8, b), c, 0, 0, 0);
}
DEV u32 opq(u32 v) { asm volatile("" : "+v"(v)); return v; }
DEV s16x8 lds_tr2(u32 base, int o0, int o1) {
  s16x4 lo = __builtin_amdgcn_ds_read_tr16_b64_v4i16((LDSP(s16x4))((LDSP(char))base + o0));
  s16x4 hi = __builtin_amdgcn_ds_read_tr16_b64_v4i16((LDSP(s16x4))((LDSP(char))base + o1));
  return __builtin_shufflevector(lo, hi, 0, 1, 2, 3, 4, 5, 6, 7);
}
DEV s16x8 lds_b128(u32 base, int o) { return *(LDSP(s16x8))((LDSP(char))base + o); }
DEV void lds_st128(u32 a, u32x4 v) { *(LDSP(u32x4))a = v; }
DEV void glds16x4(u32 v0, u32 v1, u32 v2, u32 v3, const void* sbase, u32 ldst) {
  asm volatile("s_mov_b32 m0, %5\n\ts_nop 0\n\tglobal_load_lds_dwordx4 %0, %4\n\tglobal_load_lds_dwordx4 %1, %4 offset:1024\n\t"
               "global_load_lds_dwordx4 %2, %4 offset:2048\n\tglobal_load_lds_dwordx4 %3, %4 offset:3072"
               ::"v"(v0), "v"(v1), "v"(v2), "v"(v3), "s"(sbase), "s"(ldst) : "memory");
}
DEV void glds16(u32 voff, const void* sbase, u32 ldst) {
  asm volatile("s_mov_b32 m0, %2\n\ts_nop 0\n\tglobal_load_lds_dwordx4 %0, %1" ::"v"(voff), "s"(sbase), "s"(ldst) : "memory");
}
DEV void glds16x4_lin(u32 v, const void* sbase, u32 ldst) { glds16x4(v, v, v, v, sbase, ldst); }
DEV void glds16x4_dev(u32 v, const void* sbase, u32 ldst) {
  asm volatile("s_mov_b32 m0, %2\n\ts_nop 0\n\tglobal_load_lds_dwordx4 %0, %1 sc1\n\tglobal_load_lds_dwordx4 %0, %1 offset:1024 sc1\n\t"
               "global_load_lds_dwordx4 %0, %1 offset:2048 sc1\n\tglobal_load_lds_dwordx4 %0, %1 offset:3072 sc1"
               ::"v"(v), "s"(sbase), "s"(ldst) : "memory");
}
DEV void glds16_dev(u32 voff, const void* sbase, u32 ldst) {
  asm volatile("s_mov_b32 m0, %2\n\ts_nop 0\n\tglobal_load_lds_dwordx4 %0, %1 sc1" ::"v"(voff), "s"(sbase), "s"(ldst) : "memory");
}
DEV void dflag_set(u32 addr, u32 val) {
  asm volatile("s_waitcnt lgkmcnt(0)\n\ts_mov_b32 m0, %2\n\ts_mov_b64 exec, 1\n\tglobal_load_lds_dword %0, %1\n\ts_mov_b64 exec, -1"
               ::"v"(0), "s"(seq_tab + val), "s"(addr) : "memory");
}
#define WAIT_VM(n) asm volatile("s_waitcnt vmcnt(%0)" ::"i"(n) : "memory")
#define WAIT_LGKM0() asm volatile("s_waitcnt lgkmcnt(0)" ::: "memory")
#define BAR() __builtin_amdgcn_s_barrier()
#define SCHED0() __builtin_amdgcn_sched_barrier(0)
#define SGB(mask, n) __builtin_amdgcn_sched_group_barrier(mask, n, 0)
DEV float silu_f(float x) { return x * __builtin_amdgcn_rcpf(1.0f + __builtin_amdgcn_exp2f(-1.44269504f * x)); }

DEV void lflag_set(u32 addr, u32 val) {
  asm volatile("s_waitcnt lgkmcnt(0)\n\tds_write_b32 %0, %1" ::"v"(addr), "v"(val) : "memory");
}
DEV void lflag_wait(u32 addr, u32 mask, u32 val) {
  const u32 a = addr + 4 * (threadIdx.x & mask);
#pragma unroll 1
  for (int i = 0; i < SPIN_CAP; ++i) {
    u32 v;
    asm volatile("ds_read_b32 %0, %1\n\ts_waitcnt lgkmcnt(0)" : "=v"(v) : "v"(a) : "memory");
    if (__builtin_amdgcn_ballot_w64(v < val) == 0) break;
    __builtin_amdgcn_s_sleep(LSLEEP);
  }
}
DEV u64 gload64(const char* p) { return __hip_atomic_load((GLBP(u64))p, __ATOMIC_RELAXED, __HIP_MEMORY_SCOPE_AGENT); }
DEV void gstore64(char* p, u64 v) { __hip_atomic_store((GLBP(u64))p, v, __ATOMIC_RELAXED, __HIP_MEMORY_SCOPE_AGENT); }
template <int SLEEP>
DEV void gflag_wait(const char* lane_ptr, u64 tag) {
  bool ok = false;
#pragma unroll 1
  for (int i = 0; i < SPIN_CAP; ++i) {
    asm volatile("" ::: "memory");
    if (!ok) ok = gload64(lane_ptr) == tag;
    if (__builtin_amdgcn_ballot_w64(!ok) == 0) break;
    __builtin_amdgcn_s_sleep(SLEEP);
  }
  asm volatile("" ::: "memory");
}
DEV void gflag_publish(char* p, u64 tag, int lane) {
  WAIT_VM(0);
  if (lane == 0) gstore64(p, tag);
}
DEV u64 tag_of(u64 seed) { return seed * 0x9E3779B97F4A7C15ull + 0xD1B54A32D192ED03ull; }
DEV u64 make_tag(const char* ws) { return tag_of(gload64(ws + WS_E)); }
typedef __amdgpu_buffer_rsrc_t rsrc_t;
DEV rsrc_t ws_rsrc(char* ws) { return __builtin_amdgcn_make_buffer_rsrc(ws, 0, WS_END, 0x00020000); }
DEV void st128_dev(rsrc_t r, u32 off, u32x4 v) { __builtin_amdgcn_raw_buffer_store_b128(v, r, off, 0, 16); }
DEV void st64_dev(rsrc_t r, u32 off, u32x2 v) { __builtin_amdgcn_raw_buffer_store_b64(v, r, off, 0, 16); }
DEV void st32_dev(rsrc_t r, u32 off, u32 v) { __builtin_amdgcn_raw_buffer_store_b32(v, r, off, 0, 16); }
DEV u32x4 ld128_dev(rsrc_t r, u32 off) { return __builtin_amdgcn_raw_buffer_load_b128(r, off, 0, 16); }
DEV u32x2 ld64_dev(rsrc_t r, u32 off) { return __builtin_amdgcn_raw_buffer_load_b64(r, off, 0, 16); }
DEV void touch(u32 voff, const void* sbase, u32 ldst) {
  asm volatile("s_mov_b32 m0, %2\n\ts_nop 0\n\tglobal_load_lds_dword %0, %1" ::"v"(voff), "s"(sbase), "s"(ldst) : "memory");
}

DEV void glds4_dev(u32 voff, const void* sbase, u32 ldst) {
  asm volatile("s_mov_b32 m0, %2\n\ts_nop 0\n\tglobal_load_lds_dword %0, %1 sc1" ::"v"(voff), "s"(sbase), "s"(ldst) : "memory");
}
DEV u32 shr16(u32 hi, u32 lo) { return __builtin_amdgcn_alignbit(hi, lo, 16); }

DEV u32 xbuf(int w) { return w < 2 ? L_USCR + (u32)w * 4096 : (w == 2 ? L_DUMMY : CV + 45056); }
DEV void conv_compute(int lane, int cw, int d, rsrc_t rs, char* hflag, u64 tag, bool pub) {
  const int ch = cw >> 1, var = cw & 1;
  const int m = lane & 15, g = lane >> 4, q4 = m >> 2, p4 = lane & 3;
  u32 aa = opq(CV + (u32)(ch * 4 + ((q4 + 1) & 3)) * CV_HSTR + (u32)(((q4 + 1) & 4) + 8 * g + 4 * p4) * 2 + var * 256);
  u32 ba = opq(CV_U + (u32)ch * CV_USTR + (u32)(240 - 16 * m + 8 * g) * 2 + var * 256);
  f32x4 acc[8] = {};
  s16x8 A[8], B[8];
#pragma unroll
  for (int p = 0; p < 8; ++p) { A[7 - p] = lds_tr2(aa, (7 - p) * 512, (7 - p) * 512 + 8); B[p] = lds_b128(ba, (7 - p) * 512); }
  if (var == 0) {
    const u32 a0 = CV + (u32)ch * 4 * CV_HSTR + (u32)(m + 1 + 8 * g) * 2;
#pragma unroll
    for (int j = 0; j < 8; ++j) { const short v = *(LDSP(short))(a0 + j * 2); A[0][j] = m + 8 * g + j >= 15 ? v : (short)0; }
  }
  SCHED0();
#pragma unroll 1
  for (int it = 0; it < 4; ++it) {
#pragma unroll
    for (int p = 7; p >= 0; --p) {
#pragma unroll
      for (int dd = 0; dd <= 7 - p; ++dd) {
        acc[p + dd] = mfma16(A[p], B[dd], acc[p + dd]);
        if (p == 0) { SCHED0(); B[dd] = lds_b128(ba, 64 + (7 - dd) * 512); SCHED0(); }
      }
      SCHED0();
      A[p] = lds_tr2(aa, 64 + p * 512, 64 + p * 512 + 8);
      SCHED0();
    }
    aa = opq(aa + 64); ba = opq(ba + 64);
    if (it == 0 && pub) gflag_publish(hflag, tag, lane);
  }
  f32x4 y[4];
#pragma unroll
  for (int i = 0; i < 4; ++i) {
    *(LDSP(f32x4))(xbuf(cw) + (u32)(i * 1024 + lane * 16)) = var ? acc[i] : acc[4 + i];
    y[i] = var ? acc[4 + i] : acc[i];
  }
  lflag_set(L_XCH + cw * 4, 1);
  const u32 dv = *(LDSP(u32))L_DD;
  const float Dd = ch ? bfhi(dv) : bflo(dv);
  u32x2 uu[4];
#pragma unroll
  for (int i = 0; i < 4; ++i) uu[i] = *(LDSP(u32x2))(CV_U + (u32)ch * CV_USTR + (u32)(2044 - (1024 * var + 256 * i + 16 * m + 4 * g)) * 2);
  lflag_wait(L_XCH + (cw ^ 1) * 4, 0, 1);
#pragma unroll
  for (int i = 0; i < 4; ++i) {
    const int t = 1024 * var + 256 * i + 16 * m + 4 * g;
    const f32x4 s = y[i] + *(LDSP(f32x4))(xbuf(cw ^ 1) + (u32)(i * 1024 + lane * 16));
    float y0 = rbf(s[0]) + rbf(bfhi(uu[i][1]) * Dd), y1 = rbf(s[1]) + rbf(bflo(uu[i][1]) * Dd);
    float y2 = rbf(s[2]) + rbf(bfhi(uu[i][0]) * Dd), y3 = rbf(s[3]) + rbf(bflo(uu[i][0]) * Dd);
    st64_dev(rs, WS_YT + (u32)(d * (S * 2) + t * 2), u32x2{pk2(y0, y1), pk2(y2, y3)});
  }
}

DEV int rot7(int i, int rot) { const int v = i + rot; return v >= 7 ? v - 7 : v; }
DEV int g2_ks(int j, int i, int rot) { const int e = 7 * j + rot7(i < 7 ? i : i - 7, rot); return 4 * (e >> 1) + (e & 1) + (i < 7 ? 0 : 2); }
DEV u32 g2_place(int j, int p) { return (p < 3 ? 2 * SLOT + (u32)p * 4096 : (u32)(p - 3) * 4096) + (u32)j * 12288; }
DEV u32 w2_voff(int lane) { return (u32)((lane >> 3) * (D * 2) + (((lane & 7) ^ (((lane >> 4) & 1) << 2)) << 4)); }

DEV void share(int b, int n, const char* base) {
  const int lane = (int)opq(threadIdx.x) & 63;
  if (lane < n) touch((u32)(b * n + lane) * 128, base, L_DUMMY);
}

__global__ __launch_bounds__(768, 3) void fused_kernel(const char* x, const char* kp, const char* vp, const char* skp,
                                                       const char* ssmk, const char* ssmD, const char* w1, const char* w3,
                                                       const char* w2, char* ws, char* out) {
  u64 pc0;
  asm volatile("s_getpc_b64 %0" : "=s"(pc0));
  const int b = blockIdx.x, xcd = b & 7, r = b >> 3;
  const int wid = __builtin_amdgcn_readfirstlane(threadIdx.x >> 6);
  const int panel = (xcd >> 1) * 4 + r / 7, ft = (xcd & 1) * 7 + r % 7;
  const int tau = xcd * 4 + r / 7, dl = r % 7;
  const int d0 = 2 * b;
  const int rot = ROT1 ? (r % 7 + 2 * (r / 7)) % 7 : 0, rot2 = ROT2 ? (r % 7 + 2 * (r / 7)) % 7 : 0;

  if (b < 224) {
    if (SHARES && wid >= 4 && wid < 7) share(b, wid == 4 ? 64 : 56, wid == 4 ? x : (wid == 5 ? w1 : w3));
    if (wid == 7) { *(LDSP(u32))(MISC + (threadIdx.x & 63) * 4) = 0; WAIT_LGKM0(); }
    BAR();
    if (wid == 7 && (threadIdx.x & 63) < 5) touch((threadIdx.x & 63) * 128, seq_tab, L_DUMMY);
    if (CODEPF && wid == 11 && (threadIdx.x & 63) < CODE_LINES / 28) touch((u32)(r * (CODE_LINES / 28) + (threadIdx.x & 63)) * 128, (const char*)pc0, L_DUMMY);
  }

  if (__builtin_expect(wid < 4 && b < 224, 1)) {
    const int lane = (int)opq(threadIdx.x) & 63;
    u64 tag = 0;
    if (__builtin_expect(wid < 2, 1)) {
      const int g = lane >> 4, l15 = lane & 15;
      u32 vx[8], vw[8];
      const u32 ox = (u32)((lane >> 3) * (D * 2) + (((lane & 7) ^ g) << 4));
      const u32 ow = (u32)(g * (F * 2) + (((l15 >> 2) ^ g) << 6) + ((lane & 3) << 4));
#pragma unroll
      for (int i = 0; i < 8; ++i) {
        vx[i] = (ox ^ (u32)((i & 1) * 64)) + i * 8 * (D * 2) - (i & 3) * 1024;
        vw[i] = ow + i * 4 * (F * 2) - (i & 3) * 1024;
      }
#pragma unroll 1
      for (int c = 0; c < 7; ++c) {
        if (c >= 3) BAR();
        const u32 slot = (u32)(c % 3) * SLOT + wid * 8192;
        const int kc = rot7(c, rot);
        const char* xb = x + (size_t)(panel * 128 + wid * 64) * (D * 2) + kc * 128;
        const char* w1b = w1 + (size_t)(kc * 64 + wid * 32) * (F * 2) + ft * 256;
        const char* w3b = w3 + (size_t)(kc * 64 + wid * 32) * (F * 2) + ft * 256;
#pragma unroll
        for (int i = 0; i < 8; i += 4) {
          WAIT_VM(PACE_L);
          glds16x4(vx[i], vx[i + 1], vx[i + 2], vx[i + 3], xb, slot + i * 1024);
          WAIT_VM(PACE_L);
          glds16x4(vw[i], vw[i + 1], vw[i + 2], vw[i + 3], w1b, slot + 16384 + i * 1024);
          WAIT_VM(PACE_L);
          glds16x4(vw[i], vw[i + 1], vw[i + 2], vw[i + 3], w3b, slot + 32768 + i * 1024);

        }
        dflag_set(L_FULL + wid * 4, c + 1);
      }
      if (wid == 0) {
        tag = make_tag(ws);
        const int k = lane & 7;
        gflag_wait<8>(ws + WS_FST + (k < 2 ? 2 * panel + k : (k == 2 ? tau : (k & 1) * 14 + ft)) * 128, tag);
        glds16_dev(lane * 16, ws + WS_STAT + panel * 1024, L_STAT);
        glds16_dev(lane * 16, ws + WS_CSUM + ft * 1024, L_CSUM);
        if (lane < 2) glds4_dev(lane * 4, ws + WS_E, L_TAG);
        if (lane < 1) glds4_dev(0, ssmD + d0 * 2, L_DD);
        dflag_set(L_EPI, 1);
      }
    } else {
      __builtin_amdgcn_s_setprio(3);
      const rsrc_t rs = ws_rsrc(ws);
      const int u = wid - 2;
      const int ti = b / 7, cj = b % 7;
      const int tg = lane >> 3, cg = lane & 7;
      const int t0 = 64 * ti + 32 * u + 4 * tg, ch0 = 64 * cj + 8 * cg;
      u32x4 kr[7], vr[4], sw[4]; u32x2 o[8];
      u64 seed;
      auto ldkv = [&]() {
        const u32 ko = (u32)(t0 * (D * 2) + ch0 * 2);
#pragma unroll
        for (int i = 0; i < 7; ++i) kr[i] = *(const u32x4*)(kp + (i < 3 && t0 == 0 ? ko : ko + (u32)((i - 3) * (D * 2))));
#pragma unroll
        for (int i = 0; i < 4; ++i) vr[i] = *(const u32x4*)(vp + (ko + (u32)(i * (D * 2))));
        SCHED0();
      };
      auto piece = [&](int p) {
#pragma unroll
        for (int c = 2 * p; c < 2 * p + 2; ++c) {
          const u32 wa = sw[p][(c & 1) * 2], wb = sw[p][(c & 1) * 2 + 1];
          const float w0 = bflo(wa), w1f = bfhi(wa), w2f = bflo(wb), w3f = bfhi(wb);
          float kf[7], ks[4];
#pragma unroll
          for (int i = 0; i < 7; ++i) kf[i] = (c & 1) ? bfhi(kr[i][p]) : bflo(kr[i][p]);
#pragma unroll
          for (int i = 0; i < 4; ++i)
            ks[i] = __builtin_fmaf(kf[i + 3], w0, __builtin_fmaf(kf[i + 2], w1f, __builtin_fmaf(kf[i + 1], w2f, kf[i] * w3f)));
#pragma unroll
          for (int i = 0; i < 2; ++i) {
            const int ta = 3 - 2 * i, tb = 2 - 2 * i;
            const u32 kk = pk2(ks[ta], ks[tb]);
            o[c][i] = pk2(bflo(kk) * ((c & 1) ? bfhi(vr[ta][p]) : bflo(vr[ta][p])), bfhi(kk) * ((c & 1) ? bfhi(vr[tb][p]) : bflo(vr[tb][p])));
          }
          asm volatile("" : "+v"(o[c]));
        }
        SCHED0();
      };
      BAR();
      ldkv();
#pragma unroll
      for (int i = 0; i < 4; ++i) sw[i] = *(const u32x4*)(skp + (u32)(ch0 * 8 + i * 16));
      seed = gload64(ws + WS_E + opq(0));
      if (FLAGPF && lane < 14) {
        const u32 fo = lane < 8 ? WS_FH + (u32)((2 * panel + (lane & 1)) * 56 + 4 * ft + (lane >> 1)) * 128
                                : (lane < 12 ? WS_FY + (u32)((lane & 1) * 448 + d0 + ((lane >> 1) & 1)) * 128 : WS_FUT + (u32)(cj * 64 + 2 * ti + (lane & 1)) * 128);
        if (u) touch(fo, ws, L_DUMMY);
      }
      SCHED0();
      BAR();
      SCHED0();
      if (KV_P2) {
#pragma unroll
        for (int i = 0; i < 3; ++i) if (t0 == 0) kr[i] = u32x4{0, 0, 0, 0};
        piece(0);
      }
      BAR();
      SCHED0();
      if (!KV_P2) {
#pragma unroll
        for (int i = 0; i < 3; ++i) if (t0 == 0) kr[i] = u32x4{0, 0, 0, 0};
      }
      piece(KV_P2);
      BAR();
      SCHED0();
#pragma unroll
      for (int c = 1 + KV_P2; c < 4; ++c) piece(c);
      tag = tag_of(seed);
      const u32 scr = L_USCR + (u32)u * 4096;
#pragma unroll
      for (int c = 0; c < 8; ++c) *(LDSP(u32x2))(scr + (u32)((cg * 8 + c) * 64 + (((7 - tg) ^ (cg & 6)) << 3))) = o[c];
      WAIT_LGKM0();
#pragma unroll
      for (int i = 0; i < 4; ++i) {
        const int dd = 16 * i + (lane >> 2), c4 = lane & 3;
        u32x4 v = *(LDSP(u32x4))(scr + (u32)(dd * 64 + ((c4 ^ (dd >> 4)) << 4)));
        st128_dev(rs, WS_UT + (u32)((64 * cj + dd) * (S * 2) + (2016 - 64 * ti - 32 * u) * 2 + c4 * 16), v);
      }
      gflag_publish(ws + WS_FUT + (cj * 64 + 2 * ti + u) * 128, tag, lane);
    }
    const int j = wid;
    const u32 o2 = w2_voff(lane);
    const char* wbase = w2 + dl * 128;
    u32x4 pk[8][4];
    auto park = [&](int i0) {
#pragma unroll
    for (int i = i0; i < i0 + 4; ++i) {
      const char* src = wbase + (size_t)g2_ks(j, i + 6, rot2) * 32 * (D * 2);
      WAIT_VM(PACE_S);
#pragma unroll
      for (int pc = 0; pc < 4; ++pc) pk[i][pc] = *(const u32x4*)(src + (o2 + (u32)(pc * 8 * (D * 2))));
    }
    SCHED0();
    };
    park(0);
    if (wid == 2) gflag_wait<8>(ws + WS_FUT + ((d0 >> 6) * 64 + lane) * 128, tag); else if (UT_FIRST) park(4);
    if (!UT_FIRST) park(4);
    lflag_wait(L_FREE, 7, 7);
    if (wid == 2) {
        if (lane < 32) {
          glds16(lane * 16, seq_tab + 32, CV_U + 4096);
          glds16(lane * 16, seq_tab + 32, CV_U + CV_USTR + 4096);
        }
        glds16x4_dev(lane * 16, ws + WS_UT + d0 * (S * 2), CV_U);
        glds16x4_dev(lane * 16, ws + WS_UT + (d0 + 1) * (S * 2), CV_U + CV_USTR);
        dflag_set(L_CONV + 8, 1);
        dflag_set(L_CONV + 12, 1);
        if (UT_FIRST) park(4);
      }
    if (wid < 2) {
      const u32 vl = lane * 16;
#pragma unroll 1
      for (int rr = 0; rr < 4; ++rr) {
        const char* src = ssmk + (size_t)(d0 + wid) * (S * 2) + (rr ? (rr - 4) * 2 : 0);
        const u32 dst = CV + (u32)(wid * 4 + rr) * CV_HSTR + (rr ? 24 : 32);
        glds16x4(rr && !lane ? 16u : vl, vl, vl, vl, src, dst);
      }
      dflag_set(L_CONV + wid * 4, 1);
    }
#pragma unroll 1
    for (int i = 0; i < 6; ++i) {
      if (i == W2_HOLD) lflag_wait(L_PUB, 3, 1);
      if (i == 3) dflag_set(L_FED + j * 4, 3);
      const char* src = wbase + (size_t)g2_ks(j, i, rot2) * 32 * (D * 2);
      WAIT_VM(PACE_S);
      glds16x4(o2, o2 + 8 * (D * 2) - 1024, o2 + 16 * (D * 2) - 2048, o2 + 24 * (D * 2) - 3072, src, g2_place(j, i));
    }
    dflag_set(L_FED + j * 4, 6);
    if (BPOLL ? wid == 2 : wid >= 2) {
      lflag_wait(L_PUB, 3, 1);
      const int e = lane < 28 ? lane : 27;
      gflag_wait<4>(ws + WS_FH + (tau * 56 + 4 * (e >> 1) + (e & 1) + 2 * (3 - wid)) * 128, tag);
      dflag_set(L_H + (3 - wid) * 4, 1);
    }
    WAIT_VM(0);
    const u32 wl = opq(lane * 16 + j * 12288);
#pragma unroll
    for (int i = 0; i < 8; i += 2) {
      lflag_wait(L_PROG + j * 4, 0, i + 2);
#pragma unroll
      for (int n = 0; n < 8; ++n) lds_st128(wl + g2_place(0, (i + (n >> 2)) % 6) + (n & 3) * 1024, pk[i + (n >> 2)][n & 3]);
      lflag_set(L_FED + j * 4, i + 8);
    }
    return;
  }
  const rsrc_t rs = ws_rsrc(ws);

  if (b >= 224) {
    if (wid >= 8 && b != 255) return;
    const int tid = (int)opq(threadIdx.x), lane = tid & 63;
    const int sp = b - 224;
    const u64 seed = gload64(ws + WS_E + opq(0));
    if (wid < 8) {
      const int row = 64 * sp + (tid >> 3), c8 = tid & 7;
      const int cu = sp < 28 ? sp : 27, mat = cu / 14, ct = cu % 14, kg = tid >> 4, c16 = tid & 15;
      const char* wp = (mat ? w3 : w1) + ct * 256;
      const u32 wo = (u32)(kg * (F * 2) + c16 * 16), xo = (u32)(row * (D * 2) + c8 * 16);
      u32x4 xv[7], wv[14];
#pragma unroll
      for (int i = 0; i < 7; ++i) xv[i] = *(const u32x4*)(x + (xo + i * 128));
#pragma unroll
      for (int i = 0; i < 14; ++i) wv[i] = *(const u32x4*)(wp + (wo + i * 32 * (F * 2)));
      SCHED0();
      float s = 0.f;
#pragma unroll
      for (int i = 0; i < 7; ++i)
#pragma unroll
        for (int j = 0; j < 4; ++j) s += bflo(xv[i][j]) + bfhi(xv[i][j]);
      const float m = sum8(s) * (1.0f / D);
      float q = 0.f;
#pragma unroll
      for (int i = 0; i < 7; ++i)
#pragma unroll
        for (int j = 0; j < 4; ++j) {
          const float aa = bflo(xv[i][j]) - m, bb = bfhi(xv[i][j]) - m;
          q = __builtin_fmaf(aa, aa, __builtin_fmaf(bb, bb, q));
        }
      const float sd = rbf(__builtin_amdgcn_sqrtf(rbf(rbf(sum8(q) * (1.0f / D)) + 1e-5f)));
      if (c8 == 0) *(LDSP(f32x2))(u32)(16384 + (tid >> 3) * 8) = f32x2{rbf(m), __builtin_amdgcn_rcpf(sd)};
      SCHED0();
      float a[8] = {};
#pragma unroll
      for (int i = 0; i < 14; ++i)
#pragma unroll
        for (int j = 0; j < 4; ++j) { a[2 * j] += bflo(wv[i][j]); a[2 * j + 1] += bfhi(wv[i][j]); }
      f32x4 t;
#pragma unroll
      for (int j = 0; j < 4; ++j) {
        auto p = __builtin_amdgcn_permlane16_swap(__builtin_bit_cast(u32, a[j]), __builtin_bit_cast(u32, a[j + 4]), false, false);
        t[j] = asf(p[0]) + asf(p[1]);
      }
      *(LDSP(f32x4))(u32)((kg >> 1) * 512 + c16 * 32 + (kg & 1) * 16) = t;
    }
    WAIT_LGKM0(); BAR();
    const u64 tag = tag_of(seed);
    if (wid == 0) {
      const int cu = sp < 28 ? sp : 27;
      f32x2 cs = {};
#pragma unroll
      for (int i = 0; i < 16; ++i) cs += *(LDSP(f32x2))(u32)(i * 512 + lane * 8);
      st64_dev(rs, WS_STAT + (u32)(64 * sp + lane) * 8, *(LDSP(u32x2))(u32)(16384 + lane * 8));
      if (sp < 28) st64_dev(rs, WS_CSUM + (u32)((cu % 14) * 1024 + (cu / 14) * 512 + lane * 8), __builtin_bit_cast(u32x2, cs));
      gflag_publish(ws + WS_FST + sp * 128, tag, lane);
    }
    WAIT_VM(0); BAR();
    if (SPPF) {
      if (tid < 392) touch((u32)(sp * 392 + tid) * 128, w2, L_DUMMY);
      if (tid < 448) touch((u32)(sp * 448 + tid) * 128, ssmk, L_DUMMY);
      BAR();
    }
    if (W2PF && wid > 0 && wid < 8) {
#pragma unroll 1
      for (int i = 0; i < 7; ++i) touch((u32)((((sp >> 3) * 7 + i) * 7 + wid - 1) * 64 + lane) * 128, w2, L_DUMMY);
      WAIT_VM(0);
    }
    if (b == 255) {
      gflag_wait<64>(ws + (tid < 480 ? WS_FUT + tid * 128 : WS_FH + ((((tid < 704 ? tid - 480 : 223) / 14) * 2) * 56 + ((tid < 704 ? tid - 480 : 223) % 14) * 4) * 128), tag);
      BAR();
      if (tid == 0) gstore64(ws + WS_E, tag);
    }
    return;
  }

  const int cw = wid - 4, th = cw & 1, fq = cw >> 1;
  {
    const int lane = (int)opq(threadIdx.x) & 63, g = lane >> 4, hh = lane >> 5, l31 = lane & 31;
    const int q4 = (lane >> 2) & 3, p4 = lane & 3;
    const u32 aA = 16384 + (u32)((8 * hh + q4) * 256 + ((fq ^ q4) << 6) + (g & 1) * 32 + p4 * 8);
    const u32 aB = (u32)((64 * th + l31) * 128 + ((hh ^ ((l31 >> 1) & 7)) << 4));
    f32x16 acc[2][2] = {};
    s16x8 fa[2][2][2], fb[2][2][2];
    u32 pa, pb[4];
    auto setslot = [&](u32 slot) {
      pa = opq(aA + slot);
#pragma unroll
      for (int k = 0; k < 4; ++k) pb[k] = opq((aB ^ (u32)(k << 5)) + slot);
    };
    auto rd = [&](int set) {
#pragma unroll
      for (int k = 0; k < 2; ++k) {
        const int ks = 2 * set + k;
        fa[set][k][0] = lds_tr2(pa, ks * 4096, ks * 4096 + 1024);
        fb[set][k][0] = lds_b128(pb[ks], 0);
        fb[set][k][1] = lds_b128(pb[ks], 4096);
        fa[set][k][1] = lds_tr2(pa, 16384 + ks * 4096, 16384 + ks * 4096 + 1024);
      }
    };
    auto mm = [&](int set, int n0, int n1) {
#pragma unroll
      for (int n = n0; n < n1; ++n) acc[(n >> 1) & 1][n & 1] = mfma32(fa[set][n >> 2][(n >> 1) & 1], fb[set][n >> 2][n & 1], acc[(n >> 1) & 1][n & 1]);
    };
    setslot(0);
    const u32 fad = opq(L_FULL + (lane & 1) * 4);
#pragma unroll
    for (int i = 0; i < 4; ++i) asm volatile("" : "+v"(acc[i >> 1][i & 1]));
    __builtin_amdgcn_s_setprio(1);
    lflag_wait(L_FULL, 1, 1);
    rd(0);
    SCHED0();
    u32 slot = 0;
#pragma unroll 1
    for (int c = 0; c < 7; ++c) {
      const u32 fl = *(volatile LDSP(u32))fad;
      rd(1); mm(0, 0, 5);
      SGB(0x008, 1); SGB(0x100, 4); SGB(0x008, 1); SGB(0x100, 3); SGB(0x008, 1); SGB(0x100, 3); SGB(0x008, 1); SGB(0x100, 3); SGB(0x008, 1);
      SCHED0();
      if (c < 6 && __builtin_amdgcn_ballot_w64(fl < (u32)(c + 2))) lflag_wait(L_FULL, 1, c + 2);
      SCHED0();
      slot = slot == 2 * SLOT ? 0 : slot + SLOT;
      mm(0, 5, 6);
      SCHED0();
      setslot(slot);
      SCHED0();
      mm(0, 6, 8);
      SCHED0();
      if (c < 4) { WAIT_LGKM0(); BAR(); } else if (c == 6) lflag_set(L_FREE + cw * 4, 7);
      SCHED0();
      rd(0); mm(1, 0, 8);
#pragma unroll
      for (int i = 0; i < 4; ++i) { SGB(0x008, 1); SGB(0x100, 3); }
      SGB(0x008, 4);
      SCHED0();
    }
    lflag_wait(L_EPI, 0, 1);
    const u32 hdst = WS_H + (u32)((2 * panel + th) * 56 + 4 * ft + fq) * 4096 + lane * 16;
    float nm[2], c1[2], irs2[2];
#pragma unroll
    for (int j = 0; j < 2; ++j) {
      const f32x2 st = *(LDSP(f32x2))(L_STAT + (u32)(64 * th + 32 * j + l31) * 8);
      nm[j] = -st[0]; c1[j] = -1.44269504f * st[1]; irs2[j] = __builtin_amdgcn_rcpf(st[1] * st[1]);
    }
#pragma unroll
    for (int s = 0; s < 2; ++s) {
      f32x4 cs[2][2];
#pragma unroll
      for (int m = 0; m < 2; ++m)
#pragma unroll
        for (int G = 0; G < 2; ++G) cs[m][G] = *(LDSP(f32x4))(L_CSUM + (u32)(m * 128 + 32 * fq + 16 * s + 8 * G + 4 * hh) * 4);
#pragma unroll
      for (int j = 0; j < 2; ++j) {
        float a[8], ex[8], hv[8];
#pragma unroll
        for (int e = 0; e < 8; ++e) a[e] = __builtin_fmaf(nm[j], cs[0][e >> 2][e & 3], acc[0][j][8 * s + e]);
#pragma unroll
        for (int e = 0; e < 8; ++e) ex[e] = a[e] * c1[j];
        SCHED0();
#pragma unroll
        for (int e = 0; e < 8; ++e) { ex[e] = __builtin_amdgcn_exp2f(ex[e]); hv[e] = __builtin_fmaf(nm[j], cs[1][e >> 2][e & 3], acc[1][j][8 * s + e]); }
        SCHED0();
#pragma unroll
        for (int e = 0; e < 8; ++e) ex[e] = __builtin_fmaf(ex[e], irs2[j], irs2[j]);
        SCHED0();
#pragma unroll
        for (int e = 0; e < 8; ++e) { ex[e] = __builtin_amdgcn_rcpf(ex[e]); hv[e] *= a[e]; }
        SCHED0();
#pragma unroll
        for (int e = 0; e < 8; ++e) hv[e] *= ex[e];
        st128_dev(rs, hdst + (s * 2 + j) * 1024, u32x4{pk2(hv[0], hv[1]), pk2(hv[2], hv[3]), pk2(hv[4], hv[5]), pk2(hv[6], hv[7])});
      }
    }
  }
  char* hflag = ws + WS_FH + ((2 * panel + th) * 56 + 4 * ft + fq) * 128;
  u32x4 xv, sv;
  if (cw < 4) {
    const int ht = (int)opq(threadIdx.x) & 255, lane = ht & 63, l15 = ht & 15;
    const u64 tag = tag_of(*(volatile LDSP(u64))L_TAG);
    const int ch = cw >> 1, var = cw & 1;
    gflag_publish(hflag, tag, lane);
    u32x4 xr[2]; u32x2 st[2];
#pragma unroll
    for (int i = 0; i < 2; ++i) {
      const int n = ht + 256 * i, row = n >> 3, c = n & 7;
      xr[i] = *(const u32x4*)(x + (u32)((64 * tau + row) * (D * 2) + (64 * dl + 8 * c) * 2));
      st[i] = ld64_dev(rs, WS_STAT + (u32)(64 * tau + row) * 8);
    }
    SCHED0();
    lflag_wait(L_CONV, 3, 1);
    asm volatile("" ::: "memory");
    conv_compute(lane, cw, d0 + ch, rs, hflag, tag, false);
    u32x4 xn[2];
#pragma unroll
    for (int i = 0; i < 2; ++i) {
      const float mb = asf(st[i][0]), rsd = asf(st[i][1]);
#pragma unroll
      for (int e = 0; e < 4; ++e) {
        const u32 dd = pk2(bflo(xr[i][e]) - mb, bfhi(xr[i][e]) - mb);
        xn[i][e] = pk2(bflo(dd) * rsd, bfhi(dd) * rsd);
      }
    }
    SCHED0();
    gflag_publish(ws + WS_FY + (var * 448 + d0 + ch) * 128, tag, lane);
    xv = xr[0]; sv = xn[0];
    lflag_wait(L_XCH, 1, 1);
    lds_st128(E_S + (u32)ht * 16, xn[1]); lds_st128(E_X + (u32)ht * 16, xr[1]);
    gflag_wait<4>(ws + WS_FY + ((tau >> 4) * 448 + 64 * dl + 16 * cw + l15) * 128, tag);
#pragma unroll
    for (int i = 0; i < 2; ++i)
      glds16_dev((u32)((lane >> 3) * (S * 2) + (((lane & 7) ^ (2 * cw + i)) << 4)),
                 ws + WS_YT + (64 * dl + 16 * cw + 8 * i) * (S * 2) + 128 * tau, xbuf(cw ^ 1) + (u32)i * 1024);
    WAIT_VM(0);
  } else {
    const int j = cw - 4;
    const int lane = (int)opq(threadIdx.x) & 63;
    lflag_set(L_PUB + j * 4, 1);
    const u64 btag = tag_of(*(volatile LDSP(u64))L_TAG);
    gflag_publish(hflag, btag, lane);
    const u32 hl = lane * 16;
    const char* hbase = ws + WS_H + (size_t)tau * 56 * 4096;
    const int q4 = (lane >> 2) & 3;
    const u32 ta = (u32)((4 * (lane >> 5) + q4) * 128 + ((q4 >> 1) << 6) + ((lane >> 4) & 1) * 32 + (lane & 3) * 8);
    s16x8 fa[2][4], fb[HDEPTH][4];
    f32x16 acc[2][2] = {};
    auto ldh = [&](int i) {
      const char* p = hbase + (size_t)g2_ks(j, i, rot2) * 4096;
      asm volatile("global_load_dwordx4 %0, %4, %5 sc1\n\tglobal_load_dwordx4 %1, %4, %5 offset:1024 sc1\n\t"
                   "global_load_dwordx4 %2, %4, %5 offset:2048 sc1\n\tglobal_load_dwordx4 %3, %4, %5 offset:3072 sc1"
                   : "=&v"(fb[i % HDEPTH][0]), "=&v"(fb[i % HDEPTH][1]), "=&v"(fb[i % HDEPTH][2]), "=&v"(fb[i % HDEPTH][3])
                   : "v"(hl), "s"(p) : "memory");
    };
    u32 tb[2];
#pragma unroll
    for (int a = 0; a < 2; ++a) tb[a] = opq((ta ^ (u32)(a << 6)) + g2_place(j, 0));
    auto rda = [&](int i) {
#pragma unroll
      for (int a = 0; a < 2; ++a)
        if (i > 0 && i % 3 == 0) tb[a] = opq(i % 6 ? tb[a] - 2 * SLOT : tb[a] + 2 * SLOT);
#pragma unroll
      for (int n = 0; n < 4; ++n) {
        const int o = (i % 3) * 4096 + (n >> 1) * 2048;
        fa[i & 1][n] = lds_tr2(tb[n & 1], o, o + 1024);
      }
    };
    const u32 fed = opq(L_FED + j * 4), prog = opq(L_PROG + j * 4);
    if (BPOLL) { const int e = 7 * j + (lane < 7 ? lane : 6); gflag_wait<2>(ws + WS_FH + (tau * 56 + 4 * (e >> 1) + (e & 1)) * 128, btag); }
    else lflag_wait(L_H, 0, 1);
#pragma unroll
    for (int i = 0; i < HDEPTH; ++i) ldh(i);
    lflag_wait(L_FED + j * 4, 0, 3);
    rda(0);
    SCHED0();
    u32 fl = 0;
#pragma unroll
    for (int i = 0; i < 14; ++i) {
      if (i == 2 || (i >= 5 && i < 13 && (i & 1))) {
        if (__builtin_amdgcn_readfirstlane(fl) < i + 2) lflag_wait(L_FED + j * 4, 0, i + 2);
      }
      asm volatile("s_waitcnt vmcnt(%4)" : "+v"(fb[i % HDEPTH][0]), "+v"(fb[i % HDEPTH][1]), "+v"(fb[i % HDEPTH][2]), "+v"(fb[i % HDEPTH][3])
                   : "i"(i + HDEPTH <= 14 ? (HDEPTH - 1) * 4 : (13 - i) * 4) : "memory");
      SCHED0();
      if (i < 13) rda(i + 1);
      if (i == 1 || (i >= 4 && i < 12 && !(i & 1))) fl = *(volatile LDSP(u32))fed;
#pragma unroll
      for (int s = 0; s < 2; ++s)
#pragma unroll
        for (int a = 0; a < 2; ++a)
#pragma unroll
          for (int t = 0; t < 2; ++t) acc[a][t] = mfma32(fa[i & 1][2 * s + a], fb[i % HDEPTH][2 * s + t], acc[a][t]);
      if (i < 13) {
#pragma unroll
        for (int n = 0; n < 5; ++n) { SGB(0x008, 1); SGB(0x100, 2); }
        SGB(0x008, 3);
      }
      SCHED0();
      if (i < 8 && (i & 1)) *(volatile LDSP(u32))prog = i + 1;
      if (i == 7 - HDEPTH) { lflag_wait(L_H + 4, 0, 1); }
      if (i + HDEPTH < 14) ldh(i + HDEPTH);
      SCHED0();

    }
    const int ln = (int)opq(threadIdx.x), lt = ln & 31, lh = (ln >> 5) & 1;
#pragma unroll
    for (int t = 0; t < 2; ++t)
#pragma unroll
      for (int a = 0; a < 2; ++a)
#pragma unroll
        for (int G = 0; G < 4; ++G)
          *(LDSP(f32x4))(g2_place(j, t ? 3 : 0) + (u32)(lt * 256 + (((8 * a + 2 * G + lh) ^ (lt & 7)) << 4))) =
              f32x4{acc[a][t][4 * G], acc[a][t][4 * G + 1], acc[a][t][4 * G + 2], acc[a][t][4 * G + 3]};
  }
  WAIT_LGKM0();
  BAR();
  {
    const int ht = (int)opq(threadIdx.x) & 255;
    const int half = cw >> 2, tk = ht >> 3, dg = ht & 7;
    if (half) { sv = *(LDSP(u32x4))(E_S + (u32)ht * 16); xv = *(LDSP(u32x4))(E_X + (u32)ht * 16); }
    f32x4 s0 = {}, s1 = {};
#pragma unroll
    for (int w = 0; w < 4; ++w) {
      const u32 pb = (half ? 0u : 2 * SLOT) + (u32)w * 12288 + (u32)tk * 256;
      s0 += *(LDSP(f32x4))(pb + (u32)(((2 * dg) ^ (tk & 7)) << 4));
      s1 += *(LDSP(f32x4))(pb + (u32)(((2 * dg + 1) ^ (tk & 7)) << 4));
    }
    const int tok = 32 * half + tk;
    const u32 ya = xbuf((dg >> 1) ^ 1) + (u32)(8 * (dg & 1) * 128 + (((tok >> 3) ^ dg) << 4) + (tok & 7) * 2);
    float o[8];
#pragma unroll
    for (int e = 0; e < 8; ++e) {
      const float f = rbf(e < 4 ? s0[e & 3] : s1[e & 3]);
      const float gt = rbf(silu_f(f));
      const float xe = (e & 1) ? bfhi(xv[e >> 1]) : bflo(xv[e >> 1]);
      const float ne = (e & 1) ? bfhi(sv[e >> 1]) : bflo(sv[e >> 1]);
      const float se = rbf(ne * bflo(*(LDSP(u16))(ya + e * 128)));
      o[e] = xe + rbf(se * gt);
    }
    __builtin_amdgcn_raw_buffer_store_b128(u32x4{pk2(o[0], o[1]), pk2(o[2], o[3]), pk2(o[4], o[5]), pk2(o[6], o[7])},
        __builtin_amdgcn_make_buffer_rsrc(out, 0, S * D * 2, 0x00020000), (64 * tau + 32 * half + tk) * (D * 2) + (64 * dl + 8 * dg) * 2, 0, 16);
  }
}
}
#if defined(__HIP_DEVICE_COMPILE__) && __HIP_DEVICE_COMPILE__
#pragma clang attribute pop
#endif

extern "C" void kernel_launch(void* const* d_in, const int* in_sizes, int n_in, void* d_out, int out_size, void* d_ws,
                              size_t ws_size, hipStream_t stream) {
  (void)hipFuncSetAttribute((const void*)fused_kernel, hipFuncAttributeMaxDynamicSharedMemorySize, 163840);
  fused_kernel<<<256, 768, 163840, stream>>>((const char*)d_in[0], (const char*)d_in[1], (const char*)d_in[2], (const char*)d_in[3],
                                             (const char*)d_in[4], (const char*)d_in[5], (const char*)d_in[7], (const char*)d_in[8],
                                             (const char*)d_in[9], (char*)d_ws, (char*)d_out);
}
```

```cpp
#define KV_P2 0
#include <hip/hip_runtime.h>
#include <hip/hip_bf16.h>
#include <stdint.h>
#pragma clang diagnostic ignored "-Wint-to-pointer-cast"

#if defined(__HIP_DEVICE_COMPILE__) && __HIP_DEVICE_COMPILE__
#pragma clang attribute push(__attribute__((target("no-packed-fp32-ops"))), apply_to = function)
#endif
namespace {
typedef unsigned short u16;
typedef unsigned int u32;
typedef unsigned long long u64;
typedef __attribute__((ext_vector_type(4))) short s16x4;
typedef __attribute__((ext_vector_type(8))) short s16x8;
typedef __attribute__((ext_vector_type(8))) __bf16 bf16x8;
typedef __attribute__((ext_vector_type(2))) __bf16 bf16x2;
typedef __attribute__((ext_vector_type(16))) float f32x16;
typedef __attribute__((ext_vector_type(4))) float f32x4;
typedef __attribute__((ext_vector_type(2))) float f32x2;
typedef __attribute__((ext_vector_type(4))) u32 u32x4;
typedef __attribute__((ext_vector_type(2))) u32 u32x2;
#define LDSP(T) __attribute__((address_space(3))) T*
#define GLBP(T) __attribute__((address_space(1))) T*
#define DEV __device__ __forceinline__

constexpr int S = 2048, D = 448, F = 1792;
constexpr u32 WS_H = 0;
constexpr u32 WS_UT = WS_H + 7340032;
constexpr u32 WS_YT = WS_UT + 1835008;
constexpr u32 WS_STAT = WS_YT + 1835008;
constexpr u32 WS_CSUM = WS_STAT + 16384;
constexpr u32 WS_E = WS_CSUM + 16384;
constexpr u32 WS_FUT = WS_E + 128;
constexpr u32 WS_FST = WS_FUT + 448 * 128;
constexpr u32 WS_FY = WS_FST + 32 * 128;
constexpr u32 WS_FH = WS_FY + 896 * 128;
constexpr u32 WS_END = WS_FH + 1792 * 128;
constexpr int CODE_LINES = 196;
constexpr u32 SLOT = 49152;
constexpr u32 MISC = 3 * SLOT;
constexpr u32 L_FULL = MISC, L_EPI = MISC + 8, L_H = MISC + 16, L_FREE = MISC + 32, L_CONV = MISC + 64, L_FED = MISC + 80,
              L_TAG = MISC + 96, L_DD = MISC + 104, L_ADONE = MISC + 112, L_PROG = MISC + 128, L_XCH = MISC + 144, L_PUB = MISC + 160, L_STAT = MISC + 256,
              L_CSUM = MISC + 1280, L_USCR = MISC + 4096, L_DUMMY = MISC + 12288;
constexpr u32 CV = SLOT, CV_HSTR = 4160, CV_U = CV + 8 * CV_HSTR, CV_USTR = 4608;
constexpr u32 E_S = SLOT, E_X = SLOT + 4096;
constexpr int SPIN_CAP = 1 << 18;
#define ROT1 0
#define ROT2 0
#define TOUCH2_AT 0
#define PACE_L 63
#define PACE_S 63
#define FREE_SET dflag_set
#define W2PF 0
#define KVPF 0
#define SHARES 1
#define CODEPF 0
#define W2_HOLD 0
#define PUB2 0
#define KV_LATE 1
#define SPPF 1
#define FLAGPF 1
#define MIDPF 1
#define BPOLL 1
#define HDEPTH 3
#define LSLEEP 1
#define KV_AT 1

__device__ const u32 seq_tab[160] __attribute__((aligned(128))) = {0, 1, 2, 3, 4, 5, 6, 7};
DEV float asf(u32 v) { return __builtin_bit_cast(float, v); }
DEV float bflo(u32 v) { return asf(v << 16); }
DEV float bfhi(u32 v) { return asf(v & 0xffff0000u); }
template <int CTRL> DEV float dpp(float v) { return asf(__builtin_amdgcn_update_dpp(0, __builtin_bit_cast(u32, v), CTRL, 0xf, 0xf, true)); }
DEV float sum8(float v) { v += dpp<0xB1>(v); v += dpp<0x4E>(v); return v + dpp<0x141>(v); }
DEV u32 pk2(float lo, float hi) {
  asm("" : "+v"(lo)); asm("" : "+v"(hi));
  bf16x2 r = {(__bf16)lo, (__bf16)hi};
  return __builtin_bit_cast(u32, r);
}
DEV float rbf(float f) { bf16x2 r = {(__bf16)f, (__bf16)f}; return bflo(__builtin_bit_cast(u32, r)); }
DEV f32x4 mfma16(s16x8 a, s16x8 b, f32x4 c) {
  return __builtin_amdgcn_mfma_f32_16x16x32_bf16(__builtin_bit_cast(bf16x8, a), __builtin_bit_cast(bf16x8, b), c, 0, 0, 0);
}
DEV f32x16 mfma32(s16x8 a, s16x8 b, f32x16 c) {
  return __builtin_amdgcn_mfma_f32_32x32x16_bf16(__builtin_bit_cast(bf16x8, a), __builtin_bit_cast(bf16x8, b), c, 0, 0, 0);
}
DEV u32 opq(u32 v) { asm volatile("" : "+v"(v)); return v; }
DEV s16x8 lds_tr2(u32 base, int o0, int o1) {
  s16x4 lo = __builtin_amdgcn_ds_read_tr16_b64_v4i16((LDSP(s16x4))((LDSP(char))base + o0));
  s16x4 hi = __builtin_amdgcn_ds_read_tr16_b64_v4i16((LDSP(s16x4))((LDSP(char))base + o1));
  return __builtin_shufflevector(lo, hi, 0, 1, 2, 3, 4, 5, 6, 7);
}
DEV s16x8 lds_b128(u32 base, int o) { return *(LDSP(s16x8))((LDSP(char))base + o); }
DEV void lds_st128(u32 a, u32x4 v) { *(LDSP(u32x4))a = v; }
DEV void glds16x4(u32 v0, u32 v1, u32 v2, u32 v3, const void* sbase, u32 ldst) {
  asm volatile("s_mov_b32 m0, %5\n\ts_nop 0\n\tglobal_load_lds_dwordx4 %0, %4\n\tglobal_load_lds_dwordx4 %1, %4 offset:1024\n\t"
               "global_load_lds_dwordx4 %2, %4 offset:2048\n\tglobal_load_lds_dwordx4 %3, %4 offset:3072"
               ::"v"(v0), "v"(v1), "v"(v2), "v"(v3), "s"(sbase), "s"(ldst) : "memory");
}
DEV void glds16(u32 voff, const void* sbase, u32 ldst) {
  asm volatile("s_mov_b32 m0, %2\n\ts_nop 0\n\tglobal_load_lds_dwordx4 %0, %1" ::"v"(voff), "s"(sbase), "s"(ldst) : "memory");
}
DEV void glds16x4_lin(u32 v, const void* sbase, u32 ldst) { glds16x4(v, v, v, v, sbase, ldst); }
DEV void glds16x4_dev(u32 v, const void* sbase, u32 ldst) {
  asm volatile("s_mov_b32 m0, %2\n\ts_nop 0\n\tglobal_load_lds_dwordx4 %0, %1 sc1\n\tglobal_load_lds_dwordx4 %0, %1 offset:1024 sc1\n\t"
               "global_load_lds_dwordx4 %0, %1 offset:2048 sc1\n\tglobal_load_lds_dwordx4 %0, %1 offset:3072 sc1"
               ::"v"(v), "s"(sbase), "s"(ldst) : "memory");
}
DEV void glds16_dev(u32 voff, const void* sbase, u32 ldst) {
  asm volatile("s_mov_b32 m0, %2\n\ts_nop 0\n\tglobal_load_lds_dwordx4 %0, %1 sc1" ::"v"(voff), "s"(sbase), "s"(ldst) : "memory");
}
DEV void dflag_set(u32 addr, u32 val) {
  asm volatile("s_waitcnt lgkmcnt(0)\n\ts_mov_b32 m0, %2\n\ts_mov_b64 exec, 1\n\tglobal_load_lds_dword %0, %1\n\ts_mov_b64 exec, -1"
               ::"v"(0), "s"(seq_tab + val), "s"(addr) : "memory");
}
#define WAIT_VM(n) asm volatile("s_waitcnt vmcnt(%0)" ::"i"(n) : "memory")
#define WAIT_LGKM0() asm volatile("s_waitcnt lgkmcnt(0)" ::: "memory")
#define BAR() __builtin_amdgcn_s_barrier()
#define SCHED0() __builtin_amdgcn_sched_barrier(0)
#define SGB(mask, n) __builtin_amdgcn_sched_group_barrier(mask, n, 0)
DEV float silu_f(float x) { return x * __builtin_amdgcn_rcpf(1.0f + __builtin_amdgcn_exp2f(-1.44269504f * x)); }

DEV void lflag_set(u32 addr, u32 val) {
  asm volatile("s_waitcnt lgkmcnt(0)\n\tds_write_b32 %0, %1" ::"v"(addr), "v"(val) : "memory");
}
DEV void lflag_wait(u32 addr, u32 mask, u32 val) {
  const u32 a = addr + 4 * (threadIdx.x & mask);
#pragma unroll 1
  for (int i = 0; i < SPIN_CAP; ++i) {
    u32 v;
    asm volatile("ds_read_b32 %0, %1\n\ts_waitcnt lgkmcnt(0)" : "=v"(v) : "v"(a) : "memory");
    if (__builtin_amdgcn_ballot_w64(v < val) == 0) break;
    __builtin_amdgcn_s_sleep(LSLEEP);
  }
}
DEV u64 gload64(const char* p) { return __hip_atomic_load((GLBP(u64))p, __ATOMIC_RELAXED, __HIP_MEMORY_SCOPE_AGENT); }
DEV void gstore64(char* p, u64 v) { __hip_atomic_store((GLBP(u64))p, v, __ATOMIC_RELAXED, __HIP_MEMORY_SCOPE_AGENT); }
template <int SLEEP>
DEV void gflag_wait(const char* lane_ptr, u64 tag) {
  bool ok = false;
#pragma unroll 1
  for (int i = 0; i < SPIN_CAP; ++i) {
    asm volatile("" ::: "memory");
    if (!ok) ok = gload64(lane_ptr) == tag;
    if (__builtin_amdgcn_ballot_w64(!ok) == 0) break;
    __builtin_amdgcn_s_sleep(SLEEP);
  }
  asm volatile("" ::: "memory");
}
DEV void gflag_publish(char* p, u64 tag, int lane) {
  WAIT_VM(0);
  if (lane == 0) gstore64(p, tag);
}
DEV u64 tag_of(u64 seed) { return seed * 0x9E3779B97F4A7C15ull + 0xD1B54A32D192ED03ull; }
DEV u64 make_tag(const char* ws) { return tag_of(gload64(ws + WS_E)); }
typedef __amdgpu_buffer_rsrc_t rsrc_t;
DEV rsrc_t ws_rsrc(char* ws) { return __builtin_amdgcn_make_buffer_rsrc(ws, 0, WS_END, 0x00020000); }
DEV void st128_dev(rsrc_t r, u32 off, u32x4 v) { __builtin_amdgcn_raw_buffer_store_b128(v, r, off, 0, 16); }
DEV void st64_dev(rsrc_t r, u32 off, u32x2 v) { __builtin_amdgcn_raw_buffer_store_b64(v, r, off, 0, 16); }
DEV void st32_dev(rsrc_t r, u32 off, u32 v) { __builtin_amdgcn_raw_buffer_store_b32(v, r, off, 0, 16); }
DEV u32x4 ld128_dev(rsrc_t r, u32 off) { return __builtin_amdgcn_raw_buffer_load_b128(r, off, 0, 16); }
DEV u32x2 ld64_dev(rsrc_t r, u32 off) { return __builtin_amdgcn_raw_buffer_load_b64(r, off, 0, 16); }
DEV void touch(u32 voff, const void* sbase, u32 ldst) {
  asm volatile("s_mov_b32 m0, %2\n\ts_nop 0\n\tglobal_load_lds_dword %0, %1" ::"v"(voff), "s"(sbase), "s"(ldst) : "memory");
}

DEV void glds4_dev(u32 voff, const void* sbase, u32 ldst) {
  asm volatile("s_mov_b32 m0, %2\n\ts_nop 0\n\tglobal_load_lds_dword %0, %1 sc1" ::"v"(voff), "s"(sbase), "s"(ldst) : "memory");
}
DEV u32 shr16(u32 hi, u32 lo) { return __builtin_amdgcn_alignbit(hi, lo, 16); }

DEV u32 xbuf(int w) { return w < 2 ? L_USCR + (u32)w * 4096 : (w == 2 ? L_DUMMY : CV + 45056); }
DEV void conv_compute(int lane, int cw, int d, rsrc_t rs, char* hflag, u64 tag, bool pub) {
  const int ch = cw >> 1, var = cw & 1;
  const int m = lane & 15, g = lane >> 4, q4 = m >> 2, p4 = lane & 3;
  u32 aa = opq(CV + (u32)(ch * 4 + ((q4 + 1) & 3)) * CV_HSTR + (u32)(((q4 + 1) & 4) + 8 * g + 4 * p4) * 2 + var * 256);
  u32 ba = opq(CV_U + (u32)ch * CV_USTR + (u32)(240 - 16 * m + 8 * g) * 2 + var * 256);
  f32x4 acc[8] = {};
  s16x8 A[8], B[8];
#pragma unroll
  for (int p = 0; p < 8; ++p) { A[7 - p] = lds_tr2(aa, (7 - p) * 512, (7 - p) * 512 + 8); B[p] = lds_b128(ba, (7 - p) * 512); }
  if (var == 0) {
    const u32 a0 = CV + (u32)ch * 4 * CV_HSTR + (u32)(m + 1 + 8 * g) * 2;
#pragma unroll
    for (int j = 0; j < 8; ++j) { const short v = *(LDSP(short))(a0 + j * 2); A[0][j] = m + 8 * g + j >= 15 ? v : (short)0; }
  }
  SCHED0();
#pragma unroll 1
  for (int it = 0; it < 4; ++it) {
#pragma unroll
    for (int p = 7; p >= 0; --p) {
#pragma unroll
      for (int dd = 0; dd <= 7 - p; ++dd) {
        acc[p + dd] = mfma16(A[p], B[dd], acc[p + dd]);
        if (p == 0) { SCHED0(); B[dd] = lds_b128(ba, 64 + (7 - dd) * 512); SCHED0(); }
      }
      SCHED0();
      A[p] = lds_tr2(aa, 64 + p * 512, 64 + p * 512 + 8);
      SCHED0();
    }
    aa = opq(aa + 64); ba = opq(ba + 64);
    if (it == 0 && pub) gflag_publish(hflag, tag, lane);
  }
  f32x4 y[4];
#pragma unroll
  for (int i = 0; i < 4; ++i) {
    *(LDSP(f32x4))(xbuf(cw) + (u32)(i * 1024 + lane * 16)) = var ? acc[i] : acc[4 + i];
    y[i] = var ? acc[4 + i] : acc[i];
  }
  lflag_set(L_XCH + cw * 4, 1);
  const u32 dv = *(LDSP(u32))L_DD;
  const float Dd = ch ? bfhi(dv) : bflo(dv);
  u32x2 uu[4];
#pragma unroll
  for (int i = 0; i < 4; ++i) uu[i] = *(LDSP(u32x2))(CV_U + (u32)ch * CV_USTR + (u32)(2044 - (1024 * var + 256 * i + 16 * m + 4 * g)) * 2);
  lflag_wait(L_XCH + (cw ^ 1) * 4, 0, 1);
#pragma unroll
  for (int i = 0; i < 4; ++i) {
    const int t = 1024 * var + 256 * i + 16 * m + 4 * g;
    const f32x4 s = y[i] + *(LDSP(f32x4))(xbuf(cw ^ 1) + (u32)(i * 1024 + lane * 16));
    float y0 = rbf(s[0]) + rbf(bfhi(uu[i][1]) * Dd), y1 = rbf(s[1]) + rbf(bflo(uu[i][1]) * Dd);
    float y2 = rbf(s[2]) + rbf(bfhi(uu[i][0]) * Dd), y3 = rbf(s[3]) + rbf(bflo(uu[i][0]) * Dd);
    st64_dev(rs, WS_YT + (u32)(d * (S * 2) + t * 2), u32x2{pk2(y0, y1), pk2(y2, y3)});
  }
}

DEV int rot7(int i, int rot) { const int v = i + rot; return v >= 7 ? v - 7 : v; }
DEV int g2_ks(int j, int i, int rot) { const int e = 7 * j + rot7(i < 7 ? i : i - 7, rot); return 4 * (e >> 1) + (e & 1) + (i < 7 ? 0 : 2); }
DEV u32 g2_place(int j, int p) { return (p < 3 ? 2 * SLOT + (u32)p * 4096 : (u32)(p - 3) * 4096) + (u32)j * 12288; }
DEV u32 w2_voff(int lane) { return (u32)((lane >> 3) * (D * 2) + (((lane & 7) ^ (((lane >> 4) & 1) << 2)) << 4)); }

DEV void share(int b, int n, const char* base) {
  const int lane = (int)opq(threadIdx.x) & 63;
  if (lane < n) touch((u32)(b * n + lane) * 128, base, L_DUMMY);
}

__global__ __launch_bounds__(768, 3) void fused_kernel(const char* x, const char* kp, const char* vp, const char* skp,
                                                       const char* ssmk, const char* ssmD, const char* w1, const char* w3,
                                                       const char* w2, char* ws, char* out) {
  u64 pc0;
  asm volatile("s_getpc_b64 %0" : "=s"(pc0));
  const int b = blockIdx.x, xcd = b & 7, r = b >> 3;
  const int wid = __builtin_amdgcn_readfirstlane(threadIdx.x >> 6);
  const int panel = (xcd >> 1) * 4 + r / 7, ft = (xcd & 1) * 7 + r % 7;
  const int tau = xcd * 4 + r / 7, dl = r % 7;
  const int d0 = 2 * b;
  const int rot = ROT1 ? (r % 7 + 2 * (r / 7)) % 7 : 0, rot2 = ROT2 ? (r % 7 + 2 * (r / 7)) % 7 : 0;

  if (b < 224) {
    if (SHARES && wid >= 4 && wid < 7) share(b, wid == 4 ? 64 : 56, wid == 4 ? x : (wid == 5 ? w1 : w3));
    if (wid == 7) { *(LDSP(u32))(MISC + (threadIdx.x & 63) * 4) = 0; WAIT_LGKM0(); }
    BAR();
    if (wid == 7 && (threadIdx.x & 63) < 5) touch((threadIdx.x & 63) * 128, seq_tab, L_DUMMY);
    if (CODEPF && wid == 11 && (threadIdx.x & 63) < CODE_LINES / 28) touch((u32)(r * (CODE_LINES / 28) + (threadIdx.x & 63)) * 128, (const char*)pc0, L_DUMMY);
  }

  if (__builtin_expect(wid < 4 && b < 224, 1)) {
    const int lane = (int)opq(threadIdx.x) & 63;
    u64 tag = 0;
    if (__builtin_expect(wid < 2, 1)) {
      const int g = lane >> 4, l15 = lane & 15;
      u32 vx[8], vw[8];
      const u32 ox = (u32)((lane >> 3) * (D * 2) + (((lane & 7) ^ g) << 4));
      const u32 ow = (u32)(g * (F * 2) + (((l15 >> 2) ^ g) << 6) + ((lane & 3) << 4));
#pragma unroll
      for (int i = 0; i < 8; ++i) {
        vx[i] = (ox ^ (u32)((i & 1) * 64)) + i * 8 * (D * 2) - (i & 3) * 1024;
        vw[i] = ow + i * 4 * (F * 2) - (i & 3) * 1024;
      }
#pragma unroll 1
      for (int c = 0; c < 7; ++c) {
        if (c >= 3) BAR();
        const u32 slot = (u32)(c % 3) * SLOT + wid * 8192;
        const int kc = rot7(c, rot);
        const char* xb = x + (size_t)(panel * 128 + wid * 64) * (D * 2) + kc * 128;
        const char* w1b = w1 + (size_t)(kc * 64 + wid * 32) * (F * 2) + ft * 256;
        const char* w3b = w3 + (size_t)(kc * 64 + wid * 32) * (F * 2) + ft * 256;
#pragma unroll
        for (int i = 0; i < 8; i += 4) {
          WAIT_VM(PACE_L);
          glds16x4(vx[i], vx[i + 1], vx[i + 2], vx[i + 3], xb, slot + i * 1024);
          WAIT_VM(PACE_L);
          glds16x4(vw[i], vw[i + 1], vw[i + 2], vw[i + 3], w1b, slot + 16384 + i * 1024);
          WAIT_VM(PACE_L);
          glds16x4(vw[i], vw[i + 1], vw[i + 2], vw[i + 3], w3b, slot + 32768 + i * 1024);

        }
        dflag_set(L_FULL + wid * 4, c + 1);
      }
      if (wid == 0) {
        tag = make_tag(ws);
        const int k = lane & 7;
        gflag_wait<8>(ws + WS_FST + (k < 2 ? 2 * panel + k : (k == 2 ? tau : (k & 1) * 14 + ft)) * 128, tag);
        glds16_dev(lane * 16, ws + WS_STAT + panel * 1024, L_STAT);
        glds16_dev(lane * 16, ws + WS_CSUM + ft * 1024, L_CSUM);
        if (lane < 2) glds4_dev(lane * 4, ws + WS_E, L_TAG);
        if (lane < 1) glds4_dev(0, ssmD + d0 * 2, L_DD);
        dflag_set(L_EPI, 1);
      }
    } else {
      __builtin_amdgcn_s_setprio(3);
      const rsrc_t rs = ws_rsrc(ws);
      const int u = wid - 2;
      const int ti = b / 7, cj = b % 7;
      const int tg = lane >> 3, cg = lane & 7;
      const int t0 = 64 * ti + 32 * u + 4 * tg, ch0 = 64 * cj + 8 * cg;
      u32x4 kr[7], vr[4], sw[4]; u32x2 o[8];
      u64 seed;
      auto ldkv = [&]() {
        const u32 ko = (u32)(t0 * (D * 2) + ch0 * 2);
#pragma unroll
        for (int i = 0; i < 7; ++i) kr[i] = *(const u32x4*)(kp + (i < 3 && t0 == 0 ? ko : ko + (u32)((i - 3) * (D * 2))));
#pragma unroll
        for (int i = 0; i < 4; ++i) vr[i] = *(const u32x4*)(vp + (ko + (u32)(i * (D * 2))));
        SCHED0();
      };
      auto piece = [&](int p) {
#pragma unroll
        for (int c = 2 * p; c < 2 * p + 2; ++c) {
          const u32 wa = sw[p][(c & 1) * 2], wb = sw[p][(c & 1) * 2 + 1];
          const float w0 = bflo(wa), w1f = bfhi(wa), w2f = bflo(wb), w3f = bfhi(wb);
          float kf[7], ks[4];
#pragma unroll
          for (int i = 0; i < 7; ++i) kf[i] = (c & 1) ? bfhi(kr[i][p]) : bflo(kr[i][p]);
#pragma unroll
          for (int i = 0; i < 4; ++i)
            ks[i] = __builtin_fmaf(kf[i + 3], w0, __builtin_fmaf(kf[i + 2], w1f, __builtin_fmaf(kf[i + 1], w2f, kf[i] * w3f)));
#pragma unroll
          for (int i = 0; i < 2; ++i) {
            const int ta = 3 - 2 * i, tb = 2 - 2 * i;
            const u32 kk = pk2(ks[ta], ks[tb]);
            o[c][i] = pk2(bflo(kk) * ((c & 1) ? bfhi(vr[ta][p]) : bflo(vr[ta][p])), bfhi(kk) * ((c & 1) ? bfhi(vr[tb][p]) : bflo(vr[tb][p])));
          }
          asm volatile("" : "+v"(o[c]));
        }
        SCHED0();
      };
      BAR();
      ldkv();
#pragma unroll
      for (int i = 0; i < 4; ++i) sw[i] = *(const u32x4*)(skp + (u32)(ch0 * 8 + i * 16));
      seed = gload64(ws + WS_E + opq(0));
      if (FLAGPF && lane < 14) {
        const u32 fo = lane < 8 ? WS_FH + (u32)((2 * panel + (lane & 1)) * 56 + 4 * ft + (lane >> 1)) * 128
                                : (lane < 12 ? WS_FY + (u32)((lane & 1) * 448 + d0 + ((lane >> 1) & 1)) * 128 : WS_FUT + (u32)(cj * 64 + 2 * ti + (lane & 1)) * 128);
        if (u) touch(fo, ws, L_DUMMY);
      }
      SCHED0();
      BAR();
      SCHED0();
      if (KV_P2) {
#pragma unroll
        for (int i = 0; i < 3; ++i) if (t0 == 0) kr[i] = u32x4{0, 0, 0, 0};
        piece(0);
      }
      BAR();
      SCHED0();
      if (!KV_P2) {
#pragma unroll
        for (int i = 0; i < 3; ++i) if (t0 == 0) kr[i] = u32x4{0, 0, 0, 0};
      }
      piece(KV_P2);
      BAR();
      SCHED0();
#pragma unroll
      for (int c = 1 + KV_P2; c < 4; ++c) piece(c);
      tag = tag_of(seed);
      const u32 scr = L_USCR + (u32)u * 4096;
#pragma unroll
      for (int c = 0; c < 8; ++c) *(LDSP(u32x2))(scr + (u32)((cg * 8 + c) * 64 + (((7 - tg) ^ (cg & 6)) << 3))) = o[c];
      WAIT_LGKM0();
#pragma unroll
      for (int i = 0; i < 4; ++i) {
        const int dd = 16 * i + (lane >> 2), c4 = lane & 3;
        u32x4 v = *(LDSP(u32x4))(scr + (u32)(dd * 64 + ((c4 ^ (dd >> 4)) << 4)));
        st128_dev(rs, WS_UT + (u32)((64 * cj + dd) * (S * 2) + (2016 - 64 * ti - 32 * u) * 2 + c4 * 16), v);
      }
      gflag_publish(ws + WS_FUT + (cj * 64 + 2 * ti + u) * 128, tag, lane);
    }
    const int j = wid;
    const u32 o2 = w2_voff(lane);
    const char* wbase = w2 + dl * 128;
    u32x4 pk[8][4];
    auto park = [&](int i0) {
#pragma unroll
    for (int i = i0; i < i0 + 4; ++i) {
      const char* src = wbase + (size_t)g2_ks(j, i + 6, rot2) * 32 * (D * 2);
      WAIT_VM(PACE_S);
#pragma unroll
      for (int pc = 0; pc < 4; ++pc) pk[i][pc] = *(const u32x4*)(src + (o2 + (u32)(pc * 8 * (D * 2))));
    }
    SCHED0();
    };
    park(0);
    park(4);
    lflag_wait(L_FREE, 7, 7);
    if (wid == 2) {
        if (lane < 32) {
          glds16(lane * 16, seq_tab + 32, CV_U + 4096);
          glds16(lane * 16, seq_tab + 32, CV_U + CV_USTR + 4096);
        }
        if (KV_LATE) gflag_wait<8>(ws + WS_FUT + ((d0 >> 6) * 64 + lane) * 128, tag);
        glds16x4_dev(lane * 16, ws + WS_UT + d0 * (S * 2), CV_U);
        glds16x4_dev(lane * 16, ws + WS_UT + (d0 + 1) * (S * 2), CV_U + CV_USTR);
        dflag_set(L_CONV + 8, 1);
        dflag_set(L_CONV + 12, 1);
      }
    if (wid < 2) {
      const u32 vl = lane * 16;
#pragma unroll 1
      for (int rr = 0; rr < 4; ++rr) {
        const char* src = ssmk + (size_t)(d0 + wid) * (S * 2) + (rr ? (rr - 4) * 2 : 0);
        const u32 dst = CV + (u32)(wid * 4 + rr) * CV_HSTR + (rr ? 24 : 32);
        glds16x4(rr && !lane ? 16u : vl, vl, vl, vl, src, dst);
      }
      dflag_set(L_CONV + wid * 4, 1);
    }
#pragma unroll 1
    for (int i = 0; i < 6; ++i) {
      if (i == W2_HOLD) lflag_wait(L_PUB, 3, 1);
      if (i == 3) dflag_set(L_FED + j * 4, 3);
      const char* src = wbase + (size_t)g2_ks(j, i, rot2) * 32 * (D * 2);
      WAIT_VM(PACE_S);
      glds16x4(o2, o2 + 8 * (D * 2) - 1024, o2 + 16 * (D * 2) - 2048, o2 + 24 * (D * 2) - 3072, src, g2_place(j, i));
    }
    dflag_set(L_FED + j * 4, 6);
    if (BPOLL ? wid == 2 : wid >= 2) {
      lflag_wait(L_PUB, 3, 1);
      const int e = lane < 28 ? lane : 27;
      gflag_wait<4>(ws + WS_FH + (tau * 56 + 4 * (e >> 1) + (e & 1) + 2 * (3 - wid)) * 128, tag);
      dflag_set(L_H + (3 - wid) * 4, 1);
    }
    WAIT_VM(0);
    const u32 wl = opq(lane * 16 + j * 12288);
#pragma unroll
    for (int i = 0; i < 8; i += 2) {
      lflag_wait(L_PROG + j * 4, 0, i + 2);
#pragma unroll
      for (int n = 0; n < 8; ++n) lds_st128(wl + g2_place(0, (i + (n >> 2)) % 6) + (n & 3) * 1024, pk[i + (n >> 2)][n & 3]);
      lflag_set(L_FED + j * 4, i + 8);
    }
    return;
  }
  const rsrc_t rs = ws_rsrc(ws);

  if (b >= 224) {
    if (wid >= 8 && b != 255) return;
    const int tid = (int)opq(threadIdx.x), lane = tid & 63;
    const int sp = b - 224;
    const u64 seed = gload64(ws + WS_E + opq(0));
    if (wid < 8) {
      const int row = 64 * sp + (tid >> 3), c8 = tid & 7;
      const int cu = sp < 28 ? sp : 27, mat = cu / 14, ct = cu % 14, kg = tid >> 4, c16 = tid & 15;
      const char* wp = (mat ? w3 : w1) + ct * 256;
      const u32 wo = (u32)(kg * (F * 2) + c16 * 16), xo = (u32)(row * (D * 2) + c8 * 16);
      u32x4 xv[7], wv[14];
#pragma unroll
      for (int i = 0; i < 7; ++i) xv[i] = *(const u32x4*)(x + (xo + i * 128));
#pragma unroll
      for (int i = 0; i < 14; ++i) wv[i] = *(const u32x4*)(wp + (wo + i * 32 * (F * 2)));
      SCHED0();
      float s = 0.f;
#pragma unroll
      for (int i = 0; i < 7; ++i)
#pragma unroll
        for (int j = 0; j < 4; ++j) s += bflo(xv[i][j]) + bfhi(xv[i][j]);
      const float m = sum8(s) * (1.0f / D);
      float q = 0.f;
#pragma unroll
      for (int i = 0; i < 7; ++i)
#pragma unroll
        for (int j = 0; j < 4; ++j) {
          const float aa = bflo(xv[i][j]) - m, bb = bfhi(xv[i][j]) - m;
          q = __builtin_fmaf(aa, aa, __builtin_fmaf(bb, bb, q));
        }
      const float sd = rbf(__builtin_amdgcn_sqrtf(rbf(rbf(sum8(q) * (1.0f / D)) + 1e-5f)));
      if (c8 == 0) *(LDSP(f32x2))(u32)(16384 + (tid >> 3) * 8) = f32x2{rbf(m), __builtin_amdgcn_rcpf(sd)};
      SCHED0();
      float a[8] = {};
#pragma unroll
      for (int i = 0; i < 14; ++i)
#pragma unroll
        for (int j = 0; j < 4; ++j) { a[2 * j] += bflo(wv[i][j]); a[2 * j + 1] += bfhi(wv[i][j]); }
      f32x4 t;
#pragma unroll
      for (int j = 0; j < 4; ++j) {
        auto p = __builtin_amdgcn_permlane16_swap(__builtin_bit_cast(u32, a[j]), __builtin_bit_cast(u32, a[j + 4]), false, false);
        t[j] = asf(p[0]) + asf(p[1]);
      }
      *(LDSP(f32x4))(u32)((kg >> 1) * 512 + c16 * 32 + (kg & 1) * 16) = t;
    }
    WAIT_LGKM0(); BAR();
    const u64 tag = tag_of(seed);
    if (wid == 0) {
      const int cu = sp < 28 ? sp : 27;
      f32x2 cs = {};
#pragma unroll
      for (int i = 0; i < 16; ++i) cs += *(LDSP(f32x2))(u32)(i * 512 + lane * 8);
      st64_dev(rs, WS_STAT + (u32)(64 * sp + lane) * 8, *(LDSP(u32x2))(u32)(16384 + lane * 8));
      if (sp < 28) st64_dev(rs, WS_CSUM + (u32)((cu % 14) * 1024 + (cu / 14) * 512 + lane * 8), __builtin_bit_cast(u32x2, cs));
      gflag_publish(ws + WS_FST + sp * 128, tag, lane);
    }
    WAIT_VM(0); BAR();
    if (SPPF) {
      if (tid < 392) touch((u32)(sp * 392 + tid) * 128, w2, L_DUMMY);
      if (tid < 448) touch((u32)(sp * 448 + tid) * 128, ssmk, L_DUMMY);
      BAR();
    }
    if (W2PF && wid > 0 && wid < 8) {
#pragma unroll 1
      for (int i = 0; i < 7; ++i) touch((u32)((((sp >> 3) * 7 + i) * 7 + wid - 1) * 64 + lane) * 128, w2, L_DUMMY);
      WAIT_VM(0);
    }
    if (b == 255) {
      gflag_wait<64>(ws + (tid < 480 ? WS_FUT + tid * 128 : WS_FH + ((((tid < 704 ? tid - 480 : 223) / 14) * 2) * 56 + ((tid < 704 ? tid - 480 : 223) % 14) * 4) * 128), tag);
      BAR();
      if (tid == 0) gstore64(ws + WS_E, tag);
    }
    return;
  }

  const int cw = wid - 4, th = cw & 1, fq = cw >> 1;
  {
    const int lane = (int)opq(threadIdx.x) & 63, g = lane >> 4, hh = lane >> 5, l31 = lane & 31;
    const int q4 = (lane >> 2) & 3, p4 = lane & 3;
    const u32 aA = 16384 + (u32)((8 * hh + q4) * 256 + ((fq ^ q4) << 6) + (g & 1) * 32 + p4 * 8);
    const u32 aB = (u32)((64 * th + l31) * 128 + ((hh ^ ((l31 >> 1) & 7)) << 4));
    f32x16 acc[2][2] = {};
    s16x8 fa[2][2][2], fb[2][2][2];
    u32 pa, pb[4];
    auto setslot = [&](u32 slot) {
      pa = opq(aA + slot);
#pragma unroll
      for (int k = 0; k < 4; ++k) pb[k] = opq((aB ^ (u32)(k << 5)) + slot);
    };
    auto rd = [&](int set) {
#pragma unroll
      for (int k = 0; k < 2; ++k) {
        const int ks = 2 * set + k;
        fa[set][k][0] = lds_tr2(pa, ks * 4096, ks * 4096 + 1024);
        fb[set][k][0] = lds_b128(pb[ks], 0);
        fb[set][k][1] = lds_b128(pb[ks], 4096);
        fa[set][k][1] = lds_tr2(pa, 16384 + ks * 4096, 16384 + ks * 4096 + 1024);
      }
    };
    auto mm = [&](int set, int n0, int n1) {
#pragma unroll
      for (int n = n0; n < n1; ++n) acc[(n >> 1) & 1][n & 1] = mfma32(fa[set][n >> 2][(n >> 1) & 1], fb[set][n >> 2][n & 1], acc[(n >> 1) & 1][n & 1]);
    };
    setslot(0);
    const u32 fad = opq(L_FULL + (lane & 1) * 4);
#pragma unroll
    for (int i = 0; i < 4; ++i) asm volatile("" : "+v"(acc[i >> 1][i & 1]));
    __builtin_amdgcn_s_setprio(1);
    lflag_wait(L_FULL, 1, 1);
    rd(0);
    SCHED0();
    u32 slot = 0;
    auto stage = [&](int c, bool bar) {
      rd(1);
      const u32 fl = *(volatile LDSP(u32))fad;
      mm(0, 0, 6);
      SGB(0x008, 1); SGB(0x100, 3); SGB(0x008, 1); SGB(0x100, 3); SGB(0x008, 1); SGB(0x100, 3); SGB(0x008, 1); SGB(0x100, 4); SGB(0x008, 2);
      SCHED0();
      slot = slot == 2 * SLOT ? 0 : slot + SLOT;
      setslot(slot);
      SCHED0();
      mm(0, 6, 7);
      SCHED0();
      __builtin_amdgcn_s_waitcnt(0xC07F);
      const bool miss = c < 6 && __builtin_amdgcn_ballot_w64(fl < (u32)(c + 2));
      SCHED0();
      mm(0, 7, 8);
      SCHED0();
      if (bar) BAR();
      SCHED0();
      mm(1, 0, 2);
      SCHED0();
      if (!bar && c == 6) lflag_set(L_FREE + cw * 4, 7);
      if (miss) lflag_wait(L_FULL, 1, c + 2);
      SCHED0();
      rd(0); mm(1, 2, 8);
#pragma unroll
      for (int i = 0; i < 4; ++i) { SGB(0x008, 1); SGB(0x100, 3); }
      SGB(0x008, 2);
      SCHED0();
    };
#pragma unroll 1
    for (int c = 0; c < 4; ++c) stage(c, true);
#pragma unroll 1
    for (int c = 4; c < 7; ++c) stage(c, false);
    lflag_wait(L_EPI, 0, 1);
    const u32 hdst = WS_H + (u32)((2 * panel + th) * 56 + 4 * ft + fq) * 4096 + lane * 16;
    float nm[2], c1[2], irs2[2];
#pragma unroll
    for (int j = 0; j < 2; ++j) {
      const f32x2 st = *(LDSP(f32x2))(L_STAT + (u32)(64 * th + 32 * j + l31) * 8);
      nm[j] = -st[0]; c1[j] = -1.44269504f * st[1]; irs2[j] = __builtin_amdgcn_rcpf(st[1] * st[1]);
    }
#pragma unroll
    for (int s = 0; s < 2; ++s) {
      f32x4 cs[2][2];
#pragma unroll
      for (int m = 0; m < 2; ++m)
#pragma unroll
        for (int G = 0; G < 2; ++G) cs[m][G] = *(LDSP(f32x4))(L_CSUM + (u32)(m * 128 + 32 * fq + 16 * s + 8 * G + 4 * hh) * 4);
#pragma unroll
      for (int j = 0; j < 2; ++j) {
        float a[8], ex[8], hv[8];
#pragma unroll
        for (int e = 0; e < 8; ++e) a[e] = __builtin_fmaf(nm[j], cs[0][e >> 2][e & 3], acc[0][j][8 * s + e]);
#pragma unroll
        for (int e = 0; e < 8; ++e) ex[e] = a[e] * c1[j];
        SCHED0();
#pragma unroll
        for (int e = 0; e < 8; ++e) { ex[e] = __builtin_amdgcn_exp2f(ex[e]); hv[e] = __builtin_fmaf(nm[j], cs[1][e >> 2][e & 3], acc[1][j][8 * s + e]); }
        SCHED0();
#pragma unroll
        for (int e = 0; e < 8; ++e) ex[e] = __builtin_fmaf(ex[e], irs2[j], irs2[j]);
        SCHED0();
#pragma unroll
        for (int e = 0; e < 8; ++e) { ex[e] = __builtin_amdgcn_rcpf(ex[e]); hv[e] *= a[e]; }
        SCHED0();
#pragma unroll
        for (int e = 0; e < 8; ++e) hv[e] *= ex[e];
        st128_dev(rs, hdst + (s * 2 + j) * 1024, u32x4{pk2(hv[0], hv[1]), pk2(hv[2], hv[3]), pk2(hv[4], hv[5]), pk2(hv[6], hv[7])});
      }
    }
  }
  char* hflag = ws + WS_FH + ((2 * panel + th) * 56 + 4 * ft + fq) * 128;
  u32x4 xv, sv;
  if (cw < 4) {
    const int ht = (int)opq(threadIdx.x) & 255, lane = ht & 63, l15 = ht & 15;
    const u64 tag = tag_of(*(volatile LDSP(u64))L_TAG);
    const int ch = cw >> 1, var = cw & 1;
    gflag_publish(hflag, tag, lane);
    u32x4 xr[2]; u32x2 st[2];
#pragma unroll
    for (int i = 0; i < 2; ++i) {
      const int n = ht + 256 * i, row = n >> 3, c = n & 7;
      xr[i] = *(const u32x4*)(x + (u32)((64 * tau + row) * (D * 2) + (64 * dl + 8 * c) * 2));
      st[i] = ld64_dev(rs, WS_STAT + (u32)(64 * tau + row) * 8);
    }
    SCHED0();
    lflag_wait(L_CONV, 3, 1);
    asm volatile("" ::: "memory");
    conv_compute(lane, cw, d0 + ch, rs, hflag, tag, false);
    u32x4 xn[2];
#pragma unroll
    for (int i = 0; i < 2; ++i) {
      const float mb = asf(st[i][0]), rsd = asf(st[i][1]);
#pragma unroll
      for (int e = 0; e < 4; ++e) {
        const u32 dd = pk2(bflo(xr[i][e]) - mb, bfhi(xr[i][e]) - mb);
        xn[i][e] = pk2(bflo(dd) * rsd, bfhi(dd) * rsd);
      }
    }
    SCHED0();
    gflag_publish(ws + WS_FY + (var * 448 + d0 + ch) * 128, tag, lane);
    xv = xr[0]; sv = xn[0];
    lflag_wait(L_XCH, 1, 1);
    lds_st128(E_S + (u32)ht * 16, xn[1]); lds_st128(E_X + (u32)ht * 16, xr[1]);
    gflag_wait<4>(ws + WS_FY + ((tau >> 4) * 448 + 64 * dl + 16 * cw + l15) * 128, tag);
#pragma unroll
    for (int i = 0; i < 2; ++i)
      glds16_dev((u32)((lane >> 3) * (S * 2) + (((lane & 7) ^ (2 * cw + i)) << 4)),
                 ws + WS_YT + (64 * dl + 16 * cw + 8 * i) * (S * 2) + 128 * tau, xbuf(cw ^ 1) + (u32)i * 1024);
    WAIT_VM(0);
  } else {
    const int j = cw - 4;
    const int lane = (int)opq(threadIdx.x) & 63;
    lflag_set(L_PUB + j * 4, 1);
    const u64 btag = tag_of(*(volatile LDSP(u64))L_TAG);
    gflag_publish(hflag, btag, lane);
    const u32 hl = lane * 16;
    const char* hbase = ws + WS_H + (size_t)tau * 56 * 4096;
    const int q4 = (lane >> 2) & 3;
    const u32 ta = (u32)((4 * (lane >> 5) + q4) * 128 + ((q4 >> 1) << 6) + ((lane >> 4) & 1) * 32 + (lane & 3) * 8);
    s16x8 fa[2][4], fb[HDEPTH][4];
    f32x16 acc[2][2] = {};
    auto ldh = [&](int i) {
      const char* p = hbase + (size_t)g2_ks(j, i, rot2) * 4096;
      asm volatile("global_load_dwordx4 %0, %4, %5 sc1\n\tglobal_load_dwordx4 %1, %4, %5 offset:1024 sc1\n\t"
                   "global_load_dwordx4 %2, %4, %5 offset:2048 sc1\n\tglobal_load_dwordx4 %3, %4, %5 offset:3072 sc1"
                   : "=&v"(fb[i % HDEPTH][0]), "=&v"(fb[i % HDEPTH][1]), "=&v"(fb[i % HDEPTH][2]), "=&v"(fb[i % HDEPTH][3])
                   : "v"(hl), "s"(p) : "memory");
    };
    u32 tb[2];
#pragma unroll
    for (int a = 0; a < 2; ++a) tb[a] = opq((ta ^ (u32)(a << 6)) + g2_place(j, 0));
    auto rda = [&](int i) {
#pragma unroll
      for (int a = 0; a < 2; ++a)
        if (i > 0 && i % 3 == 0) tb[a] = opq(i % 6 ? tb[a] - 2 * SLOT : tb[a] + 2 * SLOT);
#pragma unroll
      for (int n = 0; n < 4; ++n) {
        const int o = (i % 3) * 4096 + (n >> 1) * 2048;
        fa[i & 1][n] = lds_tr2(tb[n & 1], o, o + 1024);
      }
    };
    const u32 fed = opq(L_FED + j * 4), prog = opq(L_PROG + j * 4);
    if (BPOLL) { const int e = 7 * j + (lane < 7 ? lane : 6); gflag_wait<2>(ws + WS_FH + (tau * 56 + 4 * (e >> 1) + (e & 1)) * 128, btag); }
    else lflag_wait(L_H, 0, 1);
#pragma unroll
    for (int i = 0; i < HDEPTH; ++i) ldh(i);
    lflag_wait(L_FED + j * 4, 0, 3);
    rda(0);
    SCHED0();
    u32 fl = 0;
#pragma unroll
    for (int i = 0; i < 14; ++i) {
      if (i == 2 || (i >= 5 && i < 13 && (i & 1))) {
        if (__builtin_amdgcn_readfirstlane(fl) < i + 2) lflag_wait(L_FED + j * 4, 0, i + 2);
      }
      asm volatile("s_waitcnt vmcnt(%4)" : "+v"(fb[i % HDEPTH][0]), "+v"(fb[i % HDEPTH][1]), "+v"(fb[i % HDEPTH][2]), "+v"(fb[i % HDEPTH][3])
                   : "i"(i + HDEPTH <= 14 ? (HDEPTH - 1) * 4 : (13 - i) * 4) : "memory");
      SCHED0();
      if (i < 13) rda(i + 1);
      if (i == 1 || (i >= 4 && i < 12 && !(i & 1))) fl = *(volatile LDSP(u32))fed;
#pragma unroll
      for (int s = 0; s < 2; ++s)
#pragma unroll
        for (int a = 0; a < 2; ++a)
#pragma unroll
          for (int t = 0; t < 2; ++t) acc[a][t] = mfma32(fa[i & 1][2 * s + a], fb[i % HDEPTH][2 * s + t], acc[a][t]);
      if (i < 13) {
#pragma unroll
        for (int n = 0; n < 5; ++n) { SGB(0x008, 1); SGB(0x100, 2); }
        SGB(0x008, 3);
      }
      SCHED0();
      if (i < 8 && (i & 1)) *(volatile LDSP(u32))prog = i + 1;
      if (i == 7 - HDEPTH) { lflag_wait(L_H + 4, 0, 1); }
      if (i + HDEPTH < 14) ldh(i + HDEPTH);
      SCHED0();

    }
    const int ln = (int)opq(threadIdx.x), lt = ln & 31, lh = (ln >> 5) & 1;
#pragma unroll
    for (int t = 0; t < 2; ++t)
#pragma unroll
      for (int a = 0; a < 2; ++a)
#pragma unroll
        for (int G = 0; G < 4; ++G)
          *(LDSP(f32x4))(g2_place(j, t ? 3 : 0) + (u32)(lt * 256 + (((8 * a + 2 * G + lh) ^ (lt & 7)) << 4))) =
              f32x4{acc[a][t][4 * G], acc[a][t][4 * G + 1], acc[a][t][4 * G + 2], acc[a][t][4 * G + 3]};
  }
  WAIT_LGKM0();
  BAR();
  {
    const int ht = (int)opq(threadIdx.x) & 255;
    const int half = cw >> 2, tk = ht >> 3, dg = ht & 7;
    if (half) { sv = *(LDSP(u32x4))(E_S + (u32)ht * 16); xv = *(LDSP(u32x4))(E_X + (u32)ht * 16); }
    f32x4 s0 = {}, s1 = {};
#pragma unroll
    for (int w = 0; w < 4; ++w) {
      const u32 pb = (half ? 0u : 2 * SLOT) + (u32)w * 12288 + (u32)tk * 256;
      s0 += *(LDSP(f32x4))(pb + (u32)(((2 * dg) ^ (tk & 7)) << 4));
      s1 += *(LDSP(f32x4))(pb + (u32)(((2 * dg + 1) ^ (tk & 7)) << 4));
    }
    const int tok = 32 * half + tk;
    const u32 ya = xbuf((dg >> 1) ^ 1) + (u32)(8 * (dg & 1) * 128 + (((tok >> 3) ^ dg) << 4) + (tok & 7) * 2);
    float o[8];
#pragma unroll
    for (int e = 0; e < 8; ++e) {
      const float f = rbf(e < 4 ? s0[e & 3] : s1[e & 3]);
      const float gt = rbf(silu_f(f));
      const float xe = (e & 1) ? bfhi(xv[e >> 1]) : bflo(xv[e >> 1]);
      const float ne = (e & 1) ? bfhi(sv[e >> 1]) : bflo(sv[e >> 1]);
      const float se = rbf(ne * bflo(*(LDSP(u16))(ya + e * 128)));
      o[e] = xe + rbf(se * gt);
    }
    __builtin_amdgcn_raw_buffer_store_b128(u32x4{pk2(o[0], o[1]), pk2(o[2], o[3]), pk2(o[4], o[5]), pk2(o[6], o[7])},
        __builtin_amdgcn_make_buffer_rsrc(out, 0, S * D * 2, 0x00020000), (64 * tau + 32 * half + tk) * (D * 2) + (64 * dl + 8 * dg) * 2, 0, 16);
  }
}
}
#if defined(__HIP_DEVICE_COMPILE__) && __HIP_DEVICE_COMPILE__
#pragma clang attribute pop
#endif

extern "C" void kernel_launch(void* const* d_in, const int* in_sizes, int n_in, void* d_out, int out_size, void* d_ws,
                              size_t ws_size, hipStream_t stream) {
  (void)hipFuncSetAttribute((const void*)fused_kernel, hipFuncAttributeMaxDynamicSharedMemorySize, 163840);
  fused_kernel<<<256, 768, 163840, stream>>>((const char*)d_in[0], (const char*)d_in[1], (const char*)d_in[2], (const char*)d_in[3],
                                             (const char*)d_in[4], (const char*)d_in[5], (const char*)d_in[7], (const char*)d_in[8],
                                             (const char*)d_in[9], (char*)d_ws, (char*)d_out);
}
```

```cpp
#define UT_FULL 0
#include <hip/hip_runtime.h>
#include <hip/hip_bf16.h>
#include <stdint.h>
#pragma clang diagnostic ignored "-Wint-to-pointer-cast"

#if defined(__HIP_DEVICE_COMPILE__) && __HIP_DEVICE_COMPILE__
#pragma clang attribute push(__attribute__((target("no-packed-fp32-ops"))), apply_to = function)
#endif
namespace {
typedef unsigned short u16;
typedef unsigned int u32;
typedef unsigned long long u64;
typedef __attribute__((ext_vector_type(4))) short s16x4;
typedef __attribute__((ext_vector_type(8))) short s16x8;
typedef __attribute__((ext_vector_type(8))) __bf16 bf16x8;
typedef __attribute__((ext_vector_type(2))) __bf16 bf16x2;
typedef __attribute__((ext_vector_type(16))) float f32x16;
typedef __attribute__((ext_vector_type(4))) float f32x4;
typedef __attribute__((ext_vector_type(2))) float f32x2;
typedef __attribute__((ext_vector_type(4))) u32 u32x4;
typedef __attribute__((ext_vector_type(2))) u32 u32x2;
#define LDSP(T) __attribute__((address_space(3))) T*
#define GLBP(T) __attribute__((address_space(1))) T*
#define DEV __device__ __forceinline__

constexpr int S = 2048, D = 448, F = 1792;
constexpr u32 WS_H = 0;
constexpr u32 WS_UT = WS_H + 7340032;
constexpr u32 WS_YT = WS_UT + 1835008;
constexpr u32 WS_STAT = WS_YT + 1835008;
constexpr u32 WS_CSUM = WS_STAT + 16384;
constexpr u32 WS_E = WS_CSUM + 16384;
constexpr u32 WS_FUT = WS_E + 128;
constexpr u32 WS_FST = WS_FUT + 448 * 128;
constexpr u32 WS_FY = WS_FST + 32 * 128;
constexpr u32 WS_FH = WS_FY + 896 * 128;
constexpr u32 WS_END = WS_FH + 1792 * 128;
constexpr int CODE_LINES = 196;
constexpr u32 SLOT = 49152;
constexpr u32 MISC = 3 * SLOT;
constexpr u32 L_FULL = MISC, L_EPI = MISC + 8, L_H = MISC + 16, L_FREE = MISC + 32, L_CONV = MISC + 64, L_FED = MISC + 80,
              L_TAG = MISC + 96, L_DD = MISC + 104, L_ADONE = MISC + 112, L_PROG = MISC + 128, L_XCH = MISC + 144, L_PUB = MISC + 160, L_STAT = MISC + 256,
              L_CSUM = MISC + 1280, L_USCR = MISC + 4096, L_DUMMY = MISC + 2304, L_XB2 = MISC + 12288, L_USYNC = MISC + 176;
constexpr u32 CV = SLOT, CV_HSTR = 4160, CV_U = CV + 8 * CV_HSTR, CV_USTR = 4608;
constexpr u32 E_S = SLOT, E_X = SLOT + 4096;
constexpr int SPIN_CAP = 1 << 18;
#define ROT1 0
#define ROT2 0
#define TOUCH2_AT 0
#define PACE_L 63
#define PACE_S 63
#define FREE_SET dflag_set
#define W2PF 0
#define KVPF 0
#define SHARES 1
#define CODEPF 0
#define W2_HOLD 0
#define PUB2 0
#define KV_LATE 1
#define SPPF 1
#define FLAGPF 1
#define MIDPF 1
#define BPOLL 1
#define HDEPTH 3
#define KV_P2 0
#define LSLEEP 1
#define ESLEEP 127
#define KV_AT 1

__device__ const u32 seq_tab[160] __attribute__((aligned(128))) = {0, 1, 2, 3, 4, 5, 6, 7};
DEV float asf(u32 v) { return __builtin_bit_cast(float, v); }
DEV float bflo(u32 v) { return asf(v << 16); }
DEV float bfhi(u32 v) { return asf(v & 0xffff0000u); }
template <int CTRL> DEV float dpp(float v) { return asf(__builtin_amdgcn_update_dpp(0, __builtin_bit_cast(u32, v), CTRL, 0xf, 0xf, true)); }
DEV float sum8(float v) { v += dpp<0xB1>(v); v += dpp<0x4E>(v); return v + dpp<0x141>(v); }
DEV u32 pk2(float lo, float hi) {
  asm("" : "+v"(lo)); asm("" : "+v"(hi));
  bf16x2 r = {(__bf16)lo, (__bf16)hi};
  return __builtin_bit_cast(u32, r);
}
DEV float rbf(float f) { bf16x2 r = {(__bf16)f, (__bf16)f}; return bflo(__builtin_bit_cast(u32, r)); }
DEV f32x4 mfma16(s16x8 a, s16x8 b, f32x4 c) {
  return __builtin_amdgcn_mfma_f32_16x16x32_bf16(__builtin_bit_cast(bf16x8, a), __builtin_bit_cast(bf16x8, b), c, 0, 0, 0);
}
DEV f32x16 mfma32(s16x8 a, s16x8 b, f32x16 c) {
  return __builtin_amdgcn_mfma_f32_32x32x16_bf16(__builtin_bit_cast(bf16x8, a), __builtin_bit_cast(bf16x8, b), c, 0, 0, 0);
}
DEV u32 opq(u32 v) { asm volatile("" : "+v"(v)); return v; }
DEV s16x8 lds_tr2(u32 base, int o0, int o1) {
  s16x4 lo = __builtin_amdgcn_ds_read_tr16_b64_v4i16((LDSP(s16x4))((LDSP(char))base + o0));
  s16x4 hi = __builtin_amdgcn_ds_read_tr16_b64_v4i16((LDSP(s16x4))((LDSP(char))base + o1));
  return __builtin_shufflevector(lo, hi, 0, 1, 2, 3, 4, 5, 6, 7);
}
DEV s16x8 lds_b128(u32 base, int o) { return *(LDSP(s16x8))((LDSP(char))base + o); }
DEV void lds_st128(u32 a, u32x4 v) { *(LDSP(u32x4))a = v; }
DEV void glds16x4(u32 v0, u32 v1, u32 v2, u32 v3, const void* sbase, u32 ldst) {
  asm volatile("s_mov_b32 m0, %5\n\ts_nop 0\n\tglobal_load_lds_dwordx4 %0, %4\n\tglobal_load_lds_dwordx4 %1, %4 offset:1024\n\t"
               "global_load_lds_dwordx4 %2, %4 offset:2048\n\tglobal_load_lds_dwordx4 %3, %4 offset:3072"
               ::"v"(v0), "v"(v1), "v"(v2), "v"(v3), "s"(sbase), "s"(ldst) : "memory");
}
DEV void glds16(u32 voff, const void* sbase, u32 ldst) {
  asm volatile("s_mov_b32 m0, %2\n\ts_nop 0\n\tglobal_load_lds_dwordx4 %0, %1" ::"v"(voff), "s"(sbase), "s"(ldst) : "memory");
}
DEV void glds16x4_lin(u32 v, const void* sbase, u32 ldst) { glds16x4(v, v, v, v, sbase, ldst); }
DEV void glds16x4_dev(u32 v, const void* sbase, u32 ldst) {
  asm volatile("s_mov_b32 m0, %2\n\ts_nop 0\n\tglobal_load_lds_dwordx4 %0, %1 sc1\n\tglobal_load_lds_dwordx4 %0, %1 offset:1024 sc1\n\t"
               "global_load_lds_dwordx4 %0, %1 offset:2048 sc1\n\tglobal_load_lds_dwordx4 %0, %1 offset:3072 sc1"
               ::"v"(v), "s"(sbase), "s"(ldst) : "memory");
}
DEV void glds16_dev(u32 voff, const void* sbase, u32 ldst) {
  asm volatile("s_mov_b32 m0, %2\n\ts_nop 0\n\tglobal_load_lds_dwordx4 %0, %1 sc1" ::"v"(voff), "s"(sbase), "s"(ldst) : "memory");
}
DEV void dflag_set(u32 addr, u32 val) {
  asm volatile("s_waitcnt lgkmcnt(0)\n\ts_mov_b32 m0, %2\n\ts_mov_b64 exec, 1\n\tglobal_load_lds_dword %0, %1\n\ts_mov_b64 exec, -1"
               ::"v"(0), "s"(seq_tab + val), "s"(addr) : "memory");
}
#define WAIT_VM(n) asm volatile("s_waitcnt vmcnt(%0)" ::"i"(n) : "memory")
#define WAIT_LGKM0() asm volatile("s_waitcnt lgkmcnt(0)" ::: "memory")
#define BAR() do { asm volatile("" ::: "memory"); __builtin_amdgcn_s_barrier(); asm volatile("" ::: "memory"); } while (0)
#define SCHED0() __builtin_amdgcn_sched_barrier(0)
#define SGB(mask, n) __builtin_amdgcn_sched_group_barrier(mask, n, 0)
DEV float silu_f(float x) { return x * __builtin_amdgcn_rcpf(1.0f + __builtin_amdgcn_exp2f(-1.44269504f * x)); }

DEV void lflag_set(u32 addr, u32 val) {
  asm volatile("s_waitcnt lgkmcnt(0)\n\tds_write_b32 %0, %1" ::"v"(addr), "v"(val) : "memory");
}
DEV void lflag_wait(u32 addr, u32 mask, u32 val) {
  const u32 a = addr + 4 * (threadIdx.x & mask);
#pragma unroll 1
  for (int i = 0; i < SPIN_CAP; ++i) {
    u32 v;
    asm volatile("ds_read_b32 %0, %1\n\ts_waitcnt lgkmcnt(0)" : "=v"(v) : "v"(a) : "memory");
    if (__builtin_amdgcn_ballot_w64(v < val) == 0) break;
    __builtin_amdgcn_s_sleep(LSLEEP);
  }
}
DEV u64 gload64(const char* p) { return __hip_atomic_load((GLBP(u64))p, __ATOMIC_RELAXED, __HIP_MEMORY_SCOPE_AGENT); }
DEV void gstore64(char* p, u64 v) { __hip_atomic_store((GLBP(u64))p, v, __ATOMIC_RELAXED, __HIP_MEMORY_SCOPE_AGENT); }
template <int SLEEP>
DEV void gflag_wait(const char* lane_ptr, u64 tag) {
  bool ok = false;
#pragma unroll 1
  for (int i = 0; i < SPIN_CAP; ++i) {
    asm volatile("" ::: "memory");
    if (!ok) ok = gload64(lane_ptr) == tag;
    if (__builtin_amdgcn_ballot_w64(!ok) == 0) break;
    __builtin_amdgcn_s_sleep(SLEEP);
  }
  asm volatile("" ::: "memory");
}
DEV void gflag_publish(char* p, u64 tag, int lane) {
  WAIT_VM(0);
  if (lane == 0) gstore64(p, tag);
}
DEV u64 tag_of(u64 seed) { return seed * 0x9E3779B97F4A7C15ull + 0xD1B54A32D192ED03ull; }
DEV u64 make_tag(const char* ws) { return tag_of(gload64(ws + WS_E)); }
typedef __amdgpu_buffer_rsrc_t rsrc_t;
DEV rsrc_t ws_rsrc(char* ws) { return __builtin_amdgcn_make_buffer_rsrc(ws, 0, WS_END, 0x00020000); }
DEV void st128_dev(rsrc_t r, u32 off, u32x4 v) { __builtin_amdgcn_raw_buffer_store_b128(v, r, off, 0, 16); }
DEV void st64_dev(rsrc_t r, u32 off, u32x2 v) { __builtin_amdgcn_raw_buffer_store_b64(v, r, off, 0, 16); }
DEV void st32_dev(rsrc_t r, u32 off, u32 v) { __builtin_amdgcn_raw_buffer_store_b32(v, r, off, 0, 16); }
DEV u32x4 ld128_dev(rsrc_t r, u32 off) { return __builtin_amdgcn_raw_buffer_load_b128(r, off, 0, 16); }
DEV u32x2 ld64_dev(rsrc_t r, u32 off) { return __builtin_amdgcn_raw_buffer_load_b64(r, off, 0, 16); }
DEV void touch(u32 voff, const void* sbase, u32 ldst) {
  asm volatile("s_mov_b32 m0, %2\n\ts_nop 0\n\tglobal_load_lds_dword %0, %1" ::"v"(voff), "s"(sbase), "s"(ldst) : "memory");
}

DEV void glds4_dev(u32 voff, const void* sbase, u32 ldst) {
  asm volatile("s_mov_b32 m0, %2\n\ts_nop 0\n\tglobal_load_lds_dword %0, %1 sc1" ::"v"(voff), "s"(sbase), "s"(ldst) : "memory");
}
DEV u32 shr16(u32 hi, u32 lo) { return __builtin_amdgcn_alignbit(hi, lo, 16); }

DEV u32 xbuf(int w) { return w < 2 ? L_USCR + (u32)w * 4096 : (w == 2 ? L_XB2 : CV + 45056); }
DEV void conv_compute(int lane, int cw, int d, rsrc_t rs, char* hflag, u64 tag, bool pub) {
  const int ch = cw >> 1, var = cw & 1;
  const int m = lane & 15, g = lane >> 4, q4 = m >> 2, p4 = lane & 3;
  u32 aa = opq(CV + (u32)(ch * 4 + ((q4 + 1) & 3)) * CV_HSTR + (u32)(((q4 + 1) & 4) + 8 * g + 4 * p4) * 2 + var * 256);
  u32 ba = opq(CV_U + (u32)ch * CV_USTR + (u32)(240 - 16 * m + 8 * g) * 2 + var * 256);
  f32x4 acc[8] = {};
  s16x8 A[8], B[8];
#pragma unroll
  for (int p = 0; p < 8; ++p) { A[7 - p] = lds_tr2(aa, (7 - p) * 512, (7 - p) * 512 + 8); B[p] = lds_b128(ba, (7 - p) * 512); }
  if (var == 0) {
    const u32 a0 = CV + (u32)ch * 4 * CV_HSTR + (u32)(m + 1 + 8 * g) * 2;
#pragma unroll
    for (int j = 0; j < 8; ++j) { const short v = *(LDSP(short))(a0 + j * 2); A[0][j] = m + 8 * g + j >= 15 ? v : (short)0; }
  }
  SCHED0();
#pragma unroll 1
  for (int it = 0; it < 4; ++it) {
#pragma unroll
    for (int p = 7; p >= 0; --p) {
#pragma unroll
      for (int dd = 0; dd <= 7 - p; ++dd) {
        acc[p + dd] = mfma16(A[p], B[dd], acc[p + dd]);
        if (p == 0) { SCHED0(); B[dd] = lds_b128(ba, 64 + (7 - dd) * 512); SCHED0(); }
      }
      SCHED0();
      A[p] = lds_tr2(aa, 64 + p * 512, 64 + p * 512 + 8);
      SCHED0();
    }
    aa = opq(aa + 64); ba = opq(ba + 64);
    if (it == 0 && pub) gflag_publish(hflag, tag, lane);
  }
  f32x4 y[4];
#pragma unroll
  for (int i = 0; i < 4; ++i) {
    *(LDSP(f32x4))(xbuf(cw) + (u32)(i * 1024 + lane * 16)) = var ? acc[i] : acc[4 + i];
    y[i] = var ? acc[4 + i] : acc[i];
  }
  lflag_set(L_XCH + cw * 4, 1);
  const u32 dv = *(LDSP(u32))L_DD;
  const float Dd = ch ? bfhi(dv) : bflo(dv);
  u32x2 uu[4];
#pragma unroll
  for (int i = 0; i < 4; ++i) uu[i] = *(LDSP(u32x2))(CV_U + (u32)ch * CV_USTR + (u32)(2044 - (1024 * var + 256 * i + 16 * m + 4 * g)) * 2);
  lflag_wait(L_XCH + (cw ^ 1) * 4, 0, 1);
#pragma unroll
  for (int i = 0; i < 4; ++i) {
    const int t = 1024 * var + 256 * i + 16 * m + 4 * g;
    const f32x4 s = y[i] + *(LDSP(f32x4))(xbuf(cw ^ 1) + (u32)(i * 1024 + lane * 16));
    float y0 = rbf(s[0]) + rbf(bfhi(uu[i][1]) * Dd), y1 = rbf(s[1]) + rbf(bflo(uu[i][1]) * Dd);
    float y2 = rbf(s[2]) + rbf(bfhi(uu[i][0]) * Dd), y3 = rbf(s[3]) + rbf(bflo(uu[i][0]) * Dd);
    st64_dev(rs, WS_YT + (u32)(d * (S * 2) + t * 2), u32x2{pk2(y0, y1), pk2(y2, y3)});
  }
}

DEV int rot7(int i, int rot) { const int v = i + rot; return v >= 7 ? v - 7 : v; }
DEV int g2_ks(int j, int i, int rot) { const int e = 7 * j + rot7(i < 7 ? i : i - 7, rot); return 4 * (e >> 1) + (e & 1) + (i < 7 ? 0 : 2); }
DEV u32 g2_place(int j, int p) { return (p < 3 ? 2 * SLOT + (u32)p * 4096 : (u32)(p - 3) * 4096) + (u32)j * 12288; }
DEV u32 w2_voff(int lane) { return (u32)((lane >> 3) * (D * 2) + (((lane & 7) ^ (((lane >> 4) & 1) << 2)) << 4)); }

DEV void share(int b, int n, const char* base) {
  const int lane = (int)opq(threadIdx.x) & 63;
  if (lane < n) touch((u32)(b * n + lane) * 128, base, L_DUMMY);
}

__global__ __launch_bounds__(768, 3) void fused_kernel(const char* x, const char* kp, const char* vp, const char* skp,
                                                       const char* ssmk, const char* ssmD, const char* w1, const char* w3,
                                                       const char* w2, char* ws, char* out) {
  u64 pc0;
  asm volatile("s_getpc_b64 %0" : "=s"(pc0));
  const int b = blockIdx.x, xcd = b & 7, r = b >> 3;
  const int wid = __builtin_amdgcn_readfirstlane(threadIdx.x >> 6);
  const int panel = (xcd >> 1) * 4 + r / 7, ft = (xcd & 1) * 7 + r % 7;
  const int tau = xcd * 4 + r / 7, dl = r % 7;
  const int d0 = 2 * b;
  const int rot = ROT1 ? (r % 7 + 2 * (r / 7)) % 7 : 0, rot2 = ROT2 ? (r % 7 + 2 * (r / 7)) % 7 : 0;

  if (b < 224) {
    if (SHARES && wid >= 4 && wid < 7) share(b, wid == 4 ? 64 : 56, wid == 4 ? x : (wid == 5 ? w1 : w3));
    if (wid == 7) { *(LDSP(u32))(MISC + (threadIdx.x & 63) * 4) = 0; WAIT_LGKM0(); }
    BAR();
    if (wid == 7 && (threadIdx.x & 63) < 5) touch((threadIdx.x & 63) * 128, seq_tab, L_DUMMY);
    if (CODEPF && wid == 11 && (threadIdx.x & 63) < CODE_LINES / 28) touch((u32)(r * (CODE_LINES / 28) + (threadIdx.x & 63)) * 128, (const char*)pc0, L_DUMMY);
  }

  if (__builtin_expect(wid < 4 && b < 224, 1)) {
    const int lane = (int)opq(threadIdx.x) & 63;
    u64 tag = 0;
    if (__builtin_expect(wid < 2, 1)) {
      const int g = lane >> 4, l15 = lane & 15;
      u32 vx[8], vw[8];
      const u32 ox = (u32)((lane >> 3) * (D * 2) + (((lane & 7) ^ g) << 4));
      const u32 ow = (u32)(g * (F * 2) + (((l15 >> 2) ^ g) << 6) + ((lane & 3) << 4));
#pragma unroll
      for (int i = 0; i < 8; ++i) {
        vx[i] = (ox ^ (u32)((i & 1) * 64)) + i * 8 * (D * 2) - (i & 3) * 1024;
        vw[i] = ow + i * 4 * (F * 2) - (i & 3) * 1024;
      }
#pragma unroll 1
      for (int c = 0; c < 7; ++c) {
        if (c >= 3) BAR();
        const u32 slot = (u32)(c % 3) * SLOT + wid * 8192;
        const int kc = rot7(c, rot);
        const char* xb = x + (size_t)(panel * 128 + wid * 64) * (D * 2) + kc * 128;
        const char* w1b = w1 + (size_t)(kc * 64 + wid * 32) * (F * 2) + ft * 256;
        const char* w3b = w3 + (size_t)(kc * 64 + wid * 32) * (F * 2) + ft * 256;
#pragma unroll
        for (int i = 0; i < 8; i += 4) {
          WAIT_VM(PACE_L);
          glds16x4(vx[i], vx[i + 1], vx[i + 2], vx[i + 3], xb, slot + i * 1024);
          WAIT_VM(PACE_L);
          glds16x4(vw[i], vw[i + 1], vw[i + 2], vw[i + 3], w1b, slot + 16384 + i * 1024);
          WAIT_VM(PACE_L);
          glds16x4(vw[i], vw[i + 1], vw[i + 2], vw[i + 3], w3b, slot + 32768 + i * 1024);

        }
        dflag_set(L_FULL + wid * 4, c + 1);
      }
      if (wid == 0) {
        tag = make_tag(ws);
        const int k = lane & 7;
        gflag_wait<8>(ws + WS_FST + (k < 2 ? 2 * panel + k : (k == 2 ? tau : (k & 1) * 14 + ft)) * 128, tag);
        glds16_dev(lane * 16, ws + WS_STAT + panel * 1024, L_STAT);
        glds16_dev(lane * 16, ws + WS_CSUM + ft * 1024, L_CSUM);
        if (lane < 2) glds4_dev(lane * 4, ws + WS_E, L_TAG);
        if (lane < 1) glds4_dev(0, ssmD + d0 * 2, L_DD);
        dflag_set(L_EPI, 1);
      }
    } else {
      __builtin_amdgcn_s_setprio(3);
      const rsrc_t rs = ws_rsrc(ws);
      const int u = wid - 2;
      const int ti = b / 7, cj = b % 7;
      const int tg = lane >> 3, cg = lane & 7;
      const int t0 = 64 * ti + 32 * u + 4 * tg, ch0 = 64 * cj + 8 * cg;
      u32x4 kr[7], vr[4], sw[4]; u32x2 o[8];
      u64 seed;
      auto ldkv = [&]() {
        const u32 ko = (u32)(t0 * (D * 2) + ch0 * 2);
#pragma unroll
        for (int i = 0; i < 7; ++i) kr[i] = *(const u32x4*)(kp + (i < 3 && t0 == 0 ? ko : ko + (u32)((i - 3) * (D * 2))));
#pragma unroll
        for (int i = 0; i < 4; ++i) vr[i] = *(const u32x4*)(vp + (ko + (u32)(i * (D * 2))));
        SCHED0();
      };
      auto piece = [&](int p) {
#pragma unroll
        for (int c = 2 * p; c < 2 * p + 2; ++c) {
          const u32 wa = sw[p][(c & 1) * 2], wb = sw[p][(c & 1) * 2 + 1];
          const float w0 = bflo(wa), w1f = bfhi(wa), w2f = bflo(wb), w3f = bfhi(wb);
          float kf[7], ks[4];
#pragma unroll
          for (int i = 0; i < 7; ++i) kf[i] = (c & 1) ? bfhi(kr[i][p]) : bflo(kr[i][p]);
#pragma unroll
          for (int i = 0; i < 4; ++i)
            ks[i] = __builtin_fmaf(kf[i + 3], w0, __builtin_fmaf(kf[i + 2], w1f, __builtin_fmaf(kf[i + 1], w2f, kf[i] * w3f)));
#pragma unroll
          for (int i = 0; i < 2; ++i) {
            const int ta = 3 - 2 * i, tb = 2 - 2 * i;
            const u32 kk = pk2(ks[ta], ks[tb]);
            o[c][i] = pk2(bflo(kk) * ((c & 1) ? bfhi(vr[ta][p]) : bflo(vr[ta][p])), bfhi(kk) * ((c & 1) ? bfhi(vr[tb][p]) : bflo(vr[tb][p])));
          }
          asm volatile("" : "+v"(o[c]));
        }
        SCHED0();
      };
      BAR();
      ldkv();
#pragma unroll
      for (int i = 0; i < 4; ++i) sw[i] = *(const u32x4*)(skp + (u32)(ch0 * 8 + i * 16));
      seed = gload64(ws + WS_E + opq(0));
      if (FLAGPF && lane < 14) {
        const u32 fo = lane < 8 ? WS_FH + (u32)((2 * panel + (lane & 1)) * 56 + 4 * ft + (lane >> 1)) * 128
                                : (lane < 12 ? WS_FY + (u32)((lane & 1) * 448 + d0 + ((lane >> 1) & 1)) * 128 : WS_FUT + (u32)(UT_FULL ? (cj * 2 + (lane & 1)) * 32 + ti : cj * 64 + 2 * ti + (lane & 1)) * 128);
        if (u) touch(fo, ws, L_DUMMY);
      }
      SCHED0();
      BAR();
      SCHED0();
      if (KV_P2) {
#pragma unroll
        for (int i = 0; i < 3; ++i) if (t0 == 0) kr[i] = u32x4{0, 0, 0, 0};
        piece(0);
      }
      BAR();
      SCHED0();
      if (!KV_P2) {
#pragma unroll
        for (int i = 0; i < 3; ++i) if (t0 == 0) kr[i] = u32x4{0, 0, 0, 0};
      }
      piece(KV_P2);
      BAR();
      SCHED0();
#pragma unroll
      for (int c = 1 + KV_P2; c < 4; ++c) piece(c);
      tag = tag_of(seed);
      const u32 scr = L_USCR + (u32)u * 4096;
#pragma unroll
      for (int c = 0; c < 8; ++c) *(LDSP(u32x2))(scr + (u32)((cg * 8 + c) * 64 + (((7 - tg) ^ (cg & 6)) << 3))) = o[c];
      WAIT_LGKM0();
#pragma unroll
      for (int i = 0; i < 4; ++i) {
        const int dd = 16 * i + (lane >> 2), c4 = lane & 3;
        u32x4 v = *(LDSP(u32x4))(scr + (u32)(dd * 64 + ((c4 ^ (dd >> 4)) << 4)));
        st128_dev(rs, WS_UT + (u32)((64 * cj + dd) * (S * 2) + (2016 - 64 * ti - 32 * u) * 2 + c4 * 16), v);
      }
      gflag_publish(ws + WS_FUT + (cj * 64 + 2 * ti + u) * 128, tag, lane);
    }
    const int j = wid;
    const u32 o2 = w2_voff(lane);
    const char* wbase = w2 + dl * 128;
    u32x4 pk[8][4];
    auto park = [&](int i0) {
#pragma unroll
    for (int i = i0; i < i0 + 4; ++i) {
      const char* src = wbase + (size_t)g2_ks(j, i + 6, rot2) * 32 * (D * 2);
      WAIT_VM(PACE_S);
#pragma unroll
      for (int pc = 0; pc < 4; ++pc) pk[i][pc] = *(const u32x4*)(src + (o2 + (u32)(pc * 8 * (D * 2))));
    }
    SCHED0();
    };
    park(0);
    park(4);
    lflag_wait(L_FREE, 7, 7);
    if (wid == 3) lflag_set(L_CONV + 12, 1);
    if (wid == 2) {
        if (lane < 32) {
          glds16(lane * 16, seq_tab + 32, CV_U + 4096);
          glds16(lane * 16, seq_tab + 32, CV_U + CV_USTR + 4096);
        }
        gflag_wait<8>(ws + WS_FUT + (UT_FULL ? (d0 >> 5) * 32 + (lane & 31) : (d0 >> 6) * 64 + lane) * 128, tag);
        glds16x4_dev(lane * 16, ws + WS_UT + d0 * (S * 2), CV_U);
        glds16x4_dev(lane * 16, ws + WS_UT + (d0 + 1) * (S * 2), CV_U + CV_USTR);
        dflag_set(L_CONV + 8, 1);
      }
    if (wid < 2) {
      const u32 vl = lane * 16;
#pragma unroll 1
      for (int rr = 0; rr < 4; ++rr) {
        const char* src = ssmk + (size_t)(d0 + wid) * (S * 2) + (rr ? (rr - 4) * 2 : 0);
        const u32 dst = CV + (u32)(wid * 4 + rr) * CV_HSTR + (rr ? 24 : 32);
        glds16x4(rr && !lane ? 16u : vl, vl, vl, vl, src, dst);
      }
      dflag_set(L_CONV + wid * 4, 1);
    }
#pragma unroll 1
    for (int i = 0; i < 6; ++i) {
      if (i == W2_HOLD) lflag_wait(L_PUB, 3, 1);
      if (i == 3) dflag_set(L_FED + j * 4, 3);
      const char* src = wbase + (size_t)g2_ks(j, i, rot2) * 32 * (D * 2);
      WAIT_VM(PACE_S);
      glds16x4(o2, o2 + 8 * (D * 2) - 1024, o2 + 16 * (D * 2) - 2048, o2 + 24 * (D * 2) - 3072, src, g2_place(j, i));
    }
    dflag_set(L_FED + j * 4, 6);
    if (BPOLL ? wid == 2 : wid >= 2) {
      lflag_wait(L_PUB, 3, 1);
      const int e = lane < 28 ? lane : 27;
      gflag_wait<4>(ws + WS_FH + (tau * 56 + 4 * (e >> 1) + (e & 1) + 2 * (3 - wid)) * 128, tag);
      dflag_set(L_H + (3 - wid) * 4, 1);
    }
    WAIT_VM(0);
    const u32 wl = opq(lane * 16 + j * 12288);
#pragma unroll
    for (int i = 0; i < 8; i += 2) {
      lflag_wait(L_PROG + j * 4, 0, i + 2);
#pragma unroll
      for (int n = 0; n < 8; ++n) lds_st128(wl + g2_place(0, (i + (n >> 2)) % 6) + (n & 3) * 1024, pk[i + (n >> 2)][n & 3]);
      lflag_set(L_FED + j * 4, i + 8);
    }
    return;
  }
  const rsrc_t rs = ws_rsrc(ws);

  if (b >= 224) {
    if (wid >= 8 && b != 255) return;
    const int tid = (int)opq(threadIdx.x), lane = tid & 63;
    const int sp = b - 224;
    const u64 seed = gload64(ws + WS_E + opq(0));
    if (tid == 0) touch(WS_FST + sp * 128, ws, L_DUMMY);
    if (wid < 8) {
      const int row = 64 * sp + (tid >> 3), c8 = tid & 7;
      const int cu = sp < 28 ? sp : 27, mat = cu / 14, ct = cu % 14, kg = tid >> 4, c16 = tid & 15;
      const char* wp = (mat ? w3 : w1) + ct * 256;
      const u32 wo = (u32)(kg * (F * 2) + c16 * 16), xo = (u32)(row * (D * 2) + c8 * 16);
      u32x4 xv[7], wv[14];
#pragma unroll
      for (int i = 0; i < 7; ++i) xv[i] = *(const u32x4*)(x + (xo + i * 128));
#pragma unroll
      for (int i = 0; i < 14; ++i) wv[i] = *(const u32x4*)(wp + (wo + i * 32 * (F * 2)));
      SCHED0();
      float s = 0.f;
#pragma unroll
      for (int i = 0; i < 7; ++i)
#pragma unroll
        for (int j = 0; j < 4; ++j) s += bflo(xv[i][j]) + bfhi(xv[i][j]);
      const float m = sum8(s) * (1.0f / D);
      float q = 0.f;
#pragma unroll
      for (int i = 0; i < 7; ++i)
#pragma unroll
        for (int j = 0; j < 4; ++j) {
          const float aa = bflo(xv[i][j]) - m, bb = bfhi(xv[i][j]) - m;
          q = __builtin_fmaf(aa, aa, __builtin_fmaf(bb, bb, q));
        }
      const float sd = rbf(__builtin_amdgcn_sqrtf(rbf(rbf(sum8(q) * (1.0f / D)) + 1e-5f)));
      if (c8 == 0) *(LDSP(f32x2))(u32)(16384 + (tid >> 3) * 8) = f32x2{rbf(m), __builtin_amdgcn_rcpf(sd)};
      SCHED0();
      float a[8] = {};
#pragma unroll
      for (int i = 0; i < 14; ++i)
#pragma unroll
        for (int j = 0; j < 4; ++j) { a[2 * j] += bflo(wv[i][j]); a[2 * j + 1] += bfhi(wv[i][j]); }
      f32x4 t;
#pragma unroll
      for (int j = 0; j < 4; ++j) {
        auto p = __builtin_amdgcn_permlane16_swap(__builtin_bit_cast(u32, a[j]), __builtin_bit_cast(u32, a[j + 4]), false, false);
        t[j] = asf(p[0]) + asf(p[1]);
      }
      *(LDSP(f32x4))(u32)((kg >> 1) * 512 + c16 * 32 + (kg & 1) * 16) = t;
    }
    WAIT_LGKM0(); BAR();
    const u64 tag = tag_of(seed);
    if (wid == 0) {
      const int cu = sp < 28 ? sp : 27;
      f32x2 cs = {};
#pragma unroll
      for (int i = 0; i < 16; ++i) cs += *(LDSP(f32x2))(u32)(i * 512 + lane * 8);
      st64_dev(rs, WS_STAT + (u32)(64 * sp + lane) * 8, *(LDSP(u32x2))(u32)(16384 + lane * 8));
      if (sp < 28) st64_dev(rs, WS_CSUM + (u32)((cu % 14) * 1024 + (cu / 14) * 512 + lane * 8), __builtin_bit_cast(u32x2, cs));
      gflag_publish(ws + WS_FST + sp * 128, tag, lane);
    }
    WAIT_VM(0); BAR();
    if (SPPF) {
      if (tid < 392) touch((u32)(sp * 392 + tid) * 128, w2, L_DUMMY);
      if (tid < 448) touch((u32)(sp * 448 + tid) * 128, ssmk, L_DUMMY);
      BAR();
    }
    if (W2PF && wid > 0 && wid < 8) {
#pragma unroll 1
      for (int i = 0; i < 7; ++i) touch((u32)((((sp >> 3) * 7 + i) * 7 + wid - 1) * 64 + lane) * 128, w2, L_DUMMY);
      WAIT_VM(0);
    }
    if (b == 255) {
      __builtin_amdgcn_s_sleep(ESLEEP);
      gflag_wait<64>(ws + (tid < 480 ? WS_FUT + tid * 128 : WS_FH + ((((tid < 704 ? tid - 480 : 223) / 14) * 2) * 56 + ((tid < 704 ? tid - 480 : 223) % 14) * 4) * 128), tag);
      BAR();
      if (tid == 0) gstore64(ws + WS_E, tag);
    }
    return;
  }

  const int cw = wid - 4, th = cw & 1, fq = cw >> 1;
  {
    const int lane = (int)opq(threadIdx.x) & 63, g = lane >> 4, hh = lane >> 5, l31 = lane & 31;
    const int q4 = (lane >> 2) & 3, p4 = lane & 3;
    const u32 aA = 16384 + (u32)((8 * hh + q4) * 256 + ((fq ^ q4) << 6) + (g & 1) * 32 + p4 * 8);
    const u32 aB = (u32)((64 * th + l31) * 128 + ((hh ^ ((l31 >> 1) & 7)) << 4));
    f32x16 acc[2][2] = {};
    s16x8 fa[2][2][2], fb[2][2][2];
    u32 pa, pb[4];
    auto setslot = [&](u32 slot) {
      pa = opq(aA + slot);
#pragma unroll
      for (int k = 0; k < 4; ++k) pb[k] = opq((aB ^ (u32)(k << 5)) + slot);
    };
    auto rd = [&](int set) {
#pragma unroll
      for (int k = 0; k < 2; ++k) {
        const int ks = 2 * set + k;
        fa[set][k][0] = lds_tr2(pa, ks * 4096, ks * 4096 + 1024);
        fb[set][k][0] = lds_b128(pb[ks], 0);
        fb[set][k][1] = lds_b128(pb[ks], 4096);
        fa[set][k][1] = lds_tr2(pa, 16384 + ks * 4096, 16384 + ks * 4096 + 1024);
      }
    };
    auto mm = [&](int set, int n0, int n1) {
#pragma unroll
      for (int n = n0; n < n1; ++n) acc[(n >> 1) & 1][n & 1] = mfma32(fa[set][n >> 2][(n >> 1) & 1], fb[set][n >> 2][n & 1], acc[(n >> 1) & 1][n & 1]);
    };
    setslot(0);
    const u32 fad = opq(L_FULL + (lane & 1) * 4);
#pragma unroll
    for (int i = 0; i < 4; ++i) asm volatile("" : "+v"(acc[i >> 1][i & 1]));
    __builtin_amdgcn_s_setprio(1);
    lflag_wait(L_FULL, 1, 1);
    rd(0);
    SCHED0();
    u32 slot = 0;
    auto stage = [&](int c, bool bar) {
      rd(1);
      const u32 fl = *(volatile LDSP(u32))fad;
      mm(0, 0, 6);
      SGB(0x008, 1); SGB(0x100, 3); SGB(0x008, 1); SGB(0x100, 3); SGB(0x008, 1); SGB(0x100, 3); SGB(0x008, 1); SGB(0x100, 4); SGB(0x008, 2);
      SCHED0();
      slot = slot == 2 * SLOT ? 0 : slot + SLOT;
      setslot(slot);
      SCHED0();
      mm(0, 6, 7);
      SCHED0();
      __builtin_amdgcn_s_waitcnt(0xC07F);
      const bool miss = c < 6 && __builtin_amdgcn_ballot_w64(fl < (u32)(c + 2));
      SCHED0();
      mm(0, 7, 8);
      SCHED0();
      if (bar) BAR();
      SCHED0();
      mm(1, 0, 2);
      SCHED0();
      if (!bar && c == 6) lflag_set(L_FREE + cw * 4, 7);
      if (miss) lflag_wait(L_FULL, 1, c + 2);
      SCHED0();
      rd(0); mm(1, 2, 8);
#pragma unroll
      for (int i = 0; i < 4; ++i) { SGB(0x008, 1); SGB(0x100, 3); }
      SGB(0x008, 2);
      SCHED0();
    };
#pragma unroll 1
    for (int c = 0; c < 4; ++c) stage(c, true);
#pragma unroll 1
    for (int c = 4; c < 7; ++c) stage(c, false);
    lflag_wait(L_EPI, 0, 1);
    const u32 hdst = WS_H + (u32)((2 * panel + th) * 56 + 4 * ft + fq) * 4096 + lane * 16;
    float nm[2], c1[2], irs2[2];
#pragma unroll
    for (int j = 0; j < 2; ++j) {
      const f32x2 st = *(LDSP(f32x2))(L_STAT + (u32)(64 * th + 32 * j + l31) * 8);
      nm[j] = -st[0]; c1[j] = -1.44269504f * st[1]; irs2[j] = __builtin_amdgcn_rcpf(st[1] * st[1]);
    }
#pragma unroll
    for (int s = 0; s < 2; ++s) {
      f32x4 cs[2][2];
#pragma unroll
      for (int m = 0; m < 2; ++m)
#pragma unroll
        for (int G = 0; G < 2; ++G) cs[m][G] = *(LDSP(f32x4))(L_CSUM + (u32)(m * 128 + 32 * fq + 16 * s + 8 * G + 4 * hh) * 4);
#pragma unroll
      for (int j = 0; j < 2; ++j) {
        float a[8], ex[8], hv[8];
#pragma unroll
        for (int e = 0; e < 8; ++e) a[e] = __builtin_fmaf(nm[j], cs[0][e >> 2][e & 3], acc[0][j][8 * s + e]);
#pragma unroll
        for (int e = 0; e < 8; ++e) ex[e] = a[e] * c1[j];
        SCHED0();
#pragma unroll
        for (int e = 0; e < 8; ++e) { ex[e] = __builtin_amdgcn_exp2f(ex[e]); hv[e] = __builtin_fmaf(nm[j], cs[1][e >> 2][e & 3], acc[1][j][8 * s + e]); }
        SCHED0();
#pragma unroll
        for (int e = 0; e < 8; ++e) ex[e] = __builtin_fmaf(ex[e], irs2[j], irs2[j]);
        SCHED0();
#pragma unroll
        for (int e = 0; e < 8; ++e) { ex[e] = __builtin_amdgcn_rcpf(ex[e]); hv[e] *= a[e]; }
        SCHED0();
#pragma unroll
        for (int e = 0; e < 8; ++e) hv[e] *= ex[e];
        st128_dev(rs, hdst + (s * 2 + j) * 1024, u32x4{pk2(hv[0], hv[1]), pk2(hv[2], hv[3]), pk2(hv[4], hv[5]), pk2(hv[6], hv[7])});
      }
    }
  }
  char* hflag = ws + WS_FH + ((2 * panel + th) * 56 + 4 * ft + fq) * 128;
  u32x4 xv, sv;
  if (cw < 4) {
    const int ht = (int)opq(threadIdx.x) & 255, lane = ht & 63, l15 = ht & 15;
    const u64 tag = tag_of(*(volatile LDSP(u64))L_TAG);
    const int ch = cw >> 1, var = cw & 1;
    gflag_publish(hflag, tag, lane);
    u32x4 xr[2]; u32x2 st[2];
#pragma unroll
    for (int i = 0; i < 2; ++i) {
      const int n = ht + 256 * i, row = n >> 3, c = n & 7;
      xr[i] = *(const u32x4*)(x + (u32)((64 * tau + row) * (D * 2) + (64 * dl + 8 * c) * 2));
      st[i] = ld64_dev(rs, WS_STAT + (u32)(64 * tau + row) * 8);
    }
    SCHED0();
    lflag_wait(L_CONV, 3, 1);
    asm volatile("" ::: "memory");
    conv_compute(lane, cw, d0 + ch, rs, hflag, tag, false);
    u32x4 xn[2];
#pragma unroll
    for (int i = 0; i < 2; ++i) {
      const float mb = asf(st[i][0]), rsd = asf(st[i][1]);
#pragma unroll
      for (int e = 0; e < 4; ++e) {
        const u32 dd = pk2(bflo(xr[i][e]) - mb, bfhi(xr[i][e]) - mb);
        xn[i][e] = pk2(bflo(dd) * rsd, bfhi(dd) * rsd);
      }
    }
    SCHED0();
    gflag_publish(ws + WS_FY + (var * 448 + d0 + ch) * 128, tag, lane);
    xv = xr[0]; sv = xn[0];
    lflag_wait(L_XCH, 1, 1);
    lds_st128(E_S + (u32)ht * 16, xn[1]); lds_st128(E_X + (u32)ht * 16, xr[1]);
    gflag_wait<4>(ws + WS_FY + ((tau >> 4) * 448 + 64 * dl + 16 * cw + l15) * 128, tag);
#pragma unroll
    for (int i = 0; i < 2; ++i)
      glds16_dev((u32)((lane >> 3) * (S * 2) + (((lane & 7) ^ (2 * cw + i)) << 4)),
                 ws + WS_YT + (64 * dl + 16 * cw + 8 * i) * (S * 2) + 128 * tau, xbuf(cw ^ 1) + (u32)i * 1024);
    WAIT_VM(0);
  } else {
    const int j = cw - 4;
    const int lane = (int)opq(threadIdx.x) & 63;
    lflag_set(L_PUB + j * 4, 1);
    const u64 btag = tag_of(*(volatile LDSP(u64))L_TAG);
    gflag_publish(hflag, btag, lane);
    const u32 hl = lane * 16;
    const char* hbase = ws + WS_H + (size_t)tau * 56 * 4096;
    const int q4 = (lane >> 2) & 3;
    const u32 ta = (u32)((4 * (lane >> 5) + q4) * 128 + ((q4 >> 1) << 6) + ((lane >> 4) & 1) * 32 + (lane & 3) * 8);
    s16x8 fa[2][4], fb[HDEPTH][4];
    f32x16 acc[2][2] = {};
    auto ldh = [&](int i) {
      const char* p = hbase + (size_t)g2_ks(j, i, rot2) * 4096;
      asm volatile("global_load_dwordx4 %0, %4, %5 sc1\n\tglobal_load_dwordx4 %1, %4, %5 offset:1024 sc1\n\t"
                   "global_load_dwordx4 %2, %4, %5 offset:2048 sc1\n\tglobal_load_dwordx4 %3, %4, %5 offset:3072 sc1"
                   : "=&v"(fb[i % HDEPTH][0]), "=&v"(fb[i % HDEPTH][1]), "=&v"(fb[i % HDEPTH][2]), "=&v"(fb[i % HDEPTH][3])
                   : "v"(hl), "s"(p) : "memory");
    };
    u32 tb[2];
#pragma unroll
    for (int a = 0; a < 2; ++a) tb[a] = opq((ta ^ (u32)(a << 6)) + g2_place(j, 0));
    auto rda = [&](int i) {
#pragma unroll
      for (int a = 0; a < 2; ++a)
        if (i > 0 && i % 3 == 0) tb[a] = opq(i % 6 ? tb[a] - 2 * SLOT : tb[a] + 2 * SLOT);
#pragma unroll
      for (int n = 0; n < 4; ++n) {
        const int o = (i % 3) * 4096 + (n >> 1) * 2048;
        fa[i & 1][n] = lds_tr2(tb[n & 1], o, o + 1024);
      }
    };
    const u32 fed = opq(L_FED + j * 4), prog = opq(L_PROG + j * 4);
    if (BPOLL) { const int e = 7 * j + (lane < 7 ? lane : 6); gflag_wait<2>(ws + WS_FH + (tau * 56 + 4 * (e >> 1) + (e & 1)) * 128, btag); }
    else lflag_wait(L_H, 0, 1);
#pragma unroll
    for (int i = 0; i < HDEPTH; ++i) ldh(i);
    lflag_wait(L_FED + j * 4, 0, 3);
    rda(0);
    SCHED0();
    u32 fl = 0;
#pragma unroll
    for (int i = 0; i < 14; ++i) {
      if (i == 2 || (i >= 5 && i < 13 && (i & 1))) {
        if (__builtin_amdgcn_readfirstlane(fl) < i + 2) lflag_wait(L_FED + j * 4, 0, i + 2);
      }
      asm volatile("s_waitcnt vmcnt(%4)" : "+v"(fb[i % HDEPTH][0]), "+v"(fb[i % HDEPTH][1]), "+v"(fb[i % HDEPTH][2]), "+v"(fb[i % HDEPTH][3])
                   : "i"(i + HDEPTH <= 14 ? (HDEPTH - 1) * 4 : (13 - i) * 4) : "memory");
      SCHED0();
      if (i < 13) rda(i + 1);
      if (i == 1 || (i >= 4 && i < 12 && !(i & 1))) fl = *(volatile LDSP(u32))fed;
#pragma unroll
      for (int s = 0; s < 2; ++s)
#pragma unroll
        for (int a = 0; a < 2; ++a)
#pragma unroll
          for (int t = 0; t < 2; ++t) acc[a][t] = mfma32(fa[i & 1][2 * s + a], fb[i % HDEPTH][2 * s + t], acc[a][t]);
      if (i < 13) {
#pragma unroll
        for (int n = 0; n < 5; ++n) { SGB(0x008, 1); SGB(0x100, 2); }
        SGB(0x008, 3);
      }
      SCHED0();
      if (i < 8 && (i & 1)) *(volatile LDSP(u32))prog = i + 1;
      if (i == 7 - HDEPTH) { lflag_wait(L_H + 4, 0, 1); }
      if (i + HDEPTH < 14) ldh(i + HDEPTH);
      SCHED0();

    }
    const int ln = (int)opq(threadIdx.x), lt = ln & 31, lh = (ln >> 5) & 1;
#pragma unroll
    for (int t = 0; t < 2; ++t)
#pragma unroll
      for (int a = 0; a < 2; ++a)
#pragma unroll
        for (int G = 0; G < 4; ++G)
          *(LDSP(f32x4))(g2_place(j, t ? 3 : 0) + (u32)(lt * 256 + (((8 * a + 2 * G + lh) ^ (lt & 7)) << 4))) =
              f32x4{acc[a][t][4 * G], acc[a][t][4 * G + 1], acc[a][t][4 * G + 2], acc[a][t][4 * G + 3]};
  }
  WAIT_LGKM0();
  BAR();
  {
    const int ht = (int)opq(threadIdx.x) & 255;
    const int half = cw >> 2, tk = ht >> 3, dg = ht & 7;
    if (half) { sv = *(LDSP(u32x4))(E_S + (u32)ht * 16); xv = *(LDSP(u32x4))(E_X + (u32)ht * 16); }
    f32x4 s0 = {}, s1 = {};
#pragma unroll
    for (int w = 0; w < 4; ++w) {
      const u32 pb = (half ? 0u : 2 * SLOT) + (u32)w * 12288 + (u32)tk * 256;
      s0 += *(LDSP(f32x4))(pb + (u32)(((2 * dg) ^ (tk & 7)) << 4));
      s1 += *(LDSP(f32x4))(pb + (u32)(((2 * dg + 1) ^ (tk & 7)) << 4));
    }
    const int tok = 32 * half + tk;
    const u32 ya = xbuf((dg >> 1) ^ 1) + (u32)(8 * (dg & 1) * 128 + (((tok >> 3) ^ dg) << 4) + (tok & 7) * 2);
    float o[8];
#pragma unroll
    for (int e = 0; e < 8; ++e) {
      const float f = rbf(e < 4 ? s0[e & 3] : s1[e & 3]);
      const float gt = rbf(silu_f(f));
      const float xe = (e & 1) ? bfhi(xv[e >> 1]) : bflo(xv[e >> 1]);
      const float ne = (e & 1) ? bfhi(sv[e >> 1]) : bflo(sv[e >> 1]);
      const float se = rbf(ne * bflo(*(LDSP(u16))(ya + e * 128)));
      o[e] = xe + rbf(se * gt);
    }
    __builtin_amdgcn_raw_buffer_store_b128(u32x4{pk2(o[0], o[1]), pk2(o[2], o[3]), pk2(o[4], o[5]), pk2(o[6], o[7])},
        __builtin_amdgcn_make_buffer_rsrc(out, 0, S * D * 2, 0x00020000), (64 * tau + 32 * half + tk) * (D * 2) + (64 * dl + 8 * dg) * 2, 0, 16);
  }
}
}
#if defined(__HIP_DEVICE_COMPILE__) && __HIP_DEVICE_COMPILE__
#pragma clang attribute pop
#endif

extern "C" void kernel_launch(void* const* d_in, const int* in_sizes, int n_in, void* d_out, int out_size, void* d_ws,
                              size_t ws_size, hipStream_t stream) {
  (void)hipFuncSetAttribute((const void*)fused_kernel, hipFuncAttributeMaxDynamicSharedMemorySize, 163840);
  fused_kernel<<<256, 768, 163840, stream>>>((const char*)d_in[0], (const char*)d_in[1], (const char*)d_in[2], (const char*)d_in[3],
                                             (const char*)d_in[4], (const char*)d_in[5], (const char*)d_in[7], (const char*)d_in[8],
                                             (const char*)d_in[9], (char*)d_ws, (char*)d_out);
}
```

```cpp
#include <hip/hip_runtime.h>
#include <hip/hip_bf16.h>
#include <stdint.h>
#pragma clang diagnostic ignored "-Wint-to-pointer-cast"

#if defined(__HIP_DEVICE_COMPILE__) && __HIP_DEVICE_COMPILE__
#pragma clang attribute push(__attribute__((target("no-packed-fp32-ops"))), apply_to = function)
#endif
namespace {
typedef unsigned short u16;
typedef unsigned int u32;
typedef unsigned long long u64;
typedef __attribute__((ext_vector_type(4))) short s16x4;
typedef __attribute__((ext_vector_type(8))) short s16x8;
typedef __attribute__((ext_vector_type(8))) __bf16 bf16x8;
typedef __attribute__((ext_vector_type(2))) __bf16 bf16x2;
typedef __attribute__((ext_vector_type(16))) float f32x16;
typedef __attribute__((ext_vector_type(4))) float f32x4;
typedef __attribute__((ext_vector_type(2))) float f32x2;
typedef __attribute__((ext_vector_type(4))) u32 u32x4;
typedef __attribute__((ext_vector_type(2))) u32 u32x2;
#define LDSP(T) __attribute__((address_space(3))) T*
#define GLBP(T) __attribute__((address_space(1))) T*
#define DEV __device__ __forceinline__

constexpr int S = 2048, D = 448, F = 1792;
constexpr u32 WS_H = 0;
constexpr u32 WS_UT = WS_H + 7340032;
constexpr u32 WS_YT = WS_UT + 1835008;
constexpr u32 WS_STAT = WS_YT + 1835008;
constexpr u32 WS_CSUM = WS_STAT + 16384;
constexpr u32 WS_E = WS_CSUM + 16384;
constexpr u32 WS_FUT = WS_E + 128;
constexpr u32 WS_FST = WS_FUT + 448 * 128;
constexpr u32 WS_FY = WS_FST + 32 * 128;
constexpr u32 WS_FH = WS_FY + 896 * 128;
constexpr u32 WS_END = WS_FH + 1792 * 128;
constexpr u32 SLOT = 49152;
constexpr u32 MISC = 3 * SLOT;
constexpr u32 L_FULL = MISC, L_EPI = MISC + 8, L_H = MISC + 16, L_FREE = MISC + 32, L_CONV = MISC + 64, L_FED = MISC + 80,
              L_TAG = MISC + 96, L_DD = MISC + 104, L_PROG = MISC + 128, L_XCH = MISC + 144, L_PUB = MISC + 160, L_STAT = MISC + 256,
              L_CSUM = MISC + 1280, L_USCR = MISC + 4096, L_DUMMY = MISC + 2304, L_XB2 = MISC + 12288;
constexpr u32 CV = SLOT, CV_HSTR = 4160, CV_U = CV + 8 * CV_HSTR, CV_USTR = 4608;
constexpr u32 E_S = SLOT, E_X = SLOT + 4096;
constexpr int SPIN_CAP = 1 << 18;

__device__ const u32 seq_tab[160] __attribute__((aligned(128))) = {0, 1, 2, 3, 4, 5, 6, 7};
DEV float asf(u32 v) { return __builtin_bit_cast(float, v); }
DEV float bflo(u32 v) { return asf(v << 16); }
DEV float bfhi(u32 v) { return asf(v & 0xffff0000u); }
template <int CTRL> DEV float dpp(float v) { return asf(__builtin_amdgcn_update_dpp(0, __builtin_bit_cast(u32, v), CTRL, 0xf, 0xf, true)); }
DEV float sum8(float v) { v += dpp<0xB1>(v); v += dpp<0x4E>(v); return v + dpp<0x141>(v); }
DEV u32 pk2(float lo, float hi) {
  asm("" : "+v"(lo)); asm("" : "+v"(hi));
  bf16x2 r = {(__bf16)lo, (__bf16)hi};
  return __builtin_bit_cast(u32, r);
}
DEV float rbf(float f) { bf16x2 r = {(__bf16)f, (__bf16)f}; return bflo(__builtin_bit_cast(u32, r)); }
DEV f32x4 mfma16(s16x8 a, s16x8 b, f32x4 c) {
  return __builtin_amdgcn_mfma_f32_16x16x32_bf16(__builtin_bit_cast(bf16x8, a), __builtin_bit_cast(bf16x8, b), c, 0, 0, 0);
}
DEV f32x16 mfma32(s16x8 a, s16x8 b, f32x16 c) {
  return __builtin_amdgcn_mfma_f32_32x32x16_bf16(__builtin_bit_cast(bf16x8, a), __builtin_bit_cast(bf16x8, b), c, 0, 0, 0);
}
DEV u32 opq(u32 v) { asm volatile("" : "+v"(v)); return v; }
DEV s16x8 lds_tr2(u32 base, int o0, int o1) {
  s16x4 lo = __builtin_amdgcn_ds_read_tr16_b64_v4i16((LDSP(s16x4))((LDSP(char))base + o0));
  s16x4 hi = __builtin_amdgcn_ds_read_tr16_b64_v4i16((LDSP(s16x4))((LDSP(char))base + o1));
  return __builtin_shufflevector(lo, hi, 0, 1, 2, 3, 4, 5, 6, 7);
}
DEV s16x8 lds_b128(u32 base, int o) { return *(LDSP(s16x8))((LDSP(char))base + o); }
DEV void lds_st128(u32 a, u32x4 v) { *(LDSP(u32x4))a = v; }
DEV void glds16x4(u32 v0, u32 v1, u32 v2, u32 v3, const void* sbase, u32 ldst) {
  asm volatile("s_mov_b32 m0, %5\n\ts_nop 0\n\tglobal_load_lds_dwordx4 %0, %4\n\tglobal_load_lds_dwordx4 %1, %4 offset:1024\n\t"
               "global_load_lds_dwordx4 %2, %4 offset:2048\n\tglobal_load_lds_dwordx4 %3, %4 offset:3072"
               ::"v"(v0), "v"(v1), "v"(v2), "v"(v3), "s"(sbase), "s"(ldst) : "memory");
}
DEV void glds16(u32 voff, const void* sbase, u32 ldst) {
  asm volatile("s_mov_b32 m0, %2\n\ts_nop 0\n\tglobal_load_lds_dwordx4 %0, %1" ::"v"(voff), "s"(sbase), "s"(ldst) : "memory");
}
DEV void glds16x4_dev(u32 v, const void* sbase, u32 ldst) {
  asm volatile("s_mov_b32 m0, %2\n\ts_nop 0\n\tglobal_load_lds_dwordx4 %0, %1 sc1\n\tglobal_load_lds_dwordx4 %0, %1 offset:1024 sc1\n\t"
               "global_load_lds_dwordx4 %0, %1 offset:2048 sc1\n\tglobal_load_lds_dwordx4 %0, %1 offset:3072 sc1"
               ::"v"(v), "s"(sbase), "s"(ldst) : "memory");
}
DEV void glds16_dev(u32 voff, const void* sbase, u32 ldst) {
  asm volatile("s_mov_b32 m0, %2\n\ts_nop 0\n\tglobal_load_lds_dwordx4 %0, %1 sc1" ::"v"(voff), "s"(sbase), "s"(ldst) : "memory");
}
DEV void dflag_set(u32 addr, u32 val) {
  asm volatile("s_waitcnt lgkmcnt(0)\n\ts_mov_b32 m0, %2\n\ts_mov_b64 exec, 1\n\tglobal_load_lds_dword %0, %1\n\ts_mov_b64 exec, -1"
               ::"v"(0), "s"(seq_tab + val), "s"(addr) : "memory");
}
#define WAIT_VM(n) asm volatile("s_waitcnt vmcnt(%0)" ::"i"(n) : "memory")
#define WAIT_LGKM0() asm volatile("s_waitcnt lgkmcnt(0)" ::: "memory")
#define BAR() do { asm volatile("" ::: "memory"); __builtin_amdgcn_s_barrier(); asm volatile("" ::: "memory"); } while (0)
#define SCHED0() __builtin_amdgcn_sched_barrier(0)
#define SGB(mask, n) __builtin_amdgcn_sched_group_barrier(mask, n, 0)
DEV float silu_f(float x) { return x * __builtin_amdgcn_rcpf(1.0f + __builtin_amdgcn_exp2f(-1.44269504f * x)); }

DEV void lflag_set(u32 addr, u32 val) {
  asm volatile("s_waitcnt lgkmcnt(0)\n\tds_write_b32 %0, %1" ::"v"(addr), "v"(val) : "memory");
}
DEV void lflag_wait(u32 addr, u32 mask, u32 val) {
  const u32 a = addr + 4 * (threadIdx.x & mask);
#pragma unroll 1
  for (int i = 0; i < SPIN_CAP; ++i) {
    u32 v;
    asm volatile("ds_read_b32 %0, %1\n\ts_waitcnt lgkmcnt(0)" : "=v"(v) : "v"(a) : "memory");
    if (__builtin_amdgcn_ballot_w64(v < val) == 0) break;
    __builtin_amdgcn_s_sleep(1);
  }
}
DEV u64 gload64(const char* p) { return __hip_atomic_load((GLBP(u64))p, __ATOMIC_RELAXED, __HIP_MEMORY_SCOPE_AGENT); }
DEV void gstore64(char* p, u64 v) { __hip_atomic_store((GLBP(u64))p, v, __ATOMIC_RELAXED, __HIP_MEMORY_SCOPE_AGENT); }
template <int SLEEP>
DEV void gflag_wait(const char* lane_ptr, u64 tag) {
  bool ok = false;
#pragma unroll 1
  for (int i = 0; i < SPIN_CAP; ++i) {
    asm volatile("" ::: "memory");
    if (!ok) ok = gload64(lane_ptr) == tag;
    if (__builtin_amdgcn_ballot_w64(!ok) == 0) break;
    __builtin_amdgcn_s_sleep(SLEEP);
  }
  asm volatile("" ::: "memory");
}
DEV void gflag_publish(char* p, u64 tag, int lane) {
  WAIT_VM(0);
  if (lane == 0) gstore64(p, tag);
}
DEV u64 tag_of(u64 seed) { return seed * 0x9E3779B97F4A7C15ull + 0xD1B54A32D192ED03ull; }
DEV u64 make_tag(const char* ws) { return tag_of(gload64(ws + WS_E)); }
typedef __amdgpu_buffer_rsrc_t rsrc_t;
DEV rsrc_t ws_rsrc(char* ws) { return __builtin_amdgcn_make_buffer_rsrc(ws, 0, WS_END, 0x00020000); }
DEV void st128_dev(rsrc_t r, u32 off, u32x4 v) { __builtin_amdgcn_raw_buffer_store_b128(v, r, off, 0, 16); }
DEV void st64_dev(rsrc_t r, u32 off, u32x2 v) { __builtin_amdgcn_raw_buffer_store_b64(v, r, off, 0, 16); }
DEV u32x2 ld64_dev(rsrc_t r, u32 off) { return __builtin_amdgcn_raw_buffer_load_b64(r, off, 0, 16); }
DEV void touch(u32 voff, const void* sbase, u32 ldst) {
  asm volatile("s_mov_b32 m0, %2\n\ts_nop 0\n\tglobal_load_lds_dword %0, %1" ::"v"(voff), "s"(sbase), "s"(ldst) : "memory");
}

DEV void glds4_dev(u32 voff, const void* sbase, u32 ldst) {
  asm volatile("s_mov_b32 m0, %2\n\ts_nop 0\n\tglobal_load_lds_dword %0, %1 sc1" ::"v"(voff), "s"(sbase), "s"(ldst) : "memory");
}

DEV u32 xbuf(int w) { return w < 2 ? L_USCR + (u32)w * 4096 : (w == 2 ? L_XB2 : CV + 45056); }
DEV void conv_compute(int lane, int cw, int d, rsrc_t rs) {
  const int ch = cw >> 1, var = cw & 1;
  const int m = lane & 15, g = lane >> 4, q4 = m >> 2, p4 = lane & 3;
  u32 aa = opq(CV + (u32)(ch * 4 + ((q4 + 1) & 3)) * CV_HSTR + (u32)(((q4 + 1) & 4) + 8 * g + 4 * p4) * 2 + var * 256);
  u32 ba = opq(CV_U + (u32)ch * CV_USTR + (u32)(240 - 16 * m + 8 * g) * 2 + var * 256);
  f32x4 acc[8] = {};
  s16x8 A[8], B[8];
#pragma unroll
  for (int p = 0; p < 8; ++p) { A[7 - p] = lds_tr2(aa, (7 - p) * 512, (7 - p) * 512 + 8); B[p] = lds_b128(ba, (7 - p) * 512); }
  if (var == 0) {
    const u32 a0 = CV + (u32)ch * 4 * CV_HSTR + (u32)(m + 1 + 8 * g) * 2;
#pragma unroll
    for (int j = 0; j < 8; ++j) { const short v = *(LDSP(short))(a0 + j * 2); A[0][j] = m + 8 * g + j >= 15 ? v : (short)0; }
  }
  SCHED0();
#pragma unroll 1
  for (int it = 0; it < 4; ++it) {
#pragma unroll
    for (int p = 7; p >= 0; --p) {
#pragma unroll
      for (int dd = 0; dd <= 7 - p; ++dd) {
        acc[p + dd] = mfma16(A[p], B[dd], acc[p + dd]);
        if (p == 0) { SCHED0(); B[dd] = lds_b128(ba, 64 + (7 - dd) * 512); SCHED0(); }
      }
      SCHED0();
      A[p] = lds_tr2(aa, 64 + p * 512, 64 + p * 512 + 8);
      SCHED0();
    }
    aa = opq(aa + 64); ba = opq(ba + 64);
  }
  f32x4 y[4];
#pragma unroll
  for (int i = 0; i < 4; ++i) {
    *(LDSP(f32x4))(xbuf(cw) + (u32)(i * 1024 + lane * 16)) = var ? acc[i] : acc[4 + i];
    y[i] = var ? acc[4 + i] : acc[i];
  }
  lflag_set(L_XCH + cw * 4, 1);
  const u32 dv = *(LDSP(u32))L_DD;
  const float Dd = ch ? bfhi(dv) : bflo(dv);
  u32x2 uu[4];
#pragma unroll
  for (int i = 0; i < 4; ++i) uu[i] = *(LDSP(u32x2))(CV_U + (u32)ch * CV_USTR + (u32)(2044 - (1024 * var + 256 * i + 16 * m + 4 * g)) * 2);
  lflag_wait(L_XCH + (cw ^ 1) * 4, 0, 1);
#pragma unroll
  for (int i = 0; i < 4; ++i) {
    const int t = 1024 * var + 256 * i + 16 * m + 4 * g;
    const f32x4 s = y[i] + *(LDSP(f32x4))(xbuf(cw ^ 1) + (u32)(i * 1024 + lane * 16));
    float y0 = rbf(s[0]) + rbf(bfhi(uu[i][1]) * Dd), y1 = rbf(s[1]) + rbf(bflo(uu[i][1]) * Dd);
    float y2 = rbf(s[2]) + rbf(bfhi(uu[i][0]) * Dd), y3 = rbf(s[3]) + rbf(bflo(uu[i][0]) * Dd);
    st64_dev(rs, WS_YT + (u32)(d * (S * 2) + t * 2), u32x2{pk2(y0, y1), pk2(y2, y3)});
  }
}

DEV int g2_ks(int j, int i) { const int e = 7 * j + (i < 7 ? i : i - 7); return 4 * (e >> 1) + (e & 1) + (i < 7 ? 0 : 2); }
DEV u32 g2_place(int j, int p) { return (p < 3 ? 2 * SLOT + (u32)p * 4096 : (u32)(p - 3) * 4096) + (u32)j * 12288; }
DEV u32 w2_voff(int lane) { return (u32)((lane >> 3) * (D * 2) + (((lane & 7) ^ (((lane >> 4) & 1) << 2)) << 4)); }

DEV void share(int b, int n, const char* base) {
  const int lane = (int)opq(threadIdx.x) & 63;
  if (lane < n) touch((u32)(b * n + lane) * 128, base, L_DUMMY);
}

__global__ __launch_bounds__(768, 3) void fused_kernel(const char* x, const char* kp, const char* vp, const char* skp,
                                                       const char* ssmk, const char* ssmD, const char* w1, const char* w3,
                                                       const char* w2, char* ws, char* out) {
  const int b = blockIdx.x, xcd = b & 7, r = b >> 3;
  const int wid = __builtin_amdgcn_readfirstlane(threadIdx.x >> 6);
  const int panel = (xcd >> 1) * 4 + r / 7, ft = (xcd & 1) * 7 + r % 7;
  const int tau = xcd * 4 + r / 7, dl = r % 7;
  const int d0 = 2 * b;

  if (b < 224) {
    if (wid >= 4 && wid < 7) share(b, wid == 4 ? 64 : 56, wid == 4 ? x : (wid == 5 ? w1 : w3));
    if (wid == 7) { *(LDSP(u32))(MISC + (threadIdx.x & 63) * 4) = 0; WAIT_LGKM0(); }
    BAR();
    if (wid < 2) {
      const int lane = (int)opq(threadIdx.x) & 63;
      const int g = lane >> 4, l15 = lane & 15;
      u32 vx[8], vw[8];
      const u32 ox = (u32)((lane >> 3) * (D * 2) + (((lane & 7) ^ g) << 4));
      const u32 ow = (u32)(g * (F * 2) + (((l15 >> 2) ^ g) << 6) + ((lane & 3) << 4));
#pragma unroll
      for (int i = 0; i < 8; ++i) {
        vx[i] = (ox ^ (u32)((i & 1) * 64)) + i * 8 * (D * 2) - (i & 3) * 1024;
        vw[i] = ow + i * 4 * (F * 2) - (i & 3) * 1024;
      }
#pragma unroll 1
      for (int c = 0; c < 7; ++c) {
        if (c >= 3) BAR();
        const u32 slot = (u32)(c % 3) * SLOT + wid * 8192;
        const char* xb = x + (size_t)(panel * 128 + wid * 64) * (D * 2) + c * 128;
        const char* w1b = w1 + (size_t)(c * 64 + wid * 32) * (F * 2) + ft * 256;
        const char* w3b = w3 + (size_t)(c * 64 + wid * 32) * (F * 2) + ft * 256;
#pragma unroll
        for (int i = 0; i < 8; i += 4) {
          glds16x4(vx[i], vx[i + 1], vx[i + 2], vx[i + 3], xb, slot + i * 1024);
          glds16x4(vw[i], vw[i + 1], vw[i + 2], vw[i + 3], w1b, slot + 16384 + i * 1024);
          glds16x4(vw[i], vw[i + 1], vw[i + 2], vw[i + 3], w3b, slot + 32768 + i * 1024);
        }
        dflag_set(L_FULL + wid * 4, c + 1);
      }
    }
    if (wid == 7 && (threadIdx.x & 63) < 5) touch((threadIdx.x & 63) * 128, seq_tab, L_DUMMY);
  }

  if (__builtin_expect(wid < 4 && b < 224, 1)) {
    const int lane = (int)opq(threadIdx.x) & 63;
    u64 tag = 0;
    if (__builtin_expect(wid < 2, 1)) {
      if (wid == 0) {
        tag = make_tag(ws);
        const int k = lane & 7;
        gflag_wait<8>(ws + WS_FST + (k < 2 ? 2 * panel + k : (k == 2 ? tau : (k & 1) * 14 + ft)) * 128, tag);
        glds16_dev(lane * 16, ws + WS_STAT + panel * 1024, L_STAT);
        glds16_dev(lane * 16, ws + WS_CSUM + ft * 1024, L_CSUM);
        if (lane < 2) glds4_dev(lane * 4, ws + WS_E, L_TAG);
        if (lane < 1) glds4_dev(0, ssmD + d0 * 2, L_DD);
        dflag_set(L_EPI, 1);
      }
    } else {
      __builtin_amdgcn_s_setprio(3);
      const rsrc_t rs = ws_rsrc(ws);
      const int u = wid - 2;
      const int ti = b / 7, cj = b % 7;
      const int tg = lane >> 3, cg = lane & 7;
      const int t0 = 64 * ti + 32 * u + 4 * tg, ch0 = 64 * cj + 8 * cg;
      u32x4 kr[7], vr[4], sw[4]; u32x2 o[8];
      u64 seed;
      auto ldkv = [&]() {
        const u32 ko = (u32)(t0 * (D * 2) + ch0 * 2);
#pragma unroll
        for (int i = 0; i < 7; ++i) kr[i] = *(const u32x4*)(kp + (i < 3 && t0 == 0 ? ko : ko + (u32)((i - 3) * (D * 2))));
#pragma unroll
        for (int i = 0; i < 4; ++i) vr[i] = *(const u32x4*)(vp + (ko + (u32)(i * (D * 2))));
        SCHED0();
      };
      auto piece = [&](int p) {
#pragma unroll
        for (int c = 2 * p; c < 2 * p + 2; ++c) {
          const u32 wa = sw[p][(c & 1) * 2], wb = sw[p][(c & 1) * 2 + 1];
          const float w0 = bflo(wa), w1f = bfhi(wa), w2f = bflo(wb), w3f = bfhi(wb);
          float kf[7], ks[4];
#pragma unroll
          for (int i = 0; i < 7; ++i) kf[i] = (c & 1) ? bfhi(kr[i][p]) : bflo(kr[i][p]);
#pragma unroll
          for (int i = 0; i < 4; ++i)
            ks[i] = __builtin_fmaf(kf[i + 3], w0, __builtin_fmaf(kf[i + 2], w1f, __builtin_fmaf(kf[i + 1], w2f, kf[i] * w3f)));
#pragma unroll
          for (int i = 0; i < 2; ++i) {
            const int ta = 3 - 2 * i, tb = 2 - 2 * i;
            const u32 kk = pk2(ks[ta], ks[tb]);
            o[c][i] = pk2(bflo(kk) * ((c & 1) ? bfhi(vr[ta][p]) : bflo(vr[ta][p])), bfhi(kk) * ((c & 1) ? bfhi(vr[tb][p]) : bflo(vr[tb][p])));
          }
          asm volatile("" : "+v"(o[c]));
        }
        SCHED0();
      };
      BAR();
      ldkv();
#pragma unroll
      for (int i = 0; i < 4; ++i) sw[i] = *(const u32x4*)(skp + (u32)(ch0 * 8 + i * 16));
      seed = gload64(ws + WS_E + opq(0));
      if (u && lane < 14) {
        const u32 fo = lane < 8 ? WS_FH + (u32)((2 * panel + (lane & 1)) * 56 + 4 * ft + (lane >> 1)) * 128
                                : (lane < 12 ? WS_FY + (u32)((lane & 1) * 448 + d0 + ((lane >> 1) & 1)) * 128 : WS_FUT + (u32)(cj * 64 + 2 * ti + (lane & 1)) * 128);
        touch(fo, ws, L_DUMMY);
      }
      SCHED0();
      BAR();
      SCHED0();
      BAR();
      SCHED0();
#pragma unroll
      for (int i = 0; i < 3; ++i) if (t0 == 0) kr[i] = u32x4{0, 0, 0, 0};
      piece(0);
      BAR();
      SCHED0();
#pragma unroll
      for (int c = 1; c < 4; ++c) piece(c);
      tag = tag_of(seed);
      const u32 scr = L_USCR + (u32)u * 4096;
#pragma unroll
      for (int c = 0; c < 8; ++c) *(LDSP(u32x2))(scr + (u32)((cg * 8 + c) * 64 + (((7 - tg) ^ (cg & 6)) << 3))) = o[c];
      WAIT_LGKM0();
#pragma unroll
      for (int i = 0; i < 4; ++i) {
        const int dd = 16 * i + (lane >> 2), c4 = lane & 3;
        u32x4 v = *(LDSP(u32x4))(scr + (u32)(dd * 64 + ((c4 ^ (dd >> 4)) << 4)));
        st128_dev(rs, WS_UT + (u32)((64 * cj + dd) * (S * 2) + (2016 - 64 * ti - 32 * u) * 2 + c4 * 16), v);
      }
      gflag_publish(ws + WS_FUT + (cj * 64 + 2 * ti + u) * 128, tag, lane);
    }
    const int j = wid;
    const u32 o2 = w2_voff(lane);
    const char* wbase = w2 + dl * 128;
    u32x4 pk[8][4];
#pragma unroll
    for (int i = 0; i < 8; ++i) {
      const char* src = wbase + (size_t)g2_ks(j, i + 6) * 32 * (D * 2);
      asm volatile("" ::: "memory");
#pragma unroll
      for (int pc = 0; pc < 4; ++pc) pk[i][pc] = *(const u32x4*)(src + (o2 + (u32)(pc * 8 * (D * 2))));
    }
    SCHED0();
    lflag_wait(L_FREE, 7, 7);
    if (wid == 3) lflag_set(L_CONV + 12, 1);
    if (wid == 2) {
        if (lane < 32) {
          glds16(lane * 16, seq_tab + 32, CV_U + 4096);
          glds16(lane * 16, seq_tab + 32, CV_U + CV_USTR + 4096);
        }
        gflag_wait<8>(ws + WS_FUT + ((d0 >> 6) * 64 + lane) * 128, tag);
        glds16x4_dev(lane * 16, ws + WS_UT + d0 * (S * 2), CV_U);
        glds16x4_dev(lane * 16, ws + WS_UT + (d0 + 1) * (S * 2), CV_U + CV_USTR);
        dflag_set(L_CONV + 8, 1);
      }
    if (wid < 2) {
      const u32 vl = lane * 16;
#pragma unroll 1
      for (int rr = 0; rr < 4; ++rr) {
        const char* src = ssmk + (size_t)(d0 + wid) * (S * 2) + (rr ? (rr - 4) * 2 : 0);
        const u32 dst = CV + (u32)(wid * 4 + rr) * CV_HSTR + (rr ? 24 : 32);
        glds16x4(rr && !lane ? 16u : vl, vl, vl, vl, src, dst);
      }
      dflag_set(L_CONV + wid * 4, 1);
    }
    lflag_wait(L_PUB, 3, 1);
#pragma unroll 1
    for (int i = 0; i < 6; ++i) {
      if (i == 3) dflag_set(L_FED + j * 4, 3);
      const char* src = wbase + (size_t)g2_ks(j, i) * 32 * (D * 2);
      glds16x4(o2, o2 + 8 * (D * 2) - 1024, o2 + 16 * (D * 2) - 2048, o2 + 24 * (D * 2) - 3072, src, g2_place(j, i));
    }
    dflag_set(L_FED + j * 4, 6);
    if (wid == 2) {
      const int e = lane < 28 ? lane : 27;
      gflag_wait<4>(ws + WS_FH + (tau * 56 + 4 * (e >> 1) + (e & 1) + 2) * 128, tag);
      dflag_set(L_H, 1);
    }
    WAIT_VM(0);
    const u32 wl = opq(lane * 16 + j * 12288);
#pragma unroll
    for (int i = 0; i < 8; i += 2) {
      lflag_wait(L_PROG + j * 4, 0, i + 2);
#pragma unroll
      for (int n = 0; n < 8; ++n) lds_st128(wl + g2_place(0, (i + (n >> 2)) % 6) + (n & 3) * 1024, pk[i + (n >> 2)][n & 3]);
      lflag_set(L_FED + j * 4, i + 8);
    }
    return;
  }
  const rsrc_t rs = ws_rsrc(ws);

  if (b >= 224) {
    if (wid >= 8 && b != 255) return;
    const int tid = (int)opq(threadIdx.x), lane = tid & 63;
    const int sp = b - 224;
    const u64 seed = gload64(ws + WS_E + opq(0));
    if (tid == 0) touch(WS_FST + sp * 128, ws, L_DUMMY);
    if (wid < 8) {
      const int row = 64 * sp + (tid >> 3), c8 = tid & 7;
      const int cu = sp < 28 ? sp : 27, mat = cu / 14, ct = cu % 14, kg = tid >> 4, c16 = tid & 15;
      const char* wp = (mat ? w3 : w1) + ct * 256;
      const u32 wo = (u32)(kg * (F * 2) + c16 * 16), xo = (u32)(row * (D * 2) + c8 * 16);
      u32x4 xv[7], wv[14];
#pragma unroll
      for (int i = 0; i < 7; ++i) xv[i] = *(const u32x4*)(x + (xo + i * 128));
#pragma unroll
      for (int i = 0; i < 14; ++i) wv[i] = *(const u32x4*)(wp + (wo + i * 32 * (F * 2)));
      SCHED0();
      float s = 0.f;
#pragma unroll
      for (int i = 0; i < 7; ++i)
#pragma unroll
        for (int j = 0; j < 4; ++j) s += bflo(xv[i][j]) + bfhi(xv[i][j]);
      const float m = sum8(s) * (1.0f / D);
      float q = 0.f;
#pragma unroll
      for (int i = 0; i < 7; ++i)
#pragma unroll
        for (int j = 0; j < 4; ++j) {
          const float aa = bflo(xv[i][j]) - m, bb = bfhi(xv[i][j]) - m;
          q = __builtin_fmaf(aa, aa, __builtin_fmaf(bb, bb, q));
        }
      const float sd = rbf(__builtin_amdgcn_sqrtf(rbf(rbf(sum8(q) * (1.0f / D)) + 1e-5f)));
      if (c8 == 0) *(LDSP(f32x2))(u32)(16384 + (tid >> 3) * 8) = f32x2{rbf(m), __builtin_amdgcn_rcpf(sd)};
      SCHED0();
      float a[8] = {};
#pragma unroll
      for (int i = 0; i < 14; ++i)
#pragma unroll
        for (int j = 0; j < 4; ++j) { a[2 * j] += bflo(wv[i][j]); a[2 * j + 1] += bfhi(wv[i][j]); }
      f32x4 t;
#pragma unroll
      for (int j = 0; j < 4; ++j) {
        auto p = __builtin_amdgcn_permlane16_swap(__builtin_bit_cast(u32, a[j]), __builtin_bit_cast(u32, a[j + 4]), false, false);
        t[j] = asf(p[0]) + asf(p[1]);
      }
      *(LDSP(f32x4))(u32)((kg >> 1) * 512 + c16 * 32 + (kg & 1) * 16) = t;
    }
    WAIT_LGKM0(); BAR();
    const u64 tag = tag_of(seed);
    if (wid == 0) {
      const int cu = sp < 28 ? sp : 27;
      f32x2 cs = {};
#pragma unroll
      for (int i = 0; i < 16; ++i) cs += *(LDSP(f32x2))(u32)(i * 512 + lane * 8);
      st64_dev(rs, WS_STAT + (u32)(64 * sp + lane) * 8, *(LDSP(u32x2))(u32)(16384 + lane * 8));
      if (sp < 28) st64_dev(rs, WS_CSUM + (u32)((cu % 14) * 1024 + (cu / 14) * 512 + lane * 8), __builtin_bit_cast(u32x2, cs));
      gflag_publish(ws + WS_FST + sp * 128, tag, lane);
    }
    WAIT_VM(0); BAR();
    if (tid < 392) touch((u32)(sp * 392 + tid) * 128, w2, L_DUMMY);
    if (tid < 448) touch((u32)(sp * 448 + tid) * 128, ssmk, L_DUMMY);
    if (b == 255) {
      __builtin_amdgcn_s_sleep(127);
      gflag_wait<64>(ws + (tid < 480 ? WS_FUT + tid * 128 : WS_FH + ((((tid < 704 ? tid - 480 : 223) / 14) * 2) * 56 + ((tid < 704 ? tid - 480 : 223) % 14) * 4) * 128), tag);
      BAR();
      if (tid == 0) gstore64(ws + WS_E, tag);
    }
    return;
  }

  const int cw = wid - 4, th = cw & 1, fq = cw >> 1;
  {
    const int lane = (int)opq(threadIdx.x) & 63, g = lane >> 4, hh = lane >> 5, l31 = lane & 31;
    const int q4 = (lane >> 2) & 3, p4 = lane & 3;
    const u32 aA = 16384 + (u32)((8 * hh + q4) * 256 + ((fq ^ q4) << 6) + (g & 1) * 32 + p4 * 8);
    const u32 aB = (u32)((64 * th + l31) * 128 + ((hh ^ ((l31 >> 1) & 7)) << 4));
    f32x16 acc[2][2] = {};
    s16x8 fa[2][2][2], fb[2][2][2];
    u32 pa, pb[4];
    auto setslot = [&](u32 slot) {
      pa = opq(aA + slot);
#pragma unroll
      for (int k = 0; k < 4; ++k) pb[k] = opq((aB ^ (u32)(k << 5)) + slot);
    };
    auto rd = [&](int set) {
#pragma unroll
      for (int k = 0; k < 2; ++k) {
        const int ks = 2 * set + k;
        fa[set][k][0] = lds_tr2(pa, ks * 4096, ks * 4096 + 1024);
        fb[set][k][0] = lds_b128(pb[ks], 0);
        fb[set][k][1] = lds_b128(pb[ks], 4096);
        fa[set][k][1] = lds_tr2(pa, 16384 + ks * 4096, 16384 + ks * 4096 + 1024);
      }
    };
    auto mm = [&](int set, int n0, int n1) {
#pragma unroll
      for (int n = n0; n < n1; ++n) acc[(n >> 1) & 1][n & 1] = mfma32(fa[set][n >> 2][(n >> 1) & 1], fb[set][n >> 2][n & 1], acc[(n >> 1) & 1][n & 1]);
    };
    setslot(0);
    const u32 fad = opq(L_FULL + (lane & 1) * 4);
#pragma unroll
    for (int i = 0; i < 4; ++i) asm volatile("" : "+v"(acc[i >> 1][i & 1]));
    __builtin_amdgcn_s_setprio(1);
    lflag_wait(L_FULL, 1, 1);
    rd(0);
    SCHED0();
    u32 slot = 0;
    auto stage = [&](int c, bool bar) {
      rd(1);
      const u32 fl = *(volatile LDSP(u32))fad;
      mm(0, 0, 6);
      SGB(0x008, 1); SGB(0x100, 3); SGB(0x008, 1); SGB(0x100, 3); SGB(0x008, 1); SGB(0x100, 3); SGB(0x008, 1); SGB(0x100, 4); SGB(0x008, 2);
      SCHED0();
      slot = slot == 2 * SLOT ? 0 : slot + SLOT;
      setslot(slot);
      SCHED0();
      mm(0, 6, 7);
      SCHED0();
      __builtin_amdgcn_s_waitcnt(0xC07F);
      const bool miss = c < 6 && __builtin_amdgcn_ballot_w64(fl < (u32)(c + 2));
      SCHED0();
      mm(0, 7, 8);
      SCHED0();
      if (bar) BAR();
      SCHED0();
      mm(1, 0, 2);
      SCHED0();
      if (!bar && c == 6) lflag_set(L_FREE + cw * 4, 7);
      if (miss) lflag_wait(L_FULL, 1, c + 2);
      SCHED0();
      rd(0); mm(1, 2, 8);
#pragma unroll
      for (int i = 0; i < 4; ++i) { SGB(0x008, 1); SGB(0x100, 3); }
      SGB(0x008, 2);
      SCHED0();
    };
#pragma unroll 1
    for (int c = 0; c < 4; ++c) stage(c, true);
#pragma unroll 1
    for (int c = 4; c < 7; ++c) stage(c, false);
    lflag_wait(L_EPI, 0, 1);
    const u32 hdst = WS_H + (u32)((2 * panel + th) * 56 + 4 * ft + fq) * 4096 + lane * 16;
    float nm[2], c1[2], irs2[2];
#pragma unroll
    for (int j = 0; j < 2; ++j) {
      const f32x2 st = *(LDSP(f32x2))(L_STAT + (u32)(64 * th + 32 * j + l31) * 8);
      nm[j] = -st[0]; c1[j] = -1.44269504f * st[1]; irs2[j] = __builtin_amdgcn_rcpf(st[1] * st[1]);
    }
#pragma unroll
    for (int s = 0; s < 2; ++s) {
      f32x4 cs[2][2];
#pragma unroll
      for (int m = 0; m < 2; ++m)
#pragma unroll
        for (int G = 0; G < 2; ++G) cs[m][G] = *(LDSP(f32x4))(L_CSUM + (u32)(m * 128 + 32 * fq + 16 * s + 8 * G + 4 * hh) * 4);
#pragma unroll
      for (int j = 0; j < 2; ++j) {
        float a[8], ex[8], hv[8];
#pragma unroll
        for (int e = 0; e < 8; ++e) a[e] = __builtin_fmaf(nm[j], cs[0][e >> 2][e & 3], acc[0][j][8 * s + e]);
#pragma unroll
        for (int e = 0; e < 8; ++e) ex[e] = a[e] * c1[j];
        SCHED0();
#pragma unroll
        for (int e = 0; e < 8; ++e) { ex[e] = __builtin_amdgcn_exp2f(ex[e]); hv[e] = __builtin_fmaf(nm[j], cs[1][e >> 2][e & 3], acc[1][j][8 * s + e]); }
        SCHED0();
#pragma unroll
        for (int e = 0; e < 8; ++e) ex[e] = __builtin_fmaf(ex[e], irs2[j], irs2[j]);
        SCHED0();
#pragma unroll
        for (int e = 0; e < 8; ++e) { ex[e] = __builtin_amdgcn_rcpf(ex[e]); hv[e] *= a[e]; }
        SCHED0();
#pragma unroll
        for (int e = 0; e < 8; ++e) hv[e] *= ex[e];
        st128_dev(rs, hdst + (s * 2 + j) * 1024, u32x4{pk2(hv[0], hv[1]), pk2(hv[2], hv[3]), pk2(hv[4], hv[5]), pk2(hv[6], hv[7])});
      }
    }
  }
  char* hflag = ws + WS_FH + ((2 * panel + th) * 56 + 4 * ft + fq) * 128;
  u32x4 xv, sv;
  if (cw < 4) {
    const int ht = (int)opq(threadIdx.x) & 255, lane = ht & 63, l15 = ht & 15;
    const u64 tag = tag_of(*(volatile LDSP(u64))L_TAG);
    const int ch = cw >> 1, var = cw & 1;
    gflag_publish(hflag, tag, lane);
    u32x4 xr[2]; u32x2 st[2];
#pragma unroll
    for (int i = 0; i < 2; ++i) {
      const int n = ht + 256 * i, row = n >> 3, c = n & 7;
      xr[i] = *(const u32x4*)(x + (u32)((64 * tau + row) * (D * 2) + (64 * dl + 8 * c) * 2));
      st[i] = ld64_dev(rs, WS_STAT + (u32)(64 * tau + row) * 8);
    }
    SCHED0();
    lflag_wait(L_CONV, 3, 1);
    asm volatile("" ::: "memory");
    conv_compute(lane, cw, d0 + ch, rs);
    u32x4 xn[2];
#pragma unroll
    for (int i = 0; i < 2; ++i) {
      const float mb = asf(st[i][0]), rsd = asf(st[i][1]);
#pragma unroll
      for (int e = 0; e < 4; ++e) {
        const u32 dd = pk2(bflo(xr[i][e]) - mb, bfhi(xr[i][e]) - mb);
        xn[i][e] = pk2(bflo(dd) * rsd, bfhi(dd) * rsd);
      }
    }
    SCHED0();
    gflag_publish(ws + WS_FY + (var * 448 + d0 + ch) * 128, tag, lane);
    xv = xr[0]; sv = xn[0];
    lflag_wait(L_XCH, 1, 1);
    lds_st128(E_S + (u32)ht * 16, xn[1]); lds_st128(E_X + (u32)ht * 16, xr[1]);
    gflag_wait<4>(ws + WS_FY + ((tau >> 4) * 448 + 64 * dl + 16 * cw + l15) * 128, tag);
#pragma unroll
    for (int i = 0; i < 2; ++i)
      glds16_dev((u32)((lane >> 3) * (S * 2) + (((lane & 7) ^ (2 * cw + i)) << 4)),
                 ws + WS_YT + (64 * dl + 16 * cw + 8 * i) * (S * 2) + 128 * tau, xbuf(cw ^ 1) + (u32)i * 1024);
    WAIT_VM(0);
  } else {
    const int j = cw - 4;
    const int lane = (int)opq(threadIdx.x) & 63;
    lflag_set(L_PUB + j * 4, 1);
    const u64 btag = tag_of(*(volatile LDSP(u64))L_TAG);
    gflag_publish(hflag, btag, lane);
    const u32 hl = lane * 16;
    const char* hbase = ws + WS_H + (size_t)tau * 56 * 4096;
    const int q4 = (lane >> 2) & 3;
    const u32 ta = (u32)((4 * (lane >> 5) + q4) * 128 + ((q4 >> 1) << 6) + ((lane >> 4) & 1) * 32 + (lane & 3) * 8);
    s16x8 fa[2][4], fb[3][4];
    f32x16 acc[2][2] = {};
    auto ldh = [&](int i) {
      const char* p = hbase + (size_t)g2_ks(j, i) * 4096;
      asm volatile("global_load_dwordx4 %0, %4, %5 sc1\n\tglobal_load_dwordx4 %1, %4, %5 offset:1024 sc1\n\t"
                   "global_load_dwordx4 %2, %4, %5 offset:2048 sc1\n\tglobal_load_dwordx4 %3, %4, %5 offset:3072 sc1"
                   : "=&v"(fb[i % 3][0]), "=&v"(fb[i % 3][1]), "=&v"(fb[i % 3][2]), "=&v"(fb[i % 3][3])
                   : "v"(hl), "s"(p) : "memory");
    };
    u32 tb[2];
#pragma unroll
    for (int a = 0; a < 2; ++a) tb[a] = opq((ta ^ (u32)(a << 6)) + g2_place(j, 0));
    auto rda = [&](int i) {
#pragma unroll
      for (int a = 0; a < 2; ++a)
        if (i > 0 && i % 3 == 0) tb[a] = opq(i % 6 ? tb[a] - 2 * SLOT : tb[a] + 2 * SLOT);
#pragma unroll
      for (int n = 0; n < 4; ++n) {
        const int o = (i % 3) * 4096 + (n >> 1) * 2048;
        fa[i & 1][n] = lds_tr2(tb[n & 1], o, o + 1024);
      }
    };
    const u32 fed = opq(L_FED + j * 4), prog = opq(L_PROG + j * 4);
    gflag_wait<2>(ws + WS_FH + (tau * 56 + g2_ks(j, lane < 7 ? lane : 6)) * 128, btag);
#pragma unroll
    for (int i = 0; i < 3; ++i) ldh(i);
    lflag_wait(L_FED + j * 4, 0, 3);
    rda(0);
    SCHED0();
    u32 fl = 0;
#pragma unroll
    for (int i = 0; i < 14; ++i) {
      if (i == 2 || (i >= 5 && i < 13 && (i & 1))) {
        if (__builtin_amdgcn_readfirstlane(fl) < i + 2) lflag_wait(L_FED + j * 4, 0, i + 2);
      }
      asm volatile("s_waitcnt vmcnt(%4)" : "+v"(fb[i % 3][0]), "+v"(fb[i % 3][1]), "+v"(fb[i % 3][2]), "+v"(fb[i % 3][3])
                   : "i"(i < 12 ? 8 : (13 - i) * 4) : "memory");
      SCHED0();
      if (i < 13) rda(i + 1);
      if (i == 1 || (i >= 4 && i < 12 && !(i & 1))) fl = *(volatile LDSP(u32))fed;
#pragma unroll
      for (int s = 0; s < 2; ++s)
#pragma unroll
        for (int a = 0; a < 2; ++a)
#pragma unroll
          for (int t = 0; t < 2; ++t) acc[a][t] = mfma32(fa[i & 1][2 * s + a], fb[i % 3][2 * s + t], acc[a][t]);
      if (i < 13) {
#pragma unroll
        for (int n = 0; n < 5; ++n) { SGB(0x008, 1); SGB(0x100, 2); }
        SGB(0x008, 3);
      }
      SCHED0();
      if (i < 8 && (i & 1)) *(volatile LDSP(u32))prog = i + 1;
      if (i == 4) lflag_wait(L_H, 0, 1);
      if (i + 3 < 14) ldh(i + 3);
      SCHED0();
    }
    const int ln = (int)opq(threadIdx.x), lt = ln & 31, lh = (ln >> 5) & 1;
#pragma unroll
    for (int t = 0; t < 2; ++t)
#pragma unroll
      for (int a = 0; a < 2; ++a)
#pragma unroll
        for (int G = 0; G < 4; ++G)
          *(LDSP(f32x4))(g2_place(j, t ? 3 : 0) + (u32)(lt * 256 + (((8 * a + 2 * G + lh) ^ (lt & 7)) << 4))) =
              f32x4{acc[a][t][4 * G], acc[a][t][4 * G + 1], acc[a][t][4 * G + 2], acc[a][t][4 * G + 3]};
  }
  WAIT_LGKM0();
  BAR();
  {
    const int ht = (int)opq(threadIdx.x) & 255;
    const int half = cw >> 2, tk = ht >> 3, dg = ht & 7;
    if (half) { sv = *(LDSP(u32x4))(E_S + (u32)ht * 16); xv = *(LDSP(u32x4))(E_X + (u32)ht * 16); }
    f32x4 s0 = {}, s1 = {};
#pragma unroll
    for (int w = 0; w < 4; ++w) {
      const u32 pb = (half ? 0u : 2 * SLOT) + (u32)w * 12288 + (u32)tk * 256;
      s0 += *(LDSP(f32x4))(pb + (u32)(((2 * dg) ^ (tk & 7)) << 4));
      s1 += *(LDSP(f32x4))(pb + (u32)(((2 * dg + 1) ^ (tk & 7)) << 4));
    }
    const int tok = 32 * half + tk;
    const u32 ya = xbuf((dg >> 1) ^ 1) + (u32)(8 * (dg & 1) * 128 + (((tok >> 3) ^ dg) << 4) + (tok & 7) * 2);
    float o[8];
#pragma unroll
    for (int e = 0; e < 8; ++e) {
      const float f = rbf(e < 4 ? s0[e & 3] : s1[e & 3]);
      const float gt = rbf(silu_f(f));
      const float xe = (e & 1) ? bfhi(xv[e >> 1]) : bflo(xv[e >> 1]);
      const float ne = (e & 1) ? bfhi(sv[e >> 1]) : bflo(sv[e >> 1]);
      const float se = rbf(ne * bflo(*(LDSP(u16))(ya + e * 128)));
      o[e] = xe + rbf(se * gt);
    }
    __builtin_amdgcn_raw_buffer_store_b128(u32x4{pk2(o[0], o[1]), pk2(o[2], o[3]), pk2(o[4], o[5]), pk2(o[6], o[7])},
        __builtin_amdgcn_make_buffer_rsrc(out, 0, S * D * 2, 0x00020000), (64 * tau + 32 * half + tk) * (D * 2) + (64 * dl + 8 * dg) * 2, 0, 16);
  }
}
}
#if defined(__HIP_DEVICE_COMPILE__) && __HIP_DEVICE_COMPILE__
#pragma clang attribute pop
#endif

extern "C" void kernel_launch(void* const* d_in, const int* in_sizes, int n_in, void* d_out, int out_size, void* d_ws,
                              size_t ws_size, hipStream_t stream) {
  (void)hipFuncSetAttribute((const void*)fused_kernel, hipFuncAttributeMaxDynamicSharedMemorySize, 163840);
  fused_kernel<<<256, 768, 163840, stream>>>((const char*)d_in[0], (const char*)d_in[1], (const char*)d_in[2], (const char*)d_in[3],
                                             (const char*)d_in[4], (const char*)d_in[5], (const char*)d_in[7], (const char*)d_in[8],
                                             (const char*)d_in[9], (char*)d_ws, (char*)d_out);
}
```

```cpp
#define PK_A 8
#include <hip/hip_runtime.h>
#include <hip/hip_bf16.h>
#include <stdint.h>
#pragma clang diagnostic ignored "-Wint-to-pointer-cast"

#if defined(__HIP_DEVICE_COMPILE__) && __HIP_DEVICE_COMPILE__
#pragma clang attribute push(__attribute__((target("no-packed-fp32-ops"))), apply_to = function)
#endif
namespace {
typedef unsigned short u16;
typedef unsigned int u32;
typedef unsigned long long u64;
typedef __attribute__((ext_vector_type(4))) short s16x4;
typedef __attribute__((ext_vector_type(8))) short s16x8;
typedef __attribute__((ext_vector_type(8))) __bf16 bf16x8;
typedef __attribute__((ext_vector_type(2))) __bf16 bf16x2;
typedef __attribute__((ext_vector_type(16))) float f32x16;
typedef __attribute__((ext_vector_type(4))) float f32x4;
typedef __attribute__((ext_vector_type(2))) float f32x2;
typedef __attribute__((ext_vector_type(4))) u32 u32x4;
typedef __attribute__((ext_vector_type(2))) u32 u32x2;
#define LDSP(T) __attribute__((address_space(3))) T*
#define GLBP(T) __attribute__((address_space(1))) T*
#define DEV __device__ __forceinline__

constexpr int S = 2048, D = 448, F = 1792;
constexpr u32 WS_H = 0;
constexpr u32 WS_UT = WS_H + 7340032;
constexpr u32 WS_YT = WS_UT + 1835008;
constexpr u32 WS_STAT = WS_YT + 1835008;
constexpr u32 WS_CSUM = WS_STAT + 16384;
constexpr u32 WS_E = WS_CSUM + 16384;
constexpr u32 WS_FUT = WS_E + 128;
constexpr u32 WS_FST = WS_FUT + 448 * 128;
constexpr u32 WS_FY = WS_FST + 32 * 128;
constexpr u32 WS_FH = WS_FY + 896 * 128;
constexpr u32 WS_END = WS_FH + 1792 * 128;
constexpr u32 SLOT = 49152;
constexpr u32 MISC = 3 * SLOT;
constexpr u32 L_FULL = MISC, L_EPI = MISC + 8, L_H = MISC + 16, L_FREE = MISC + 32, L_CONV = MISC + 64, L_FED = MISC + 80,
              L_TAG = MISC + 96, L_DD = MISC + 104, L_PROG = MISC + 128, L_XCH = MISC + 144, L_PUB = MISC + 160, L_STAT = MISC + 256,
              L_CSUM = MISC + 1280, L_USCR = MISC + 4096, L_DUMMY = MISC + 2304, L_XB2 = MISC + 12288;
constexpr u32 CV = SLOT, CV_HSTR = 4160, CV_U = CV + 8 * CV_HSTR, CV_USTR = 4608;
constexpr u32 E_S = SLOT, E_X = SLOT + 4096;
constexpr int SPIN_CAP = 1 << 18;

__device__ const u32 seq_tab[160] __attribute__((aligned(128))) = {0, 1, 2, 3, 4, 5, 6, 7};
DEV float asf(u32 v) { return __builtin_bit_cast(float, v); }
DEV float bflo(u32 v) { return asf(v << 16); }
DEV float bfhi(u32 v) { return asf(v & 0xffff0000u); }
template <int CTRL> DEV float dpp(float v) { return asf(__builtin_amdgcn_update_dpp(0, __builtin_bit_cast(u32, v), CTRL, 0xf, 0xf, true)); }
DEV float sum8(float v) { v += dpp<0xB1>(v); v += dpp<0x4E>(v); return v + dpp<0x141>(v); }
DEV u32 pk2(float lo, float hi) {
  asm("" : "+v"(lo)); asm("" : "+v"(hi));
  bf16x2 r = {(__bf16)lo, (__bf16)hi};
  return __builtin_bit_cast(u32, r);
}
DEV float rbf(float f) { bf16x2 r = {(__bf16)f, (__bf16)f}; return bflo(__builtin_bit_cast(u32, r)); }
DEV f32x4 mfma16(s16x8 a, s16x8 b, f32x4 c) {
  return __builtin_amdgcn_mfma_f32_16x16x32_bf16(__builtin_bit_cast(bf16x8, a), __builtin_bit_cast(bf16x8, b), c, 0, 0, 0);
}
DEV f32x16 mfma32(s16x8 a, s16x8 b, f32x16 c) {
  return __builtin_amdgcn_mfma_f32_32x32x16_bf16(__builtin_bit_cast(bf16x8, a), __builtin_bit_cast(bf16x8, b), c, 0, 0, 0);
}
DEV u32 opq(u32 v) { asm volatile("" : "+v"(v)); return v; }
DEV s16x8 lds_tr2(u32 base, int o0, int o1) {
  s16x4 lo = __builtin_amdgcn_ds_read_tr16_b64_v4i16((LDSP(s16x4))((LDSP(char))base + o0));
  s16x4 hi = __builtin_amdgcn_ds_read_tr16_b64_v4i16((LDSP(s16x4))((LDSP(char))base + o1));
  return __builtin_shufflevector(lo, hi, 0, 1, 2, 3, 4, 5, 6, 7);
}
DEV s16x8 lds_b128(u32 base, int o) { return *(LDSP(s16x8))((LDSP(char))base + o); }
DEV void lds_st128(u32 a, u32x4 v) { *(LDSP(u32x4))a = v; }
DEV void glds16x4(u32 v0, u32 v1, u32 v2, u32 v3, const void* sbase, u32 ldst) {
  asm volatile("s_mov_b32 m0, %5\n\ts_nop 0\n\tglobal_load_lds_dwordx4 %0, %4\n\tglobal_load_lds_dwordx4 %1, %4 offset:1024\n\t"
               "global_load_lds_dwordx4 %2, %4 offset:2048\n\tglobal_load_lds_dwordx4 %3, %4 offset:3072"
               ::"v"(v0), "v"(v1), "v"(v2), "v"(v3), "s"(sbase), "s"(ldst) : "memory");
}
DEV void glds16(u32 voff, const void* sbase, u32 ldst) {
  asm volatile("s_mov_b32 m0, %2\n\ts_nop 0\n\tglobal_load_lds_dwordx4 %0, %1" ::"v"(voff), "s"(sbase), "s"(ldst) : "memory");
}
DEV void glds16x4_dev(u32 v, const void* sbase, u32 ldst) {
  asm volatile("s_mov_b32 m0, %2\n\ts_nop 0\n\tglobal_load_lds_dwordx4 %0, %1 sc1\n\tglobal_load_lds_dwordx4 %0, %1 offset:1024 sc1\n\t"
               "global_load_lds_dwordx4 %0, %1 offset:2048 sc1\n\tglobal_load_lds_dwordx4 %0, %1 offset:3072 sc1"
               ::"v"(v), "s"(sbase), "s"(ldst) : "memory");
}
DEV void glds16_dev(u32 voff, const void* sbase, u32 ldst) {
  asm volatile("s_mov_b32 m0, %2\n\ts_nop 0\n\tglobal_load_lds_dwordx4 %0, %1 sc1" ::"v"(voff), "s"(sbase), "s"(ldst) : "memory");
}
DEV void dflag_set(u32 addr, u32 val) {
  asm volatile("s_waitcnt lgkmcnt(0)\n\ts_mov_b32 m0, %2\n\ts_mov_b64 exec, 1\n\tglobal_load_lds_dword %0, %1\n\ts_mov_b64 exec, -1"
               ::"v"(0), "s"(seq_tab + val), "s"(addr) : "memory");
}
#define WAIT_VM(n) asm volatile("s_waitcnt vmcnt(%0)" ::"i"(n) : "memory")
#define WAIT_LGKM0() asm volatile("s_waitcnt lgkmcnt(0)" ::: "memory")
#define BAR() do { asm volatile("" ::: "memory"); __builtin_amdgcn_s_barrier(); asm volatile("" ::: "memory"); } while (0)
#define SCHED0() __builtin_amdgcn_sched_barrier(0)
#define SGB(mask, n) __builtin_amdgcn_sched_group_barrier(mask, n, 0)
DEV float silu_f(float x) { return x * __builtin_amdgcn_rcpf(1.0f + __builtin_amdgcn_exp2f(-1.44269504f * x)); }

DEV void lflag_set(u32 addr, u32 val) {
  asm volatile("s_waitcnt lgkmcnt(0)\n\tds_write_b32 %0, %1" ::"v"(addr), "v"(val) : "memory");
}
DEV void lflag_wait(u32 addr, u32 mask, u32 val) {
  const u32 a = addr + 4 * (threadIdx.x & mask);
#pragma unroll 1
  for (int i = 0; i < SPIN_CAP; ++i) {
    u32 v;
    asm volatile("ds_read_b32 %0, %1\n\ts_waitcnt lgkmcnt(0)" : "=v"(v) : "v"(a) : "memory");
    if (__builtin_amdgcn_ballot_w64(v < val) == 0) break;
    __builtin_amdgcn_s_sleep(1);
  }
}
DEV u64 gload64(const char* p) { return __hip_atomic_load((GLBP(u64))p, __ATOMIC_RELAXED, __HIP_MEMORY_SCOPE_AGENT); }
DEV void gstore64(char* p, u64 v) { __hip_atomic_store((GLBP(u64))p, v, __ATOMIC_RELAXED, __HIP_MEMORY_SCOPE_AGENT); }
template <int SLEEP>
DEV void gflag_wait(const char* lane_ptr, u64 tag) {
  bool ok = false;
#pragma unroll 1
  for (int i = 0; i < SPIN_CAP; ++i) {
    asm volatile("" ::: "memory");
    if (!ok) ok = gload64(lane_ptr) == tag;
    if (__builtin_amdgcn_ballot_w64(!ok) == 0) break;
    __builtin_amdgcn_s_sleep(SLEEP);
  }
  asm volatile("" ::: "memory");
}
DEV void gflag_publish(char* p, u64 tag, int lane) {
  WAIT_VM(0);
  if (lane == 0) gstore64(p, tag);
}
DEV u64 tag_of(u64 seed) { return seed * 0x9E3779B97F4A7C15ull + 0xD1B54A32D192ED03ull; }
DEV u64 make_tag(const char* ws) { return tag_of(gload64(ws + WS_E)); }
typedef __amdgpu_buffer_rsrc_t rsrc_t;
DEV rsrc_t ws_rsrc(char* ws) { return __builtin_amdgcn_make_buffer_rsrc(ws, 0, WS_END, 0x00020000); }
DEV void st128_dev(rsrc_t r, u32 off, u32x4 v) { __builtin_amdgcn_raw_buffer_store_b128(v, r, off, 0, 16); }
DEV void st64_dev(rsrc_t r, u32 off, u32x2 v) { __builtin_amdgcn_raw_buffer_store_b64(v, r, off, 0, 16); }
DEV u32x2 ld64_dev(rsrc_t r, u32 off) { return __builtin_amdgcn_raw_buffer_load_b64(r, off, 0, 16); }
DEV void touch(u32 voff, const void* sbase, u32 ldst) {
  asm volatile("s_mov_b32 m0, %2\n\ts_nop 0\n\tglobal_load_lds_dword %0, %1" ::"v"(voff), "s"(sbase), "s"(ldst) : "memory");
}

DEV void glds4_dev(u32 voff, const void* sbase, u32 ldst) {
  asm volatile("s_mov_b32 m0, %2\n\ts_nop 0\n\tglobal_load_lds_dword %0, %1 sc1" ::"v"(voff), "s"(sbase), "s"(ldst) : "memory");
}

DEV u32 xbuf(int w) { return w < 2 ? L_USCR + (u32)w * 4096 : (w == 2 ? L_XB2 : CV + 45056); }
DEV void conv_compute(int lane, int cw, int d, rsrc_t rs) {
  const int ch = cw >> 1, var = cw & 1;
  const int m = lane & 15, g = lane >> 4, q4 = m >> 2, p4 = lane & 3;
  u32 aa = opq(CV + (u32)(ch * 4 + ((q4 + 1) & 3)) * CV_HSTR + (u32)(((q4 + 1) & 4) + 8 * g + 4 * p4) * 2 + var * 256);
  u32 ba = opq(CV_U + (u32)ch * CV_USTR + (u32)(240 - 16 * m + 8 * g) * 2 + var * 256);
  f32x4 acc[8] = {};
  s16x8 A[8], B[8];
#pragma unroll
  for (int p = 0; p < 8; ++p) { A[7 - p] = lds_tr2(aa, (7 - p) * 512, (7 - p) * 512 + 8); B[p] = lds_b128(ba, (7 - p) * 512); }
  if (var == 0) {
    const u32 a0 = CV + (u32)ch * 4 * CV_HSTR + (u32)(m + 1 + 8 * g) * 2;
#pragma unroll
    for (int j = 0; j < 8; ++j) { const short v = *(LDSP(short))(a0 + j * 2); A[0][j] = m + 8 * g + j >= 15 ? v : (short)0; }
  }
  SCHED0();
#pragma unroll 1
  for (int it = 0; it < 4; ++it) {
#pragma unroll
    for (int p = 7; p >= 0; --p) {
#pragma unroll
      for (int dd = 0; dd <= 7 - p; ++dd) {
        acc[p + dd] = mfma16(A[p], B[dd], acc[p + dd]);
        if (p == 0) { SCHED0(); B[dd] = lds_b128(ba, 64 + (7 - dd) * 512); SCHED0(); }
      }
      SCHED0();
      A[p] = lds_tr2(aa, 64 + p * 512, 64 + p * 512 + 8);
      SCHED0();
    }
    aa = opq(aa + 64); ba = opq(ba + 64);
  }
  f32x4 y[4];
#pragma unroll
  for (int i = 0; i < 4; ++i) {
    *(LDSP(f32x4))(xbuf(cw) + (u32)(i * 1024 + lane * 16)) = var ? acc[i] : acc[4 + i];
    y[i] = var ? acc[4 + i] : acc[i];
  }
  lflag_set(L_XCH + cw * 4, 1);
  const u32 dv = *(LDSP(u32))L_DD;
  const float Dd = ch ? bfhi(dv) : bflo(dv);
  u32x2 uu[4];
#pragma unroll
  for (int i = 0; i < 4; ++i) uu[i] = *(LDSP(u32x2))(CV_U + (u32)ch * CV_USTR + (u32)(2044 - (1024 * var + 256 * i + 16 * m + 4 * g)) * 2);
  lflag_wait(L_XCH + (cw ^ 1) * 4, 0, 1);
#pragma unroll
  for (int i = 0; i < 4; ++i) {
    const int t = 1024 * var + 256 * i + 16 * m + 4 * g;
    const f32x4 s = y[i] + *(LDSP(f32x4))(xbuf(cw ^ 1) + (u32)(i * 1024 + lane * 16));
    float y0 = rbf(s[0]) + rbf(bfhi(uu[i][1]) * Dd), y1 = rbf(s[1]) + rbf(bflo(uu[i][1]) * Dd);
    float y2 = rbf(s[2]) + rbf(bfhi(uu[i][0]) * Dd), y3 = rbf(s[3]) + rbf(bflo(uu[i][0]) * Dd);
    st64_dev(rs, WS_YT + (u32)(d * (S * 2) + t * 2), u32x2{pk2(y0, y1), pk2(y2, y3)});
  }
}

DEV int g2_ks(int j, int i) { const int e = 7 * j + (i < 7 ? i : i - 7); return 4 * (e >> 1) + (e & 1) + (i < 7 ? 0 : 2); }
DEV u32 g2_place(int j, int p) { return (p < 3 ? 2 * SLOT + (u32)p * 4096 : (u32)(p - 3) * 4096) + (u32)j * 12288; }
DEV u32 w2_voff(int lane) { return (u32)((lane >> 3) * (D * 2) + (((lane & 7) ^ (((lane >> 4) & 1) << 2)) << 4)); }

DEV void share(int b, int n, const char* base) {
  const int lane = (int)opq(threadIdx.x) & 63;
  if (lane < n) touch((u32)(b * n + lane) * 128, base, L_DUMMY);
}

__global__ __launch_bounds__(768, 3) void fused_kernel(const char* x, const char* kp, const char* vp, const char* skp,
                                                       const char* ssmk, const char* ssmD, const char* w1, const char* w3,
                                                       const char* w2, char* ws, char* out) {
  const int b = blockIdx.x, xcd = b & 7, r = b >> 3;
  const int wid = __builtin_amdgcn_readfirstlane(threadIdx.x >> 6);
  const int panel = (xcd >> 1) * 4 + r / 7, ft = (xcd & 1) * 7 + r % 7;
  const int tau = xcd * 4 + r / 7, dl = r % 7;
  const int d0 = 2 * b;

  if (b < 224) {
    if (wid >= 4 && wid < 7) share(b, wid == 4 ? 64 : 56, wid == 4 ? x : (wid == 5 ? w1 : w3));
    if (wid == 7) { *(LDSP(u32))(MISC + (threadIdx.x & 63) * 4) = 0; WAIT_LGKM0(); }
    BAR();
    if (wid < 2) {
      const int lane = (int)opq(threadIdx.x) & 63;
      const int g = lane >> 4, l15 = lane & 15;
      u32 vx[8], vw[8];
      const u32 ox = (u32)((lane >> 3) * (D * 2) + (((lane & 7) ^ g) << 4));
      const u32 ow = (u32)(g * (F * 2) + (((l15 >> 2) ^ g) << 6) + ((lane & 3) << 4));
#pragma unroll
      for (int i = 0; i < 8; ++i) {
        vx[i] = (ox ^ (u32)((i & 1) * 64)) + i * 8 * (D * 2) - (i & 3) * 1024;
        vw[i] = ow + i * 4 * (F * 2) - (i & 3) * 1024;
      }
#pragma unroll 1
      for (int c = 0; c < 7; ++c) {
        if (c >= 3) BAR();
        const u32 slot = (u32)(c % 3) * SLOT + wid * 8192;
        const char* xb = x + (size_t)(panel * 128 + wid * 64) * (D * 2) + c * 128;
        const char* w1b = w1 + (size_t)(c * 64 + wid * 32) * (F * 2) + ft * 256;
        const char* w3b = w3 + (size_t)(c * 64 + wid * 32) * (F * 2) + ft * 256;
#pragma unroll
        for (int i = 0; i < 8; i += 4) {
          glds16x4(vx[i], vx[i + 1], vx[i + 2], vx[i + 3], xb, slot + i * 1024);
          glds16x4(vw[i], vw[i + 1], vw[i + 2], vw[i + 3], w1b, slot + 16384 + i * 1024);
          glds16x4(vw[i], vw[i + 1], vw[i + 2], vw[i + 3], w3b, slot + 32768 + i * 1024);
        }
        dflag_set(L_FULL + wid * 4, c + 1);
      }
    }
    if (wid == 7 && (threadIdx.x & 63) < 5) touch((threadIdx.x & 63) * 128, seq_tab, L_DUMMY);
  }

  if (__builtin_expect(wid < 4 && b < 224, 1)) {
    const int lane = (int)opq(threadIdx.x) & 63;
    u64 tag = 0;
    if (__builtin_expect(wid < 2, 1)) {
      if (wid == 0) {
        tag = make_tag(ws);
        const int k = lane & 7;
        gflag_wait<8>(ws + WS_FST + (k < 2 ? 2 * panel + k : (k == 2 ? tau : (k & 1) * 14 + ft)) * 128, tag);
        glds16_dev(lane * 16, ws + WS_STAT + panel * 1024, L_STAT);
        glds16_dev(lane * 16, ws + WS_CSUM + ft * 1024, L_CSUM);
        if (lane < 2) glds4_dev(lane * 4, ws + WS_E, L_TAG);
        if (lane < 1) glds4_dev(0, ssmD + d0 * 2, L_DD);
        dflag_set(L_EPI, 1);
      }
    } else {
      __builtin_amdgcn_s_setprio(3);
      const rsrc_t rs = ws_rsrc(ws);
      const int u = wid - 2;
      const int ti = b / 7, cj = b % 7;
      const int tg = lane >> 3, cg = lane & 7;
      const int t0 = 64 * ti + 32 * u + 4 * tg, ch0 = 64 * cj + 8 * cg;
      u32x4 kr[7], vr[4], sw[4]; u32x2 o[8];
      u64 seed;
      auto ldkv = [&]() {
        const u32 ko = (u32)(t0 * (D * 2) + ch0 * 2);
#pragma unroll
        for (int i = 0; i < 7; ++i) kr[i] = *(const u32x4*)(kp + (i < 3 && t0 == 0 ? ko : ko + (u32)((i - 3) * (D * 2))));
#pragma unroll
        for (int i = 0; i < 4; ++i) vr[i] = *(const u32x4*)(vp + (ko + (u32)(i * (D * 2))));
        SCHED0();
      };
      auto piece = [&](int p) {
#pragma unroll
        for (int c = 2 * p; c < 2 * p + 2; ++c) {
          const u32 wa = sw[p][(c & 1) * 2], wb = sw[p][(c & 1) * 2 + 1];
          const float w0 = bflo(wa), w1f = bfhi(wa), w2f = bflo(wb), w3f = bfhi(wb);
          float kf[7], ks[4];
#pragma unroll
          for (int i = 0; i < 7; ++i) kf[i] = (c & 1) ? bfhi(kr[i][p]) : bflo(kr[i][p]);
#pragma unroll
          for (int i = 0; i < 4; ++i)
            ks[i] = __builtin_fmaf(kf[i + 3], w0, __builtin_fmaf(kf[i + 2], w1f, __builtin_fmaf(kf[i + 1], w2f, kf[i] * w3f)));
#pragma unroll
          for (int i = 0; i < 2; ++i) {
            const int ta = 3 - 2 * i, tb = 2 - 2 * i;
            const u32 kk = pk2(ks[ta], ks[tb]);
            o[c][i] = pk2(bflo(kk) * ((c & 1) ? bfhi(vr[ta][p]) : bflo(vr[ta][p])), bfhi(kk) * ((c & 1) ? bfhi(vr[tb][p]) : bflo(vr[tb][p])));
          }
          asm volatile("" : "+v"(o[c]));
        }
        SCHED0();
      };
      BAR();
      ldkv();
#pragma unroll
      for (int i = 0; i < 4; ++i) sw[i] = *(const u32x4*)(skp + (u32)(ch0 * 8 + i * 16));
      seed = gload64(ws + WS_E + opq(0));
      if (u && lane < 14) {
        const u32 fo = lane < 8 ? WS_FH + (u32)((2 * panel + (lane & 1)) * 56 + 4 * ft + (lane >> 1)) * 128
                                : (lane < 12 ? WS_FY + (u32)((lane & 1) * 448 + d0 + ((lane >> 1) & 1)) * 128 : WS_FUT + (u32)(cj * 64 + 2 * ti + (lane & 1)) * 128);
        touch(fo, ws, L_DUMMY);
      }
      SCHED0();
      BAR();
      SCHED0();
      BAR();
      SCHED0();
#pragma unroll
      for (int i = 0; i < 3; ++i) if (t0 == 0) kr[i] = u32x4{0, 0, 0, 0};
      piece(0);
      BAR();
      SCHED0();
#pragma unroll
      for (int c = 1; c < 4; ++c) piece(c);
      tag = tag_of(seed);
      const u32 scr = L_USCR + (u32)u * 4096;
#pragma unroll
      for (int c = 0; c < 8; ++c) *(LDSP(u32x2))(scr + (u32)((cg * 8 + c) * 64 + (((7 - tg) ^ (cg & 6)) << 3))) = o[c];
      WAIT_LGKM0();
#pragma unroll
      for (int i = 0; i < 4; ++i) {
        const int dd = 16 * i + (lane >> 2), c4 = lane & 3;
        u32x4 v = *(LDSP(u32x4))(scr + (u32)(dd * 64 + ((c4 ^ (dd >> 4)) << 4)));
        st128_dev(rs, WS_UT + (u32)((64 * cj + dd) * (S * 2) + (2016 - 64 * ti - 32 * u) * 2 + c4 * 16), v);
      }
      gflag_publish(ws + WS_FUT + (cj * 64 + 2 * ti + u) * 128, tag, lane);
    }
    const int j = wid;
    const u32 o2 = w2_voff(lane);
    const char* wbase = w2 + dl * 128;
    u32x4 pk[8][4];
    auto park = [&](int i0, int i1) {
#pragma unroll
      for (int i = i0; i < i1; ++i) {
        const char* src = wbase + (size_t)g2_ks(j, i + 6) * 32 * (D * 2);
        asm volatile("" ::: "memory");
#pragma unroll
        for (int pc = 0; pc < 4; ++pc) pk[i][pc] = *(const u32x4*)(src + (o2 + (u32)(pc * 8 * (D * 2))));
      }
      SCHED0();
    };
    park(0, PK_A);
    if (wid == 2) {
      gflag_wait<8>(ws + WS_FUT + ((d0 >> 6) * 64 + lane) * 128, tag);
      lflag_wait(L_FREE, 7, 7);
      glds16x4_dev(lane * 16, ws + WS_UT + d0 * (S * 2), CV_U);
      glds16x4_dev(lane * 16, ws + WS_UT + (d0 + 1) * (S * 2), CV_U + CV_USTR);
      if (lane < 32) {
        glds16(lane * 16, seq_tab + 32, CV_U + 4096);
        glds16(lane * 16, seq_tab + 32, CV_U + CV_USTR + 4096);
      }
      dflag_set(L_CONV + 8, 1);
    }
    if (wid == 3) lflag_set(L_CONV + 12, 1);
    park(PK_A, 8);
    if (wid == 1) {
      lflag_wait(L_FREE, 7, 7);
      const u32 vl = lane * 16;
#pragma unroll 1
      for (int n = 0; n < 8; ++n) {
        const int rr = n & 3;
        const char* src = ssmk + (size_t)(d0 + (n >> 2)) * (S * 2) + (rr ? (rr - 4) * 2 : 0);
        glds16x4(rr && !lane ? 16u : vl, vl, vl, vl, src, CV + (u32)n * CV_HSTR + (rr ? 24 : 32));
        if (rr == 3) dflag_set(L_CONV + (n >> 2) * 4, 1);
      }
    }
    lflag_wait(L_FREE, 7, 7);
    lflag_wait(L_PUB, 3, 1);
#pragma unroll 1
    for (int i = 0; i < 6; ++i) {
      if (i == 3) dflag_set(L_FED + j * 4, 3);
      const char* src = wbase + (size_t)g2_ks(j, i) * 32 * (D * 2);
      glds16x4(o2, o2 + 8 * (D * 2) - 1024, o2 + 16 * (D * 2) - 2048, o2 + 24 * (D * 2) - 3072, src, g2_place(j, i));
    }
    dflag_set(L_FED + j * 4, 6);
    if (wid == 3) {
      const int e = lane < 28 ? lane : 27;
      gflag_wait<4>(ws + WS_FH + (tau * 56 + 4 * (e >> 1) + (e & 1) + 2) * 128, tag);
      dflag_set(L_H, 1);
    }
    __builtin_amdgcn_s_setprio(0);
    WAIT_VM(0);
    const u32 wl = opq(lane * 16 + j * 12288);
#pragma unroll
    for (int i = 0; i < 8; i += 2) {
      lflag_wait(L_PROG + j * 4, 0, i + 2);
#pragma unroll
      for (int n = 0; n < 8; ++n) lds_st128(wl + g2_place(0, (i + (n >> 2)) % 6) + (n & 3) * 1024, pk[i + (n >> 2)][n & 3]);
      lflag_set(L_FED + j * 4, i + 8);
    }
    return;
  }
  const rsrc_t rs = ws_rsrc(ws);

  if (b >= 224) {
    if (wid >= 8 && b != 255) return;
    const int tid = (int)opq(threadIdx.x), lane = tid & 63;
    const int sp = b - 224;
    const u64 seed = gload64(ws + WS_E + opq(0));
    if (tid == 0) touch(WS_FST + sp * 128, ws, L_DUMMY);
    if (wid < 8) {
      const int row = 64 * sp + (tid >> 3), c8 = tid & 7;
      const int cu = sp < 28 ? sp : 27, mat = cu / 14, ct = cu % 14, kg = tid >> 4, c16 = tid & 15;
      const char* wp = (mat ? w3 : w1) + ct * 256;
      const u32 wo = (u32)(kg * (F * 2) + c16 * 16), xo = (u32)(row * (D * 2) + c8 * 16);
      u32x4 xv[7], wv[14];
#pragma unroll
      for (int i = 0; i < 7; ++i) xv[i] = *(const u32x4*)(x + (xo + i * 128));
#pragma unroll
      for (int i = 0; i < 14; ++i) wv[i] = *(const u32x4*)(wp + (wo + i * 32 * (F * 2)));
      SCHED0();
      float s = 0.f;
#pragma unroll
      for (int i = 0; i < 7; ++i)
#pragma unroll
        for (int j = 0; j < 4; ++j) s += bflo(xv[i][j]) + bfhi(xv[i][j]);
      const float m = sum8(s) * (1.0f / D);
      float q = 0.f;
#pragma unroll
      for (int i = 0; i < 7; ++i)
#pragma unroll
        for (int j = 0; j < 4; ++j) {
          const float aa = bflo(xv[i][j]) - m, bb = bfhi(xv[i][j]) - m;
          q = __builtin_fmaf(aa, aa, __builtin_fmaf(bb, bb, q));
        }
      const float sd = rbf(__builtin_amdgcn_sqrtf(rbf(rbf(sum8(q) * (1.0f / D)) + 1e-5f)));
      if (c8 == 0) *(LDSP(f32x2))(u32)(16384 + (tid >> 3) * 8) = f32x2{rbf(m), __builtin_amdgcn_rcpf(sd)};
      SCHED0();
      float a[8] = {};
#pragma unroll
      for (int i = 0; i < 14; ++i)
#pragma unroll
        for (int j = 0; j < 4; ++j) { a[2 * j] += bflo(wv[i][j]); a[2 * j + 1] += bfhi(wv[i][j]); }
      f32x4 t;
#pragma unroll
      for (int j = 0; j < 4; ++j) {
        auto p = __builtin_amdgcn_permlane16_swap(__builtin_bit_cast(u32, a[j]), __builtin_bit_cast(u32, a[j + 4]), false, false);
        t[j] = asf(p[0]) + asf(p[1]);
      }
      *(LDSP(f32x4))(u32)((kg >> 1) * 512 + c16 * 32 + (kg & 1) * 16) = t;
    }
    WAIT_LGKM0(); BAR();
    const u64 tag = tag_of(seed);
    if (wid == 0) {
      const int cu = sp < 28 ? sp : 27;
      f32x2 cs = {};
#pragma unroll
      for (int i = 0; i < 16; ++i) cs += *(LDSP(f32x2))(u32)(i * 512 + lane * 8);
      st64_dev(rs, WS_STAT + (u32)(64 * sp + lane) * 8, *(LDSP(u32x2))(u32)(16384 + lane * 8));
      if (sp < 28) st64_dev(rs, WS_CSUM + (u32)((cu % 14) * 1024 + (cu / 14) * 512 + lane * 8), __builtin_bit_cast(u32x2, cs));
      gflag_publish(ws + WS_FST + sp * 128, tag, lane);
    }
    WAIT_VM(0); BAR();
    if (tid < 392) touch((u32)(sp * 392 + tid) * 128, w2, L_DUMMY);
    if (tid < 448) touch((u32)(sp * 448 + tid) * 128, ssmk, L_DUMMY);
    if (b == 255) {
      __builtin_amdgcn_s_sleep(127);
      gflag_wait<64>(ws + (tid < 480 ? WS_FUT + tid * 128 : WS_FH + ((((tid < 704 ? tid - 480 : 223) / 14) * 2) * 56 + ((tid < 704 ? tid - 480 : 223) % 14) * 4) * 128), tag);
      BAR();
      if (tid == 0) gstore64(ws + WS_E, tag);
    }
    return;
  }

  const int cw = wid - 4, th = cw & 1, fq = cw >> 1;
  {
    const int lane = (int)opq(threadIdx.x) & 63, g = lane >> 4, hh = lane >> 5, l31 = lane & 31;
    const int q4 = (lane >> 2) & 3, p4 = lane & 3;
    const u32 aA = 16384 + (u32)((8 * hh + q4) * 256 + ((fq ^ q4) << 6) + (g & 1) * 32 + p4 * 8);
    const u32 aB = (u32)((64 * th + l31) * 128 + ((hh ^ ((l31 >> 1) & 7)) << 4));
    f32x16 acc[2][2] = {};
    s16x8 fa[2][2][2], fb[2][2][2];
    u32 pa, pb[4];
    auto setslot = [&](u32 slot) {
      pa = opq(aA + slot);
#pragma unroll
      for (int k = 0; k < 4; ++k) pb[k] = opq((aB ^ (u32)(k << 5)) + slot);
    };
    auto rd = [&](int set) {
#pragma unroll
      for (int k = 0; k < 2; ++k) {
        const int ks = 2 * set + k;
        fa[set][k][0] = lds_tr2(pa, ks * 4096, ks * 4096 + 1024);
        fb[set][k][0] = lds_b128(pb[ks], 0);
        fb[set][k][1] = lds_b128(pb[ks], 4096);
        fa[set][k][1] = lds_tr2(pa, 16384 + ks * 4096, 16384 + ks * 4096 + 1024);
      }
    };
    auto mm = [&](int set, int n0, int n1) {
#pragma unroll
      for (int n = n0; n < n1; ++n) acc[(n >> 1) & 1][n & 1] = mfma32(fa[set][n >> 2][(n >> 1) & 1], fb[set][n >> 2][n & 1], acc[(n >> 1) & 1][n & 1]);
    };
    setslot(0);
    const u32 fad = opq(L_FULL + (lane & 1) * 4);
#pragma unroll
    for (int i = 0; i < 4; ++i) asm volatile("" : "+v"(acc[i >> 1][i & 1]));
    __builtin_amdgcn_s_setprio(1);
    lflag_wait(L_FULL, 1, 1);
    rd(0);
    SCHED0();
    u32 slot = 0;
    auto stage = [&](int c, bool bar) {
      rd(1);
      const u32 fl = *(volatile LDSP(u32))fad;
      mm(0, 0, 6);
      SGB(0x008, 1); SGB(0x100, 3); SGB(0x008, 1); SGB(0x100, 3); SGB(0x008, 1); SGB(0x100, 3); SGB(0x008, 1); SGB(0x100, 4); SGB(0x008, 2);
      SCHED0();
      slot = slot == 2 * SLOT ? 0 : slot + SLOT;
      setslot(slot);
      SCHED0();
      mm(0, 6, 7);
      SCHED0();
      __builtin_amdgcn_s_waitcnt(0xC07F);
      const bool miss = c < 6 && __builtin_amdgcn_ballot_w64(fl < (u32)(c + 2));
      SCHED0();
      mm(0, 7, 8);
      SCHED0();
      if (bar) BAR();
      SCHED0();
      mm(1, 0, 2);
      SCHED0();
      if (!bar && c == 6) lflag_set(L_FREE + cw * 4, 7);
      if (miss) lflag_wait(L_FULL, 1, c + 2);
      SCHED0();
      rd(0); mm(1, 2, 8);
#pragma unroll
      for (int i = 0; i < 4; ++i) { SGB(0x008, 1); SGB(0x100, 3); }
      SGB(0x008, 2);
      SCHED0();
    };
#pragma unroll 1
    for (int c = 0; c < 4; ++c) stage(c, true);
#pragma unroll 1
    for (int c = 4; c < 7; ++c) stage(c, false);
    lflag_wait(L_EPI, 0, 1);
    const u32 hdst = WS_H + (u32)((2 * panel + th) * 56 + 4 * ft + fq) * 4096 + lane * 16;
    float nm[2], c1[2], irs2[2];
#pragma unroll
    for (int j = 0; j < 2; ++j) {
      const f32x2 st = *(LDSP(f32x2))(L_STAT + (u32)(64 * th + 32 * j + l31) * 8);
      nm[j] = -st[0]; c1[j] = -1.44269504f * st[1]; irs2[j] = __builtin_amdgcn_rcpf(st[1] * st[1]);
    }
#pragma unroll
    for (int s = 0; s < 2; ++s) {
      f32x4 cs[2][2];
#pragma unroll
      for (int m = 0; m < 2; ++m)
#pragma unroll
        for (int G = 0; G < 2; ++G) cs[m][G] = *(LDSP(f32x4))(L_CSUM + (u32)(m * 128 + 32 * fq + 16 * s + 8 * G + 4 * hh) * 4);
#pragma unroll
      for (int j = 0; j < 2; ++j) {
        float a[8], ex[8], hv[8];
#pragma unroll
        for (int e = 0; e < 8; ++e) a[e] = __builtin_fmaf(nm[j], cs[0][e >> 2][e & 3], acc[0][j][8 * s + e]);
#pragma unroll
        for (int e = 0; e < 8; ++e) ex[e] = a[e] * c1[j];
        SCHED0();
#pragma unroll
        for (int e = 0; e < 8; ++e) { ex[e] = __builtin_amdgcn_exp2f(ex[e]); hv[e] = __builtin_fmaf(nm[j], cs[1][e >> 2][e & 3], acc[1][j][8 * s + e]); }
        SCHED0();
#pragma unroll
        for (int e = 0; e < 8; ++e) ex[e] = __builtin_fmaf(ex[e], irs2[j], irs2[j]);
        SCHED0();
#pragma unroll
        for (int e = 0; e < 8; ++e) { ex[e] = __builtin_amdgcn_rcpf(ex[e]); hv[e] *= a[e]; }
        SCHED0();
#pragma unroll
        for (int e = 0; e < 8; ++e) hv[e] *= ex[e];
        st128_dev(rs, hdst + (s * 2 + j) * 1024, u32x4{pk2(hv[0], hv[1]), pk2(hv[2], hv[3]), pk2(hv[4], hv[5]), pk2(hv[6], hv[7])});
      }
    }
  }
  char* hflag = ws + WS_FH + ((2 * panel + th) * 56 + 4 * ft + fq) * 128;
  u32x4 xv, sv;
  if (cw < 4) {
    const int ht = (int)opq(threadIdx.x) & 255, lane = ht & 63, l15 = ht & 15;
    const u64 tag = tag_of(*(volatile LDSP(u64))L_TAG);
    const int ch = cw >> 1, var = cw & 1;
    gflag_publish(hflag, tag, lane);
    u32x4 xr[2]; u32x2 st[2];
#pragma unroll
    for (int i = 0; i < 2; ++i) {
      const int n = ht + 256 * i, row = n >> 3, c = n & 7;
      xr[i] = *(const u32x4*)(x + (u32)((64 * tau + row) * (D * 2) + (64 * dl + 8 * c) * 2));
      st[i] = ld64_dev(rs, WS_STAT + (u32)(64 * tau + row) * 8);
    }
    SCHED0();
    lflag_wait(L_CONV, 3, 1);
    asm volatile("" ::: "memory");
    conv_compute(lane, cw, d0 + ch, rs);
    u32x4 xn[2];
#pragma unroll
    for (int i = 0; i < 2; ++i) {
      const float mb = asf(st[i][0]), rsd = asf(st[i][1]);
#pragma unroll
      for (int e = 0; e < 4; ++e) {
        const u32 dd = pk2(bflo(xr[i][e]) - mb, bfhi(xr[i][e]) - mb);
        xn[i][e] = pk2(bflo(dd) * rsd, bfhi(dd) * rsd);
      }
    }
    SCHED0();
    gflag_publish(ws + WS_FY + (var * 448 + d0 + ch) * 128, tag, lane);
    xv = xr[0]; sv = xn[0];
    lflag_wait(L_XCH, 1, 1);
    lds_st128(E_S + (u32)ht * 16, xn[1]); lds_st128(E_X + (u32)ht * 16, xr[1]);
    gflag_wait<4>(ws + WS_FY + ((tau >> 4) * 448 + 64 * dl + 16 * cw + l15) * 128, tag);
#pragma unroll
    for (int i = 0; i < 2; ++i)
      glds16_dev((u32)((lane >> 3) * (S * 2) + (((lane & 7) ^ (2 * cw + i)) << 4)),
                 ws + WS_YT + (64 * dl + 16 * cw + 8 * i) * (S * 2) + 128 * tau, xbuf(cw ^ 1) + (u32)i * 1024);
    WAIT_VM(0);
  } else {
    const int j = cw - 4;
    const int lane = (int)opq(threadIdx.x) & 63;
    lflag_set(L_PUB + j * 4, 1);
    const u64 btag = tag_of(*(volatile LDSP(u64))L_TAG);
    gflag_publish(hflag, btag, lane);
    const u32 hl = lane * 16;
    const char* hbase = ws + WS_H + (size_t)tau * 56 * 4096;
    const int q4 = (lane >> 2) & 3;
    const u32 ta = (u32)((4 * (lane >> 5) + q4) * 128 + ((q4 >> 1) << 6) + ((lane >> 4) & 1) * 32 + (lane & 3) * 8);
    s16x8 fa[2][4], fb[3][4];
    f32x16 acc[2][2] = {};
    auto ldh = [&](int i) {
      const char* p = hbase + (size_t)g2_ks(j, i) * 4096;
      asm volatile("global_load_dwordx4 %0, %4, %5 sc1\n\tglobal_load_dwordx4 %1, %4, %5 offset:1024 sc1\n\t"
                   "global_load_dwordx4 %2, %4, %5 offset:2048 sc1\n\tglobal_load_dwordx4 %3, %4, %5 offset:3072 sc1"
                   : "=&v"(fb[i % 3][0]), "=&v"(fb[i % 3][1]), "=&v"(fb[i % 3][2]), "=&v"(fb[i % 3][3])
                   : "v"(hl), "s"(p) : "memory");
    };
    u32 tb[2];
#pragma unroll
    for (int a = 0; a < 2; ++a) tb[a] = opq((ta ^ (u32)(a << 6)) + g2_place(j, 0));
    auto rda = [&](int i) {
#pragma unroll
      for (int a = 0; a < 2; ++a)
        if (i > 0 && i % 3 == 0) tb[a] = opq(i % 6 ? tb[a] - 2 * SLOT : tb[a] + 2 * SLOT);
#pragma unroll
      for (int n = 0; n < 4; ++n) {
        const int o = (i % 3) * 4096 + (n >> 1) * 2048;
        fa[i & 1][n] = lds_tr2(tb[n & 1], o, o + 1024);
      }
    };
    const u32 fed = opq(L_FED + j * 4), prog = opq(L_PROG + j * 4);
    gflag_wait<2>(ws + WS_FH + (tau * 56 + g2_ks(j, lane < 7 ? lane : 6)) * 128, btag);
#pragma unroll
    for (int i = 0; i < 3; ++i) ldh(i);
    lflag_wait(L_FED + j * 4, 0, 3);
    rda(0);
    SCHED0();
    u32 fl = 0;
#pragma unroll
    for (int i = 0; i < 14; ++i) {
      if (i == 2 || (i >= 5 && i < 13 && (i & 1))) {
        if (__builtin_amdgcn_readfirstlane(fl) < i + 2) lflag_wait(L_FED + j * 4, 0, i + 2);
      }
      asm volatile("s_waitcnt vmcnt(%4)" : "+v"(fb[i % 3][0]), "+v"(fb[i % 3][1]), "+v"(fb[i % 3][2]), "+v"(fb[i % 3][3])
                   : "i"(i < 12 ? 8 : (13 - i) * 4) : "memory");
      SCHED0();
      if (i < 13) rda(i + 1);
      if (i == 1 || (i >= 4 && i < 12 && !(i & 1))) fl = *(volatile LDSP(u32))fed;
#pragma unroll
      for (int s = 0; s < 2; ++s)
#pragma unroll
        for (int a = 0; a < 2; ++a)
#pragma unroll
          for (int t = 0; t < 2; ++t) acc[a][t] = mfma32(fa[i & 1][2 * s + a], fb[i % 3][2 * s + t], acc[a][t]);
      if (i < 13) {
#pragma unroll
        for (int n = 0; n < 5; ++n) { SGB(0x008, 1); SGB(0x100, 2); }
        SGB(0x008, 3);
      }
      SCHED0();
      if (i < 8 && (i & 1)) *(volatile LDSP(u32))prog = i + 1;
      if (i == 4) lflag_wait(L_H, 0, 1);
      if (i + 3 < 14) ldh(i + 3);
      SCHED0();
    }
    const int ln = (int)opq(threadIdx.x), lt = ln & 31, lh = (ln >> 5) & 1;
#pragma unroll
    for (int t = 0; t < 2; ++t)
#pragma unroll
      for (int a = 0; a < 2; ++a)
#pragma unroll
        for (int G = 0; G < 4; ++G)
          *(LDSP(f32x4))(g2_place(j, t ? 3 : 0) + (u32)(lt * 256 + (((8 * a + 2 * G + lh) ^ (lt & 7)) << 4))) =
              f32x4{acc[a][t][4 * G], acc[a][t][4 * G + 1], acc[a][t][4 * G + 2], acc[a][t][4 * G + 3]};
  }
  WAIT_LGKM0();
  BAR();
  {
    const int ht = (int)opq(threadIdx.x) & 255;
    const int half = cw >> 2, tk = ht >> 3, dg = ht & 7;
    if (half) { sv = *(LDSP(u32x4))(E_S + (u32)ht * 16); xv = *(LDSP(u32x4))(E_X + (u32)ht * 16); }
    f32x4 s0 = {}, s1 = {};
#pragma unroll
    for (int w = 0; w < 4; ++w) {
      const u32 pb = (half ? 0u : 2 * SLOT) + (u32)w * 12288 + (u32)tk * 256;
      s0 += *(LDSP(f32x4))(pb + (u32)(((2 * dg) ^ (tk & 7)) << 4));
      s1 += *(LDSP(f32x4))(pb + (u32)(((2 * dg + 1) ^ (tk & 7)) << 4));
    }
    const int tok = 32 * half + tk;
    const u32 ya = xbuf((dg >> 1) ^ 1) + (u32)(8 * (dg & 1) * 128 + (((tok >> 3) ^ dg) << 4) + (tok & 7) * 2);
    float o[8];
#pragma unroll
    for (int e = 0; e < 8; ++e) {
      const float f = rbf(e < 4 ? s0[e & 3] : s1[e & 3]);
      const float gt = rbf(silu_f(f));
      const float xe = (e & 1) ? bfhi(xv[e >> 1]) : bflo(xv[e >> 1]);
      const float ne = (e & 1) ? bfhi(sv[e >> 1]) : bflo(sv[e >> 1]);
      const float se = rbf(ne * bflo(*(LDSP(u16))(ya + e * 128)));
      o[e] = xe + rbf(se * gt);
    }
    __builtin_amdgcn_raw_buffer_store_b128(u32x4{pk2(o[0], o[1]), pk2(o[2], o[3]), pk2(o[4], o[5]), pk2(o[6], o[7])},
        __builtin_amdgcn_make_buffer_rsrc(out, 0, S * D * 2, 0x00020000), (64 * tau + 32 * half + tk) * (D * 2) + (64 * dl + 8 * dg) * 2, 0, 16);
  }
}
}
#if defined(__HIP_DEVICE_COMPILE__) && __HIP_DEVICE_COMPILE__
#pragma clang attribute pop
#endif

extern "C" void kernel_launch(void* const* d_in, const int* in_sizes, int n_in, void* d_out, int out_size, void* d_ws,
                              size_t ws_size, hipStream_t stream) {
  (void)hipFuncSetAttribute((const void*)fused_kernel, hipFuncAttributeMaxDynamicSharedMemorySize, 163840);
  fused_kernel<<<256, 768, 163840, stream>>>((const char*)d_in[0], (const char*)d_in[1], (const char*)d_in[2], (const char*)d_in[3],
                                             (const char*)d_in[4], (const char*)d_in[5], (const char*)d_in[7], (const char*)d_in[8],
                                             (const char*)d_in[9], (char*)d_ws, (char*)d_out);
}
```

```cpp
#define PROBE_ID 1
#define PROBE_W 4
#define PROBE_B (blockIdx.x == 100)
#include <hip/hip_runtime.h>
#include <hip/hip_bf16.h>
#include <stdint.h>
#define DIAG_DECL const unsigned long long pr_t0 = __builtin_amdgcn_s_memrealtime(); unsigned long long pr_t = pr_t0;
#define STAMP(i) do { if ((i) == PROBE_ID) pr_t = __builtin_amdgcn_s_memrealtime(); } while (0)
#define CYC_BEGIN
#define CYC_END
#define DIAG_FLUSH(w) do { if (PROBE_B && wid == PROBE_W) { const unsigned long long e = pr_t0 + 4000 + 4 * (pr_t - pr_t0); \
    for (int pi = 0; pi < 100000 && __builtin_amdgcn_s_memrealtime() < e; ++pi) __builtin_amdgcn_s_sleep(2); } } while (0)
#pragma clang diagnostic ignored "-Wint-to-pointer-cast"

#if defined(__HIP_DEVICE_COMPILE__) && __HIP_DEVICE_COMPILE__
#pragma clang attribute push(__attribute__((target("no-packed-fp32-ops"))), apply_to = function)
#endif
namespace {
typedef unsigned short u16;
typedef unsigned int u32;
typedef unsigned long long u64;
typedef __attribute__((ext_vector_type(4))) short s16x4;
typedef __attribute__((ext_vector_type(8))) short s16x8;
typedef __attribute__((ext_vector_type(8))) __bf16 bf16x8;
typedef __attribute__((ext_vector_type(2))) __bf16 bf16x2;
typedef __attribute__((ext_vector_type(16))) float f32x16;
typedef __attribute__((ext_vector_type(4))) float f32x4;
typedef __attribute__((ext_vector_type(2))) float f32x2;
typedef __attribute__((ext_vector_type(4))) u32 u32x4;
typedef __attribute__((ext_vector_type(2))) u32 u32x2;
#define LDSP(T) __attribute__((address_space(3))) T*
#define GLBP(T) __attribute__((address_space(1))) T*
#define DEV __device__ __forceinline__

constexpr int S = 2048, D = 448, F = 1792;
constexpr u32 WS_H = 0;
constexpr u32 WS_UT = WS_H + 7340032;
constexpr u32 WS_YT = WS_UT + 1835008;
constexpr u32 WS_STAT = WS_YT + 1835008;
constexpr u32 WS_CSUM = WS_STAT + 16384;
constexpr u32 WS_E = WS_CSUM + 16384;
constexpr u32 WS_FUT = WS_E + 128;
constexpr u32 WS_FST = WS_FUT + 448 * 128;
constexpr u32 WS_FY = WS_FST + 32 * 128;
constexpr u32 WS_FH = WS_FY + 896 * 128;
constexpr u32 WS_END = WS_FH + 1792 * 128;
constexpr int CODE_LINES = 196;
constexpr u32 SLOT = 49152;
constexpr u32 MISC = 3 * SLOT;
constexpr u32 L_FULL = MISC, L_EPI = MISC + 8, L_H = MISC + 16, L_FREE = MISC + 32, L_CONV = MISC + 64, L_FED = MISC + 80,
              L_TAG = MISC + 96, L_DD = MISC + 104, L_ADONE = MISC + 112, L_PROG = MISC + 128, L_XCH = MISC + 144, L_PUB = MISC + 160, L_STAT = MISC + 256,
              L_CSUM = MISC + 1280, L_USCR = MISC + 4096, L_DUMMY = MISC + 2304, L_XB2 = MISC + 12288, L_USYNC = MISC + 176;
constexpr u32 CV = SLOT, CV_HSTR = 4160, CV_U = CV + 8 * CV_HSTR, CV_USTR = 4608;
constexpr u32 E_S = SLOT, E_X = SLOT + 4096;
constexpr int SPIN_CAP = 1 << 18;
#define ROT1 0
#define ROT2 0
#define TOUCH2_AT 0
#define PACE_L 63
#define PACE_S 63
#define FREE_SET dflag_set
#define W2PF 0
#define KVPF 0
#define SHARES 1
#define CODEPF 0
#define W2_HOLD 0
#define PUB2 0
#define KV_LATE 1
#define SPPF 1
#define FLAGPF 1
#define MIDPF 1
#define BPOLL 1
#define HDEPTH 3
#define KV_P2 0
#define LSLEEP 1
#define UT_FULL 0
#define ESLEEP 127
#define KV_AT 1

__device__ const u32 seq_tab[160] __attribute__((aligned(128))) = {0, 1, 2, 3, 4, 5, 6, 7};
DEV float asf(u32 v) { return __builtin_bit_cast(float, v); }
DEV float bflo(u32 v) { return asf(v << 16); }
DEV float bfhi(u32 v) { return asf(v & 0xffff0000u); }
template <int CTRL> DEV float dpp(float v) { return asf(__builtin_amdgcn_update_dpp(0, __builtin_bit_cast(u32, v), CTRL, 0xf, 0xf, true)); }
DEV float sum8(float v) { v += dpp<0xB1>(v); v += dpp<0x4E>(v); return v + dpp<0x141>(v); }
DEV u32 pk2(float lo, float hi) {
  asm("" : "+v"(lo)); asm("" : "+v"(hi));
  bf16x2 r = {(__bf16)lo, (__bf16)hi};
  return __builtin_bit_cast(u32, r);
}
DEV float rbf(float f) { bf16x2 r = {(__bf16)f, (__bf16)f}; return bflo(__builtin_bit_cast(u32, r)); }
DEV f32x4 mfma16(s16x8 a, s16x8 b, f32x4 c) {
  return __builtin_amdgcn_mfma_f32_16x16x32_bf16(__builtin_bit_cast(bf16x8, a), __builtin_bit_cast(bf16x8, b), c, 0, 0, 0);
}
DEV f32x16 mfma32(s16x8 a, s16x8 b, f32x16 c) {
  return __builtin_amdgcn_mfma_f32_32x32x16_bf16(__builtin_bit_cast(bf16x8, a), __builtin_bit_cast(bf16x8, b), c, 0, 0, 0);
}
DEV u32 opq(u32 v) { asm volatile("" : "+v"(v)); return v; }
DEV s16x8 lds_tr2(u32 base, int o0, int o1) {
  s16x4 lo = __builtin_amdgcn_ds_read_tr16_b64_v4i16((LDSP(s16x4))((LDSP(char))base + o0));
  s16x4 hi = __builtin_amdgcn_ds_read_tr16_b64_v4i16((LDSP(s16x4))((LDSP(char))base + o1));
  return __builtin_shufflevector(lo, hi, 0, 1, 2, 3, 4, 5, 6, 7);
}
DEV s16x8 lds_b128(u32 base, int o) { return *(LDSP(s16x8))((LDSP(char))base + o); }
DEV void lds_st128(u32 a, u32x4 v) { *(LDSP(u32x4))a = v; }
DEV void glds16x4(u32 v0, u32 v1, u32 v2, u32 v3, const void* sbase, u32 ldst) {
  asm volatile("s_mov_b32 m0, %5\n\ts_nop 0\n\tglobal_load_lds_dwordx4 %0, %4\n\tglobal_load_lds_dwordx4 %1, %4 offset:1024\n\t"
               "global_load_lds_dwordx4 %2, %4 offset:2048\n\tglobal_load_lds_dwordx4 %3, %4 offset:3072"
               ::"v"(v0), "v"(v1), "v"(v2), "v"(v3), "s"(sbase), "s"(ldst) : "memory");
}
DEV void glds16(u32 voff, const void* sbase, u32 ldst) {
  asm volatile("s_mov_b32 m0, %2\n\ts_nop 0\n\tglobal_load_lds_dwordx4 %0, %1" ::"v"(voff), "s"(sbase), "s"(ldst) : "memory");
}
DEV void glds16x4_lin(u32 v, const void* sbase, u32 ldst) { glds16x4(v, v, v, v, sbase, ldst); }
DEV void glds16x4_dev(u32 v, const void* sbase, u32 ldst) {
  asm volatile("s_mov_b32 m0, %2\n\ts_nop 0\n\tglobal_load_lds_dwordx4 %0, %1 sc1\n\tglobal_load_lds_dwordx4 %0, %1 offset:1024 sc1\n\t"
               "global_load_lds_dwordx4 %0, %1 offset:2048 sc1\n\tglobal_load_lds_dwordx4 %0, %1 offset:3072 sc1"
               ::"v"(v), "s"(sbase), "s"(ldst) : "memory");
}
DEV void glds16_dev(u32 voff, const void* sbase, u32 ldst) {
  asm volatile("s_mov_b32 m0, %2\n\ts_nop 0\n\tglobal_load_lds_dwordx4 %0, %1 sc1" ::"v"(voff), "s"(sbase), "s"(ldst) : "memory");
}
DEV void dflag_set(u32 addr, u32 val) {
  asm volatile("s_waitcnt lgkmcnt(0)\n\ts_mov_b32 m0, %2\n\ts_mov_b64 exec, 1\n\tglobal_load_lds_dword %0, %1\n\ts_mov_b64 exec, -1"
               ::"v"(0), "s"(seq_tab + val), "s"(addr) : "memory");
}
#define WAIT_VM(n) asm volatile("s_waitcnt vmcnt(%0)" ::"i"(n) : "memory")
#define WAIT_LGKM0() asm volatile("s_waitcnt lgkmcnt(0)" ::: "memory")
#define BAR() do { asm volatile("" ::: "memory"); __builtin_amdgcn_s_barrier(); asm volatile("" ::: "memory"); } while (0)
#define SCHED0() __builtin_amdgcn_sched_barrier(0)
#define SGB(mask, n) __builtin_amdgcn_sched_group_barrier(mask, n, 0)
DEV float silu_f(float x) { return x * __builtin_amdgcn_rcpf(1.0f + __builtin_amdgcn_exp2f(-1.44269504f * x)); }

DEV void lflag_set(u32 addr, u32 val) {
  asm volatile("s_waitcnt lgkmcnt(0)\n\tds_write_b32 %0, %1" ::"v"(addr), "v"(val) : "memory");
}
DEV void lflag_wait(u32 addr, u32 mask, u32 val) {
  const u32 a = addr + 4 * (threadIdx.x & mask);
#pragma unroll 1
  for (int i = 0; i < SPIN_CAP; ++i) {
    u32 v;
    asm volatile("ds_read_b32 %0, %1\n\ts_waitcnt lgkmcnt(0)" : "=v"(v) : "v"(a) : "memory");
    if (__builtin_amdgcn_ballot_w64(v < val) == 0) break;
    __builtin_amdgcn_s_sleep(LSLEEP);
  }
}
DEV u64 gload64(const char* p) { return __hip_atomic_load((GLBP(u64))p, __ATOMIC_RELAXED, __HIP_MEMORY_SCOPE_AGENT); }
DEV void gstore64(char* p, u64 v) { __hip_atomic_store((GLBP(u64))p, v, __ATOMIC_RELAXED, __HIP_MEMORY_SCOPE_AGENT); }
template <int SLEEP>
DEV void gflag_wait(const char* lane_ptr, u64 tag) {
  bool ok = false;
#pragma unroll 1
  for (int i = 0; i < SPIN_CAP; ++i) {
    asm volatile("" ::: "memory");
    if (!ok) ok = gload64(lane_ptr) == tag;
    if (__builtin_amdgcn_ballot_w64(!ok) == 0) break;
    __builtin_amdgcn_s_sleep(SLEEP);
  }
  asm volatile("" ::: "memory");
}
DEV void gflag_publish(char* p, u64 tag, int lane) {
  WAIT_VM(0);
  if (lane == 0) gstore64(p, tag);
}
DEV u64 tag_of(u64 seed) { return seed * 0x9E3779B97F4A7C15ull + 0xD1B54A32D192ED03ull; }
DEV u64 make_tag(const char* ws) { return tag_of(gload64(ws + WS_E)); }
typedef __amdgpu_buffer_rsrc_t rsrc_t;
DEV rsrc_t ws_rsrc(char* ws) { return __builtin_amdgcn_make_buffer_rsrc(ws, 0, WS_END, 0x00020000); }
DEV void st128_dev(rsrc_t r, u32 off, u32x4 v) { __builtin_amdgcn_raw_buffer_store_b128(v, r, off, 0, 16); }
DEV void st64_dev(rsrc_t r, u32 off, u32x2 v) { __builtin_amdgcn_raw_buffer_store_b64(v, r, off, 0, 16); }
DEV void st32_dev(rsrc_t r, u32 off, u32 v) { __builtin_amdgcn_raw_buffer_store_b32(v, r, off, 0, 16); }
DEV u32x4 ld128_dev(rsrc_t r, u32 off) { return __builtin_amdgcn_raw_buffer_load_b128(r, off, 0, 16); }
DEV u32x2 ld64_dev(rsrc_t r, u32 off) { return __builtin_amdgcn_raw_buffer_load_b64(r, off, 0, 16); }
DEV void touch(u32 voff, const void* sbase, u32 ldst) {
  asm volatile("s_mov_b32 m0, %2\n\ts_nop 0\n\tglobal_load_lds_dword %0, %1" ::"v"(voff), "s"(sbase), "s"(ldst) : "memory");
}

DEV void glds4_dev(u32 voff, const void* sbase, u32 ldst) {
  asm volatile("s_mov_b32 m0, %2\n\ts_nop 0\n\tglobal_load_lds_dword %0, %1 sc1" ::"v"(voff), "s"(sbase), "s"(ldst) : "memory");
}
DEV u32 shr16(u32 hi, u32 lo) { return __builtin_amdgcn_alignbit(hi, lo, 16); }

DEV u32 xbuf(int w) { return w < 2 ? L_USCR + (u32)w * 4096 : (w == 2 ? L_XB2 : CV + 45056); }
DEV void conv_compute(int lane, int cw, int d, rsrc_t rs, char* hflag, u64 tag, bool pub) {
  const int ch = cw >> 1, var = cw & 1;
  const int m = lane & 15, g = lane >> 4, q4 = m >> 2, p4 = lane & 3;
  u32 aa = opq(CV + (u32)(ch * 4 + ((q4 + 1) & 3)) * CV_HSTR + (u32)(((q4 + 1) & 4) + 8 * g + 4 * p4) * 2 + var * 256);
  u32 ba = opq(CV_U + (u32)ch * CV_USTR + (u32)(240 - 16 * m + 8 * g) * 2 + var * 256);
  f32x4 acc[8] = {};
  s16x8 A[8], B[8];
#pragma unroll
  for (int p = 0; p < 8; ++p) { A[7 - p] = lds_tr2(aa, (7 - p) * 512, (7 - p) * 512 + 8); B[p] = lds_b128(ba, (7 - p) * 512); }
  if (var == 0) {
    const u32 a0 = CV + (u32)ch * 4 * CV_HSTR + (u32)(m + 1 + 8 * g) * 2;
#pragma unroll
    for (int j = 0; j < 8; ++j) { const short v = *(LDSP(short))(a0 + j * 2); A[0][j] = m + 8 * g + j >= 15 ? v : (short)0; }
  }
  SCHED0();
#pragma unroll 1
  for (int it = 0; it < 4; ++it) {
#pragma unroll
    for (int p = 7; p >= 0; --p) {
#pragma unroll
      for (int dd = 0; dd <= 7 - p; ++dd) {
        acc[p + dd] = mfma16(A[p], B[dd], acc[p + dd]);
        if (p == 0) { SCHED0(); B[dd] = lds_b128(ba, 64 + (7 - dd) * 512); SCHED0(); }
      }
      SCHED0();
      A[p] = lds_tr2(aa, 64 + p * 512, 64 + p * 512 + 8);
      SCHED0();
    }
    aa = opq(aa + 64); ba = opq(ba + 64);
    if (it == 0 && pub) gflag_publish(hflag, tag, lane);
  }
  f32x4 y[4];
#pragma unroll
  for (int i = 0; i < 4; ++i) {
    *(LDSP(f32x4))(xbuf(cw) + (u32)(i * 1024 + lane * 16)) = var ? acc[i] : acc[4 + i];
    y[i] = var ? acc[4 + i] : acc[i];
  }
  lflag_set(L_XCH + cw * 4, 1);
  const u32 dv = *(LDSP(u32))L_DD;
  const float Dd = ch ? bfhi(dv) : bflo(dv);
  u32x2 uu[4];
#pragma unroll
  for (int i = 0; i < 4; ++i) uu[i] = *(LDSP(u32x2))(CV_U + (u32)ch * CV_USTR + (u32)(2044 - (1024 * var + 256 * i + 16 * m + 4 * g)) * 2);
  lflag_wait(L_XCH + (cw ^ 1) * 4, 0, 1);
#pragma unroll
  for (int i = 0; i < 4; ++i) {
    const int t = 1024 * var + 256 * i + 16 * m + 4 * g;
    const f32x4 s = y[i] + *(LDSP(f32x4))(xbuf(cw ^ 1) + (u32)(i * 1024 + lane * 16));
    float y0 = rbf(s[0]) + rbf(bfhi(uu[i][1]) * Dd), y1 = rbf(s[1]) + rbf(bflo(uu[i][1]) * Dd);
    float y2 = rbf(s[2]) + rbf(bfhi(uu[i][0]) * Dd), y3 = rbf(s[3]) + rbf(bflo(uu[i][0]) * Dd);
    st64_dev(rs, WS_YT + (u32)(d * (S * 2) + t * 2), u32x2{pk2(y0, y1), pk2(y2, y3)});
  }
}

DEV int rot7(int i, int rot) { const int v = i + rot; return v >= 7 ? v - 7 : v; }
DEV int g2_ks(int j, int i, int rot) { const int e = 7 * j + rot7(i < 7 ? i : i - 7, rot); return 4 * (e >> 1) + (e & 1) + (i < 7 ? 0 : 2); }
DEV u32 g2_place(int j, int p) { return (p < 3 ? 2 * SLOT + (u32)p * 4096 : (u32)(p - 3) * 4096) + (u32)j * 12288; }
DEV u32 w2_voff(int lane) { return (u32)((lane >> 3) * (D * 2) + (((lane & 7) ^ (((lane >> 4) & 1) << 2)) << 4)); }

DEV void share(int b, int n, const char* base) {
  const int lane = (int)opq(threadIdx.x) & 63;
  if (lane < n) touch((u32)(b * n + lane) * 128, base, L_DUMMY);
}

__global__ __launch_bounds__(768, 3) void fused_kernel(const char* x, const char* kp, const char* vp, const char* skp,
                                                       const char* ssmk, const char* ssmD, const char* w1, const char* w3,
                                                       const char* w2, char* ws, char* out) {
  u64 pc0;
  asm volatile("s_getpc_b64 %0" : "=s"(pc0));
  DIAG_DECL STAMP(0);
  const int b = blockIdx.x, xcd = b & 7, r = b >> 3;
  const int wid = __builtin_amdgcn_readfirstlane(threadIdx.x >> 6);
  const int panel = (xcd >> 1) * 4 + r / 7, ft = (xcd & 1) * 7 + r % 7;
  const int tau = xcd * 4 + r / 7, dl = r % 7;
  const int d0 = 2 * b;
  const int rot = ROT1 ? (r % 7 + 2 * (r / 7)) % 7 : 0, rot2 = ROT2 ? (r % 7 + 2 * (r / 7)) % 7 : 0;

  if (b < 224) {
    if (SHARES && wid >= 4 && wid < 7) share(b, wid == 4 ? 64 : 56, wid == 4 ? x : (wid == 5 ? w1 : w3));
    if (wid == 7) { *(LDSP(u32))(MISC + (threadIdx.x & 63) * 4) = 0; WAIT_LGKM0(); }
    BAR();
    if (wid == 7 && (threadIdx.x & 63) < 5) touch((threadIdx.x & 63) * 128, seq_tab, L_DUMMY);
    if (CODEPF && wid == 11 && (threadIdx.x & 63) < CODE_LINES / 28) touch((u32)(r * (CODE_LINES / 28) + (threadIdx.x & 63)) * 128, (const char*)pc0, L_DUMMY);
  }

  if (__builtin_expect(wid < 4 && b < 224, 1)) {
    const int lane = (int)opq(threadIdx.x) & 63;
    u64 tag = 0;
    if (__builtin_expect(wid < 2, 1)) {
      const int g = lane >> 4, l15 = lane & 15;
      u32 vx[8], vw[8];
      const u32 ox = (u32)((lane >> 3) * (D * 2) + (((lane & 7) ^ g) << 4));
      const u32 ow = (u32)(g * (F * 2) + (((l15 >> 2) ^ g) << 6) + ((lane & 3) << 4));
#pragma unroll
      for (int i = 0; i < 8; ++i) {
        vx[i] = (ox ^ (u32)((i & 1) * 64)) + i * 8 * (D * 2) - (i & 3) * 1024;
        vw[i] = ow + i * 4 * (F * 2) - (i & 3) * 1024;
      }
#pragma unroll 1
      for (int c = 0; c < 7; ++c) {
        if (c >= 3) BAR();
        const u32 slot = (u32)(c % 3) * SLOT + wid * 8192;
        const int kc = rot7(c, rot);
        const char* xb = x + (size_t)(panel * 128 + wid * 64) * (D * 2) + kc * 128;
        const char* w1b = w1 + (size_t)(kc * 64 + wid * 32) * (F * 2) + ft * 256;
        const char* w3b = w3 + (size_t)(kc * 64 + wid * 32) * (F * 2) + ft * 256;
#pragma unroll
        for (int i = 0; i < 8; i += 4) {
          WAIT_VM(PACE_L);
          glds16x4(vx[i], vx[i + 1], vx[i + 2], vx[i + 3], xb, slot + i * 1024);
          WAIT_VM(PACE_L);
          glds16x4(vw[i], vw[i + 1], vw[i + 2], vw[i + 3], w1b, slot + 16384 + i * 1024);
          WAIT_VM(PACE_L);
          glds16x4(vw[i], vw[i + 1], vw[i + 2], vw[i + 3], w3b, slot + 32768 + i * 1024);

        }
        dflag_set(L_FULL + wid * 4, c + 1);
        if (c == 0) STAMP(1); else if (c == 2) STAMP(2); else if (c == 4) STAMP(3); else if (c == 6) STAMP(4);
      }
      if (wid == 0) {
        tag = make_tag(ws);
        const int k = lane & 7;
        gflag_wait<8>(ws + WS_FST + (k < 2 ? 2 * panel + k : (k == 2 ? tau : (k & 1) * 14 + ft)) * 128, tag);
        glds16_dev(lane * 16, ws + WS_STAT + panel * 1024, L_STAT);
        glds16_dev(lane * 16, ws + WS_CSUM + ft * 1024, L_CSUM);
        if (lane < 2) glds4_dev(lane * 4, ws + WS_E, L_TAG);
        if (lane < 1) glds4_dev(0, ssmD + d0 * 2, L_DD);
        dflag_set(L_EPI, 1);
        STAMP(5);
      }
    } else {
      __builtin_amdgcn_s_setprio(3);
      const rsrc_t rs = ws_rsrc(ws);
      const int u = wid - 2;
      const int ti = b / 7, cj = b % 7;
      const int tg = lane >> 3, cg = lane & 7;
      const int t0 = 64 * ti + 32 * u + 4 * tg, ch0 = 64 * cj + 8 * cg;
      u32x4 kr[7], vr[4], sw[4]; u32x2 o[8];
      u64 seed;
      auto ldkv = [&]() {
        const u32 ko = (u32)(t0 * (D * 2) + ch0 * 2);
#pragma unroll
        for (int i = 0; i < 7; ++i) kr[i] = *(const u32x4*)(kp + (i < 3 && t0 == 0 ? ko : ko + (u32)((i - 3) * (D * 2))));
#pragma unroll
        for (int i = 0; i < 4; ++i) vr[i] = *(const u32x4*)(vp + (ko + (u32)(i * (D * 2))));
        SCHED0();
      };
      auto piece = [&](int p) {
#pragma unroll
        for (int c = 2 * p; c < 2 * p + 2; ++c) {
          const u32 wa = sw[p][(c & 1) * 2], wb = sw[p][(c & 1) * 2 + 1];
          const float w0 = bflo(wa), w1f = bfhi(wa), w2f = bflo(wb), w3f = bfhi(wb);
          float kf[7], ks[4];
#pragma unroll
          for (int i = 0; i < 7; ++i) kf[i] = (c & 1) ? bfhi(kr[i][p]) : bflo(kr[i][p]);
#pragma unroll
          for (int i = 0; i < 4; ++i)
            ks[i] = __builtin_fmaf(kf[i + 3], w0, __builtin_fmaf(kf[i + 2], w1f, __builtin_fmaf(kf[i + 1], w2f, kf[i] * w3f)));
#pragma unroll
          for (int i = 0; i < 2; ++i) {
            const int ta = 3 - 2 * i, tb = 2 - 2 * i;
            const u32 kk = pk2(ks[ta], ks[tb]);
            o[c][i] = pk2(bflo(kk) * ((c & 1) ? bfhi(vr[ta][p]) : bflo(vr[ta][p])), bfhi(kk) * ((c & 1) ? bfhi(vr[tb][p]) : bflo(vr[tb][p])));
          }
          asm volatile("" : "+v"(o[c]));
        }
        SCHED0();
      };
      BAR();
      ldkv();
#pragma unroll
      for (int i = 0; i < 4; ++i) sw[i] = *(const u32x4*)(skp + (u32)(ch0 * 8 + i * 16));
      seed = gload64(ws + WS_E + opq(0));
      if (FLAGPF && lane < 14) {
        const u32 fo = lane < 8 ? WS_FH + (u32)((2 * panel + (lane & 1)) * 56 + 4 * ft + (lane >> 1)) * 128
                                : (lane < 12 ? WS_FY + (u32)((lane & 1) * 448 + d0 + ((lane >> 1) & 1)) * 128 : WS_FUT + (u32)(UT_FULL ? (cj * 2 + (lane & 1)) * 32 + ti : cj * 64 + 2 * ti + (lane & 1)) * 128);
        if (u) touch(fo, ws, L_DUMMY);
      }
      SCHED0();
      STAMP(1);
      BAR();
      SCHED0();
      if (KV_P2) {
#pragma unroll
        for (int i = 0; i < 3; ++i) if (t0 == 0) kr[i] = u32x4{0, 0, 0, 0};
        piece(0);
      }
      BAR();
      SCHED0();
      if (!KV_P2) {
#pragma unroll
        for (int i = 0; i < 3; ++i) if (t0 == 0) kr[i] = u32x4{0, 0, 0, 0};
      }
      piece(KV_P2);
      BAR();
      SCHED0();
      STAMP(3);
#pragma unroll
      for (int c = 1 + KV_P2; c < 4; ++c) piece(c);
      tag = tag_of(seed);
      const u32 scr = L_USCR + (u32)u * 4096;
#pragma unroll
      for (int c = 0; c < 8; ++c) *(LDSP(u32x2))(scr + (u32)((cg * 8 + c) * 64 + (((7 - tg) ^ (cg & 6)) << 3))) = o[c];
      WAIT_LGKM0();
#pragma unroll
      for (int i = 0; i < 4; ++i) {
        const int dd = 16 * i + (lane >> 2), c4 = lane & 3;
        u32x4 v = *(LDSP(u32x4))(scr + (u32)(dd * 64 + ((c4 ^ (dd >> 4)) << 4)));
        st128_dev(rs, WS_UT + (u32)((64 * cj + dd) * (S * 2) + (2016 - 64 * ti - 32 * u) * 2 + c4 * 16), v);
      }
      STAMP(4);
      gflag_publish(ws + WS_FUT + (cj * 64 + 2 * ti + u) * 128, tag, lane);
      STAMP(5);
      STAMP(6);
    }
    const int j = wid;
    const u32 o2 = w2_voff(lane);
    const char* wbase = w2 + dl * 128;
    u32x4 pk[8][4];
    auto park = [&](int i0) {
#pragma unroll
    for (int i = i0; i < i0 + 4; ++i) {
      const char* src = wbase + (size_t)g2_ks(j, i + 6, rot2) * 32 * (D * 2);
      WAIT_VM(PACE_S);
#pragma unroll
      for (int pc = 0; pc < 4; ++pc) pk[i][pc] = *(const u32x4*)(src + (o2 + (u32)(pc * 8 * (D * 2))));
    }
    SCHED0();
    STAMP(8);
    };
    park(0);
    park(4);
    lflag_wait(L_FREE, 7, 7);
    STAMP(13);
    if (wid == 3) lflag_set(L_CONV + 12, 1);
    if (wid == 2) {
        if (lane < 32) {
          glds16(lane * 16, seq_tab + 32, CV_U + 4096);
          glds16(lane * 16, seq_tab + 32, CV_U + CV_USTR + 4096);
        }
        gflag_wait<8>(ws + WS_FUT + (UT_FULL ? (d0 >> 5) * 32 + (lane & 31) : (d0 >> 6) * 64 + lane) * 128, tag);
        glds16x4_dev(lane * 16, ws + WS_UT + d0 * (S * 2), CV_U);
        glds16x4_dev(lane * 16, ws + WS_UT + (d0 + 1) * (S * 2), CV_U + CV_USTR);
        dflag_set(L_CONV + 8, 1);
        STAMP(7);
      }
    if (wid < 2) {
      const u32 vl = lane * 16;
#pragma unroll 1
      for (int rr = 0; rr < 4; ++rr) {
        const char* src = ssmk + (size_t)(d0 + wid) * (S * 2) + (rr ? (rr - 4) * 2 : 0);
        const u32 dst = CV + (u32)(wid * 4 + rr) * CV_HSTR + (rr ? 24 : 32);
        glds16x4(rr && !lane ? 16u : vl, vl, vl, vl, src, dst);
      }
      dflag_set(L_CONV + wid * 4, 1);
      STAMP(9);
    }
#pragma unroll 1
    for (int i = 0; i < 6; ++i) {
      if (i == W2_HOLD) lflag_wait(L_PUB, 3, 1);
      if (i == 3) dflag_set(L_FED + j * 4, 3);
      const char* src = wbase + (size_t)g2_ks(j, i, rot2) * 32 * (D * 2);
      WAIT_VM(PACE_S);
      glds16x4(o2, o2 + 8 * (D * 2) - 1024, o2 + 16 * (D * 2) - 2048, o2 + 24 * (D * 2) - 3072, src, g2_place(j, i));
    }
    dflag_set(L_FED + j * 4, 6);
    STAMP(10);
    if (BPOLL ? wid == 2 : wid >= 2) {
      lflag_wait(L_PUB, 3, 1);
      const int e = lane < 28 ? lane : 27;
      gflag_wait<4>(ws + WS_FH + (tau * 56 + 4 * (e >> 1) + (e & 1) + 2 * (3 - wid)) * 128, tag);
      dflag_set(L_H + (3 - wid) * 4, 1);
    }
    WAIT_VM(0);
    STAMP(11);
    const u32 wl = opq(lane * 16 + j * 12288);
#pragma unroll
    for (int i = 0; i < 8; i += 2) {
      lflag_wait(L_PROG + j * 4, 0, i + 2);
#pragma unroll
      for (int n = 0; n < 8; ++n) lds_st128(wl + g2_place(0, (i + (n >> 2)) % 6) + (n & 3) * 1024, pk[i + (n >> 2)][n & 3]);
      lflag_set(L_FED + j * 4, i + 8);
    }
    STAMP(12);
    DIAG_FLUSH(ws);
    return;
  }
  const rsrc_t rs = ws_rsrc(ws);

  if (b >= 224) {
    if (wid >= 8 && b != 255) return;
    const int tid = (int)opq(threadIdx.x), lane = tid & 63;
    const int sp = b - 224;
    const u64 seed = gload64(ws + WS_E + opq(0));
    if (tid == 0) touch(WS_FST + sp * 128, ws, L_DUMMY);
    if (wid < 8) {
      const int row = 64 * sp + (tid >> 3), c8 = tid & 7;
      const int cu = sp < 28 ? sp : 27, mat = cu / 14, ct = cu % 14, kg = tid >> 4, c16 = tid & 15;
      const char* wp = (mat ? w3 : w1) + ct * 256;
      const u32 wo = (u32)(kg * (F * 2) + c16 * 16), xo = (u32)(row * (D * 2) + c8 * 16);
      u32x4 xv[7], wv[14];
#pragma unroll
      for (int i = 0; i < 7; ++i) xv[i] = *(const u32x4*)(x + (xo + i * 128));
#pragma unroll
      for (int i = 0; i < 14; ++i) wv[i] = *(const u32x4*)(wp + (wo + i * 32 * (F * 2)));
      SCHED0();
      float s = 0.f;
#pragma unroll
      for (int i = 0; i < 7; ++i)
#pragma unroll
        for (int j = 0; j < 4; ++j) s += bflo(xv[i][j]) + bfhi(xv[i][j]);
      const float m = sum8(s) * (1.0f / D);
      float q = 0.f;
#pragma unroll
      for (int i = 0; i < 7; ++i)
#pragma unroll
        for (int j = 0; j < 4; ++j) {
          const float aa = bflo(xv[i][j]) - m, bb = bfhi(xv[i][j]) - m;
          q = __builtin_fmaf(aa, aa, __builtin_fmaf(bb, bb, q));
        }
      const float sd = rbf(__builtin_amdgcn_sqrtf(rbf(rbf(sum8(q) * (1.0f / D)) + 1e-5f)));
      if (c8 == 0) *(LDSP(f32x2))(u32)(16384 + (tid >> 3) * 8) = f32x2{rbf(m), __builtin_amdgcn_rcpf(sd)};
      SCHED0();
      float a[8] = {};
#pragma unroll
      for (int i = 0; i < 14; ++i)
#pragma unroll
        for (int j = 0; j < 4; ++j) { a[2 * j] += bflo(wv[i][j]); a[2 * j + 1] += bfhi(wv[i][j]); }
      f32x4 t;
#pragma unroll
      for (int j = 0; j < 4; ++j) {
        auto p = __builtin_amdgcn_permlane16_swap(__builtin_bit_cast(u32, a[j]), __builtin_bit_cast(u32, a[j + 4]), false, false);
        t[j] = asf(p[0]) + asf(p[1]);
      }
      *(LDSP(f32x4))(u32)((kg >> 1) * 512 + c16 * 32 + (kg & 1) * 16) = t;
      STAMP(1);
    }
    WAIT_LGKM0(); BAR();
    const u64 tag = tag_of(seed);
    if (wid == 0) {
      const int cu = sp < 28 ? sp : 27;
      f32x2 cs = {};
#pragma unroll
      for (int i = 0; i < 16; ++i) cs += *(LDSP(f32x2))(u32)(i * 512 + lane * 8);
      st64_dev(rs, WS_STAT + (u32)(64 * sp + lane) * 8, *(LDSP(u32x2))(u32)(16384 + lane * 8));
      if (sp < 28) st64_dev(rs, WS_CSUM + (u32)((cu % 14) * 1024 + (cu / 14) * 512 + lane * 8), __builtin_bit_cast(u32x2, cs));
      gflag_publish(ws + WS_FST + sp * 128, tag, lane);
    }
    STAMP(2);
    WAIT_VM(0); BAR();
    if (SPPF) {
      if (tid < 392) touch((u32)(sp * 392 + tid) * 128, w2, L_DUMMY);
      if (tid < 448) touch((u32)(sp * 448 + tid) * 128, ssmk, L_DUMMY);
      BAR();
    }
    if (W2PF && wid > 0 && wid < 8) {
#pragma unroll 1
      for (int i = 0; i < 7; ++i) touch((u32)((((sp >> 3) * 7 + i) * 7 + wid - 1) * 64 + lane) * 128, w2, L_DUMMY);
      WAIT_VM(0);
    }
    STAMP(4);
    if (b == 255) {
      __builtin_amdgcn_s_sleep(ESLEEP);
      gflag_wait<64>(ws + (tid < 480 ? WS_FUT + tid * 128 : WS_FH + ((((tid < 704 ? tid - 480 : 223) / 14) * 2) * 56 + ((tid < 704 ? tid - 480 : 223) % 14) * 4) * 128), tag);
      BAR();
      if (tid == 0) gstore64(ws + WS_E, tag);
    }
    STAMP(3);
    DIAG_FLUSH(ws);
    return;
  }

  const int cw = wid - 4, th = cw & 1, fq = cw >> 1;
  {
    const int lane = (int)opq(threadIdx.x) & 63, g = lane >> 4, hh = lane >> 5, l31 = lane & 31;
    const int q4 = (lane >> 2) & 3, p4 = lane & 3;
    const u32 aA = 16384 + (u32)((8 * hh + q4) * 256 + ((fq ^ q4) << 6) + (g & 1) * 32 + p4 * 8);
    const u32 aB = (u32)((64 * th + l31) * 128 + ((hh ^ ((l31 >> 1) & 7)) << 4));
    f32x16 acc[2][2] = {};
    s16x8 fa[2][2][2], fb[2][2][2];
    u32 pa, pb[4];
    auto setslot = [&](u32 slot) {
      pa = opq(aA + slot);
#pragma unroll
      for (int k = 0; k < 4; ++k) pb[k] = opq((aB ^ (u32)(k << 5)) + slot);
    };
    auto rd = [&](int set) {
#pragma unroll
      for (int k = 0; k < 2; ++k) {
        const int ks = 2 * set + k;
        fa[set][k][0] = lds_tr2(pa, ks * 4096, ks * 4096 + 1024);
        fb[set][k][0] = lds_b128(pb[ks], 0);
        fb[set][k][1] = lds_b128(pb[ks], 4096);
        fa[set][k][1] = lds_tr2(pa, 16384 + ks * 4096, 16384 + ks * 4096 + 1024);
      }
    };
    auto mm = [&](int set, int n0, int n1) {
#pragma unroll
      for (int n = n0; n < n1; ++n) acc[(n >> 1) & 1][n & 1] = mfma32(fa[set][n >> 2][(n >> 1) & 1], fb[set][n >> 2][n & 1], acc[(n >> 1) & 1][n & 1]);
    };
    setslot(0);
    const u32 fad = opq(L_FULL + (lane & 1) * 4);
#pragma unroll
    for (int i = 0; i < 4; ++i) asm volatile("" : "+v"(acc[i >> 1][i & 1]));
    __builtin_amdgcn_s_setprio(1);
    lflag_wait(L_FULL, 1, 1);
    STAMP(1);
    CYC_BEGIN
    rd(0);
    SCHED0();
    u32 slot = 0;
    auto stage = [&](int c, bool bar) {
      rd(1);
      const u32 fl = *(volatile LDSP(u32))fad;
      mm(0, 0, 6);
      SGB(0x008, 1); SGB(0x100, 3); SGB(0x008, 1); SGB(0x100, 3); SGB(0x008, 1); SGB(0x100, 3); SGB(0x008, 1); SGB(0x100, 4); SGB(0x008, 2);
      SCHED0();
      slot = slot == 2 * SLOT ? 0 : slot + SLOT;
      setslot(slot);
      SCHED0();
      mm(0, 6, 7);
      SCHED0();
      __builtin_amdgcn_s_waitcnt(0xC07F);
      const bool miss = c < 6 && __builtin_amdgcn_ballot_w64(fl < (u32)(c + 2));
      SCHED0();
      mm(0, 7, 8);
      SCHED0();
      if (bar) BAR();
      SCHED0();
      mm(1, 0, 2);
      SCHED0();
      if (!bar && c == 6) lflag_set(L_FREE + cw * 4, 7);
      if (miss) lflag_wait(L_FULL, 1, c + 2);
      SCHED0();
      rd(0); mm(1, 2, 8);
#pragma unroll
      for (int i = 0; i < 4; ++i) { SGB(0x008, 1); SGB(0x100, 3); }
      SGB(0x008, 2);
      SCHED0();
    };
#pragma unroll 1
    for (int c = 0; c < 4; ++c) stage(c, true);
#pragma unroll 1
    for (int c = 4; c < 7; ++c) stage(c, false);
    STAMP(3);
    CYC_END;
    lflag_wait(L_EPI, 0, 1);
    STAMP(14);
    const u32 hdst = WS_H + (u32)((2 * panel + th) * 56 + 4 * ft + fq) * 4096 + lane * 16;
    float nm[2], c1[2], irs2[2];
#pragma unroll
    for (int j = 0; j < 2; ++j) {
      const f32x2 st = *(LDSP(f32x2))(L_STAT + (u32)(64 * th + 32 * j + l31) * 8);
      nm[j] = -st[0]; c1[j] = -1.44269504f * st[1]; irs2[j] = __builtin_amdgcn_rcpf(st[1] * st[1]);
    }
#pragma unroll
    for (int s = 0; s < 2; ++s) {
      f32x4 cs[2][2];
#pragma unroll
      for (int m = 0; m < 2; ++m)
#pragma unroll
        for (int G = 0; G < 2; ++G) cs[m][G] = *(LDSP(f32x4))(L_CSUM + (u32)(m * 128 + 32 * fq + 16 * s + 8 * G + 4 * hh) * 4);
#pragma unroll
      for (int j = 0; j < 2; ++j) {
        float a[8], ex[8], hv[8];
#pragma unroll
        for (int e = 0; e < 8; ++e) a[e] = __builtin_fmaf(nm[j], cs[0][e >> 2][e & 3], acc[0][j][8 * s + e]);
#pragma unroll
        for (int e = 0; e < 8; ++e) ex[e] = a[e] * c1[j];
        SCHED0();
#pragma unroll
        for (int e = 0; e < 8; ++e) { ex[e] = __builtin_amdgcn_exp2f(ex[e]); hv[e] = __builtin_fmaf(nm[j], cs[1][e >> 2][e & 3], acc[1][j][8 * s + e]); }
        SCHED0();
#pragma unroll
        for (int e = 0; e < 8; ++e) ex[e] = __builtin_fmaf(ex[e], irs2[j], irs2[j]);
        SCHED0();
#pragma unroll
        for (int e = 0; e < 8; ++e) { ex[e] = __builtin_amdgcn_rcpf(ex[e]); hv[e] *= a[e]; }
        SCHED0();
#pragma unroll
        for (int e = 0; e < 8; ++e) hv[e] *= ex[e];
        st128_dev(rs, hdst + (s * 2 + j) * 1024, u32x4{pk2(hv[0], hv[1]), pk2(hv[2], hv[3]), pk2(hv[4], hv[5]), pk2(hv[6], hv[7])});
      }
    }
    STAMP(4);
  }
  char* hflag = ws + WS_FH + ((2 * panel + th) * 56 + 4 * ft + fq) * 128;
  u32x4 xv, sv;
  if (cw < 4) {
    const int ht = (int)opq(threadIdx.x) & 255, lane = ht & 63, l15 = ht & 15;
    const u64 tag = tag_of(*(volatile LDSP(u64))L_TAG);
    const int ch = cw >> 1, var = cw & 1;
    gflag_publish(hflag, tag, lane);
    u32x4 xr[2]; u32x2 st[2];
#pragma unroll
    for (int i = 0; i < 2; ++i) {
      const int n = ht + 256 * i, row = n >> 3, c = n & 7;
      xr[i] = *(const u32x4*)(x + (u32)((64 * tau + row) * (D * 2) + (64 * dl + 8 * c) * 2));
      st[i] = ld64_dev(rs, WS_STAT + (u32)(64 * tau + row) * 8);
    }
    SCHED0();
    lflag_wait(L_CONV, 3, 1);
    asm volatile("" ::: "memory");
    STAMP(5);
    conv_compute(lane, cw, d0 + ch, rs, hflag, tag, false);
    STAMP(6);
    u32x4 xn[2];
#pragma unroll
    for (int i = 0; i < 2; ++i) {
      const float mb = asf(st[i][0]), rsd = asf(st[i][1]);
#pragma unroll
      for (int e = 0; e < 4; ++e) {
        const u32 dd = pk2(bflo(xr[i][e]) - mb, bfhi(xr[i][e]) - mb);
        xn[i][e] = pk2(bflo(dd) * rsd, bfhi(dd) * rsd);
      }
    }
    SCHED0();
    gflag_publish(ws + WS_FY + (var * 448 + d0 + ch) * 128, tag, lane);
    STAMP(7);
    xv = xr[0]; sv = xn[0];
    lflag_wait(L_XCH, 1, 1);
    lds_st128(E_S + (u32)ht * 16, xn[1]); lds_st128(E_X + (u32)ht * 16, xr[1]);
    gflag_wait<4>(ws + WS_FY + ((tau >> 4) * 448 + 64 * dl + 16 * cw + l15) * 128, tag);
    STAMP(8);
#pragma unroll
    for (int i = 0; i < 2; ++i)
      glds16_dev((u32)((lane >> 3) * (S * 2) + (((lane & 7) ^ (2 * cw + i)) << 4)),
                 ws + WS_YT + (64 * dl + 16 * cw + 8 * i) * (S * 2) + 128 * tau, xbuf(cw ^ 1) + (u32)i * 1024);
    WAIT_VM(0);
    STAMP(10);
  } else {
    const int j = cw - 4;
    const int lane = (int)opq(threadIdx.x) & 63;
    lflag_set(L_PUB + j * 4, 1);
    const u64 btag = tag_of(*(volatile LDSP(u64))L_TAG);
    gflag_publish(hflag, btag, lane);
    STAMP(5);
    const u32 hl = lane * 16;
    const char* hbase = ws + WS_H + (size_t)tau * 56 * 4096;
    const int q4 = (lane >> 2) & 3;
    const u32 ta = (u32)((4 * (lane >> 5) + q4) * 128 + ((q4 >> 1) << 6) + ((lane >> 4) & 1) * 32 + (lane & 3) * 8);
    s16x8 fa[2][4], fb[HDEPTH][4];
    f32x16 acc[2][2] = {};
    auto ldh = [&](int i) {
      const char* p = hbase + (size_t)g2_ks(j, i, rot2) * 4096;
      asm volatile("global_load_dwordx4 %0, %4, %5 sc1\n\tglobal_load_dwordx4 %1, %4, %5 offset:1024 sc1\n\t"
                   "global_load_dwordx4 %2, %4, %5 offset:2048 sc1\n\tglobal_load_dwordx4 %3, %4, %5 offset:3072 sc1"
                   : "=&v"(fb[i % HDEPTH][0]), "=&v"(fb[i % HDEPTH][1]), "=&v"(fb[i % HDEPTH][2]), "=&v"(fb[i % HDEPTH][3])
                   : "v"(hl), "s"(p) : "memory");
    };
    u32 tb[2];
#pragma unroll
    for (int a = 0; a < 2; ++a) tb[a] = opq((ta ^ (u32)(a << 6)) + g2_place(j, 0));
    auto rda = [&](int i) {
#pragma unroll
      for (int a = 0; a < 2; ++a)
        if (i > 0 && i % 3 == 0) tb[a] = opq(i % 6 ? tb[a] - 2 * SLOT : tb[a] + 2 * SLOT);
#pragma unroll
      for (int n = 0; n < 4; ++n) {
        const int o = (i % 3) * 4096 + (n >> 1) * 2048;
        fa[i & 1][n] = lds_tr2(tb[n & 1], o, o + 1024);
      }
    };
    const u32 fed = opq(L_FED + j * 4), prog = opq(L_PROG + j * 4);
    if (BPOLL) { const int e = 7 * j + (lane < 7 ? lane : 6); gflag_wait<2>(ws + WS_FH + (tau * 56 + 4 * (e >> 1) + (e & 1)) * 128, btag); }
    else lflag_wait(L_H, 0, 1);
    STAMP(6);
#pragma unroll
    for (int i = 0; i < HDEPTH; ++i) ldh(i);
    lflag_wait(L_FED + j * 4, 0, 3);
    rda(0);
    SCHED0();
    u32 fl = 0;
#pragma unroll
    for (int i = 0; i < 14; ++i) {
      if (i == 2 || (i >= 5 && i < 13 && (i & 1))) {
        if (__builtin_amdgcn_readfirstlane(fl) < i + 2) lflag_wait(L_FED + j * 4, 0, i + 2);
      }
      asm volatile("s_waitcnt vmcnt(%4)" : "+v"(fb[i % HDEPTH][0]), "+v"(fb[i % HDEPTH][1]), "+v"(fb[i % HDEPTH][2]), "+v"(fb[i % HDEPTH][3])
                   : "i"(i + HDEPTH <= 14 ? (HDEPTH - 1) * 4 : (13 - i) * 4) : "memory");
      SCHED0();
      if (i < 13) rda(i + 1);
      if (i == 1 || (i >= 4 && i < 12 && !(i & 1))) fl = *(volatile LDSP(u32))fed;
#pragma unroll
      for (int s = 0; s < 2; ++s)
#pragma unroll
        for (int a = 0; a < 2; ++a)
#pragma unroll
          for (int t = 0; t < 2; ++t) acc[a][t] = mfma32(fa[i & 1][2 * s + a], fb[i % HDEPTH][2 * s + t], acc[a][t]);
      if (i < 13) {
#pragma unroll
        for (int n = 0; n < 5; ++n) { SGB(0x008, 1); SGB(0x100, 2); }
        SGB(0x008, 3);
      }
      SCHED0();
      if (i < 8 && (i & 1)) *(volatile LDSP(u32))prog = i + 1;
      if (i == 7 - HDEPTH) { lflag_wait(L_H + 4, 0, 1); STAMP(8); }
      if (i + HDEPTH < 14) ldh(i + HDEPTH);
      SCHED0();
      if (i == 0) STAMP(7);
      if (i == 8) STAMP(9);
    }
    STAMP(10);
    const int ln = (int)opq(threadIdx.x), lt = ln & 31, lh = (ln >> 5) & 1;
#pragma unroll
    for (int t = 0; t < 2; ++t)
#pragma unroll
      for (int a = 0; a < 2; ++a)
#pragma unroll
        for (int G = 0; G < 4; ++G)
          *(LDSP(f32x4))(g2_place(j, t ? 3 : 0) + (u32)(lt * 256 + (((8 * a + 2 * G + lh) ^ (lt & 7)) << 4))) =
              f32x4{acc[a][t][4 * G], acc[a][t][4 * G + 1], acc[a][t][4 * G + 2], acc[a][t][4 * G + 3]};
  }
  WAIT_LGKM0();
  STAMP(11);
  BAR();
  STAMP(12);
  {
    const int ht = (int)opq(threadIdx.x) & 255;
    const int half = cw >> 2, tk = ht >> 3, dg = ht & 7;
    if (half) { sv = *(LDSP(u32x4))(E_S + (u32)ht * 16); xv = *(LDSP(u32x4))(E_X + (u32)ht * 16); }
    f32x4 s0 = {}, s1 = {};
#pragma unroll
    for (int w = 0; w < 4; ++w) {
      const u32 pb = (half ? 0u : 2 * SLOT) + (u32)w * 12288 + (u32)tk * 256;
      s0 += *(LDSP(f32x4))(pb + (u32)(((2 * dg) ^ (tk & 7)) << 4));
      s1 += *(LDSP(f32x4))(pb + (u32)(((2 * dg + 1) ^ (tk & 7)) << 4));
    }
    const int tok = 32 * half + tk;
    const u32 ya = xbuf((dg >> 1) ^ 1) + (u32)(8 * (dg & 1) * 128 + (((tok >> 3) ^ dg) << 4) + (tok & 7) * 2);
    float o[8];
#pragma unroll
    for (int e = 0; e < 8; ++e) {
      const float f = rbf(e < 4 ? s0[e & 3] : s1[e & 3]);
      const float gt = rbf(silu_f(f));
      const float xe = (e & 1) ? bfhi(xv[e >> 1]) : bflo(xv[e >> 1]);
      const float ne = (e & 1) ? bfhi(sv[e >> 1]) : bflo(sv[e >> 1]);
      const float se = rbf(ne * bflo(*(LDSP(u16))(ya + e * 128)));
      o[e] = xe + rbf(se * gt);
    }
    __builtin_amdgcn_raw_buffer_store_b128(u32x4{pk2(o[0], o[1]), pk2(o[2], o[3]), pk2(o[4], o[5]), pk2(o[6], o[7])},
        __builtin_amdgcn_make_buffer_rsrc(out, 0, S * D * 2, 0x00020000), (64 * tau + 32 * half + tk) * (D * 2) + (64 * dl + 8 * dg) * 2, 0, 16);
  }
  STAMP(13);
  DIAG_FLUSH(ws);
}
}
#if defined(__HIP_DEVICE_COMPILE__) && __HIP_DEVICE_COMPILE__
#pragma clang attribute pop
#endif

extern "C" void kernel_launch(void* const* d_in, const int* in_sizes, int n_in, void* d_out, int out_size, void* d_ws,
                              size_t ws_size, hipStream_t stream) {
  (void)hipFuncSetAttribute((const void*)fused_kernel, hipFuncAttributeMaxDynamicSharedMemorySize, 163840);
  fused_kernel<<<256, 768, 163840, stream>>>((const char*)d_in[0], (const char*)d_in[1], (const char*)d_in[2], (const char*)d_in[3],
                                             (const char*)d_in[4], (const char*)d_in[5], (const char*)d_in[7], (const char*)d_in[8],
                                             (const char*)d_in[9], (char*)d_ws, (char*)d_out);
}
```

```cpp
#include <hip/hip_runtime.h>
#include <hip/hip_bf16.h>
#include <stdint.h>
#pragma clang diagnostic ignored "-Wint-to-pointer-cast"

#if defined(__HIP_DEVICE_COMPILE__) && __HIP_DEVICE_COMPILE__
#pragma clang attribute push(__attribute__((target("no-packed-fp32-ops"))), apply_to = function)
#endif
namespace {
typedef unsigned short u16;
typedef unsigned int u32;
typedef unsigned long long u64;
typedef __attribute__((ext_vector_type(4))) short s16x4;
typedef __attribute__((ext_vector_type(8))) short s16x8;
typedef __attribute__((ext_vector_type(8))) __bf16 bf16x8;
typedef __attribute__((ext_vector_type(2))) __bf16 bf16x2;
typedef __attribute__((ext_vector_type(16))) float f32x16;
typedef __attribute__((ext_vector_type(4))) float f32x4;
typedef __attribute__((ext_vector_type(2))) float f32x2;
typedef __attribute__((ext_vector_type(4))) u32 u32x4;
typedef __attribute__((ext_vector_type(2))) u32 u32x2;
#define LDSP(T) __attribute__((address_space(3))) T*
#define GLBP(T) __attribute__((address_space(1))) T*
#define DEV __device__ __forceinline__

constexpr int S = 2048, D = 448, F = 1792;
constexpr u32 WS_H = 0;
constexpr u32 WS_UT = WS_H + 7340032;
constexpr u32 WS_YT = WS_UT + 1835008;
constexpr u32 WS_STAT = WS_YT + 1835008;
constexpr u32 WS_CSUM = WS_STAT + 16384;
constexpr u32 WS_E = WS_CSUM + 16384;
constexpr u32 WS_FUT = WS_E + 128;
constexpr u32 WS_FST = WS_FUT + 448 * 128;
constexpr u32 WS_FY = WS_FST + 32 * 128;
constexpr u32 WS_FH = WS_FY + 896 * 128;
constexpr u32 WS_END = WS_FH + 1792 * 128;
constexpr u32 SLOT = 49152;
constexpr u32 MISC = 3 * SLOT;
constexpr u32 L_FULL = MISC, L_EPI = MISC + 8, L_H = MISC + 16, L_FREE = MISC + 32, L_CONV = MISC + 64, L_FED = MISC + 80,
              L_TAG = MISC + 96, L_DD = MISC + 104, L_PROG = MISC + 128, L_XCH = MISC + 144, L_PUB = MISC + 160, L_STAT = MISC + 256,
              L_CSUM = MISC + 1280, L_USCR = MISC + 4096, L_DUMMY = MISC + 2304, L_XB2 = MISC + 12288;
constexpr u32 CV = SLOT, CV_HSTR = 4160, CV_U = CV + 8 * CV_HSTR, CV_USTR = 4608;
constexpr u32 E_S = SLOT, E_X = SLOT + 4096;
constexpr int SPIN_CAP = 1 << 18;

__device__ const u32 seq_tab[160] __attribute__((aligned(128))) = {0, 1, 2, 3, 4, 5, 6, 7};
DEV float asf(u32 v) { return __builtin_bit_cast(float, v); }
DEV float bflo(u32 v) { return asf(v << 16); }
DEV float bfhi(u32 v) { return asf(v & 0xffff0000u); }
template <int CTRL> DEV float dpp(float v) { return asf(__builtin_amdgcn_update_dpp(0, __builtin_bit_cast(u32, v), CTRL, 0xf, 0xf, true)); }
DEV float sum8(float v) { v += dpp<0xB1>(v); v += dpp<0x4E>(v); return v + dpp<0x141>(v); }
DEV u32 pk2(float lo, float hi) {
  asm("" : "+v"(lo)); asm("" : "+v"(hi));
  bf16x2 r = {(__bf16)lo, (__bf16)hi};
  return __builtin_bit_cast(u32, r);
}
DEV float rbf(float f) { bf16x2 r = {(__bf16)f, (__bf16)f}; return bflo(__builtin_bit_cast(u32, r)); }
DEV f32x4 mfma16(s16x8 a, s16x8 b, f32x4 c) {
  return __builtin_amdgcn_mfma_f32_16x16x32_bf16(__builtin_bit_cast(bf16x8, a), __builtin_bit_cast(bf16x8, b), c, 0, 0, 0);
}
DEV f32x16 mfma32(s16x8 a, s16x8 b, f32x16 c) {
  return __builtin_amdgcn_mfma_f32_32x32x16_bf16(__builtin_bit_cast(bf16x8, a), __builtin_bit_cast(bf16x8, b), c, 0, 0, 0);
}
DEV u32 opq(u32 v) { asm volatile("" : "+v"(v)); return v; }
DEV s16x8 lds_tr2(u32 base, int o0, int o1) {
  s16x4 lo = __builtin_amdgcn_ds_read_tr16_b64_v4i16((LDSP(s16x4))((LDSP(char))base + o0));
  s16x4 hi = __builtin_amdgcn_ds_read_tr16_b64_v4i16((LDSP(s16x4))((LDSP(char))base + o1));
  return __builtin_shufflevector(lo, hi, 0, 1, 2, 3, 4, 5, 6, 7);
}
DEV s16x8 lds_b128(u32 base, int o) { return *(LDSP(s16x8))((LDSP(char))base + o); }
DEV void lds_st128(u32 a, u32x4 v) { *(LDSP(u32x4))a = v; }
DEV void glds16x4(u32 v0, u32 v1, u32 v2, u32 v3, const void* sbase, u32 ldst) {
  asm volatile("s_mov_b32 m0, %5\n\ts_nop 0\n\tglobal_load_lds_dwordx4 %0, %4\n\tglobal_load_lds_dwordx4 %1, %4 offset:1024\n\t"
               "global_load_lds_dwordx4 %2, %4 offset:2048\n\tglobal_load_lds_dwordx4 %3, %4 offset:3072"
               ::"v"(v0), "v"(v1), "v"(v2), "v"(v3), "s"(sbase), "s"(ldst) : "memory");
}
DEV void glds16(u32 voff, const void* sbase, u32 ldst) {
  asm volatile("s_mov_b32 m0, %2\n\ts_nop 0\n\tglobal_load_lds_dwordx4 %0, %1" ::"v"(voff), "s"(sbase), "s"(ldst) : "memory");
}
DEV void glds16x4_dev(u32 v, const void* sbase, u32 ldst) {
  asm volatile("s_mov_b32 m0, %2\n\ts_nop 0\n\tglobal_load_lds_dwordx4 %0, %1 sc1\n\tglobal_load_lds_dwordx4 %0, %1 offset:1024 sc1\n\t"
               "global_load_lds_dwordx4 %0, %1 offset:2048 sc1\n\tglobal_load_lds_dwordx4 %0, %1 offset:3072 sc1"
               ::"v"(v), "s"(sbase), "s"(ldst) : "memory");
}
DEV void glds16_dev(u32 voff, const void* sbase, u32 ldst) {
  asm volatile("s_mov_b32 m0, %2\n\ts_nop 0\n\tglobal_load_lds_dwordx4 %0, %1 sc1" ::"v"(voff), "s"(sbase), "s"(ldst) : "memory");
}
DEV void dflag_set(u32 addr, u32 val) {
  asm volatile("s_waitcnt lgkmcnt(0)\n\ts_mov_b32 m0, %2\n\ts_mov_b64 exec, 1\n\tglobal_load_lds_dword %0, %1\n\ts_mov_b64 exec, -1"
               ::"v"(0), "s"(seq_tab + val), "s"(addr) : "memory");
}
#define WAIT_VM(n) asm volatile("s_waitcnt vmcnt(%0)" ::"i"(n) : "memory")
#define WAIT_LGKM0() asm volatile("s_waitcnt lgkmcnt(0)" ::: "memory")
#define BAR() do { asm volatile("" ::: "memory"); __builtin_amdgcn_s_barrier(); asm volatile("" ::: "memory"); } while (0)
#define SCHED0() __builtin_amdgcn_sched_barrier(0)
#define SGB(mask, n) __builtin_amdgcn_sched_group_barrier(mask, n, 0)
DEV float silu_f(float x) { return x * __builtin_amdgcn_rcpf(1.0f + __builtin_amdgcn_exp2f(-1.44269504f * x)); }

DEV void lflag_set(u32 addr, u32 val) {
  asm volatile("s_waitcnt lgkmcnt(0)\n\tds_write_b32 %0, %1" ::"v"(addr), "v"(val) : "memory");
}
DEV void lflag_wait(u32 addr, u32 mask, u32 val) {
  const u32 a = addr + 4 * (threadIdx.x & mask);
#pragma unroll 1
  for (int i = 0; i < SPIN_CAP; ++i) {
    u32 v;
    asm volatile("ds_read_b32 %0, %1\n\ts_waitcnt lgkmcnt(0)" : "=v"(v) : "v"(a) : "memory");
    if (__builtin_amdgcn_ballot_w64(v < val) == 0) break;
    __builtin_amdgcn_s_sleep(1);
  }
}
DEV u64 gload64(const char* p) { return __hip_atomic_load((GLBP(u64))p, __ATOMIC_RELAXED, __HIP_MEMORY_SCOPE_AGENT); }
DEV void gstore64(char* p, u64 v) { __hip_atomic_store((GLBP(u64))p, v, __ATOMIC_RELAXED, __HIP_MEMORY_SCOPE_AGENT); }
template <int SLEEP>
DEV void gflag_wait(const char* lane_ptr, u64 tag) {
  bool ok = false;
#pragma unroll 1
  for (int i = 0; i < SPIN_CAP; ++i) {
    asm volatile("" ::: "memory");
    if (!ok) ok = gload64(lane_ptr) == tag;
    if (__builtin_amdgcn_ballot_w64(!ok) == 0) break;
    __builtin_amdgcn_s_sleep(SLEEP);
  }
  asm volatile("" ::: "memory");
}
DEV void gflag_publish(char* p, u64 tag, int lane) {
  WAIT_VM(0);
  if (lane == 0) gstore64(p, tag);
}
DEV u64 tag_of(u64 seed) { return seed * 0x9E3779B97F4A7C15ull + 0xD1B54A32D192ED03ull; }
DEV u64 make_tag(const char* ws) { return tag_of(gload64(ws + WS_E)); }
typedef __amdgpu_buffer_rsrc_t rsrc_t;
DEV rsrc_t ws_rsrc(char* ws) { return __builtin_amdgcn_make_buffer_rsrc(ws, 0, WS_END, 0x00020000); }
DEV void st128_dev(rsrc_t r, u32 off, u32x4 v) { __builtin_amdgcn_raw_buffer_store_b128(v, r, off, 0, 16); }
DEV void st64_dev(rsrc_t r, u32 off, u32x2 v) { __builtin_amdgcn_raw_buffer_store_b64(v, r, off, 0, 16); }
DEV u32x2 ld64_dev(rsrc_t r, u32 off) { return __builtin_amdgcn_raw_buffer_load_b64(r, off, 0, 16); }
DEV void touch(u32 voff, const void* sbase, u32 ldst) {
  asm volatile("s_mov_b32 m0, %2\n\ts_nop 0\n\tglobal_load_lds_dword %0, %1" ::"v"(voff), "s"(sbase), "s"(ldst) : "memory");
}

DEV void glds4_dev(u32 voff, const void* sbase, u32 ldst) {
  asm volatile("s_mov_b32 m0, %2\n\ts_nop 0\n\tglobal_load_lds_dword %0, %1 sc1" ::"v"(voff), "s"(sbase), "s"(ldst) : "memory");
}

DEV u32 xbuf(int w) { return w < 2 ? L_USCR + (u32)w * 4096 : (w == 2 ? L_XB2 : CV + 45056); }
DEV void conv_compute(int lane, int cw, int d, rsrc_t rs) {
  const int ch = cw >> 1, var = cw & 1;
  const int m = lane & 15, g = lane >> 4, q4 = m >> 2, p4 = lane & 3;
  u32 aa = opq(CV + (u32)(ch * 4 + ((q4 + 1) & 3)) * CV_HSTR + (u32)(((q4 + 1) & 4) + 8 * g + 4 * p4) * 2 + var * 256);
  u32 ba = opq(CV_U + (u32)ch * CV_USTR + (u32)(240 - 16 * m + 8 * g) * 2 + var * 256);
  f32x4 acc[8] = {};
  s16x8 A[8], B[8];
#pragma unroll
  for (int p = 0; p < 8; ++p) { A[7 - p] = lds_tr2(aa, (7 - p) * 512, (7 - p) * 512 + 8); B[p] = lds_b128(ba, (7 - p) * 512); }
  if (var == 0) {
    const u32 a0 = CV + (u32)ch * 4 * CV_HSTR + (u32)(m + 1 + 8 * g) * 2;
#pragma unroll
    for (int j = 0; j < 8; ++j) { const short v = *(LDSP(short))(a0 + j * 2); A[0][j] = m + 8 * g + j >= 15 ? v : (short)0; }
  }
  SCHED0();
#pragma unroll 1
  for (int it = 0; it < 4; ++it) {
#pragma unroll
    for (int p = 7; p >= 0; --p) {
#pragma unroll
      for (int dd = 0; dd <= 7 - p; ++dd) {
        acc[p + dd] = mfma16(A[p], B[dd], acc[p + dd]);
        if (p == 0) { SCHED0(); B[dd] = lds_b128(ba, 64 + (7 - dd) * 512); SCHED0(); }
      }
      SCHED0();
      A[p] = lds_tr2(aa, 64 + p * 512, 64 + p * 512 + 8);
      SCHED0();
    }
    aa = opq(aa + 64); ba = opq(ba + 64);
  }
  f32x4 y[4];
#pragma unroll
  for (int i = 0; i < 4; ++i) {
    *(LDSP(f32x4))(xbuf(cw) + (u32)(i * 1024 + lane * 16)) = var ? acc[i] : acc[4 + i];
    y[i] = var ? acc[4 + i] : acc[i];
  }
  lflag_set(L_XCH + cw * 4, 1);
  const u32 dv = *(LDSP(u32))L_DD;
  const float Dd = ch ? bfhi(dv) : bflo(dv);
  u32x2 uu[4];
#pragma unroll
  for (int i = 0; i < 4; ++i) uu[i] = *(LDSP(u32x2))(CV_U + (u32)ch * CV_USTR + (u32)(2044 - (1024 * var + 256 * i + 16 * m + 4 * g)) * 2);
  lflag_wait(L_XCH + (cw ^ 1) * 4, 0, 1);
#pragma unroll
  for (int i = 0; i < 4; ++i) {
    const int t = 1024 * var + 256 * i + 16 * m + 4 * g;
    const f32x4 s = y[i] + *(LDSP(f32x4))(xbuf(cw ^ 1) + (u32)(i * 1024 + lane * 16));
    float y0 = rbf(s[0]) + rbf(bfhi(uu[i][1]) * Dd), y1 = rbf(s[1]) + rbf(bflo(uu[i][1]) * Dd);
    float y2 = rbf(s[2]) + rbf(bfhi(uu[i][0]) * Dd), y3 = rbf(s[3]) + rbf(bflo(uu[i][0]) * Dd);
    st64_dev(rs, WS_YT + (u32)(d * (S * 2) + t * 2), u32x2{pk2(y0, y1), pk2(y2, y3)});
  }
}

DEV int g2_ks(int j, int i) { const int e = 7 * j + (i < 7 ? i : i - 7); return 4 * (e >> 1) + (e & 1) + (i < 7 ? 0 : 2); }
DEV u32 g2_place(int j, int p) { return (p < 3 ? 2 * SLOT + (u32)p * 4096 : (u32)(p - 3) * 4096) + (u32)j * 12288; }
DEV u32 w2_voff(int lane) { return (u32)((lane >> 3) * (D * 2) + (((lane & 7) ^ (((lane >> 4) & 1) << 2)) << 4)); }

DEV void share(int b, int n, const char* base) {
  const int lane = (int)opq(threadIdx.x) & 63;
  if (lane < n) touch((u32)(b * n + lane) * 128, base, L_DUMMY);
}

__global__ __launch_bounds__(768, 3) void fused_kernel(const char* x, const char* kp, const char* vp, const char* skp,
                                                       const char* ssmk, const char* ssmD, const char* w1, const char* w3,
                                                       const char* w2, char* ws, char* out) {
  const int b = blockIdx.x, xcd = b & 7, r = b >> 3;
  const int wid = __builtin_amdgcn_readfirstlane(threadIdx.x >> 6);
  const int panel = (xcd >> 1) * 4 + r / 7, ft = (xcd & 1) * 7 + r % 7;
  const int tau = xcd * 4 + r / 7, dl = r % 7;
  const int d0 = 2 * b;

  if (b < 224) {
    if (wid >= 4 && wid < 7) share(b, wid == 4 ? 64 : 56, wid == 4 ? x : (wid == 5 ? w1 : w3));
    if (wid == 7) { *(LDSP(u32))(MISC + (threadIdx.x & 63) * 4) = 0; WAIT_LGKM0(); }
    BAR();
    if (wid < 2) {
      const int lane = (int)opq(threadIdx.x) & 63;
      const int g = lane >> 4, l15 = lane & 15;
      u32 vx[8], vw[8];
      const u32 ox = (u32)((lane >> 3) * (D * 2) + (((lane & 7) ^ g) << 4));
      const u32 ow = (u32)(g * (F * 2) + (((l15 >> 2) ^ g) << 6) + ((lane & 3) << 4));
#pragma unroll
      for (int i = 0; i < 8; ++i) {
        vx[i] = (ox ^ (u32)((i & 1) * 64)) + i * 8 * (D * 2) - (i & 3) * 1024;
        vw[i] = ow + i * 4 * (F * 2) - (i & 3) * 1024;
      }
#pragma unroll 1
      for (int c = 0; c < 7; ++c) {
        if (c >= 3) BAR();
        const u32 slot = (u32)(c % 3) * SLOT + wid * 8192;
        const char* xb = x + (size_t)(panel * 128 + wid * 64) * (D * 2) + c * 128;
        const char* w1b = w1 + (size_t)(c * 64 + wid * 32) * (F * 2) + ft * 256;
        const char* w3b = w3 + (size_t)(c * 64 + wid * 32) * (F * 2) + ft * 256;
#pragma unroll
        for (int i = 0; i < 8; i += 4) {
          glds16x4(vx[i], vx[i + 1], vx[i + 2], vx[i + 3], xb, slot + i * 1024);
          glds16x4(vw[i], vw[i + 1], vw[i + 2], vw[i + 3], w1b, slot + 16384 + i * 1024);
          glds16x4(vw[i], vw[i + 1], vw[i + 2], vw[i + 3], w3b, slot + 32768 + i * 1024);
        }
        dflag_set(L_FULL + wid * 4, c + 1);
      }
    }
    if (wid == 7 && (threadIdx.x & 63) < 5) touch((threadIdx.x & 63) * 128, seq_tab, L_DUMMY);
  }

  if (__builtin_expect(wid < 4 && b < 224, 1)) {
    const int lane = (int)opq(threadIdx.x) & 63;
    u64 tag = 0;
    if (wid < 2) {
      if (wid == 0) {
        tag = make_tag(ws);
        const int k = lane & 7;
        gflag_wait<8>(ws + WS_FST + (k < 2 ? 2 * panel + k : (k == 2 ? tau : (k & 1) * 14 + ft)) * 128, tag);
        glds16_dev(lane * 16, ws + WS_STAT + panel * 1024, L_STAT);
        glds16_dev(lane * 16, ws + WS_CSUM + ft * 1024, L_CSUM);
        if (lane < 2) glds4_dev(lane * 4, ws + WS_E, L_TAG);
        if (lane < 1) glds4_dev(0, ssmD + d0 * 2, L_DD);
        dflag_set(L_EPI, 1);
      }
    } else {
      __builtin_amdgcn_s_setprio(3);
      const rsrc_t rs = ws_rsrc(ws);
      const int u = wid - 2;
      const int ti = b / 7, cj = b % 7;
      const int tg = lane >> 3, cg = lane & 7;
      const int t0 = 64 * ti + 32 * u + 4 * tg, ch0 = 64 * cj + 8 * cg;
      u32x4 kr[7], vr[4], sw[4]; u32x2 o[8];
      u64 seed;
      auto ldkv = [&]() {
        const u32 ko = (u32)(t0 * (D * 2) + ch0 * 2);
#pragma unroll
        for (int i = 0; i < 7; ++i) kr[i] = *(const u32x4*)(kp + (i < 3 && t0 == 0 ? ko : ko + (u32)((i - 3) * (D * 2))));
#pragma unroll
        for (int i = 0; i < 4; ++i) vr[i] = *(const u32x4*)(vp + (ko + (u32)(i * (D * 2))));
        SCHED0();
      };
      auto piece = [&](int p) {
#pragma unroll
        for (int c = 2 * p; c < 2 * p + 2; ++c) {
          const u32 wa = sw[p][(c & 1) * 2], wb = sw[p][(c & 1) * 2 + 1];
          const float w0 = bflo(wa), w1f = bfhi(wa), w2f = bflo(wb), w3f = bfhi(wb);
          float kf[7], ks[4];
#pragma unroll
          for (int i = 0; i < 7; ++i) kf[i] = (c & 1) ? bfhi(kr[i][p]) : bflo(kr[i][p]);
#pragma unroll
          for (int i = 0; i < 4; ++i)
            ks[i] = __builtin_fmaf(kf[i + 3], w0, __builtin_fmaf(kf[i + 2], w1f, __builtin_fmaf(kf[i + 1], w2f, kf[i] * w3f)));
#pragma unroll
          for (int i = 0; i < 2; ++i) {
            const int ta = 3 - 2 * i, tb = 2 - 2 * i;
            const u32 kk = pk2(ks[ta], ks[tb]);
            o[c][i] = pk2(bflo(kk) * ((c & 1) ? bfhi(vr[ta][p]) : bflo(vr[ta][p])), bfhi(kk) * ((c & 1) ? bfhi(vr[tb][p]) : bflo(vr[tb][p])));
          }
          asm volatile("" : "+v"(o[c]));
        }
        SCHED0();
      };
      BAR();
      ldkv();
#pragma unroll
      for (int i = 0; i < 4; ++i) sw[i] = *(const u32x4*)(skp + (u32)(ch0 * 8 + i * 16));
      seed = gload64(ws + WS_E + opq(0));
      if (u && lane < 14) {
        const u32 fo = lane < 8 ? WS_FH + (u32)((2 * panel + (lane & 1)) * 56 + 4 * ft + (lane >> 1)) * 128
                                : (lane < 12 ? WS_FY + (u32)((lane & 1) * 448 + d0 + ((lane >> 1) & 1)) * 128 : WS_FUT + (u32)(cj * 64 + 2 * ti + (lane & 1)) * 128);
        touch(fo, ws, L_DUMMY);
      }
      SCHED0();
      BAR();
      SCHED0();
      BAR();
      SCHED0();
#pragma unroll
      for (int i = 0; i < 3; ++i) if (t0 == 0) kr[i] = u32x4{0, 0, 0, 0};
      piece(0);
      BAR();
      SCHED0();
#pragma unroll
      for (int c = 1; c < 4; ++c) piece(c);
      tag = tag_of(seed);
      const u32 scr = L_USCR + (u32)u * 4096;
#pragma unroll
      for (int c = 0; c < 8; ++c) *(LDSP(u32x2))(scr + (u32)((cg * 8 + c) * 64 + (((7 - tg) ^ (cg & 6)) << 3))) = o[c];
      WAIT_LGKM0();
#pragma unroll
      for (int i = 0; i < 4; ++i) {
        const int dd = 16 * i + (lane >> 2), c4 = lane & 3;
        u32x4 v = *(LDSP(u32x4))(scr + (u32)(dd * 64 + ((c4 ^ (dd >> 4)) << 4)));
        st128_dev(rs, WS_UT + (u32)((64 * cj + dd) * (S * 2) + (2016 - 64 * ti - 32 * u) * 2 + c4 * 16), v);
      }
      gflag_publish(ws + WS_FUT + (cj * 64 + 2 * ti + u) * 128, tag, lane);
    }
    const int j = wid;
    const u32 o2 = w2_voff(lane);
    const char* wbase = w2 + dl * 128;
    u32x4 pk[8][4];
    auto park = [&](int i0, int i1) {
#pragma unroll
      for (int i = i0; i < i1; ++i) {
        const char* src = wbase + (size_t)g2_ks(j, i + 6) * 32 * (D * 2);
        asm volatile("" ::: "memory");
#pragma unroll
        for (int pc = 0; pc < 4; ++pc) pk[i][pc] = *(const u32x4*)(src + (o2 + (u32)(pc * 8 * (D * 2))));
      }
      SCHED0();
    };
    park(0, 6);
    if (wid == 2) {
      gflag_wait<8>(ws + WS_FUT + ((d0 >> 6) * 64 + lane) * 128, tag);
      lflag_wait(L_FREE, 7, 7);
      glds16x4_dev(lane * 16, ws + WS_UT + d0 * (S * 2), CV_U);
      glds16x4_dev(lane * 16, ws + WS_UT + (d0 + 1) * (S * 2), CV_U + CV_USTR);
      if (lane < 32) {
        glds16(lane * 16, seq_tab + 32, CV_U + 4096);
        glds16(lane * 16, seq_tab + 32, CV_U + CV_USTR + 4096);
      }
      dflag_set(L_CONV + 8, 1);
    }
    if (wid == 3) lflag_set(L_CONV + 12, 1);
    park(6, 8);
    if (wid == 1) {
      lflag_wait(L_FREE, 7, 7);
      const u32 vl = lane * 16;
#pragma unroll 1
      for (int n = 0; n < 8; ++n) {
        const int rr = n & 3;
        const char* src = ssmk + (size_t)(d0 + (n >> 2)) * (S * 2) + (rr ? (rr - 4) * 2 : 0);
        glds16x4(rr && !lane ? 16u : vl, vl, vl, vl, src, CV + (u32)n * CV_HSTR + (rr ? 24 : 32));
        if (rr == 3) dflag_set(L_CONV + (n >> 2) * 4, 1);
      }
    }
    lflag_wait(L_FREE, 7, 7);
    lflag_wait(L_PUB, 3, 1);
#pragma unroll 1
    for (int i = 0; i < 6; ++i) {
      if (i == 3) dflag_set(L_FED + j * 4, 3);
      const char* src = wbase + (size_t)g2_ks(j, i) * 32 * (D * 2);
      glds16x4(o2, o2 + 8 * (D * 2) - 1024, o2 + 16 * (D * 2) - 2048, o2 + 24 * (D * 2) - 3072, src, g2_place(j, i));
    }
    dflag_set(L_FED + j * 4, 6);
    if (wid == 3) {
      const int e = lane < 28 ? lane : 27;
      gflag_wait<4>(ws + WS_FH + (tau * 56 + 4 * (e >> 1) + (e & 1) + 2) * 128, tag);
      dflag_set(L_H, 1);
    }
    __builtin_amdgcn_s_setprio(0);
    WAIT_VM(0);
    const u32 wl = opq(lane * 16 + j * 12288);
#pragma unroll
    for (int i = 0; i < 8; i += 2) {
      lflag_wait(L_PROG + j * 4, 0, i + 2);
#pragma unroll
      for (int n = 0; n < 8; ++n) lds_st128(wl + g2_place(0, (i + (n >> 2)) % 6) + (n & 3) * 1024, pk[i + (n >> 2)][n & 3]);
      lflag_set(L_FED + j * 4, i + 8);
    }
    return;
  }
  const rsrc_t rs = ws_rsrc(ws);

  if (b >= 224) {
    if (wid >= 8 && b != 255) return;
    const int tid = (int)opq(threadIdx.x), lane = tid & 63;
    const int sp = b - 224;
    const u64 seed = gload64(ws + WS_E + opq(0));
    if (tid == 0) touch(WS_FST + sp * 128, ws, L_DUMMY);
    if (wid < 8) {
      const int row = 64 * sp + (tid >> 3), c8 = tid & 7;
      const int cu = sp < 28 ? sp : 27, mat = cu / 14, ct = cu % 14, kg = tid >> 4, c16 = tid & 15;
      const char* wp = (mat ? w3 : w1) + ct * 256;
      const u32 wo = (u32)(kg * (F * 2) + c16 * 16), xo = (u32)(row * (D * 2) + c8 * 16);
      u32x4 xv[7], wv[14];
#pragma unroll
      for (int i = 0; i < 7; ++i) xv[i] = *(const u32x4*)(x + (xo + i * 128));
#pragma unroll
      for (int i = 0; i < 14; ++i) wv[i] = *(const u32x4*)(wp + (wo + i * 32 * (F * 2)));
      SCHED0();
      float s = 0.f;
#pragma unroll
      for (int i = 0; i < 7; ++i)
#pragma unroll
        for (int j = 0; j < 4; ++j) s += bflo(xv[i][j]) + bfhi(xv[i][j]);
      const float m = sum8(s) * (1.0f / D);
      float q = 0.f;
#pragma unroll
      for (int i = 0; i < 7; ++i)
#pragma unroll
        for (int j = 0; j < 4; ++j) {
          const float aa = bflo(xv[i][j]) - m, bb = bfhi(xv[i][j]) - m;
          q = __builtin_fmaf(aa, aa, __builtin_fmaf(bb, bb, q));
        }
      const float sd = rbf(__builtin_amdgcn_sqrtf(rbf(rbf(sum8(q) * (1.0f / D)) + 1e-5f)));
      if (c8 == 0) *(LDSP(f32x2))(u32)(16384 + (tid >> 3) * 8) = f32x2{rbf(m), __builtin_amdgcn_rcpf(sd)};
      SCHED0();
      float a[8] = {};
#pragma unroll
      for (int i = 0; i < 14; ++i)
#pragma unroll
        for (int j = 0; j < 4; ++j) { a[2 * j] += bflo(wv[i][j]); a[2 * j + 1] += bfhi(wv[i][j]); }
      f32x4 t;
#pragma unroll
      for (int j = 0; j < 4; ++j) {
        auto p = __builtin_amdgcn_permlane16_swap(__builtin_bit_cast(u32, a[j]), __builtin_bit_cast(u32, a[j + 4]), false, false);
        t[j] = asf(p[0]) + asf(p[1]);
      }
      *(LDSP(f32x4))(u32)((kg >> 1) * 512 + c16 * 32 + (kg & 1) * 16) = t;
    }
    WAIT_LGKM0(); BAR();
    const u64 tag = tag_of(seed);
    if (wid == 0) {
      const int cu = sp < 28 ? sp : 27;
      f32x2 cs = {};
#pragma unroll
      for (int i = 0; i < 16; ++i) cs += *(LDSP(f32x2))(u32)(i * 512 + lane * 8);
      st64_dev(rs, WS_STAT + (u32)(64 * sp + lane) * 8, *(LDSP(u32x2))(u32)(16384 + lane * 8));
      if (sp < 28) st64_dev(rs, WS_CSUM + (u32)((cu % 14) * 1024 + (cu / 14) * 512 + lane * 8), __builtin_bit_cast(u32x2, cs));
      gflag_publish(ws + WS_FST + sp * 128, tag, lane);
    }
    WAIT_VM(0); BAR();
    if (tid < 392) touch((u32)(sp * 392 + tid) * 128, w2, L_DUMMY);
    if (tid < 448) touch((u32)(sp * 448 + tid) * 128, ssmk, L_DUMMY);
    if (b == 255) {
      __builtin_amdgcn_s_sleep(127);
      gflag_wait<64>(ws + (tid < 480 ? WS_FUT + tid * 128 : WS_FH + ((((tid < 704 ? tid - 480 : 223) / 14) * 2) * 56 + ((tid < 704 ? tid - 480 : 223) % 14) * 4) * 128), tag);
      BAR();
      if (tid == 0) gstore64(ws + WS_E, tag);
    }
    return;
  }

  const int cw = wid - 4, th = cw & 1, fq = cw >> 1;
  {
    const int lane = (int)opq(threadIdx.x) & 63, g = lane >> 4, hh = lane >> 5, l31 = lane & 31;
    const int q4 = (lane >> 2) & 3, p4 = lane & 3;
    const u32 aA = 16384 + (u32)((8 * hh + q4) * 256 + ((fq ^ q4) << 6) + (g & 1) * 32 + p4 * 8);
    const u32 aB = (u32)((64 * th + l31) * 128 + ((hh ^ ((l31 >> 1) & 7)) << 4));
    f32x16 acc[2][2] = {};
    s16x8 fa[2][2][2], fb[2][2][2];
    u32 pa, pb[4];
    auto setslot = [&](u32 slot) {
      pa = opq(aA + slot);
#pragma unroll
      for (int k = 0; k < 4; ++k) pb[k] = opq((aB ^ (u32)(k << 5)) + slot);
    };
    auto rd = [&](int set) {
#pragma unroll
      for (int k = 0; k < 2; ++k) {
        const int ks = 2 * set + k;
        fa[set][k][0] = lds_tr2(pa, ks * 4096, ks * 4096 + 1024);
        fb[set][k][0] = lds_b128(pb[ks], 0);
        fb[set][k][1] = lds_b128(pb[ks], 4096);
        fa[set][k][1] = lds_tr2(pa, 16384 + ks * 4096, 16384 + ks * 4096 + 1024);
      }
    };
    auto mm = [&](int set, int n0, int n1) {
#pragma unroll
      for (int n = n0; n < n1; ++n) acc[(n >> 1) & 1][n & 1] = mfma32(fa[set][n >> 2][(n >> 1) & 1], fb[set][n >> 2][n & 1], acc[(n >> 1) & 1][n & 1]);
    };
    setslot(0);
    const u32 fad = opq(L_FULL + (lane & 1) * 4);
#pragma unroll
    for (int i = 0; i < 4; ++i) asm volatile("" : "+v"(acc[i >> 1][i & 1]));
    __builtin_amdgcn_s_setprio(1);
    lflag_wait(L_FULL, 1, 1);
    rd(0);
    SCHED0();
    u32 slot = 0;
    auto stage = [&](int c, bool bar) {
      rd(1);
      const u32 fl = *(volatile LDSP(u32))fad;
      mm(0, 0, 6);
      SGB(0x008, 1); SGB(0x100, 3); SGB(0x008, 1); SGB(0x100, 3); SGB(0x008, 1); SGB(0x100, 3); SGB(0x008, 1); SGB(0x100, 4); SGB(0x008, 2);
      SCHED0();
      slot = slot == 2 * SLOT ? 0 : slot + SLOT;
      setslot(slot);
      SCHED0();
      mm(0, 6, 7);
      SCHED0();
      __builtin_amdgcn_s_waitcnt(0xC07F);
      const bool miss = c < 6 && __builtin_amdgcn_ballot_w64(fl < (u32)(c + 2));
      SCHED0();
      mm(0, 7, 8);
      SCHED0();
      if (bar) BAR();
      SCHED0();
      mm(1, 0, 2);
      SCHED0();
      if (!bar && c == 6) lflag_set(L_FREE + cw * 4, 7);
      if (miss) lflag_wait(L_FULL, 1, c + 2);
      SCHED0();
      rd(0); mm(1, 2, 8);
#pragma unroll
      for (int i = 0; i < 4; ++i) { SGB(0x008, 1); SGB(0x100, 3); }
      SGB(0x008, 2);
      SCHED0();
    };
#pragma unroll 1
    for (int c = 0; c < 4; ++c) stage(c, true);
#pragma unroll 1
    for (int c = 4; c < 7; ++c) stage(c, false);
    lflag_wait(L_EPI, 0, 1);
    const u32 hdst = WS_H + (u32)((2 * panel + th) * 56 + 4 * ft + fq) * 4096 + lane * 16;
    float nm[2], c1[2], irs2[2];
#pragma unroll
    for (int j = 0; j < 2; ++j) {
      const f32x2 st = *(LDSP(f32x2))(L_STAT + (u32)(64 * th + 32 * j + l31) * 8);
      nm[j] = -st[0]; c1[j] = -1.44269504f * st[1]; irs2[j] = __builtin_amdgcn_rcpf(st[1] * st[1]);
    }
#pragma unroll
    for (int s = 0; s < 2; ++s) {
      f32x4 cs[2][2];
#pragma unroll
      for (int m = 0; m < 2; ++m)
#pragma unroll
        for (int G = 0; G < 2; ++G) cs[m][G] = *(LDSP(f32x4))(L_CSUM + (u32)(m * 128 + 32 * fq + 16 * s + 8 * G + 4 * hh) * 4);
#pragma unroll
      for (int j = 0; j < 2; ++j) {
        float a[8], ex[8], hv[8];
#pragma unroll
        for (int e = 0; e < 8; ++e) a[e] = __builtin_fmaf(nm[j], cs[0][e >> 2][e & 3], acc[0][j][8 * s + e]);
#pragma unroll
        for (int e = 0; e < 8; ++e) ex[e] = a[e] * c1[j];
        SCHED0();
#pragma unroll
        for (int e = 0; e < 8; ++e) { ex[e] = __builtin_amdgcn_exp2f(ex[e]); hv[e] = __builtin_fmaf(nm[j], cs[1][e >> 2][e & 3], acc[1][j][8 * s + e]); }
        SCHED0();
#pragma unroll
        for (int e = 0; e < 8; ++e) ex[e] = __builtin_fmaf(ex[e], irs2[j], irs2[j]);
        SCHED0();
#pragma unroll
        for (int e = 0; e < 8; ++e) { ex[e] = __builtin_amdgcn_rcpf(ex[e]); hv[e] *= a[e]; }
        SCHED0();
#pragma unroll
        for (int e = 0; e < 8; ++e) hv[e] *= ex[e];
        st128_dev(rs, hdst + (s * 2 + j) * 1024, u32x4{pk2(hv[0], hv[1]), pk2(hv[2], hv[3]), pk2(hv[4], hv[5]), pk2(hv[6], hv[7])});
      }
    }
  }
  char* hflag = ws + WS_FH + ((2 * panel + th) * 56 + 4 * ft + fq) * 128;
  u32x4 xv, sv;
  if (cw < 4) {
    const int ht = (int)opq(threadIdx.x) & 255, lane = ht & 63, l15 = ht & 15;
    const u64 tag = tag_of(*(volatile LDSP(u64))L_TAG);
    const int ch = cw >> 1, var = cw & 1;
    gflag_publish(hflag, tag, lane);
    u32x4 xr[2]; u32x2 st[2];
#pragma unroll
    for (int i = 0; i < 2; ++i) {
      const int n = ht + 256 * i, row = n >> 3, c = n & 7;
      xr[i] = *(const u32x4*)(x + (u32)((64 * tau + row) * (D * 2) + (64 * dl + 8 * c) * 2));
      st[i] = ld64_dev(rs, WS_STAT + (u32)(64 * tau + row) * 8);
    }
    SCHED0();
    lflag_wait(L_CONV, 3, 1);
    asm volatile("" ::: "memory");
    conv_compute(lane, cw, d0 + ch, rs);
    u32x4 xn[2];
#pragma unroll
    for (int i = 0; i < 2; ++i) {
      const float mb = asf(st[i][0]), rsd = asf(st[i][1]);
#pragma unroll
      for (int e = 0; e < 4; ++e) {
        const u32 dd = pk2(bflo(xr[i][e]) - mb, bfhi(xr[i][e]) - mb);
        xn[i][e] = pk2(bflo(dd) * rsd, bfhi(dd) * rsd);
      }
    }
    SCHED0();
    gflag_publish(ws + WS_FY + (var * 448 + d0 + ch) * 128, tag, lane);
    xv = xr[0]; sv = xn[0];
    lflag_wait(L_XCH, 1, 1);
    lds_st128(E_S + (u32)ht * 16, xn[1]); lds_st128(E_X + (u32)ht * 16, xr[1]);
    gflag_wait<4>(ws + WS_FY + ((tau >> 4) * 448 + 64 * dl + 16 * cw + l15) * 128, tag);
#pragma unroll
    for (int i = 0; i < 2; ++i)
      glds16_dev((u32)((lane >> 3) * (S * 2) + (((lane & 7) ^ (2 * cw + i)) << 4)),
                 ws + WS_YT + (64 * dl + 16 * cw + 8 * i) * (S * 2) + 128 * tau, xbuf(cw ^ 1) + (u32)i * 1024);
    WAIT_VM(0);
  } else {
    const int j = cw - 4;
    const int lane = (int)opq(threadIdx.x) & 63;
    lflag_set(L_PUB + j * 4, 1);
    const u64 btag = tag_of(*(volatile LDSP(u64))L_TAG);
    gflag_publish(hflag, btag, lane);
    const u32 hl = lane * 16;
    const char* hbase = ws + WS_H + (size_t)tau * 56 * 4096;
    const int q4 = (lane >> 2) & 3;
    const u32 ta = (u32)((4 * (lane >> 5) + q4) * 128 + ((q4 >> 1) << 6) + ((lane >> 4) & 1) * 32 + (lane & 3) * 8);
    s16x8 fa[2][4], fb[3][4];
    f32x16 acc[2][2] = {};
    auto ldh = [&](int i) {
      const char* p = hbase + (size_t)g2_ks(j, i) * 4096;
      asm volatile("global_load_dwordx4 %0, %4, %5 sc1\n\tglobal_load_dwordx4 %1, %4, %5 offset:1024 sc1\n\t"
                   "global_load_dwordx4 %2, %4, %5 offset:2048 sc1\n\tglobal_load_dwordx4 %3, %4, %5 offset:3072 sc1"
                   : "=&v"(fb[i % 3][0]), "=&v"(fb[i % 3][1]), "=&v"(fb[i % 3][2]), "=&v"(fb[i % 3][3])
                   : "v"(hl), "s"(p) : "memory");
    };
    u32 tb[2];
#pragma unroll
    for (int a = 0; a < 2; ++a) tb[a] = opq((ta ^ (u32)(a << 6)) + g2_place(j, 0));
    auto rda = [&](int i) {
#pragma unroll
      for (int a = 0; a < 2; ++a)
        if (i > 0 && i % 3 == 0) tb[a] = opq(i % 6 ? tb[a] - 2 * SLOT : tb[a] + 2 * SLOT);
#pragma unroll
      for (int n = 0; n < 4; ++n) {
        const int o = (i % 3) * 4096 + (n >> 1) * 2048;
        fa[i & 1][n] = lds_tr2(tb[n & 1], o, o + 1024);
      }
    };
    const u32 fed = opq(L_FED + j * 4), prog = opq(L_PROG + j * 4);
    gflag_wait<2>(ws + WS_FH + (tau * 56 + g2_ks(j, lane < 7 ? lane : 6)) * 128, btag);
#pragma unroll
    for (int i = 0; i < 3; ++i) ldh(i);
    lflag_wait(L_FED + j * 4, 0, 3);
    rda(0);
    SCHED0();
    u32 fl = 0;
#pragma unroll
    for (int i = 0; i < 14; ++i) {
      if (i == 2 || (i >= 5 && i < 13 && (i & 1))) {
        if (__builtin_amdgcn_readfirstlane(fl) < i + 2) lflag_wait(L_FED + j * 4, 0, i + 2);
      }
      asm volatile("s_waitcnt vmcnt(%4)" : "+v"(fb[i % 3][0]), "+v"(fb[i % 3][1]), "+v"(fb[i % 3][2]), "+v"(fb[i % 3][3])
                   : "i"(i < 12 ? 8 : (13 - i) * 4) : "memory");
      SCHED0();
      if (i < 13) rda(i + 1);
      if (i == 1 || (i >= 4 && i < 12 && !(i & 1))) fl = *(volatile LDSP(u32))fed;
#pragma unroll
      for (int s = 0; s < 2; ++s)
#pragma unroll
        for (int a = 0; a < 2; ++a)
#pragma unroll
          for (int t = 0; t < 2; ++t) acc[a][t] = mfma32(fa[i & 1][2 * s + a], fb[i % 3][2 * s + t], acc[a][t]);
      if (i < 13) {
#pragma unroll
        for (int n = 0; n < 5; ++n) { SGB(0x008, 1); SGB(0x100, 2); }
        SGB(0x008, 3);
      }
      SCHED0();
      if (i < 8 && (i & 1)) *(volatile LDSP(u32))prog = i + 1;
      if (i == 4) lflag_wait(L_H, 0, 1);
      if (i + 3 < 14) ldh(i + 3);
      SCHED0();
    }
    const int ln = (int)opq(threadIdx.x), lt = ln & 31, lh = (ln >> 5) & 1;
#pragma unroll
    for (int t = 0; t < 2; ++t)
#pragma unroll
      for (int a = 0; a < 2; ++a)
#pragma unroll
        for (int G = 0; G < 4; ++G)
          *(LDSP(f32x4))(g2_place(j, t ? 3 : 0) + (u32)(lt * 256 + (((8 * a + 2 * G + lh) ^ (lt & 7)) << 4))) =
              f32x4{acc[a][t][4 * G], acc[a][t][4 * G + 1], acc[a][t][4 * G + 2], acc[a][t][4 * G + 3]};
  }
  WAIT_LGKM0();
  BAR();
  {
    const int ht = (int)opq(threadIdx.x) & 255;
    const int half = cw >> 2, tk = ht >> 3, dg = ht & 7;
    if (half) { sv = *(LDSP(u32x4))(E_S + (u32)ht * 16); xv = *(LDSP(u32x4))(E_X + (u32)ht * 16); }
    f32x4 s0 = {}, s1 = {};
#pragma unroll
    for (int w = 0; w < 4; ++w) {
      const u32 pb = (half ? 0u : 2 * SLOT) + (u32)w * 12288 + (u32)tk * 256;
      s0 += *(LDSP(f32x4))(pb + (u32)(((2 * dg) ^ (tk & 7)) << 4));
      s1 += *(LDSP(f32x4))(pb + (u32)(((2 * dg + 1) ^ (tk & 7)) << 4));
    }
    const int tok = 32 * half + tk;
    const u32 ya = xbuf((dg >> 1) ^ 1) + (u32)(8 * (dg & 1) * 128 + (((tok >> 3) ^ dg) << 4) + (tok & 7) * 2);
    float o[8];
#pragma unroll
    for (int e = 0; e < 8; ++e) {
      const float f = rbf(e < 4 ? s0[e & 3] : s1[e & 3]);
      const float gt = rbf(silu_f(f));
      const float xe = (e & 1) ? bfhi(xv[e >> 1]) : bflo(xv[e >> 1]);
      const float ne = (e & 1) ? bfhi(sv[e >> 1]) : bflo(sv[e >> 1]);
      const float se = rbf(ne * bflo(*(LDSP(u16))(ya + e * 128)));
      o[e] = xe + rbf(se * gt);
    }
    __builtin_amdgcn_raw_buffer_store_b128(u32x4{pk2(o[0], o[1]), pk2(o[2], o[3]), pk2(o[4], o[5]), pk2(o[6], o[7])},
        __builtin_amdgcn_make_buffer_rsrc(out, 0, S * D * 2, 0x00020000), (64 * tau + 32 * half + tk) * (D * 2) + (64 * dl + 8 * dg) * 2, 0, 16);
  }
}
}
#if defined(__HIP_DEVICE_COMPILE__) && __HIP_DEVICE_COMPILE__
#pragma clang attribute pop
#endif

extern "C" void kernel_launch(void* const* d_in, const int* in_sizes, int n_in, void* d_out, int out_size, void* d_ws,
                              size_t ws_size, hipStream_t stream) {
  (void)hipFuncSetAttribute((const void*)fused_kernel, hipFuncAttributeMaxDynamicSharedMemorySize, 163840);
  fused_kernel<<<256, 768, 163840, stream>>>((const char*)d_in[0], (const char*)d_in[1], (const char*)d_in[2], (const char*)d_in[3],
                                             (const char*)d_in[4], (const char*)d_in[5], (const char*)d_in[7], (const char*)d_in[8],
                                             (const char*)d_in[9], (char*)d_ws, (char*)d_out);
}
```

```cpp
#include <hip/hip_runtime.h>
#include <hip/hip_bf16.h>
#include <stdint.h>
#pragma clang diagnostic ignored "-Wint-to-pointer-cast"

#if defined(__HIP_DEVICE_COMPILE__) && __HIP_DEVICE_COMPILE__
#pragma clang attribute push(__attribute__((target("no-packed-fp32-ops"))), apply_to = function)
#endif
namespace {
typedef unsigned short u16;
typedef unsigned int u32;
typedef unsigned long long u64;
typedef __attribute__((ext_vector_type(4))) short s16x4;
typedef __attribute__((ext_vector_type(8))) short s16x8;
typedef __attribute__((ext_vector_type(8))) __bf16 bf16x8;
typedef __attribute__((ext_vector_type(2))) __bf16 bf16x2;
typedef __attribute__((ext_vector_type(16))) float f32x16;
typedef __attribute__((ext_vector_type(4))) float f32x4;
typedef __attribute__((ext_vector_type(2))) float f32x2;
typedef __attribute__((ext_vector_type(4))) u32 u32x4;
typedef __attribute__((ext_vector_type(2))) u32 u32x2;
#define LDSP(T) __attribute__((address_space(3))) T*
#define GLBP(T) __attribute__((address_space(1))) T*
#define DEV __device__ __forceinline__

constexpr int S = 2048, D = 448, F = 1792;
constexpr u32 WS_H = 0;
constexpr u32 WS_UT = WS_H + 7340032;
constexpr u32 WS_YT = WS_UT + 1835008;
constexpr u32 WS_STAT = WS_YT + 1835008;
constexpr u32 WS_CSUM = WS_STAT + 16384;
constexpr u32 WS_E = WS_CSUM + 16384;
constexpr u32 WS_FUT = WS_E + 128;
constexpr u32 WS_FST = WS_FUT + 448 * 128;
constexpr u32 WS_FY = WS_FST + 32 * 128;
constexpr u32 WS_FH = WS_FY + 896 * 128;
constexpr u32 WS_END = WS_FH + 1792 * 128;
constexpr u32 SLOT = 49152;
constexpr u32 MISC = 3 * SLOT;
constexpr u32 L_FULL = MISC, L_EPI = MISC + 8, L_H = MISC + 16, L_FREE = MISC + 32, L_CONV = MISC + 64, L_FED = MISC + 80,
              L_TAG = MISC + 96, L_DD = MISC + 104, L_PROG = MISC + 128, L_XCH = MISC + 144, L_PUB = MISC + 160, L_STAT = MISC + 256,
              L_CSUM = MISC + 1280, L_USCR = MISC + 4096, L_DUMMY = MISC + 2304, L_XB2 = MISC + 12288;
constexpr u32 CV = SLOT, CV_HSTR = 4160, CV_U = CV + 8 * CV_HSTR, CV_USTR = 4608;
constexpr u32 E_S = SLOT, E_X = SLOT + 4096;
constexpr int SPIN_CAP = 1 << 18;

__device__ const u32 seq_tab[160] __attribute__((aligned(128))) = {0, 1, 2, 3, 4, 5, 6, 7};
DEV float asf(u32 v) { return __builtin_bit_cast(float, v); }
DEV float bflo(u32 v) { return asf(v << 16); }
DEV float bfhi(u32 v) { return asf(v & 0xffff0000u); }
template <int CTRL> DEV float dpp(float v) { return asf(__builtin_amdgcn_update_dpp(0, __builtin_bit_cast(u32, v), CTRL, 0xf, 0xf, true)); }
DEV float sum8(float v) { v += dpp<0xB1>(v); v += dpp<0x4E>(v); return v + dpp<0x141>(v); }
DEV u32 pk2(float lo, float hi) {
  asm("" : "+v"(lo)); asm("" : "+v"(hi));
  bf16x2 r = {(__bf16)lo, (__bf16)hi};
  u32 v = __builtin_bit_cast(u32, r);
  asm("" : "+v"(v));
  return v;
}
DEV float rbf(float f) { bf16x2 r = {(__bf16)f, (__bf16)f}; return bflo(__builtin_bit_cast(u32, r)); }
DEV f32x4 mfma16(s16x8 a, s16x8 b, f32x4 c) {
  return __builtin_amdgcn_mfma_f32_16x16x32_bf16(__builtin_bit_cast(bf16x8, a), __builtin_bit_cast(bf16x8, b), c, 0, 0, 0);
}
DEV f32x16 mfma32(s16x8 a, s16x8 b, f32x16 c) {
  return __builtin_amdgcn_mfma_f32_32x32x16_bf16(__builtin_bit_cast(bf16x8, a), __builtin_bit_cast(bf16x8, b), c, 0, 0, 0);
}
DEV u32 opq(u32 v) { asm volatile("" : "+v"(v)); return v; }
DEV s16x8 lds_tr2(u32 base, int o0, int o1) {
  s16x4 lo = __builtin_amdgcn_ds_read_tr16_b64_v4i16((LDSP(s16x4))((LDSP(char))base + o0));
  s16x4 hi = __builtin_amdgcn_ds_read_tr16_b64_v4i16((LDSP(s16x4))((LDSP(char))base + o1));
  return __builtin_shufflevector(lo, hi, 0, 1, 2, 3, 4, 5, 6, 7);
}
DEV s16x8 lds_b128(u32 base, int o) { return *(LDSP(s16x8))((LDSP(char))base + o); }
DEV void lds_st128(u32 a, u32x4 v) { *(LDSP(u32x4))a = v; }
DEV void glds16x4(u32 v0, u32 v1, u32 v2, u32 v3, const void* sbase, u32 ldst) {
  asm volatile("s_mov_b32 m0, %5\n\ts_nop 0\n\tglobal_load_lds_dwordx4 %0, %4\n\tglobal_load_lds_dwordx4 %1, %4 offset:1024\n\t"
               "global_load_lds_dwordx4 %2, %4 offset:2048\n\tglobal_load_lds_dwordx4 %3, %4 offset:3072"
               ::"v"(v0), "v"(v1), "v"(v2), "v"(v3), "s"(sbase), "s"(ldst) : "memory");
}
DEV void glds16(u32 voff, const void* sbase, u32 ldst) {
  asm volatile("s_mov_b32 m0, %2\n\ts_nop 0\n\tglobal_load_lds_dwordx4 %0, %1" ::"v"(voff), "s"(sbase), "s"(ldst) : "memory");
}
DEV void glds16x4_dev(u32 v, const void* sbase, u32 ldst) {
  asm volatile("s_mov_b32 m0, %2\n\ts_nop 0\n\tglobal_load_lds_dwordx4 %0, %1 sc1\n\tglobal_load_lds_dwordx4 %0, %1 offset:1024 sc1\n\t"
               "global_load_lds_dwordx4 %0, %1 offset:2048 sc1\n\tglobal_load_lds_dwordx4 %0, %1 offset:3072 sc1"
               ::"v"(v), "s"(sbase), "s"(ldst) : "memory");
}
DEV void glds16_dev(u32 voff, const void* sbase, u32 ldst) {
  asm volatile("s_mov_b32 m0, %2\n\ts_nop 0\n\tglobal_load_lds_dwordx4 %0, %1 sc1" ::"v"(voff), "s"(sbase), "s"(ldst) : "memory");
}
DEV void dflag_set(u32 addr, u32 val) {
  asm volatile("s_waitcnt lgkmcnt(0)\n\ts_mov_b32 m0, %2\n\ts_mov_b64 exec, 1\n\tglobal_load_lds_dword %0, %1\n\ts_mov_b64 exec, -1"
               ::"v"(0), "s"(seq_tab + val), "s"(addr) : "memory");
}
#define WAIT_VM(n) asm volatile("s_waitcnt vmcnt(%0)" ::"i"(n) : "memory")
#define WAIT_LGKM0() asm volatile("s_waitcnt lgkmcnt(0)" ::: "memory")
#define BAR() do { asm volatile("" ::: "memory"); __builtin_amdgcn_s_barrier(); asm volatile("" ::: "memory"); } while (0)
#define SCHED0() __builtin_amdgcn_sched_barrier(0)
#define SGB(mask, n) __builtin_amdgcn_sched_group_barrier(mask, n, 0)

DEV void lflag_set(u32 addr, u32 val) {
  asm volatile("s_waitcnt lgkmcnt(0)\n\tds_write_b32 %0, %1" ::"v"(addr), "v"(val) : "memory");
}
DEV void lflag_wait(u32 addr, u32 mask, u32 val) {
  const u32 a = addr + 4 * (threadIdx.x & mask);
#pragma unroll 1
  for (int i = 0; i < SPIN_CAP; ++i) {
    u32 v;
    asm volatile("ds_read_b32 %0, %1\n\ts_waitcnt lgkmcnt(0)" : "=v"(v) : "v"(a) : "memory");
    if (__builtin_amdgcn_ballot_w64(v < val) == 0) break;
    __builtin_amdgcn_s_sleep(1);
  }
}
DEV u64 gload64(const char* p) { return __hip_atomic_load((GLBP(u64))p, __ATOMIC_RELAXED, __HIP_MEMORY_SCOPE_AGENT); }
DEV void gstore64(char* p, u64 v) { __hip_atomic_store((GLBP(u64))p, v, __ATOMIC_RELAXED, __HIP_MEMORY_SCOPE_AGENT); }
template <int SLEEP>
DEV void gflag_wait(const char* lane_ptr, u64 tag) {
  bool ok = false;
#pragma unroll 1
  for (int i = 0; i < SPIN_CAP; ++i) {
    asm volatile("" ::: "memory");
    if (!ok) ok = gload64(lane_ptr) == tag;
    if (__builtin_amdgcn_ballot_w64(!ok) == 0) break;
    __builtin_amdgcn_s_sleep(SLEEP);
  }
  asm volatile("" ::: "memory");
}
DEV void gflag_publish(char* p, u64 tag, int lane) {
  WAIT_VM(0);
  if (lane == 0) gstore64(p, tag);
}
DEV u64 tag_of(u64 seed) { return seed * 0x9E3779B97F4A7C15ull + 0xD1B54A32D192ED03ull; }
DEV u64 make_tag(const char* ws) { return tag_of(gload64(ws + WS_E)); }
typedef __amdgpu_buffer_rsrc_t rsrc_t;
DEV rsrc_t ws_rsrc(char* ws) { return __builtin_amdgcn_make_buffer_rsrc(ws, 0, WS_END, 0x00020000); }
DEV void st128_dev(rsrc_t r, u32 off, u32x4 v) { __builtin_amdgcn_raw_buffer_store_b128(v, r, off, 0, 16); }
DEV void st64_dev(rsrc_t r, u32 off, u32x2 v) { __builtin_amdgcn_raw_buffer_store_b64(v, r, off, 0, 16); }
DEV u32x2 ld64_dev(rsrc_t r, u32 off) { return __builtin_amdgcn_raw_buffer_load_b64(r, off, 0, 16); }
DEV void touch(u32 voff, const void* sbase, u32 ldst) {
  asm volatile("s_mov_b32 m0, %2\n\ts_nop 0\n\tglobal_load_lds_dword %0, %1" ::"v"(voff), "s"(sbase), "s"(ldst) : "memory");
}

DEV void glds4_dev(u32 voff, const void* sbase, u32 ldst) {
  asm volatile("s_mov_b32 m0, %2\n\ts_nop 0\n\tglobal_load_lds_dword %0, %1 sc1" ::"v"(voff), "s"(sbase), "s"(ldst) : "memory");
}

DEV u32 xbuf(int w) { return w < 2 ? L_USCR + (u32)w * 4096 : (w == 2 ? L_XB2 : CV + 45056); }
DEV void conv_compute(int lane, int cw, int d, rsrc_t rs) {
  const int ch = cw >> 1, var = cw & 1;
  const int m = lane & 15, g = lane >> 4, q4 = m >> 2, p4 = lane & 3;
  u32 aa = opq(CV + (u32)(ch * 4 + ((q4 + 1) & 3)) * CV_HSTR + (u32)(((q4 + 1) & 4) + 8 * g + 4 * p4) * 2 + var * 256);
  u32 ba = opq(CV_U + (u32)ch * CV_USTR + (u32)(240 - 16 * m + 8 * g) * 2 + var * 256);
  f32x4 acc[8] = {};
  s16x8 A[8], B[8];
#pragma unroll
  for (int p = 0; p < 8; ++p) { A[7 - p] = lds_tr2(aa, (7 - p) * 512, (7 - p) * 512 + 8); B[p] = lds_b128(ba, (7 - p) * 512); }
  if (var == 0) {
    const u32 a0 = CV + (u32)ch * 4 * CV_HSTR + (u32)(m + 1 + 8 * g) * 2;
#pragma unroll
    for (int j = 0; j < 8; ++j) { const short v = *(LDSP(short))(a0 + j * 2); A[0][j] = m + 8 * g + j >= 15 ? v : (short)0; }
  }
  SCHED0();
#pragma unroll 1
  for (int it = 0; it < 4; ++it) {
#pragma unroll
    for (int p = 7; p >= 0; --p) {
#pragma unroll
      for (int dd = 0; dd <= 7 - p; ++dd) {
        acc[p + dd] = mfma16(A[p], B[dd], acc[p + dd]);
        if (p == 0) { SCHED0(); B[dd] = lds_b128(ba, 64 + (7 - dd) * 512); SCHED0(); }
      }
      SCHED0();
      A[p] = lds_tr2(aa, 64 + p * 512, 64 + p * 512 + 8);
      SCHED0();
    }
    aa = opq(aa + 64); ba = opq(ba + 64);
  }
  f32x4 y[4];
#pragma unroll
  for (int i = 0; i < 4; ++i) {
    *(LDSP(f32x4))(xbuf(cw) + (u32)(i * 1024 + lane * 16)) = var ? acc[i] : acc[4 + i];
    y[i] = var ? acc[4 + i] : acc[i];
  }
  lflag_set(L_XCH + cw * 4, 1);
  const u32 dv = *(LDSP(u32))L_DD;
  const float Dd = ch ? bfhi(dv) : bflo(dv);
  u32x2 uu[4];
#pragma unroll
  for (int i = 0; i < 4; ++i) uu[i] = *(LDSP(u32x2))(CV_U + (u32)ch * CV_USTR + (u32)(2044 - (1024 * var + 256 * i + 16 * m + 4 * g)) * 2);
  u32x2 sk[4];
#pragma unroll
  for (int i = 0; i < 4; ++i) sk[i] = u32x2{pk2(bfhi(uu[i][1]) * Dd, bflo(uu[i][1]) * Dd), pk2(bfhi(uu[i][0]) * Dd, bflo(uu[i][0]) * Dd)};
  const u32 xa = opq(xbuf(cw ^ 1) + (u32)lane * 16), yo = opq(WS_YT + (u32)(d * (S * 2) + (1024 * var + 16 * m + 4 * g) * 2));
  SCHED0();
  lflag_wait(L_XCH + (cw ^ 1) * 4, 0, 1);
  f32x4 pv[4];
#pragma unroll
  for (int i = 0; i < 4; ++i) pv[i] = *(LDSP(f32x4))((LDSP(char))xa + i * 1024);
  SCHED0();
#pragma unroll
  for (int i = 0; i < 4; ++i) {
    const f32x4 s = y[i] + pv[i];
    const u32 c0 = pk2(s[0], s[1]), c1 = pk2(s[2], s[3]);
    st64_dev(rs, yo + i * 512, u32x2{pk2(bflo(c0) + bflo(sk[i][0]), bfhi(c0) + bfhi(sk[i][0])), pk2(bflo(c1) + bflo(sk[i][1]), bfhi(c1) + bfhi(sk[i][1]))});
  }
}

DEV int g2_ks(int j, int i) { const int e = 7 * j + (i < 7 ? i : i - 7); return 4 * (e >> 1) + (e & 1) + (i < 7 ? 0 : 2); }
DEV u32 g2_place(int j, int p) { return (p < 3 ? 2 * SLOT + (u32)p * 4096 : (u32)(p - 3) * 4096) + (u32)j * 12288; }
DEV u32 w2_voff(int lane) { return (u32)((lane >> 3) * (D * 2) + (((lane & 7) ^ (((lane >> 4) & 1) << 2)) << 4)); }

DEV void share(int b, int n, const char* base) {
  const int lane = (int)opq(threadIdx.x) & 63;
  if (lane < n) touch((u32)(b * n + lane) * 128, base, L_DUMMY);
}

__global__ __launch_bounds__(768, 3) void fused_kernel(const char* x, const char* kp, const char* vp, const char* skp,
                                                       const char* ssmk, const char* ssmD, const char* w1, const char* w3,
                                                       const char* w2, char* ws, char* out) {
  const int b = blockIdx.x, xcd = b & 7, r = b >> 3;
  const int wid = __builtin_amdgcn_readfirstlane(threadIdx.x >> 6);
  const int panel = (xcd >> 1) * 4 + r / 7, ft = (xcd & 1) * 7 + r % 7;
  const int tau = xcd * 4 + r / 7, dl = r % 7;
  const int d0 = 2 * b;

  if (b < 224) {
    if (wid >= 4 && wid < 7) share(b, wid == 4 ? 64 : 56, wid == 4 ? x : (wid == 5 ? w1 : w3));
    if (wid == 7) { *(LDSP(u32))(MISC + (threadIdx.x & 63) * 4) = 0; WAIT_LGKM0(); }
    BAR();
    if (wid < 2) {
      const int lane = (int)opq(threadIdx.x) & 63;
      const int g = lane >> 4, l15 = lane & 15;
      u32 vx[8], vw[8];
      const u32 ox = (u32)((lane >> 3) * (D * 2) + (((lane & 7) ^ g) << 4));
      const u32 ow = (u32)(g * (F * 2) + (((l15 >> 2) ^ g) << 6) + ((lane & 3) << 4));
#pragma unroll
      for (int i = 0; i < 8; ++i) {
        vx[i] = (ox ^ (u32)((i & 1) * 64)) + i * 8 * (D * 2) - (i & 3) * 1024;
        vw[i] = ow + i * 4 * (F * 2) - (i & 3) * 1024;
      }
#pragma unroll 1
      for (int c = 0; c < 7; ++c) {
        if (c >= 3) BAR();
        const u32 slot = (u32)(c % 3) * SLOT + wid * 8192;
        const char* xb = x + (size_t)(panel * 128 + wid * 64) * (D * 2) + c * 128;
        const char* w1b = w1 + (size_t)(c * 64 + wid * 32) * (F * 2) + ft * 256;
        const char* w3b = w3 + (size_t)(c * 64 + wid * 32) * (F * 2) + ft * 256;
#pragma unroll
        for (int i = 0; i < 8; i += 4) {
          glds16x4(vx[i], vx[i + 1], vx[i + 2], vx[i + 3], xb, slot + i * 1024);
          glds16x4(vw[i], vw[i + 1], vw[i + 2], vw[i + 3], w1b, slot + 16384 + i * 1024);
          glds16x4(vw[i], vw[i + 1], vw[i + 2], vw[i + 3], w3b, slot + 32768 + i * 1024);
        }
        dflag_set(L_FULL + wid * 4, c + 1);
      }
    }
    if (wid == 7 && (threadIdx.x & 63) < 5) touch((threadIdx.x & 63) * 128, seq_tab, L_DUMMY);
  }

  if (__builtin_expect(wid < 4 && b < 224, 1)) {
    const int lane = (int)opq(threadIdx.x) & 63;
    u64 tag = 0;
    if (wid < 2) {
      if (wid == 0) {
        tag = make_tag(ws);
        const int k = lane & 7;
        gflag_wait<8>(ws + WS_FST + (k < 2 ? 2 * panel + k : (k == 2 ? tau : (k & 1) * 14 + ft)) * 128, tag);
        glds16_dev(lane * 16, ws + WS_STAT + panel * 1024, L_STAT);
        glds16_dev(lane * 16, ws + WS_CSUM + ft * 1024, L_CSUM);
        if (lane < 2) glds4_dev(lane * 4, ws + WS_E, L_TAG);
        if (lane < 1) glds4_dev(0, ssmD + d0 * 2, L_DD);
        dflag_set(L_EPI, 1);
      }
    } else {
      __builtin_amdgcn_s_setprio(3);
      const rsrc_t rs = ws_rsrc(ws);
      const int u = wid - 2;
      const int ti = b / 7, cj = b % 7;
      const int tg = lane >> 3, cg = lane & 7;
      const int t0 = 64 * ti + 32 * u + 4 * tg, ch0 = 64 * cj + 8 * cg;
      u32x4 kr[7], vr[4], sw[4]; u32x2 o[8];
      u64 seed;
      auto ldkv = [&]() {
        const u32 ko = (u32)(t0 * (D * 2) + ch0 * 2);
#pragma unroll
        for (int i = 0; i < 7; ++i) kr[i] = *(const u32x4*)(kp + (i < 3 && t0 == 0 ? ko : ko + (u32)((i - 3) * (D * 2))));
#pragma unroll
        for (int i = 0; i < 4; ++i) vr[i] = *(const u32x4*)(vp + (ko + (u32)(i * (D * 2))));
        SCHED0();
      };
      auto piece = [&](int p) {
#pragma unroll
        for (int c = 2 * p; c < 2 * p + 2; ++c) {
          const u32 wa = sw[p][(c & 1) * 2], wb = sw[p][(c & 1) * 2 + 1];
          const float w0 = bflo(wa), w1f = bfhi(wa), w2f = bflo(wb), w3f = bfhi(wb);
          float kf[7], ks[4];
#pragma unroll
          for (int i = 0; i < 7; ++i) kf[i] = (c & 1) ? bfhi(kr[i][p]) : bflo(kr[i][p]);
#pragma unroll
          for (int i = 0; i < 4; ++i)
            ks[i] = __builtin_fmaf(kf[i + 3], w0, __builtin_fmaf(kf[i + 2], w1f, __builtin_fmaf(kf[i + 1], w2f, kf[i] * w3f)));
#pragma unroll
          for (int i = 0; i < 2; ++i) {
            const int ta = 3 - 2 * i, tb = 2 - 2 * i;
            const u32 kk = pk2(ks[ta], ks[tb]);
            o[c][i] = pk2(bflo(kk) * ((c & 1) ? bfhi(vr[ta][p]) : bflo(vr[ta][p])), bfhi(kk) * ((c & 1) ? bfhi(vr[tb][p]) : bflo(vr[tb][p])));
          }
          asm volatile("" : "+v"(o[c]));
        }
        SCHED0();
      };
      BAR();
      ldkv();
#pragma unroll
      for (int i = 0; i < 4; ++i) sw[i] = *(const u32x4*)(skp + (u32)(ch0 * 8 + i * 16));
      seed = gload64(ws + WS_E + opq(0));
      if (u && lane < 14) {
        const u32 fo = lane < 8 ? WS_FH + (u32)((2 * panel + (lane & 1)) * 56 + 4 * ft + (lane >> 1)) * 128
                                : (lane < 12 ? WS_FY + (u32)((lane & 1) * 448 + d0 + ((lane >> 1) & 1)) * 128 : WS_FUT + (u32)(cj * 64 + 2 * ti + (lane & 1)) * 128);
        touch(fo, ws, L_DUMMY);
      }
      SCHED0();
      BAR();
      SCHED0();
      BAR();
      SCHED0();
#pragma unroll
      for (int i = 0; i < 3; ++i) if (t0 == 0) kr[i] = u32x4{0, 0, 0, 0};
      piece(0);
      BAR();
      SCHED0();
#pragma unroll
      for (int c = 1; c < 4; ++c) piece(c);
      tag = tag_of(seed);
      const u32 scr = L_USCR + (u32)u * 4096;
#pragma unroll
      for (int c = 0; c < 8; ++c) *(LDSP(u32x2))(scr + (u32)((cg * 8 + c) * 64 + (((7 - tg) ^ (cg & 6)) << 3))) = o[c];
      WAIT_LGKM0();
#pragma unroll
      for (int i = 0; i < 4; ++i) {
        const int dd = 16 * i + (lane >> 2), c4 = lane & 3;
        u32x4 v = *(LDSP(u32x4))(scr + (u32)(dd * 64 + ((c4 ^ (dd >> 4)) << 4)));
        st128_dev(rs, WS_UT + (u32)((64 * cj + dd) * (S * 2) + (2016 - 64 * ti - 32 * u) * 2 + c4 * 16), v);
      }
      gflag_publish(ws + WS_FUT + (cj * 64 + 2 * ti + u) * 128, tag, lane);
    }
    const int j = wid;
    const u32 o2 = w2_voff(lane);
    const char* wbase = w2 + dl * 128;
    u32x4 pk[8][4];
    auto park = [&](int i0, int i1) {
#pragma unroll
      for (int i = i0; i < i1; ++i) {
        const char* src = wbase + (size_t)g2_ks(j, i + 6) * 32 * (D * 2);
        asm volatile("" ::: "memory");
#pragma unroll
        for (int pc = 0; pc < 4; ++pc) pk[i][pc] = *(const u32x4*)(src + (o2 + (u32)(pc * 8 * (D * 2))));
      }
      SCHED0();
    };
    park(0, 6);
    if (wid == 2) {
      gflag_wait<8>(ws + WS_FUT + ((d0 >> 6) * 64 + lane) * 128, tag);
      lflag_wait(L_FREE, 7, 7);
      glds16x4_dev(lane * 16, ws + WS_UT + d0 * (S * 2), CV_U);
      glds16x4_dev(lane * 16, ws + WS_UT + (d0 + 1) * (S * 2), CV_U + CV_USTR);
      if (lane < 32) {
        glds16(lane * 16, seq_tab + 32, CV_U + 4096);
        glds16(lane * 16, seq_tab + 32, CV_U + CV_USTR + 4096);
      }
      dflag_set(L_CONV + 8, 1);
    }
    if (wid == 3) lflag_set(L_CONV + 12, 1);
    park(6, 8);
    if (wid == 1) {
      lflag_wait(L_FREE, 7, 7);
      const u32 vl = lane * 16;
#pragma unroll 1
      for (int n = 0; n < 8; ++n) {
        const int rr = n & 3;
        const char* src = ssmk + (size_t)(d0 + (n >> 2)) * (S * 2) + (rr ? (rr - 4) * 2 : 0);
        glds16x4(rr && !lane ? 16u : vl, vl, vl, vl, src, CV + (u32)n * CV_HSTR + (rr ? 24 : 32));
        if (rr == 3) dflag_set(L_CONV + (n >> 2) * 4, 1);
      }
    }
    lflag_wait(L_FREE, 7, 7);
    lflag_wait(L_PUB, 3, 1);
#pragma unroll 1
    for (int i = 0; i < 6; ++i) {
      if (i == 3) dflag_set(L_FED + j * 4, 3);
      const char* src = wbase + (size_t)g2_ks(j, i) * 32 * (D * 2);
      glds16x4(o2, o2 + 8 * (D * 2) - 1024, o2 + 16 * (D * 2) - 2048, o2 + 24 * (D * 2) - 3072, src, g2_place(j, i));
    }
    dflag_set(L_FED + j * 4, 6);
    if (wid == 3) {
      const int e = lane < 28 ? lane : 27;
      gflag_wait<4>(ws + WS_FH + (tau * 56 + 4 * (e >> 1) + (e & 1) + 2) * 128, tag);
      dflag_set(L_H, 1);
    }
    __builtin_amdgcn_s_setprio(0);
    WAIT_VM(0);
    const u32 wl = opq(lane * 16 + j * 12288);
#pragma unroll
    for (int i = 0; i < 8; i += 2) {
      lflag_wait(L_PROG + j * 4, 0, i + 2);
#pragma unroll
      for (int n = 0; n < 8; ++n) lds_st128(wl + g2_place(0, (i + (n >> 2)) % 6) + (n & 3) * 1024, pk[i + (n >> 2)][n & 3]);
      lflag_set(L_FED + j * 4, i + 8);
    }
    return;
  }
  const rsrc_t rs = ws_rsrc(ws);

  if (b >= 224) {
    if (wid >= 8 && b != 255) return;
    const int tid = (int)opq(threadIdx.x), lane = tid & 63;
    const int sp = b - 224;
    const u64 seed = gload64(ws + WS_E + opq(0));
    if (tid == 0) touch(WS_FST + sp * 128, ws, L_DUMMY);
    if (wid < 8) {
      const int row = 64 * sp + (tid >> 3), c8 = tid & 7;
      const int cu = sp < 28 ? sp : 27, mat = cu / 14, ct = cu % 14, kg = tid >> 4, c16 = tid & 15;
      const char* wp = (mat ? w3 : w1) + ct * 256;
      const u32 wo = (u32)(kg * (F * 2) + c16 * 16), xo = (u32)(row * (D * 2) + c8 * 16);
      u32x4 xv[7], wv[14];
#pragma unroll
      for (int i = 0; i < 7; ++i) xv[i] = *(const u32x4*)(x + (xo + i * 128));
#pragma unroll
      for (int i = 0; i < 14; ++i) wv[i] = *(const u32x4*)(wp + (wo + i * 32 * (F * 2)));
      SCHED0();
      float s = 0.f;
#pragma unroll
      for (int i = 0; i < 7; ++i)
#pragma unroll
        for (int j = 0; j < 4; ++j) s += bflo(xv[i][j]) + bfhi(xv[i][j]);
      const float m = sum8(s) * (1.0f / D);
      float q = 0.f;
#pragma unroll
      for (int i = 0; i < 7; ++i)
#pragma unroll
        for (int j = 0; j < 4; ++j) {
          const float aa = bflo(xv[i][j]) - m, bb = bfhi(xv[i][j]) - m;
          q = __builtin_fmaf(aa, aa, __builtin_fmaf(bb, bb, q));
        }
      const float sd = rbf(__builtin_amdgcn_sqrtf(rbf(rbf(sum8(q) * (1.0f / D)) + 1e-5f)));
      if (c8 == 0) *(LDSP(f32x2))(u32)(16384 + (tid >> 3) * 8) = f32x2{rbf(m), __builtin_amdgcn_rcpf(sd)};
      SCHED0();
      float a[8] = {};
#pragma unroll
      for (int i = 0; i < 14; ++i)
#pragma unroll
        for (int j = 0; j < 4; ++j) { a[2 * j] += bflo(wv[i][j]); a[2 * j + 1] += bfhi(wv[i][j]); }
      f32x4 t;
#pragma unroll
      for (int j = 0; j < 4; ++j) {
        auto p = __builtin_amdgcn_permlane16_swap(__builtin_bit_cast(u32, a[j]), __builtin_bit_cast(u32, a[j + 4]), false, false);
        t[j] = asf(p[0]) + asf(p[1]);
      }
      *(LDSP(f32x4))(u32)((kg >> 1) * 512 + c16 * 32 + (kg & 1) * 16) = t;
    }
    WAIT_LGKM0(); BAR();
    const u64 tag = tag_of(seed);
    if (wid == 0) {
      const int cu = sp < 28 ? sp : 27;
      f32x2 cs = {};
#pragma unroll
      for (int i = 0; i < 16; ++i) cs += *(LDSP(f32x2))(u32)(i * 512 + lane * 8);
      st64_dev(rs, WS_STAT + (u32)(64 * sp + lane) * 8, *(LDSP(u32x2))(u32)(16384 + lane * 8));
      if (sp < 28) st64_dev(rs, WS_CSUM + (u32)((cu % 14) * 1024 + (cu / 14) * 512 + lane * 8), __builtin_bit_cast(u32x2, cs));
      gflag_publish(ws + WS_FST + sp * 128, tag, lane);
    }
    WAIT_VM(0); BAR();
    if (tid < 392) touch((u32)(sp * 392 + tid) * 128, w2, L_DUMMY);
    if (tid < 448) touch((u32)(sp * 448 + tid) * 128, ssmk, L_DUMMY);
    if (b == 255) {
      __builtin_amdgcn_s_sleep(127);
      gflag_wait<64>(ws + (tid < 480 ? WS_FUT + tid * 128 : WS_FH + ((((tid < 704 ? tid - 480 : 223) / 14) * 2) * 56 + ((tid < 704 ? tid - 480 : 223) % 14) * 4) * 128), tag);
      BAR();
      if (tid == 0) gstore64(ws + WS_E, tag);
    }
    return;
  }

  const int cw = wid - 4, th = cw & 1, fq = cw >> 1;
  {
    const int lane = (int)opq(threadIdx.x) & 63, g = lane >> 4, hh = lane >> 5, l31 = lane & 31;
    const int q4 = (lane >> 2) & 3, p4 = lane & 3;
    const u32 aA = 16384 + (u32)((8 * hh + q4) * 256 + ((fq ^ q4) << 6) + (g & 1) * 32 + p4 * 8);
    const u32 aB = (u32)((64 * th + l31) * 128 + ((hh ^ ((l31 >> 1) & 7)) << 4));
    f32x16 acc[2][2] = {};
    s16x8 fa[2][2][2], fb[2][2][2];
    u32 pa, pb[4];
    auto setslot = [&](u32 slot) {
      pa = opq(aA + slot);
#pragma unroll
      for (int k = 0; k < 4; ++k) pb[k] = opq((aB ^ (u32)(k << 5)) + slot);
    };
    auto rd = [&](int set) {
#pragma unroll
      for (int k = 0; k < 2; ++k) {
        const int ks = 2 * set + k;
        fa[set][k][0] = lds_tr2(pa, ks * 4096, ks * 4096 + 1024);
        fb[set][k][0] = lds_b128(pb[ks], 0);
        fb[set][k][1] = lds_b128(pb[ks], 4096);
        fa[set][k][1] = lds_tr2(pa, 16384 + ks * 4096, 16384 + ks * 4096 + 1024);
      }
    };
    auto mm = [&](int set, int n0, int n1) {
#pragma unroll
      for (int n = n0; n < n1; ++n) acc[(n >> 1) & 1][n & 1] = mfma32(fa[set][n >> 2][(n >> 1) & 1], fb[set][n >> 2][n & 1], acc[(n >> 1) & 1][n & 1]);
    };
    setslot(0);
    const u32 fad = opq(L_FULL + (lane & 1) * 4);
#pragma unroll
    for (int i = 0; i < 4; ++i) asm volatile("" : "+v"(acc[i >> 1][i & 1]));
    __builtin_amdgcn_s_setprio(1);
    lflag_wait(L_FULL, 1, 1);
    rd(0);
    SCHED0();
    u32 slot = 0;
    auto stage = [&](int c, bool bar) {
      rd(1);
      const u32 fl = *(volatile LDSP(u32))fad;
      mm(0, 0, 6);
      SGB(0x008, 1); SGB(0x100, 3); SGB(0x008, 1); SGB(0x100, 3); SGB(0x008, 1); SGB(0x100, 3); SGB(0x008, 1); SGB(0x100, 4); SGB(0x008, 2);
      SCHED0();
      slot = slot == 2 * SLOT ? 0 : slot + SLOT;
      setslot(slot);
      SCHED0();
      mm(0, 6, 7);
      SCHED0();
      __builtin_amdgcn_s_waitcnt(0xC07F);
      const bool miss = c < 6 && __builtin_amdgcn_ballot_w64(fl < (u32)(c + 2));
      SCHED0();
      mm(0, 7, 8);
      SCHED0();
      if (bar) BAR();
      SCHED0();
      mm(1, 0, 2);
      SCHED0();
      if (!bar && c == 6) lflag_set(L_FREE + cw * 4, 7);
      if (miss) lflag_wait(L_FULL, 1, c + 2);
      SCHED0();
      rd(0); mm(1, 2, 8);
#pragma unroll
      for (int i = 0; i < 4; ++i) { SGB(0x008, 1); SGB(0x100, 3); }
      SGB(0x008, 2);
      SCHED0();
    };
#pragma unroll 1
    for (int c = 0; c < 4; ++c) stage(c, true);
#pragma unroll 1
    for (int c = 4; c < 7; ++c) stage(c, false);
    lflag_wait(L_EPI, 0, 1);
    const u32 hdst = WS_H + (u32)((2 * panel + th) * 56 + 4 * ft + fq) * 4096 + lane * 16;
    float nm[2], c1[2], irs2[2];
#pragma unroll
    for (int j = 0; j < 2; ++j) {
      const f32x2 st = *(LDSP(f32x2))(L_STAT + (u32)(64 * th + 32 * j + l31) * 8);
      nm[j] = -st[0]; c1[j] = -1.44269504f * st[1]; irs2[j] = __builtin_amdgcn_rcpf(st[1] * st[1]);
    }
#pragma unroll
    for (int s = 0; s < 2; ++s) {
      f32x4 cs[2][2];
#pragma unroll
      for (int m = 0; m < 2; ++m)
#pragma unroll
        for (int G = 0; G < 2; ++G) cs[m][G] = *(LDSP(f32x4))(L_CSUM + (u32)(m * 128 + 32 * fq + 16 * s + 8 * G + 4 * hh) * 4);
#pragma unroll
      for (int j = 0; j < 2; ++j) {
        float a[8], ex[8], hv[8];
#pragma unroll
        for (int e = 0; e < 8; ++e) a[e] = __builtin_fmaf(nm[j], cs[0][e >> 2][e & 3], acc[0][j][8 * s + e]);
#pragma unroll
        for (int e = 0; e < 8; ++e) ex[e] = a[e] * c1[j];
        SCHED0();
#pragma unroll
        for (int e = 0; e < 8; ++e) { ex[e] = __builtin_amdgcn_exp2f(ex[e]); hv[e] = __builtin_fmaf(nm[j], cs[1][e >> 2][e & 3], acc[1][j][8 * s + e]); }
        SCHED0();
#pragma unroll
        for (int e = 0; e < 8; ++e) ex[e] = __builtin_fmaf(ex[e], irs2[j], irs2[j]);
        SCHED0();
#pragma unroll
        for (int e = 0; e < 8; ++e) { ex[e] = __builtin_amdgcn_rcpf(ex[e]); hv[e] *= a[e]; }
        SCHED0();
#pragma unroll
        for (int e = 0; e < 8; ++e) hv[e] *= ex[e];
        st128_dev(rs, hdst + (s * 2 + j) * 1024, u32x4{pk2(hv[0], hv[1]), pk2(hv[2], hv[3]), pk2(hv[4], hv[5]), pk2(hv[6], hv[7])});
      }
    }
  }
  char* hflag = ws + WS_FH + ((2 * panel + th) * 56 + 4 * ft + fq) * 128;
  u32x4 xv, sv;
  if (cw < 4) {
    const int ht = (int)opq(threadIdx.x) & 255, lane = ht & 63, l15 = ht & 15;
    const u64 tag = tag_of(*(volatile LDSP(u64))L_TAG);
    const int ch = cw >> 1, var = cw & 1;
    gflag_publish(hflag, tag, lane);
    u32x4 xr[2]; u32x2 st[2];
#pragma unroll
    for (int i = 0; i < 2; ++i) {
      const int n = ht + 256 * i, row = n >> 3, c = n & 7;
      xr[i] = *(const u32x4*)(x + (u32)((64 * tau + row) * (D * 2) + (64 * dl + 8 * c) * 2));
      st[i] = ld64_dev(rs, WS_STAT + (u32)(64 * tau + row) * 8);
    }
    SCHED0();
    lflag_wait(L_CONV, 3, 1);
    asm volatile("" ::: "memory");
    conv_compute(lane, cw, d0 + ch, rs);
    u32x4 xn[2];
#pragma unroll
    for (int i = 0; i < 2; ++i) {
      const float mb = asf(st[i][0]), rsd = asf(st[i][1]);
#pragma unroll
      for (int e = 0; e < 4; ++e) {
        const u32 dd = pk2(bflo(xr[i][e]) - mb, bfhi(xr[i][e]) - mb);
        xn[i][e] = pk2(bflo(dd) * rsd, bfhi(dd) * rsd);
      }
    }
    SCHED0();
    gflag_publish(ws + WS_FY + (var * 448 + d0 + ch) * 128, tag, lane);
    xv = xr[0]; sv = xn[0];
    lflag_wait(L_XCH, 1, 1);
    lds_st128(E_S + (u32)ht * 16, xn[1]); lds_st128(E_X + (u32)ht * 16, xr[1]);
    gflag_wait<4>(ws + WS_FY + ((tau >> 4) * 448 + 64 * dl + 16 * cw + l15) * 128, tag);
#pragma unroll
    for (int i = 0; i < 2; ++i)
      glds16_dev((u32)((lane >> 3) * (S * 2) + (((lane & 7) ^ (2 * cw + i)) << 4)),
                 ws + WS_YT + (64 * dl + 16 * cw + 8 * i) * (S * 2) + 128 * tau, xbuf(cw ^ 1) + (u32)i * 1024);
    WAIT_VM(0);
  } else {
    const int j = cw - 4;
    const int lane = (int)opq(threadIdx.x) & 63;
    lflag_set(L_PUB + j * 4, 1);
    const u64 btag = tag_of(*(volatile LDSP(u64))L_TAG);
    gflag_publish(hflag, btag, lane);
    const u32 hl = lane * 16;
    const char* hbase = ws + WS_H + (size_t)tau * 56 * 4096;
    const int q4 = (lane >> 2) & 3;
    const u32 ta = (u32)((4 * (lane >> 5) + q4) * 128 + ((q4 >> 1) << 6) + ((lane >> 4) & 1) * 32 + (lane & 3) * 8);
    s16x8 fa[2][4], fb[3][4];
    f32x16 acc[2][2] = {};
    auto ldh = [&](int i) {
      const char* p = hbase + (size_t)g2_ks(j, i) * 4096;
      asm volatile("global_load_dwordx4 %0, %4, %5 sc1\n\tglobal_load_dwordx4 %1, %4, %5 offset:1024 sc1\n\t"
                   "global_load_dwordx4 %2, %4, %5 offset:2048 sc1\n\tglobal_load_dwordx4 %3, %4, %5 offset:3072 sc1"
                   : "=&v"(fb[i % 3][0]), "=&v"(fb[i % 3][1]), "=&v"(fb[i % 3][2]), "=&v"(fb[i % 3][3])
                   : "v"(hl), "s"(p) : "memory");
    };
    u32 tb[2];
#pragma unroll
    for (int a = 0; a < 2; ++a) tb[a] = opq((ta ^ (u32)(a << 6)) + g2_place(j, 0));
    auto rda = [&](int i) {
#pragma unroll
      for (int a = 0; a < 2; ++a)
        if (i > 0 && i % 3 == 0) tb[a] = opq(i % 6 ? tb[a] - 2 * SLOT : tb[a] + 2 * SLOT);
#pragma unroll
      for (int n = 0; n < 4; ++n) {
        const int o = (i % 3) * 4096 + (n >> 1) * 2048;
        fa[i & 1][n] = lds_tr2(tb[n & 1], o, o + 1024);
      }
    };
    const u32 fed = opq(L_FED + j * 4), prog = opq(L_PROG + j * 4);
    gflag_wait<2>(ws + WS_FH + (tau * 56 + g2_ks(j, lane < 7 ? lane : 6)) * 128, btag);
#pragma unroll
    for (int i = 0; i < 3; ++i) ldh(i);
    lflag_wait(L_FED + j * 4, 0, 3);
    rda(0);
    SCHED0();
    u32 fl = 0;
#pragma unroll
    for (int i = 0; i < 14; ++i) {
      if (i == 2 || (i >= 5 && i < 13 && (i & 1))) {
        if (__builtin_amdgcn_readfirstlane(fl) < i + 2) lflag_wait(L_FED + j * 4, 0, i + 2);
      }
      asm volatile("s_waitcnt vmcnt(%4)" : "+v"(fb[i % 3][0]), "+v"(fb[i % 3][1]), "+v"(fb[i % 3][2]), "+v"(fb[i % 3][3])
                   : "i"(i < 12 ? 8 : (13 - i) * 4) : "memory");
      SCHED0();
      if (i < 13) rda(i + 1);
      if (i == 1 || (i >= 4 && i < 12 && !(i & 1))) fl = *(volatile LDSP(u32))fed;
      if (i < 13) {
#pragma unroll
        for (int s = 0; s < 2; ++s)
#pragma unroll
          for (int a = 0; a < 2; ++a)
#pragma unroll
            for (int t = 0; t < 2; ++t) acc[a][t] = mfma32(fa[i & 1][2 * s + a], fb[i % 3][2 * s + t], acc[a][t]);
#pragma unroll
        for (int n = 0; n < 5; ++n) { SGB(0x008, 1); SGB(0x100, 2); }
        SGB(0x008, 3);
      } else {
        __builtin_amdgcn_s_waitcnt(0xC07F);
        const int ln = (int)opq(threadIdx.x), lt = ln & 31, lh = (ln >> 5) & 1;
#pragma unroll
        for (int n = 0; n < 5; ++n) {
          if (n < 4) {
#pragma unroll
            for (int s = 0; s < 2; ++s) acc[n & 1][n >> 1] = mfma32(fa[i & 1][2 * s + (n & 1)], fb[i % 3][2 * s + (n >> 1)], acc[n & 1][n >> 1]);
          }
          SCHED0();
          if (n > 0) {
            const int a = (n - 1) & 1, t = (n - 1) >> 1;
#pragma unroll
            for (int G = 0; G < 4; ++G)
              *(LDSP(f32x4))(g2_place(j, t ? 3 : 0) + (u32)(lt * 256 + (((8 * a + 2 * G + lh) ^ (lt & 7)) << 4))) =
                  f32x4{acc[a][t][4 * G], acc[a][t][4 * G + 1], acc[a][t][4 * G + 2], acc[a][t][4 * G + 3]};
          }
          SCHED0();
        }
      }
      SCHED0();
      if (i < 8 && (i & 1)) *(volatile LDSP(u32))prog = i + 1;
      if (i == 4) lflag_wait(L_H, 0, 1);
      if (i + 3 < 14) ldh(i + 3);
      SCHED0();
    }
  }
  {
    const int ht = (int)opq(threadIdx.x) & 255;
    const int half = cw >> 2, tk = ht >> 3, dg = ht & 7, tok = 32 * half + tk;
    const u32 pa0 = opq((half ? 0u : 2 * SLOT) + (u32)(tk * 256 + (((2 * dg) ^ (tk & 7)) << 4))), pa1 = opq(pa0 ^ 16u);
    const u32 ya = opq(xbuf((dg >> 1) ^ 1) + (u32)(8 * (dg & 1) * 128 + (((tok >> 3) ^ dg) << 4) + (tok & 7) * 2));
    const u32 ea = opq(E_S + (u32)ht * 16);
    const u32 oo = opq((u32)((64 * tau + tok) * (D * 2) + (64 * dl + 8 * dg) * 2));
    WAIT_LGKM0();
    BAR();
    f32x4 p0[4], p1[4];
    u32 yv[8];
#pragma unroll
    for (int w = 0; w < 4; ++w) {
      p0[w] = *(LDSP(f32x4))((LDSP(char))pa0 + w * 12288);
      p1[w] = *(LDSP(f32x4))((LDSP(char))pa1 + w * 12288);
    }
#pragma unroll
    for (int e = 0; e < 8; ++e) yv[e] = *(LDSP(u16))((LDSP(char))ya + e * 128);
    SCHED0();
    if (half) { sv = *(LDSP(u32x4))ea; xv = *(LDSP(u32x4))((LDSP(char))ea + (E_X - E_S)); }
    const f32x4 s0 = (p0[0] + p0[1]) + (p0[2] + p0[3]), s1 = (p1[0] + p1[1]) + (p1[2] + p1[3]);
    u32 f[4];
    float ex[8];
#pragma unroll
    for (int e = 0; e < 4; ++e) f[e] = e < 2 ? pk2(s0[2 * e], s0[2 * e + 1]) : pk2(s1[2 * e - 4], s1[2 * e - 3]);
#pragma unroll
    for (int e = 0; e < 8; ++e) ex[e] = __builtin_amdgcn_exp2f(-1.44269504f * ((e & 1) ? bfhi(f[e >> 1]) : bflo(f[e >> 1])));
    SCHED0();
    u32 se[4];
#pragma unroll
    for (int e = 0; e < 4; ++e) se[e] = pk2(bflo(sv[e]) * bflo(yv[2 * e]), bfhi(sv[e]) * bflo(yv[2 * e + 1]));
#pragma unroll
    for (int e = 0; e < 8; ++e) ex[e] = __builtin_amdgcn_rcpf(1.0f + ex[e]);
    SCHED0();
    u32x4 o;
#pragma unroll
    for (int e = 0; e < 4; ++e) {
      const u32 gt = pk2(bflo(f[e]) * ex[2 * e], bfhi(f[e]) * ex[2 * e + 1]);
      const u32 p = pk2(bflo(se[e]) * bflo(gt), bfhi(se[e]) * bfhi(gt));
      o[e] = pk2(bflo(xv[e]) + bflo(p), bfhi(xv[e]) + bfhi(p));
    }
    __builtin_amdgcn_raw_buffer_store_b128(o, __builtin_amdgcn_make_buffer_rsrc(out, 0, S * D * 2, 0x00020000), oo, 0, 16);
  }
}
}
#if defined(__HIP_DEVICE_COMPILE__) && __HIP_DEVICE_COMPILE__
#pragma clang attribute pop
#endif

extern "C" void kernel_launch(void* const* d_in, const int* in_sizes, int n_in, void* d_out, int out_size, void* d_ws,
                              size_t ws_size, hipStream_t stream) {
  (void)hipFuncSetAttribute((const void*)fused_kernel, hipFuncAttributeMaxDynamicSharedMemorySize, 163840);
  fused_kernel<<<256, 768, 163840, stream>>>((const char*)d_in[0], (const char*)d_in[1], (const char*)d_in[2], (const char*)d_in[3],
                                             (const char*)d_in[4], (const char*)d_in[5], (const char*)d_in[7], (const char*)d_in[8],
                                             (const char*)d_in[9], (char*)d_ws, (char*)d_out);
}
```

```cpp
#include <hip/hip_runtime.h>
#include <hip/hip_bf16.h>
#include <stdint.h>
#pragma clang diagnostic ignored "-Wint-to-pointer-cast"

#if defined(__HIP_DEVICE_COMPILE__) && __HIP_DEVICE_COMPILE__
#pragma clang attribute push(__attribute__((target("no-packed-fp32-ops"))), apply_to = function)
#endif
namespace {
typedef unsigned short u16;
typedef unsigned int u32;
typedef unsigned long long u64;
typedef __attribute__((ext_vector_type(4))) short s16x4;
typedef __attribute__((ext_vector_type(8))) short s16x8;
typedef __attribute__((ext_vector_type(8))) __bf16 bf16x8;
typedef __attribute__((ext_vector_type(2))) __bf16 bf16x2;
typedef __attribute__((ext_vector_type(16))) float f32x16;
typedef __attribute__((ext_vector_type(4))) float f32x4;
typedef __attribute__((ext_vector_type(2))) float f32x2;
typedef __attribute__((ext_vector_type(4))) u32 u32x4;
typedef __attribute__((ext_vector_type(2))) u32 u32x2;
#define LDSP(T) __attribute__((address_space(3))) T*
#define GLBP(T) __attribute__((address_space(1))) T*
#define DEV __device__ __forceinline__

constexpr int S = 2048, D = 448, F = 1792;
constexpr u32 WS_H = 0;
constexpr u32 WS_UT = WS_H + 7340032;
constexpr u32 WS_YT = WS_UT + 1835008;
constexpr u32 WS_STAT = WS_YT + 1835008;
constexpr u32 WS_CSUM = WS_STAT + 16384;
constexpr u32 WS_E = WS_CSUM + 16384;
constexpr u32 WS_FUT = WS_E + 128;
constexpr u32 WS_FST = WS_FUT + 448 * 128;
constexpr u32 WS_FY = WS_FST + 32 * 128;
constexpr u32 WS_FH = WS_FY + 896 * 128;
constexpr u32 WS_END = WS_FH + 1792 * 128;
constexpr u32 SLOT = 49152;
constexpr u32 MISC = 3 * SLOT;
constexpr u32 L_FULL = MISC, L_EPI = MISC + 8, L_H = MISC + 16, L_FREE = MISC + 32, L_CONV = MISC + 64, L_FED = MISC + 80,
              L_TAG = MISC + 96, L_DD = MISC + 104, L_PROG = MISC + 128, L_XCH = MISC + 144, L_PUB = MISC + 160, L_STAT = MISC + 256,
              L_CSUM = MISC + 1280, L_USCR = MISC + 4096, L_DUMMY = MISC + 2304, L_XB2 = MISC + 12288;
constexpr u32 CV = SLOT, CV_HSTR = 4160, CV_U = CV + 8 * CV_HSTR, CV_USTR = 4608;
constexpr u32 E_S = SLOT, E_X = SLOT + 4096;
constexpr int SPIN_CAP = 1 << 18;

__device__ const u32 seq_tab[160] __attribute__((aligned(128))) = {0, 1, 2, 3, 4, 5, 6, 7};
DEV float asf(u32 v) { return __builtin_bit_cast(float, v); }
DEV float bflo(u32 v) { return asf(v << 16); }
DEV float bfhi(u32 v) { return asf(v & 0xffff0000u); }
template <int CTRL> DEV float dpp(float v) { return asf(__builtin_amdgcn_update_dpp(0, __builtin_bit_cast(u32, v), CTRL, 0xf, 0xf, true)); }
DEV float sum8(float v) { v += dpp<0xB1>(v); v += dpp<0x4E>(v); return v + dpp<0x141>(v); }
DEV u32 pk2(float lo, float hi) {
  asm("" : "+v"(lo)); asm("" : "+v"(hi));
  bf16x2 r = {(__bf16)lo, (__bf16)hi};
  u32 v = __builtin_bit_cast(u32, r);
  asm("" : "+v"(v));
  return v;
}
DEV float rbf(float f) { bf16x2 r = {(__bf16)f, (__bf16)f}; return bflo(__builtin_bit_cast(u32, r)); }
DEV f32x4 mfma16(s16x8 a, s16x8 b, f32x4 c) {
  return __builtin_amdgcn_mfma_f32_16x16x32_bf16(__builtin_bit_cast(bf16x8, a), __builtin_bit_cast(bf16x8, b), c, 0, 0, 0);
}
DEV f32x16 mfma32(s16x8 a, s16x8 b, f32x16 c) {
  return __builtin_amdgcn_mfma_f32_32x32x16_bf16(__builtin_bit_cast(bf16x8, a), __builtin_bit_cast(bf16x8, b), c, 0, 0, 0);
}
DEV u32 opq(u32 v) { asm volatile("" : "+v"(v)); return v; }
DEV s16x8 lds_tr2(u32 base, int o0, int o1) {
  s16x4 lo = __builtin_amdgcn_ds_read_tr16_b64_v4i16((LDSP(s16x4))((LDSP(char))base + o0));
  s16x4 hi = __builtin_amdgcn_ds_read_tr16_b64_v4i16((LDSP(s16x4))((LDSP(char))base + o1));
  return __builtin_shufflevector(lo, hi, 0, 1, 2, 3, 4, 5, 6, 7);
}
DEV s16x8 lds_b128(u32 base, int o) { return *(LDSP(s16x8))((LDSP(char))base + o); }
DEV void lds_st128(u32 a, u32x4 v) { *(LDSP(u32x4))a = v; }
DEV void glds16x4(u32 v0, u32 v1, u32 v2, u32 v3, const void* sbase, u32 ldst) {
  asm volatile("s_mov_b32 m0, %5\n\ts_nop 0\n\tglobal_load_lds_dwordx4 %0, %4\n\tglobal_load_lds_dwordx4 %1, %4 offset:1024\n\t"
               "global_load_lds_dwordx4 %2, %4 offset:2048\n\tglobal_load_lds_dwordx4 %3, %4 offset:3072"
               ::"v"(v0), "v"(v1), "v"(v2), "v"(v3), "s"(sbase), "s"(ldst) : "memory");
}
DEV void glds16(u32 voff, const void* sbase, u32 ldst) {
  asm volatile("s_mov_b32 m0, %2\n\ts_nop 0\n\tglobal_load_lds_dwordx4 %0, %1" ::"v"(voff), "s"(sbase), "s"(ldst) : "memory");
}
DEV void glds16x4_dev(u32 v, const void* sbase, u32 ldst) {
  asm volatile("s_mov_b32 m0, %2\n\ts_nop 0\n\tglobal_load_lds_dwordx4 %0, %1 sc1\n\tglobal_load_lds_dwordx4 %0, %1 offset:1024 sc1\n\t"
               "global_load_lds_dwordx4 %0, %1 offset:2048 sc1\n\tglobal_load_lds_dwordx4 %0, %1 offset:3072 sc1"
               ::"v"(v), "s"(sbase), "s"(ldst) : "memory");
}
DEV void glds16_dev(u32 voff, const void* sbase, u32 ldst) {
  asm volatile("s_mov_b32 m0, %2\n\ts_nop 0\n\tglobal_load_lds_dwordx4 %0, %1 sc1" ::"v"(voff), "s"(sbase), "s"(ldst) : "memory");
}
DEV void dflag_set(u32 addr, u32 val) {
  asm volatile("s_waitcnt lgkmcnt(0)\n\ts_mov_b32 m0, %2\n\ts_mov_b64 exec, 1\n\tglobal_load_lds_dword %0, %1\n\ts_mov_b64 exec, -1"
               ::"v"(0), "s"(seq_tab + val), "s"(addr) : "memory");
}
#define WAIT_VM(n) asm volatile("s_waitcnt vmcnt(%0)" ::"i"(n) : "memory")
#define WAIT_LGKM0() asm volatile("s_waitcnt lgkmcnt(0)" ::: "memory")
#define BAR() do { asm volatile("" ::: "memory"); __builtin_amdgcn_s_barrier(); asm volatile("" ::: "memory"); } while (0)
#define SCHED0() __builtin_amdgcn_sched_barrier(0)
#define SGB(mask, n) __builtin_amdgcn_sched_group_barrier(mask, n, 0)

DEV void lflag_set(u32 addr, u32 val) {
  asm volatile("s_waitcnt lgkmcnt(0)\n\tds_write_b32 %0, %1" ::"v"(addr), "v"(val) : "memory");
}
DEV void lflag_wait(u32 addr, u32 mask, u32 val) {
  const u32 a = addr + 4 * (threadIdx.x & mask);
#pragma unroll 1
  for (int i = 0; i < SPIN_CAP; ++i) {
    u32 v;
    asm volatile("ds_read_b32 %0, %1\n\ts_waitcnt lgkmcnt(0)" : "=v"(v) : "v"(a) : "memory");
    if (__builtin_amdgcn_ballot_w64(v < val) == 0) break;
    __builtin_amdgcn_s_sleep(1);
  }
}
DEV u64 gload64(const char* p) { return __hip_atomic_load((GLBP(u64))p, __ATOMIC_RELAXED, __HIP_MEMORY_SCOPE_AGENT); }
DEV void gstore64(char* p, u64 v) { __hip_atomic_store((GLBP(u64))p, v, __ATOMIC_RELAXED, __HIP_MEMORY_SCOPE_AGENT); }
template <int SLEEP>
DEV void gflag_wait(const char* lane_ptr, u64 tag) {
  bool ok = false;
#pragma unroll 1
  for (int i = 0; i < SPIN_CAP; ++i) {
    asm volatile("" ::: "memory");
    if (!ok) ok = gload64(lane_ptr) == tag;
    if (__builtin_amdgcn_ballot_w64(!ok) == 0) break;
    __builtin_amdgcn_s_sleep(SLEEP);
  }
  asm volatile("" ::: "memory");
}
DEV void gflag_publish(char* p, u64 tag, int lane) {
  WAIT_VM(0);
  if (lane == 0) gstore64(p, tag);
}
DEV u64 tag_of(u64 seed) { return seed * 0x9E3779B97F4A7C15ull + 0xD1B54A32D192ED03ull; }
DEV u64 make_tag(const char* ws) { return tag_of(gload64(ws + WS_E)); }
typedef __amdgpu_buffer_rsrc_t rsrc_t;
DEV rsrc_t ws_rsrc(char* ws) { return __builtin_amdgcn_make_buffer_rsrc(ws, 0, WS_END, 0x00020000); }
DEV void st128_dev(rsrc_t r, u32 off, u32x4 v) { __builtin_amdgcn_raw_buffer_store_b128(v, r, off, 0, 16); }
DEV void st64_dev(rsrc_t r, u32 off, u32x2 v) { __builtin_amdgcn_raw_buffer_store_b64(v, r, off, 0, 16); }
DEV u32x2 ld64_dev(rsrc_t r, u32 off) { return __builtin_amdgcn_raw_buffer_load_b64(r, off, 0, 16); }
DEV void touch(u32 voff, const void* sbase, u32 ldst) {
  asm volatile("s_mov_b32 m0, %2\n\ts_nop 0\n\tglobal_load_lds_dword %0, %1" ::"v"(voff), "s"(sbase), "s"(ldst) : "memory");
}

DEV void glds4_dev(u32 voff, const void* sbase, u32 ldst) {
  asm volatile("s_mov_b32 m0, %2\n\ts_nop 0\n\tglobal_load_lds_dword %0, %1 sc1" ::"v"(voff), "s"(sbase), "s"(ldst) : "memory");
}

DEV u32 xbuf(int w) { return w < 2 ? L_USCR + (u32)w * 4096 : (w == 2 ? L_XB2 : CV + 45056); }
DEV void conv_compute(int lane, int cw, int d, rsrc_t rs) {
  const int ch = cw >> 1, var = cw & 1;
  const int m = lane & 15, g = lane >> 4, q4 = m >> 2, p4 = lane & 3;
  u32 aa = opq(CV + (u32)(ch * 4 + ((q4 + 1) & 3)) * CV_HSTR + (u32)(((q4 + 1) & 4) + 8 * g + 4 * p4) * 2 + var * 256);
  u32 ba = opq(CV_U + (u32)ch * CV_USTR + (u32)(240 - 16 * m + 8 * g) * 2 + var * 256);
  f32x4 acc[8] = {};
  s16x8 A[8], B[8];
  const u32 a0 = opq(CV + (u32)ch * 4 * CV_HSTR + (u32)(m + 1 + 8 * g) * 2);
  u32x4 msk;
#pragma unroll
  for (int i = 0; i < 4; ++i) { const int e = m + 8 * g + 2 * i - 15; msk[i] = e >= 0 ? ~0u : (e == -1 ? 0xffff0000u : 0u); }
#pragma unroll
  for (int i = 0; i < 8; ++i) asm volatile("" : "+v"(acc[i]));
  asm volatile("" : "+v"(msk));
  lflag_wait(L_CONV, 3, 1);
  u32 h0[8];
#pragma unroll
  for (int j = 0; j < 8; ++j) h0[j] = *(LDSP(u16))((LDSP(char))a0 + j * 2);
  SCHED0();
#pragma unroll
  for (int p = 0; p < 8; ++p) { A[7 - p] = lds_tr2(aa, (7 - p) * 512, (7 - p) * 512 + 8); B[p] = lds_b128(ba, (7 - p) * 512); }
  SCHED0();
  if (var == 0) {
    u32x4 f;
#pragma unroll
    for (int i = 0; i < 4; ++i) f[i] = (h0[2 * i] | (h0[2 * i + 1] << 16)) & msk[i];
    A[0] = __builtin_bit_cast(s16x8, f);
  }
  SCHED0();
#pragma unroll 1
  for (int it = 0; it < 4; ++it) {
#pragma unroll
    for (int p = 7; p >= 0; --p) {
#pragma unroll
      for (int dd = 0; dd <= 7 - p; ++dd) {
        acc[p + dd] = mfma16(A[p], B[dd], acc[p + dd]);
        if (p == 0) { SCHED0(); B[dd] = lds_b128(ba, 64 + (7 - dd) * 512); SCHED0(); }
      }
      SCHED0();
      A[p] = lds_tr2(aa, 64 + p * 512, 64 + p * 512 + 8);
      SCHED0();
    }
    aa = opq(aa + 64); ba = opq(ba + 64);
  }
  f32x4 y[4];
#pragma unroll
  for (int i = 0; i < 4; ++i) {
    *(LDSP(f32x4))(xbuf(cw) + (u32)(i * 1024 + lane * 16)) = var ? acc[i] : acc[4 + i];
    y[i] = var ? acc[4 + i] : acc[i];
  }
  lflag_set(L_XCH + cw * 4, 1);
  const u32 dv = *(LDSP(u32))L_DD;
  const float Dd = ch ? bfhi(dv) : bflo(dv);
  u32x2 uu[4];
#pragma unroll
  for (int i = 0; i < 4; ++i) uu[i] = *(LDSP(u32x2))(CV_U + (u32)ch * CV_USTR + (u32)(2044 - (1024 * var + 256 * i + 16 * m + 4 * g)) * 2);
  u32x2 sk[4];
#pragma unroll
  for (int i = 0; i < 4; ++i) sk[i] = u32x2{pk2(bfhi(uu[i][1]) * Dd, bflo(uu[i][1]) * Dd), pk2(bfhi(uu[i][0]) * Dd, bflo(uu[i][0]) * Dd)};
  const u32 xa = opq(xbuf(cw ^ 1) + (u32)lane * 16), yo = opq(WS_YT + (u32)(d * (S * 2) + (1024 * var + 16 * m + 4 * g) * 2));
  SCHED0();
  lflag_wait(L_XCH + (cw ^ 1) * 4, 0, 1);
  f32x4 pv[4];
#pragma unroll
  for (int i = 0; i < 4; ++i) pv[i] = *(LDSP(f32x4))((LDSP(char))xa + i * 1024);
  SCHED0();
#pragma unroll
  for (int i = 0; i < 4; ++i) {
    const f32x4 s = y[i] + pv[i];
    const u32 c0 = pk2(s[0], s[1]), c1 = pk2(s[2], s[3]);
    st64_dev(rs, yo + i * 512, u32x2{pk2(bflo(c0) + bflo(sk[i][0]), bfhi(c0) + bfhi(sk[i][0])), pk2(bflo(c1) + bflo(sk[i][1]), bfhi(c1) + bfhi(sk[i][1]))});
  }
}

DEV int g2_ks(int j, int i) { const int e = 7 * j + (i < 7 ? i : i - 7); return 4 * (e >> 1) + (e & 1) + (i < 7 ? 0 : 2); }
DEV u32 g2_place(int j, int p) { return (p < 3 ? 2 * SLOT + (u32)p * 4096 : (u32)(p - 3) * 4096) + (u32)j * 12288; }
DEV u32 w2_voff(int lane) { return (u32)((lane >> 3) * (D * 2) + (((lane & 7) ^ (((lane >> 4) & 1) << 2)) << 4)); }

DEV void share(int b, int n, const char* base) {
  const int lane = (int)opq(threadIdx.x) & 63;
  if (lane < n) touch((u32)(b * n + lane) * 128, base, L_DUMMY);
}

__global__ __launch_bounds__(768, 3) void fused_kernel(const char* x, const char* kp, const char* vp, const char* skp,
                                                       const char* ssmk, const char* ssmD, const char* w1, const char* w3,
                                                       const char* w2, char* ws, char* out) {
  const int b = blockIdx.x, xcd = b & 7, r = b >> 3;
  const int wid = __builtin_amdgcn_readfirstlane(threadIdx.x >> 6);
  const int panel = (xcd >> 1) * 4 + r / 7, ft = (xcd & 1) * 7 + r % 7;
  const int tau = xcd * 4 + r / 7, dl = r % 7;
  const int d0 = 2 * b;

  if (b < 224) {
    if (wid >= 4 && wid < 7) share(b, wid == 4 ? 64 : 56, wid == 4 ? x : (wid == 5 ? w1 : w3));
    if (wid == 7) { *(LDSP(u32))(MISC + (threadIdx.x & 63) * 4) = 0; WAIT_LGKM0(); }
    BAR();
    if (wid < 2) {
      const int lane = (int)opq(threadIdx.x) & 63;
      const int g = lane >> 4, l15 = lane & 15;
      u32 vx[8], vw[8];
      const u32 ox = (u32)((lane >> 3) * (D * 2) + (((lane & 7) ^ g) << 4));
      const u32 ow = (u32)(g * (F * 2) + (((l15 >> 2) ^ g) << 6) + ((lane & 3) << 4));
#pragma unroll
      for (int i = 0; i < 8; ++i) {
        vx[i] = (ox ^ (u32)((i & 1) * 64)) + i * 8 * (D * 2) - (i & 3) * 1024;
        vw[i] = ow + i * 4 * (F * 2) - (i & 3) * 1024;
      }
#pragma unroll 1
      for (int c = 0; c < 7; ++c) {
        if (c >= 3) BAR();
        const u32 slot = (u32)(c % 3) * SLOT + wid * 8192;
        const char* xb = x + (size_t)(panel * 128 + wid * 64) * (D * 2) + c * 128;
        const char* w1b = w1 + (size_t)(c * 64 + wid * 32) * (F * 2) + ft * 256;
        const char* w3b = w3 + (size_t)(c * 64 + wid * 32) * (F * 2) + ft * 256;
#pragma unroll
        for (int i = 0; i < 8; i += 4) {
          glds16x4(vx[i], vx[i + 1], vx[i + 2], vx[i + 3], xb, slot + i * 1024);
          glds16x4(vw[i], vw[i + 1], vw[i + 2], vw[i + 3], w1b, slot + 16384 + i * 1024);
          glds16x4(vw[i], vw[i + 1], vw[i + 2], vw[i + 3], w3b, slot + 32768 + i * 1024);
        }
        dflag_set(L_FULL + wid * 4, c + 1);
      }
    }
    if (wid == 7 && (threadIdx.x & 63) < 5) touch((threadIdx.x & 63) * 128, seq_tab, L_DUMMY);
  }

  if (__builtin_expect(wid < 4 && b < 224, 1)) {
    const int lane = (int)opq(threadIdx.x) & 63;
    u64 tag = 0;
    if (wid < 2) {
      if (wid == 0) {
        tag = make_tag(ws);
        const int k = lane & 7;
        gflag_wait<8>(ws + WS_FST + (k < 2 ? 2 * panel + k : (k == 2 ? tau : (k & 1) * 14 + ft)) * 128, tag);
        glds16_dev(lane * 16, ws + WS_STAT + panel * 1024, L_STAT);
        glds16_dev(lane * 16, ws + WS_CSUM + ft * 1024, L_CSUM);
        if (lane < 2) glds4_dev(lane * 4, ws + WS_E, L_TAG);
        if (lane < 1) glds4_dev(0, ssmD + d0 * 2, L_DD);
        dflag_set(L_EPI, 1);
      }
    } else {
      __builtin_amdgcn_s_setprio(3);
      const rsrc_t rs = ws_rsrc(ws);
      const int u = wid - 2;
      const int ti = b / 7, cj = b % 7;
      const int tg = lane >> 3, cg = lane & 7;
      const int t0 = 64 * ti + 32 * u + 4 * tg, ch0 = 64 * cj + 8 * cg;
      u32x4 kr[7], vr[4], sw[4]; u32x2 o[8];
      u64 seed;
      auto ldkv = [&]() {
        const u32 ko = (u32)(t0 * (D * 2) + ch0 * 2);
#pragma unroll
        for (int i = 0; i < 7; ++i) kr[i] = *(const u32x4*)(kp + (i < 3 && t0 == 0 ? ko : ko + (u32)((i - 3) * (D * 2))));
#pragma unroll
        for (int i = 0; i < 4; ++i) vr[i] = *(const u32x4*)(vp + (ko + (u32)(i * (D * 2))));
        SCHED0();
      };
      auto piece = [&](int p) {
#pragma unroll
        for (int c = 2 * p; c < 2 * p + 2; ++c) {
          const u32 wa = sw[p][(c & 1) * 2], wb = sw[p][(c & 1) * 2 + 1];
          const float w0 = bflo(wa), w1f = bfhi(wa), w2f = bflo(wb), w3f = bfhi(wb);
          float kf[7], ks[4];
#pragma unroll
          for (int i = 0; i < 7; ++i) kf[i] = (c & 1) ? bfhi(kr[i][p]) : bflo(kr[i][p]);
#pragma unroll
          for (int i = 0; i < 4; ++i)
            ks[i] = __builtin_fmaf(kf[i + 3], w0, __builtin_fmaf(kf[i + 2], w1f, __builtin_fmaf(kf[i + 1], w2f, kf[i] * w3f)));
#pragma unroll
          for (int i = 0; i < 2; ++i) {
            const int ta = 3 - 2 * i, tb = 2 - 2 * i;
            const u32 kk = pk2(ks[ta], ks[tb]);
            o[c][i] = pk2(bflo(kk) * ((c & 1) ? bfhi(vr[ta][p]) : bflo(vr[ta][p])), bfhi(kk) * ((c & 1) ? bfhi(vr[tb][p]) : bflo(vr[tb][p])));
          }
          asm volatile("" : "+v"(o[c]));
        }
        SCHED0();
      };
      BAR();
      ldkv();
#pragma unroll
      for (int i = 0; i < 4; ++i) sw[i] = *(const u32x4*)(skp + (u32)(ch0 * 8 + i * 16));
      seed = gload64(ws + WS_E + opq(0));
      if (u && lane < 14) {
        const u32 fo = lane < 8 ? WS_FH + (u32)((2 * panel + (lane & 1)) * 56 + 4 * ft + (lane >> 1)) * 128
                                : (lane < 12 ? WS_FY + (u32)((lane & 1) * 448 + d0 + ((lane >> 1) & 1)) * 128 : WS_FUT + (u32)(cj * 64 + 2 * ti + (lane & 1)) * 128);
        touch(fo, ws, L_DUMMY);
      }
      SCHED0();
      BAR();
      SCHED0();
      BAR();
      SCHED0();
#pragma unroll
      for (int i = 0; i < 3; ++i) if (t0 == 0) kr[i] = u32x4{0, 0, 0, 0};
      piece(0);
      BAR();
      SCHED0();
#pragma unroll
      for (int c = 1; c < 4; ++c) piece(c);
      tag = tag_of(seed);
      const u32 scr = L_USCR + (u32)u * 4096;
#pragma unroll
      for (int c = 0; c < 8; ++c) *(LDSP(u32x2))(scr + (u32)((cg * 8 + c) * 64 + (((7 - tg) ^ (cg & 6)) << 3))) = o[c];
      WAIT_LGKM0();
#pragma unroll
      for (int i = 0; i < 4; ++i) {
        const int dd = 16 * i + (lane >> 2), c4 = lane & 3;
        u32x4 v = *(LDSP(u32x4))(scr + (u32)(dd * 64 + ((c4 ^ (dd >> 4)) << 4)));
        st128_dev(rs, WS_UT + (u32)((64 * cj + dd) * (S * 2) + (2016 - 64 * ti - 32 * u) * 2 + c4 * 16), v);
      }
      gflag_publish(ws + WS_FUT + (cj * 64 + 2 * ti + u) * 128, tag, lane);
    }
    const int j = wid;
    const u32 o2 = w2_voff(lane);
    const char* wbase = w2 + dl * 128;
    u32x4 pk[8][4];
    auto park = [&](int i0, int i1) {
#pragma unroll
      for (int i = i0; i < i1; ++i) {
        const char* src = wbase + (size_t)g2_ks(j, i + 6) * 32 * (D * 2);
        asm volatile("" ::: "memory");
#pragma unroll
        for (int pc = 0; pc < 4; ++pc) pk[i][pc] = *(const u32x4*)(src + (o2 + (u32)(pc * 8 * (D * 2))));
      }
      SCHED0();
    };
    park(0, 6);
    if (wid == 2) {
      gflag_wait<8>(ws + WS_FUT + ((d0 >> 6) * 64 + lane) * 128, tag);
      lflag_wait(L_FREE, 7, 7);
      glds16x4_dev(lane * 16, ws + WS_UT + d0 * (S * 2), CV_U);
      glds16x4_dev(lane * 16, ws + WS_UT + (d0 + 1) * (S * 2), CV_U + CV_USTR);
      if (lane < 32) {
        glds16(lane * 16, seq_tab + 32, CV_U + 4096);
        glds16(lane * 16, seq_tab + 32, CV_U + CV_USTR + 4096);
      }
      dflag_set(L_CONV + 8, 1);
    }
    if (wid == 3) lflag_set(L_CONV + 12, 1);
    park(6, 8);
    if (wid == 1) {
      lflag_wait(L_FREE, 7, 7);
      const u32 vl = lane * 16;
#pragma unroll 1
      for (int n = 0; n < 8; ++n) {
        const int rr = n & 3;
        const char* src = ssmk + (size_t)(d0 + (n >> 2)) * (S * 2) + (rr ? (rr - 4) * 2 : 0);
        glds16x4(rr && !lane ? 16u : vl, vl, vl, vl, src, CV + (u32)n * CV_HSTR + (rr ? 24 : 32));
        if (rr == 3) dflag_set(L_CONV + (n >> 2) * 4, 1);
      }
    }
    lflag_wait(L_FREE, 7, 7);
    lflag_wait(L_PUB, 3, 1);
#pragma unroll 1
    for (int i = 0; i < 6; ++i) {
      if (i == 3) dflag_set(L_FED + j * 4, 3);
      const char* src = wbase + (size_t)g2_ks(j, i) * 32 * (D * 2);
      glds16x4(o2, o2 + 8 * (D * 2) - 1024, o2 + 16 * (D * 2) - 2048, o2 + 24 * (D * 2) - 3072, src, g2_place(j, i));
    }
    dflag_set(L_FED + j * 4, 6);
    if (wid == 3) {
      const int e = lane < 28 ? lane : 27;
      gflag_wait<4>(ws + WS_FH + (tau * 56 + 4 * (e >> 1) + (e & 1) + 2) * 128, tag);
      dflag_set(L_H, 1);
    }
    __builtin_amdgcn_s_setprio(0);
    WAIT_VM(0);
    const u32 wl = opq(lane * 16 + j * 12288);
#pragma unroll
    for (int i = 0; i < 8; i += 2) {
      lflag_wait(L_PROG + j * 4, 0, i + 2);
#pragma unroll
      for (int n = 0; n < 8; ++n) lds_st128(wl + g2_place(0, (i + (n >> 2)) % 6) + (n & 3) * 1024, pk[i + (n >> 2)][n & 3]);
      lflag_set(L_FED + j * 4, i + 8);
    }
    return;
  }
  const rsrc_t rs = ws_rsrc(ws);

  if (b >= 224) {
    if (wid >= 8 && b != 255) return;
    const int tid = (int)opq(threadIdx.x), lane = tid & 63;
    const int sp = b - 224;
    const u64 seed = gload64(ws + WS_E + opq(0));
    if (tid == 0) touch(WS_FST + sp * 128, ws, L_DUMMY);
    if (wid < 8) {
      const int row = 64 * sp + (tid >> 3), c8 = tid & 7;
      const int cu = sp < 28 ? sp : 27, mat = cu / 14, ct = cu % 14, kg = tid >> 4, c16 = tid & 15;
      const char* wp = (mat ? w3 : w1) + ct * 256;
      const u32 wo = (u32)(kg * (F * 2) + c16 * 16), xo = (u32)(row * (D * 2) + c8 * 16);
      u32x4 xv[7], wv[14];
#pragma unroll
      for (int i = 0; i < 7; ++i) xv[i] = *(const u32x4*)(x + (xo + i * 128));
#pragma unroll
      for (int i = 0; i < 14; ++i) wv[i] = *(const u32x4*)(wp + (wo + i * 32 * (F * 2)));
      SCHED0();
      float s = 0.f;
#pragma unroll
      for (int i = 0; i < 7; ++i)
#pragma unroll
        for (int j = 0; j < 4; ++j) s += bflo(xv[i][j]) + bfhi(xv[i][j]);
      const float m = sum8(s) * (1.0f / D);
      float q = 0.f;
#pragma unroll
      for (int i = 0; i < 7; ++i)
#pragma unroll
        for (int j = 0; j < 4; ++j) {
          const float aa = bflo(xv[i][j]) - m, bb = bfhi(xv[i][j]) - m;
          q = __builtin_fmaf(aa, aa, __builtin_fmaf(bb, bb, q));
        }
      const float sd = rbf(__builtin_amdgcn_sqrtf(rbf(rbf(sum8(q) * (1.0f / D)) + 1e-5f)));
      if (c8 == 0) *(LDSP(f32x2))(u32)(16384 + (tid >> 3) * 8) = f32x2{rbf(m), __builtin_amdgcn_rcpf(sd)};
      SCHED0();
      float a[8] = {};
#pragma unroll
      for (int i = 0; i < 14; ++i)
#pragma unroll
        for (int j = 0; j < 4; ++j) { a[2 * j] += bflo(wv[i][j]); a[2 * j + 1] += bfhi(wv[i][j]); }
      f32x4 t;
#pragma unroll
      for (int j = 0; j < 4; ++j) {
        auto p = __builtin_amdgcn_permlane16_swap(__builtin_bit_cast(u32, a[j]), __builtin_bit_cast(u32, a[j + 4]), false, false);
        t[j] = asf(p[0]) + asf(p[1]);
      }
      *(LDSP(f32x4))(u32)((kg >> 1) * 512 + c16 * 32 + (kg & 1) * 16) = t;
    }
    WAIT_LGKM0(); BAR();
    const u64 tag = tag_of(seed);
    if (wid == 0) {
      const int cu = sp < 28 ? sp : 27;
      f32x2 cs = {};
#pragma unroll
      for (int i = 0; i < 16; ++i) cs += *(LDSP(f32x2))(u32)(i * 512 + lane * 8);
      st64_dev(rs, WS_STAT + (u32)(64 * sp + lane) * 8, *(LDSP(u32x2))(u32)(16384 + lane * 8));
      if (sp < 28) st64_dev(rs, WS_CSUM + (u32)((cu % 14) * 1024 + (cu / 14) * 512 + lane * 8), __builtin_bit_cast(u32x2, cs));
      gflag_publish(ws + WS_FST + sp * 128, tag, lane);
    }
    WAIT_VM(0); BAR();
    if (tid < 392) touch((u32)(sp * 392 + tid) * 128, w2, L_DUMMY);
    if (tid < 448) touch((u32)(sp * 448 + tid) * 128, ssmk, L_DUMMY);
    if (b == 255) {
      __builtin_amdgcn_s_sleep(127);
      gflag_wait<64>(ws + (tid < 480 ? WS_FUT + tid * 128 : WS_FH + ((((tid < 704 ? tid - 480 : 223) / 14) * 2) * 56 + ((tid < 704 ? tid - 480 : 223) % 14) * 4) * 128), tag);
      BAR();
      if (tid == 0) gstore64(ws + WS_E, tag);
    }
    return;
  }

  const int cw = wid - 4, th = cw & 1, fq = cw >> 1;
  {
    const int lane = (int)opq(threadIdx.x) & 63, g = lane >> 4, hh = lane >> 5, l31 = lane & 31;
    const int q4 = (lane >> 2) & 3, p4 = lane & 3;
    const u32 aA = 16384 + (u32)((8 * hh + q4) * 256 + ((fq ^ q4) << 6) + (g & 1) * 32 + p4 * 8);
    const u32 aB = (u32)((64 * th + l31) * 128 + ((hh ^ ((l31 >> 1) & 7)) << 4));
    f32x16 acc[2][2] = {};
    s16x8 fa[2][2][2], fb[2][2][2];
    u32 pa, pb[4];
    auto setslot = [&](u32 slot) {
      pa = opq(aA + slot);
#pragma unroll
      for (int k = 0; k < 4; ++k) pb[k] = opq((aB ^ (u32)(k << 5)) + slot);
    };
    auto rd = [&](int set) {
#pragma unroll
      for (int k = 0; k < 2; ++k) {
        const int ks = 2 * set + k;
        fa[set][k][0] = lds_tr2(pa, ks * 4096, ks * 4096 + 1024);
        fb[set][k][0] = lds_b128(pb[ks], 0);
        fb[set][k][1] = lds_b128(pb[ks], 4096);
        fa[set][k][1] = lds_tr2(pa, 16384 + ks * 4096, 16384 + ks * 4096 + 1024);
      }
    };
    auto mm = [&](int set, int n0, int n1) {
#pragma unroll
      for (int n = n0; n < n1; ++n) acc[(n >> 1) & 1][n & 1] = mfma32(fa[set][n >> 2][(n >> 1) & 1], fb[set][n >> 2][n & 1], acc[(n >> 1) & 1][n & 1]);
    };
    setslot(0);
    const u32 fad = opq(L_FULL + (lane & 1) * 4);
#pragma unroll
    for (int i = 0; i < 4; ++i) asm volatile("" : "+v"(acc[i >> 1][i & 1]));
    __builtin_amdgcn_s_setprio(1);
    lflag_wait(L_FULL, 1, 1);
    rd(0);
    SCHED0();
    u32 slot = 0;
    auto stage = [&](int c, bool bar) {
      rd(1);
      const u32 fl = *(volatile LDSP(u32))fad;
      mm(0, 0, 6);
      SGB(0x008, 1); SGB(0x100, 3); SGB(0x008, 1); SGB(0x100, 3); SGB(0x008, 1); SGB(0x100, 3); SGB(0x008, 1); SGB(0x100, 4); SGB(0x008, 2);
      SCHED0();
      slot = slot == 2 * SLOT ? 0 : slot + SLOT;
      setslot(slot);
      SCHED0();
      mm(0, 6, 7);
      SCHED0();
      __builtin_amdgcn_s_waitcnt(0xC07F);
      const bool miss = c < 6 && __builtin_amdgcn_ballot_w64(fl < (u32)(c + 2));
      SCHED0();
      mm(0, 7, 8);
      SCHED0();
      if (bar) BAR();
      SCHED0();
      mm(1, 0, 2);
      SCHED0();
      if (!bar && c == 6) lflag_set(L_FREE + cw * 4, 7);
      if (miss) lflag_wait(L_FULL, 1, c + 2);
      SCHED0();
      rd(0); mm(1, 2, 8);
#pragma unroll
      for (int i = 0; i < 4; ++i) { SGB(0x008, 1); SGB(0x100, 3); }
      SGB(0x008, 2);
      SCHED0();
    };
#pragma unroll 1
    for (int c = 0; c < 4; ++c) stage(c, true);
#pragma unroll 1
    for (int c = 4; c < 7; ++c) stage(c, false);
    const u32 hdst = WS_H + (u32)((2 * panel + th) * 56 + 4 * ft + fq) * 4096 + lane * 16;
    const u32 sa = opq(L_STAT + (u32)(64 * th + l31) * 8), ca = opq(L_CSUM + (u32)(32 * fq + 4 * hh) * 4);
    f32x2 st[2]; f32x4 csa[2][2][2];
    auto rdc = [&]() {
#pragma unroll
      for (int j = 0; j < 2; ++j) st[j] = *(volatile LDSP(f32x2))((LDSP(char))sa + j * 256);
#pragma unroll
      for (int n = 0; n < 8; ++n) csa[n >> 2][(n >> 1) & 1][n & 1] = *(volatile LDSP(f32x4))((LDSP(char))ca + ((n >> 1) & 1) * 512 + (n >> 2) * 64 + (n & 1) * 32);
    };
    const u32 ef = *(volatile LDSP(u32))L_EPI;
    rdc();
    if (__builtin_amdgcn_readfirstlane(ef) == 0) { lflag_wait(L_EPI, 0, 1); rdc(); }
    float nm[2], c1[2], irs2[2];
#pragma unroll
    for (int j = 0; j < 2; ++j) { nm[j] = -st[j][0]; c1[j] = -1.44269504f * st[j][1]; irs2[j] = __builtin_amdgcn_rcpf(st[j][1] * st[j][1]); }
#pragma unroll
    for (int s = 0; s < 2; ++s) {
      const f32x4 (&cs)[2][2] = csa[s];
#pragma unroll
      for (int j = 0; j < 2; ++j) {
        float a[8], ex[8], hv[8];
#pragma unroll
        for (int e = 0; e < 8; ++e) a[e] = __builtin_fmaf(nm[j], cs[0][e >> 2][e & 3], acc[0][j][8 * s + e]);
#pragma unroll
        for (int e = 0; e < 8; ++e) ex[e] = a[e] * c1[j];
        SCHED0();
#pragma unroll
        for (int e = 0; e < 8; ++e) { ex[e] = __builtin_amdgcn_exp2f(ex[e]); hv[e] = __builtin_fmaf(nm[j], cs[1][e >> 2][e & 3], acc[1][j][8 * s + e]); }
        SCHED0();
#pragma unroll
        for (int e = 0; e < 8; ++e) ex[e] = __builtin_fmaf(ex[e], irs2[j], irs2[j]);
        SCHED0();
#pragma unroll
        for (int e = 0; e < 8; ++e) { ex[e] = __builtin_amdgcn_rcpf(ex[e]); hv[e] *= a[e]; }
        SCHED0();
#pragma unroll
        for (int e = 0; e < 8; ++e) hv[e] *= ex[e];
        st128_dev(rs, hdst + (s * 2 + j) * 1024, u32x4{pk2(hv[0], hv[1]), pk2(hv[2], hv[3]), pk2(hv[4], hv[5]), pk2(hv[6], hv[7])});
      }
    }
  }
  char* hflag = ws + WS_FH + ((2 * panel + th) * 56 + 4 * ft + fq) * 128;
  u32x4 xv, sv;
  if (cw < 4) {
    const int ht = (int)opq(threadIdx.x) & 255, lane = ht & 63, l15 = ht & 15;
    const u64 tag = tag_of(*(volatile LDSP(u64))L_TAG);
    const int ch = cw >> 1, var = cw & 1;
    gflag_publish(hflag, tag, lane);
    u32x4 xr[2]; u32x2 st[2];
#pragma unroll
    for (int i = 0; i < 2; ++i) {
      const int n = ht + 256 * i, row = n >> 3, c = n & 7;
      xr[i] = *(const u32x4*)(x + (u32)((64 * tau + row) * (D * 2) + (64 * dl + 8 * c) * 2));
      st[i] = ld64_dev(rs, WS_STAT + (u32)(64 * tau + row) * 8);
    }
    SCHED0();
    conv_compute(lane, cw, d0 + ch, rs);
    u32x4 xn[2];
#pragma unroll
    for (int i = 0; i < 2; ++i) {
      const float mb = asf(st[i][0]), rsd = asf(st[i][1]);
#pragma unroll
      for (int e = 0; e < 4; ++e) {
        const u32 dd = pk2(bflo(xr[i][e]) - mb, bfhi(xr[i][e]) - mb);
        xn[i][e] = pk2(bflo(dd) * rsd, bfhi(dd) * rsd);
      }
    }
    asm volatile("" : "+v"(xn[0]), "+v"(xn[1]));
    SCHED0();
    gflag_publish(ws + WS_FY + (var * 448 + d0 + ch) * 128, tag, lane);
    xv = xr[0]; sv = xn[0];
    lflag_wait(L_XCH, 1, 1);
    lds_st128(E_S + (u32)ht * 16, xn[1]); lds_st128(E_X + (u32)ht * 16, xr[1]);
    gflag_wait<4>(ws + WS_FY + ((tau >> 4) * 448 + 64 * dl + 16 * cw + l15) * 128, tag);
#pragma unroll
    for (int i = 0; i < 2; ++i)
      glds16_dev((u32)((lane >> 3) * (S * 2) + (((lane & 7) ^ (2 * cw + i)) << 4)),
                 ws + WS_YT + (64 * dl + 16 * cw + 8 * i) * (S * 2) + 128 * tau, xbuf(cw ^ 1) + (u32)i * 1024);
    WAIT_VM(0);
  } else {
    const int j = cw - 4;
    const int lane = (int)opq(threadIdx.x) & 63;
    lflag_set(L_PUB + j * 4, 1);
    const u64 btag = tag_of(*(volatile LDSP(u64))L_TAG);
    gflag_publish(hflag, btag, lane);
    const u32 hl = lane * 16;
    const char* hbase = ws + WS_H + (size_t)tau * 56 * 4096;
    const int q4 = (lane >> 2) & 3;
    const u32 ta = (u32)((4 * (lane >> 5) + q4) * 128 + ((q4 >> 1) << 6) + ((lane >> 4) & 1) * 32 + (lane & 3) * 8);
    s16x8 fa[2][4], fb[3][4];
    f32x16 acc[2][2] = {};
    auto ldh = [&](int i) {
      const char* p = hbase + (size_t)g2_ks(j, i) * 4096;
      asm volatile("global_load_dwordx4 %0, %4, %5 sc1\n\tglobal_load_dwordx4 %1, %4, %5 offset:1024 sc1\n\t"
                   "global_load_dwordx4 %2, %4, %5 offset:2048 sc1\n\tglobal_load_dwordx4 %3, %4, %5 offset:3072 sc1"
                   : "=&v"(fb[i % 3][0]), "=&v"(fb[i % 3][1]), "=&v"(fb[i % 3][2]), "=&v"(fb[i % 3][3])
                   : "v"(hl), "s"(p) : "memory");
    };
    u32 tb[2];
#pragma unroll
    for (int a = 0; a < 2; ++a) tb[a] = opq((ta ^ (u32)(a << 6)) + g2_place(j, 0));
    auto rda = [&](int i) {
#pragma unroll
      for (int a = 0; a < 2; ++a)
        if (i > 0 && i % 3 == 0) tb[a] = opq(i % 6 ? tb[a] - 2 * SLOT : tb[a] + 2 * SLOT);
#pragma unroll
      for (int n = 0; n < 4; ++n) {
        const int o = (i % 3) * 4096 + (n >> 1) * 2048;
        fa[i & 1][n] = lds_tr2(tb[n & 1], o, o + 1024);
      }
    };
    const u32 fed = opq(L_FED + j * 4), prog = opq(L_PROG + j * 4);
    gflag_wait<2>(ws + WS_FH + (tau * 56 + g2_ks(j, lane < 7 ? lane : 6)) * 128, btag);
#pragma unroll
    for (int i = 0; i < 3; ++i) ldh(i);
    lflag_wait(L_FED + j * 4, 0, 3);
    rda(0);
    SCHED0();
    u32 fl = 0;
#pragma unroll
    for (int i = 0; i < 14; ++i) {
      if (i == 2 || (i >= 5 && i < 13 && (i & 1))) {
        if (__builtin_amdgcn_readfirstlane(fl) < i + 2) lflag_wait(L_FED + j * 4, 0, i + 2);
      }
      asm volatile("s_waitcnt vmcnt(%4)" : "+v"(fb[i % 3][0]), "+v"(fb[i % 3][1]), "+v"(fb[i % 3][2]), "+v"(fb[i % 3][3])
                   : "i"(i < 12 ? 8 : (13 - i) * 4) : "memory");
      SCHED0();
      if (i < 13) rda(i + 1);
      if (i == 1 || (i >= 4 && i < 12 && !(i & 1))) fl = *(volatile LDSP(u32))fed;
      if (i < 13) {
#pragma unroll
        for (int s = 0; s < 2; ++s)
#pragma unroll
          for (int a = 0; a < 2; ++a)
#pragma unroll
            for (int t = 0; t < 2; ++t) acc[a][t] = mfma32(fa[i & 1][2 * s + a], fb[i % 3][2 * s + t], acc[a][t]);
#pragma unroll
        for (int n = 0; n < 5; ++n) { SGB(0x008, 1); SGB(0x100, 2); }
        SGB(0x008, 3);
      } else {
        __builtin_amdgcn_s_waitcnt(0xC07F);
        const int ln = (int)opq(threadIdx.x), lt = ln & 31, lh = (ln >> 5) & 1;
#pragma unroll
        for (int n = 0; n < 5; ++n) {
          if (n < 4) {
#pragma unroll
            for (int s = 0; s < 2; ++s) acc[n & 1][n >> 1] = mfma32(fa[i & 1][2 * s + (n & 1)], fb[i % 3][2 * s + (n >> 1)], acc[n & 1][n >> 1]);
          }
          SCHED0();
          if (n > 0) {
            const int a = (n - 1) & 1, t = (n - 1) >> 1;
#pragma unroll
            for (int G = 0; G < 4; ++G)
              *(LDSP(f32x4))(g2_place(j, t ? 3 : 0) + (u32)(lt * 256 + (((8 * a + 2 * G + lh) ^ (lt & 7)) << 4))) =
                  f32x4{acc[a][t][4 * G], acc[a][t][4 * G + 1], acc[a][t][4 * G + 2], acc[a][t][4 * G + 3]};
          }
          SCHED0();
        }
      }
      SCHED0();
      if (i < 8 && (i & 1)) *(volatile LDSP(u32))prog = i + 1;
      if (i == 4) lflag_wait(L_H, 0, 1);
      if (i + 3 < 14) ldh(i + 3);
      SCHED0();
    }
  }
  {
    const int ht = (int)opq(threadIdx.x) & 255;
    const int half = cw >> 2, tk = ht >> 3, dg = ht & 7, tok = 32 * half + tk;
    const u32 pa0 = opq((half ? 0u : 2 * SLOT) + (u32)(tk * 256 + (((2 * dg) ^ (tk & 7)) << 4))), pa1 = opq(pa0 ^ 16u);
    const u32 ya = opq(xbuf((dg >> 1) ^ 1) + (u32)(8 * (dg & 1) * 128 + (((tok >> 3) ^ dg) << 4) + (tok & 7) * 2));
    const u32 ea = opq(E_S + (u32)ht * 16);
    const u32 oo = opq((u32)((64 * tau + tok) * (D * 2) + (64 * dl + 8 * dg) * 2));
    WAIT_LGKM0();
    BAR();
    f32x4 p0[4], p1[4];
    u32 yv[8];
#pragma unroll
    for (int w = 0; w < 4; ++w) {
      p0[w] = *(LDSP(f32x4))((LDSP(char))pa0 + w * 12288);
      p1[w] = *(LDSP(f32x4))((LDSP(char))pa1 + w * 12288);
    }
#pragma unroll
    for (int e = 0; e < 8; ++e) yv[e] = *(LDSP(u16))((LDSP(char))ya + e * 128);
    SCHED0();
    if (half) { sv = *(LDSP(u32x4))ea; xv = *(LDSP(u32x4))((LDSP(char))ea + (E_X - E_S)); }
    const f32x4 s0 = (p0[0] + p0[1]) + (p0[2] + p0[3]), s1 = (p1[0] + p1[1]) + (p1[2] + p1[3]);
    u32 f[4];
    float ex[8];
#pragma unroll
    for (int e = 0; e < 4; ++e) f[e] = e < 2 ? pk2(s0[2 * e], s0[2 * e + 1]) : pk2(s1[2 * e - 4], s1[2 * e - 3]);
#pragma unroll
    for (int e = 0; e < 8; ++e) ex[e] = __builtin_amdgcn_exp2f(-1.44269504f * ((e & 1) ? bfhi(f[e >> 1]) : bflo(f[e >> 1])));
    SCHED0();
    u32 se[4];
#pragma unroll
    for (int e = 0; e < 4; ++e) se[e] = pk2(bflo(sv[e]) * bflo(yv[2 * e]), bfhi(sv[e]) * bflo(yv[2 * e + 1]));
#pragma unroll
    for (int e = 0; e < 8; ++e) ex[e] = __builtin_amdgcn_rcpf(1.0f + ex[e]);
    SCHED0();
    u32x4 o;
#pragma unroll
    for (int e = 0; e < 4; ++e) {
      const u32 gt = pk2(bflo(f[e]) * ex[2 * e], bfhi(f[e]) * ex[2 * e + 1]);
      const u32 p = pk2(bflo(se[e]) * bflo(gt), bfhi(se[e]) * bfhi(gt));
      o[e] = pk2(bflo(xv[e]) + bflo(p), bfhi(xv[e]) + bfhi(p));
    }
    __builtin_amdgcn_raw_buffer_store_b128(o, __builtin_amdgcn_make_buffer_rsrc(out, 0, S * D * 2, 0x00020000), oo, 0, 16);
  }
}
}
#if defined(__HIP_DEVICE_COMPILE__) && __HIP_DEVICE_COMPILE__
#pragma clang attribute pop
#endif

extern "C" void kernel_launch(void* const* d_in, const int* in_sizes, int n_in, void* d_out, int out_size, void* d_ws,
                              size_t ws_size, hipStream_t stream) {
  (void)hipFuncSetAttribute((const void*)fused_kernel, hipFuncAttributeMaxDynamicSharedMemorySize, 163840);
  fused_kernel<<<256, 768, 163840, stream>>>((const char*)d_in[0], (const char*)d_in[1], (const char*)d_in[2], (const char*)d_in[3],
                                             (const char*)d_in[4], (const char*)d_in[5], (const char*)d_in[7], (const char*)d_in[8],
                                             (const char*)d_in[9], (char*)d_ws, (char*)d_out);
}
```

```cpp
#include <hip/hip_runtime.h>
#include <hip/hip_bf16.h>
#include <stdint.h>
#pragma clang diagnostic ignored "-Wint-to-pointer-cast"

#if defined(__HIP_DEVICE_COMPILE__) && __HIP_DEVICE_COMPILE__
#pragma clang attribute push(__attribute__((target("no-packed-fp32-ops"))), apply_to = function)
#endif
namespace {
typedef unsigned short u16;
typedef unsigned int u32;
typedef unsigned long long u64;
typedef __attribute__((ext_vector_type(4))) short s16x4;
typedef __attribute__((ext_vector_type(8))) short s16x8;
typedef __attribute__((ext_vector_type(8))) __bf16 bf16x8;
typedef __attribute__((ext_vector_type(2))) __bf16 bf16x2;
typedef __attribute__((ext_vector_type(16))) float f32x16;
typedef __attribute__((ext_vector_type(4))) float f32x4;
typedef __attribute__((ext_vector_type(2))) float f32x2;
typedef __attribute__((ext_vector_type(4))) u32 u32x4;
typedef __attribute__((ext_vector_type(2))) u32 u32x2;
#define LDSP(T) __attribute__((address_space(3))) T*
#define GLBP(T) __attribute__((address_space(1))) T*
#define DEV __device__ __forceinline__

constexpr int S = 2048, D = 448, F = 1792;
constexpr u32 WS_H = 0;
constexpr u32 WS_UT = WS_H + 7340032;
constexpr u32 WS_YT = WS_UT + 1835008;
constexpr u32 WS_STAT = WS_YT + 1835008;
constexpr u32 WS_CSUM = WS_STAT + 16384;
constexpr u32 WS_E = WS_CSUM + 16384;
constexpr u32 WS_FUT = WS_E + 128;
constexpr u32 WS_FST = WS_FUT + 448 * 128;
constexpr u32 WS_FY = WS_FST + 32 * 128;
constexpr u32 WS_FH = WS_FY + 896 * 128;
constexpr u32 WS_END = WS_FH + 1792 * 128;
constexpr u32 SLOT = 49152;
constexpr u32 MISC = 3 * SLOT;
constexpr u32 L_FULL = MISC, L_EPI = MISC + 8, L_H = MISC + 16, L_FREE = MISC + 32, L_CONV = MISC + 64, L_FED = MISC + 80,
              L_TAG = MISC + 96, L_DD = MISC + 104, L_PROG = MISC + 128, L_XCH = MISC + 144, L_PUB = MISC + 160, L_PUBA = MISC + 176, L_STAT = MISC + 256,
              L_CSUM = MISC + 1280, L_USCR = MISC + 4096, L_DUMMY = MISC + 2304, L_XB2 = MISC + 12288;
constexpr u32 CV = SLOT, CV_HSTR = 4160, CV_U = CV + 8 * CV_HSTR, CV_USTR = 4608;
constexpr u32 E_S = SLOT, E_X = SLOT + 4096;
constexpr int SPIN_CAP = 1 << 18;

__device__ const u32 seq_tab[160] __attribute__((aligned(128))) = {0, 1, 2, 3, 4, 5, 6, 7};
DEV float asf(u32 v) { return __builtin_bit_cast(float, v); }
DEV float bflo(u32 v) { return asf(v << 16); }
DEV float bfhi(u32 v) { return asf(v & 0xffff0000u); }
template <int CTRL> DEV float dpp(float v) { return asf(__builtin_amdgcn_update_dpp(0, __builtin_bit_cast(u32, v), CTRL, 0xf, 0xf, true)); }
DEV float sum8(float v) { v += dpp<0xB1>(v); v += dpp<0x4E>(v); return v + dpp<0x141>(v); }
DEV u32 pk2(float lo, float hi) {
  asm("" : "+v"(lo)); asm("" : "+v"(hi));
  bf16x2 r = {(__bf16)lo, (__bf16)hi};
  u32 v = __builtin_bit_cast(u32, r);
  asm("" : "+v"(v));
  return v;
}
DEV float rbf(float f) { bf16x2 r = {(__bf16)f, (__bf16)f}; return bflo(__builtin_bit_cast(u32, r)); }
DEV f32x4 mfma16(s16x8 a, s16x8 b, f32x4 c) {
  return __builtin_amdgcn_mfma_f32_16x16x32_bf16(__builtin_bit_cast(bf16x8, a), __builtin_bit_cast(bf16x8, b), c, 0, 0, 0);
}
DEV f32x16 mfma32(s16x8 a, s16x8 b, f32x16 c) {
  return __builtin_amdgcn_mfma_f32_32x32x16_bf16(__builtin_bit_cast(bf16x8, a), __builtin_bit_cast(bf16x8, b), c, 0, 0, 0);
}
DEV u32 opq(u32 v) { asm volatile("" : "+v"(v)); return v; }
DEV s16x8 lds_tr2(u32 base, int o0, int o1) {
  s16x4 lo = __builtin_amdgcn_ds_read_tr16_b64_v4i16((LDSP(s16x4))((LDSP(char))base + o0));
  s16x4 hi = __builtin_amdgcn_ds_read_tr16_b64_v4i16((LDSP(s16x4))((LDSP(char))base + o1));
  return __builtin_shufflevector(lo, hi, 0, 1, 2, 3, 4, 5, 6, 7);
}
DEV s16x8 lds_b128(u32 base, int o) { return *(LDSP(s16x8))((LDSP(char))base + o); }
DEV void lds_st128(u32 a, u32x4 v) { *(LDSP(u32x4))a = v; }
DEV void glds16x4(u32 v0, u32 v1, u32 v2, u32 v3, const void* sbase, u32 ldst) {
  asm volatile("s_mov_b32 m0, %5\n\ts_nop 0\n\tglobal_load_lds_dwordx4 %0, %4\n\tglobal_load_lds_dwordx4 %1, %4 offset:1024\n\t"
               "global_load_lds_dwordx4 %2, %4 offset:2048\n\tglobal_load_lds_dwordx4 %3, %4 offset:3072"
               ::"v"(v0), "v"(v1), "v"(v2), "v"(v3), "s"(sbase), "s"(ldst) : "memory");
}
DEV void glds16(u32 voff, const void* sbase, u32 ldst) {
  asm volatile("s_mov_b32 m0, %2\n\ts_nop 0\n\tglobal_load_lds_dwordx4 %0, %1" ::"v"(voff), "s"(sbase), "s"(ldst) : "memory");
}
DEV void glds16x4_dev(u32 v, const void* sbase, u32 ldst) {
  asm volatile("s_mov_b32 m0, %2\n\ts_nop 0\n\tglobal_load_lds_dwordx4 %0, %1 sc1\n\tglobal_load_lds_dwordx4 %0, %1 offset:1024 sc1\n\t"
               "global_load_lds_dwordx4 %0, %1 offset:2048 sc1\n\tglobal_load_lds_dwordx4 %0, %1 offset:3072 sc1"
               ::"v"(v), "s"(sbase), "s"(ldst) : "memory");
}
DEV void glds16_dev(u32 voff, const void* sbase, u32 ldst) {
  asm volatile("s_mov_b32 m0, %2\n\ts_nop 0\n\tglobal_load_lds_dwordx4 %0, %1 sc1" ::"v"(voff), "s"(sbase), "s"(ldst) : "memory");
}
DEV void dflag_set(u32 addr, u32 val) {
  asm volatile("s_waitcnt lgkmcnt(0)\n\ts_mov_b32 m0, %2\n\ts_mov_b64 exec, 1\n\tglobal_load_lds_dword %0, %1\n\ts_mov_b64 exec, -1"
               ::"v"(0), "s"(seq_tab + val), "s"(addr) : "memory");
}
#define WAIT_VM(n) asm volatile("s_waitcnt vmcnt(%0)" ::"i"(n) : "memory")
#define WAIT_LGKM0() asm volatile("s_waitcnt lgkmcnt(0)" ::: "memory")
#define BAR() do { asm volatile("" ::: "memory"); __builtin_amdgcn_s_barrier(); asm volatile("" ::: "memory"); } while (0)
#define SCHED0() __builtin_amdgcn_sched_barrier(0)
#define SGB(mask, n) __builtin_amdgcn_sched_group_barrier(mask, n, 0)

DEV void lflag_set(u32 addr, u32 val) {
  asm volatile("s_waitcnt lgkmcnt(0)\n\tds_write_b32 %0, %1" ::"v"(addr), "v"(val) : "memory");
}
DEV void lflag_wait(u32 addr, u32 mask, u32 val) {
  const u32 a = addr + 4 * (threadIdx.x & mask);
#pragma unroll 1
  for (int i = 0; i < SPIN_CAP; ++i) {
    u32 v;
    asm volatile("ds_read_b32 %0, %1\n\ts_waitcnt lgkmcnt(0)" : "=v"(v) : "v"(a) : "memory");
    if (__builtin_amdgcn_ballot_w64(v < val) == 0) break;
    __builtin_amdgcn_s_sleep(1);
  }
}
DEV u64 gload64(const char* p) { return __hip_atomic_load((GLBP(u64))p, __ATOMIC_RELAXED, __HIP_MEMORY_SCOPE_AGENT); }
DEV void gstore64(char* p, u64 v) { __hip_atomic_store((GLBP(u64))p, v, __ATOMIC_RELAXED, __HIP_MEMORY_SCOPE_AGENT); }
template <int SLEEP>
DEV void gflag_wait(const char* lane_ptr, u64 tag) {
  bool ok = false;
#pragma unroll 1
  for (int i = 0; i < SPIN_CAP; ++i) {
    asm volatile("" ::: "memory");
    if (!ok) ok = gload64(lane_ptr) == tag;
    if (__builtin_amdgcn_ballot_w64(!ok) == 0) break;
    __builtin_amdgcn_s_sleep(SLEEP);
  }
  asm volatile("" ::: "memory");
}
DEV void gflag_publish(char* p, u64 tag, int lane) {
  WAIT_VM(0);
  if (lane == 0) gstore64(p, tag);
}
DEV u64 tag_of(u64 seed) { return seed * 0x9E3779B97F4A7C15ull + 0xD1B54A32D192ED03ull; }
DEV u64 make_tag(const char* ws) { return tag_of(gload64(ws + WS_E)); }
typedef __amdgpu_buffer_rsrc_t rsrc_t;
DEV rsrc_t ws_rsrc(char* ws) { return __builtin_amdgcn_make_buffer_rsrc(ws, 0, WS_END, 0x00020000); }
DEV void st128_dev(rsrc_t r, u32 off, u32x4 v) { __builtin_amdgcn_raw_buffer_store_b128(v, r, off, 0, 16); }
DEV void st64_dev(rsrc_t r, u32 off, u32x2 v) { __builtin_amdgcn_raw_buffer_store_b64(v, r, off, 0, 16); }
DEV u32x2 ld64_dev(rsrc_t r, u32 off) { return __builtin_amdgcn_raw_buffer_load_b64(r, off, 0, 16); }
DEV void touch(u32 voff, const void* sbase, u32 ldst) {
  asm volatile("s_mov_b32 m0, %2\n\ts_nop 0\n\tglobal_load_lds_dword %0, %1" ::"v"(voff), "s"(sbase), "s"(ldst) : "memory");
}

DEV void glds4_dev(u32 voff, const void* sbase, u32 ldst) {
  asm volatile("s_mov_b32 m0, %2\n\ts_nop 0\n\tglobal_load_lds_dword %0, %1 sc1" ::"v"(voff), "s"(sbase), "s"(ldst) : "memory");
}

DEV u32 xbuf(int w) { return w < 2 ? L_USCR + (u32)w * 4096 : (w == 2 ? L_XB2 : CV + 45056); }
DEV void conv_compute(int lane, int cw, int d, rsrc_t rs) {
  const int ch = cw >> 1, var = cw & 1;
  const int m = lane & 15, g = lane >> 4, q4 = m >> 2, p4 = lane & 3;
  u32 aa = opq(CV + (u32)(ch * 4 + ((q4 + 1) & 3)) * CV_HSTR + (u32)(((q4 + 1) & 4) + 8 * g + 4 * p4) * 2 + var * 256);
  u32 ba = opq(CV_U + (u32)ch * CV_USTR + (u32)(240 - 16 * m + 8 * g) * 2 + var * 256);
  f32x4 acc[8] = {};
  s16x8 A[8], B[8];
  const u32 a0 = opq(CV + (u32)ch * 4 * CV_HSTR + (u32)(m + 1 + 8 * g) * 2);
  u32x4 msk;
#pragma unroll
  for (int i = 0; i < 4; ++i) { const int e = m + 8 * g + 2 * i - 15; msk[i] = e >= 0 ? ~0u : (e == -1 ? 0xffff0000u : 0u); }
#pragma unroll
  for (int i = 0; i < 8; ++i) asm volatile("" : "+v"(acc[i]));
  asm volatile("" : "+v"(msk));
  lflag_wait(L_CONV, 3, 1);
  u32 h0[8];
#pragma unroll
  for (int j = 0; j < 8; ++j) h0[j] = *(LDSP(u16))((LDSP(char))a0 + j * 2);
  SCHED0();
#pragma unroll
  for (int p = 0; p < 8; ++p) { A[7 - p] = lds_tr2(aa, (7 - p) * 512, (7 - p) * 512 + 8); B[p] = lds_b128(ba, (7 - p) * 512); }
  SCHED0();
  if (var == 0) {
    u32x4 f;
#pragma unroll
    for (int i = 0; i < 4; ++i) f[i] = (h0[2 * i] | (h0[2 * i + 1] << 16)) & msk[i];
    A[0] = __builtin_bit_cast(s16x8, f);
  }
  SCHED0();
#pragma unroll 1
  for (int it = 0; it < 4; ++it) {
#pragma unroll
    for (int p = 7; p >= 0; --p) {
#pragma unroll
      for (int dd = 0; dd <= 7 - p; ++dd) {
        acc[p + dd] = mfma16(A[p], B[dd], acc[p + dd]);
        if (p == 0) { SCHED0(); B[dd] = lds_b128(ba, 64 + (7 - dd) * 512); SCHED0(); }
      }
      SCHED0();
      A[p] = lds_tr2(aa, 64 + p * 512, 64 + p * 512 + 8);
      SCHED0();
    }
    aa = opq(aa + 64); ba = opq(ba + 64);
  }
  f32x4 y[4];
#pragma unroll
  for (int i = 0; i < 4; ++i) {
    *(LDSP(f32x4))(xbuf(cw) + (u32)(i * 1024 + lane * 16)) = var ? acc[i] : acc[4 + i];
    y[i] = var ? acc[4 + i] : acc[i];
  }
  lflag_set(L_XCH + cw * 4, 1);
  const u32 dv = *(LDSP(u32))L_DD;
  const float Dd = ch ? bfhi(dv) : bflo(dv);
  u32x2 uu[4];
#pragma unroll
  for (int i = 0; i < 4; ++i) uu[i] = *(LDSP(u32x2))(CV_U + (u32)ch * CV_USTR + (u32)(2044 - (1024 * var + 256 * i + 16 * m + 4 * g)) * 2);
  u32x2 sk[4];
#pragma unroll
  for (int i = 0; i < 4; ++i) sk[i] = u32x2{pk2(bfhi(uu[i][1]) * Dd, bflo(uu[i][1]) * Dd), pk2(bfhi(uu[i][0]) * Dd, bflo(uu[i][0]) * Dd)};
  const u32 xa = opq(xbuf(cw ^ 1) + (u32)lane * 16), yo = opq(WS_YT + (u32)(d * (S * 2) + (1024 * var + 16 * m + 4 * g) * 2));
  SCHED0();
  lflag_wait(L_XCH + (cw ^ 1) * 4, 0, 1);
  f32x4 pv[4];
#pragma unroll
  for (int i = 0; i < 4; ++i) pv[i] = *(LDSP(f32x4))((LDSP(char))xa + i * 1024);
  SCHED0();
#pragma unroll
  for (int i = 0; i < 4; ++i) {
    const f32x4 s = y[i] + pv[i];
    const u32 c0 = pk2(s[0], s[1]), c1 = pk2(s[2], s[3]);
    st64_dev(rs, yo + i * 512, u32x2{pk2(bflo(c0) + bflo(sk[i][0]), bfhi(c0) + bfhi(sk[i][0])), pk2(bflo(c1) + bflo(sk[i][1]), bfhi(c1) + bfhi(sk[i][1]))});
  }
}

DEV int g2_ks(int j, int i) { const int e = 7 * j + (i < 7 ? i : i - 7); return 4 * (e >> 1) + (e & 1) + (i < 7 ? 0 : 2); }
DEV u32 g2_place(int j, int p) { return (p < 3 ? 2 * SLOT + (u32)p * 4096 : (u32)(p - 3) * 4096) + (u32)j * 12288; }
DEV u32 w2_voff(int lane) { return (u32)((lane >> 3) * (D * 2) + (((lane & 7) ^ (((lane >> 4) & 1) << 2)) << 4)); }

DEV void share(int b, int n, const char* base) {
  const int lane = (int)opq(threadIdx.x) & 63;
  if (lane < n) touch((u32)(b * n + lane) * 128, base, L_DUMMY);
}

__global__ __launch_bounds__(768, 3) void fused_kernel(const char* x, const char* kp, const char* vp, const char* skp,
                                                       const char* ssmk, const char* ssmD, const char* w1, const char* w3,
                                                       const char* w2, char* ws, char* out) {
  const int b = blockIdx.x, xcd = b & 7, r = b >> 3;
  const int wid = __builtin_amdgcn_readfirstlane(threadIdx.x >> 6);
  const int panel = (xcd >> 1) * 4 + r / 7, ft = (xcd & 1) * 7 + r % 7;
  const int tau = xcd * 4 + r / 7, dl = r % 7;
  const int d0 = 2 * b;

  if (b < 224) {
    if (wid >= 4 && wid < 7) share(b, wid == 4 ? 64 : 56, wid == 4 ? x : (wid == 5 ? w1 : w3));
    if (wid == 7) { *(LDSP(u32))(MISC + (threadIdx.x & 63) * 4) = 0; WAIT_LGKM0(); }
    BAR();
    if (wid < 2) {
      const int lane = (int)opq(threadIdx.x) & 63;
      const int g = lane >> 4, l15 = lane & 15;
      u32 vx[8], vw[8];
      const u32 ox = (u32)((lane >> 3) * (D * 2) + (((lane & 7) ^ g) << 4));
      const u32 ow = (u32)(g * (F * 2) + (((l15 >> 2) ^ g) << 6) + ((lane & 3) << 4));
#pragma unroll
      for (int i = 0; i < 8; ++i) {
        vx[i] = (ox ^ (u32)((i & 1) * 64)) + i * 8 * (D * 2) - (i & 3) * 1024;
        vw[i] = ow + i * 4 * (F * 2) - (i & 3) * 1024;
      }
#pragma unroll 1
      for (int c = 0; c < 7; ++c) {
        if (c >= 3) BAR();
        const u32 slot = (u32)(c % 3) * SLOT + wid * 8192;
        const char* xb = x + (size_t)(panel * 128 + wid * 64) * (D * 2) + c * 128;
        const char* w1b = w1 + (size_t)(c * 64 + wid * 32) * (F * 2) + ft * 256;
        const char* w3b = w3 + (size_t)(c * 64 + wid * 32) * (F * 2) + ft * 256;
#pragma unroll
        for (int i = 0; i < 8; i += 4) {
          glds16x4(vx[i], vx[i + 1], vx[i + 2], vx[i + 3], xb, slot + i * 1024);
          glds16x4(vw[i], vw[i + 1], vw[i + 2], vw[i + 3], w1b, slot + 16384 + i * 1024);
          glds16x4(vw[i], vw[i + 1], vw[i + 2], vw[i + 3], w3b, slot + 32768 + i * 1024);
        }
        dflag_set(L_FULL + wid * 4, c + 1);
      }
    }
    if (wid == 7 && (threadIdx.x & 63) < 5) touch((threadIdx.x & 63) * 128, seq_tab, L_DUMMY);
  }

  if (__builtin_expect(wid < 4 && b < 224, 1)) {
    const int lane = (int)opq(threadIdx.x) & 63;
    u64 tag = 0;
    if (wid < 2) {
      if (wid == 0) {
        tag = make_tag(ws);
        const int k = lane & 7;
        gflag_wait<8>(ws + WS_FST + (k < 2 ? 2 * panel + k : (k == 2 ? tau : (k & 1) * 14 + ft)) * 128, tag);
        glds16_dev(lane * 16, ws + WS_STAT + panel * 1024, L_STAT);
        glds16_dev(lane * 16, ws + WS_CSUM + ft * 1024, L_CSUM);
        if (lane < 2) glds4_dev(lane * 4, ws + WS_E, L_TAG);
        if (lane < 1) glds4_dev(0, ssmD + d0 * 2, L_DD);
        dflag_set(L_EPI, 1);
      }
    } else {
      __builtin_amdgcn_s_setprio(3);
      const rsrc_t rs = ws_rsrc(ws);
      const int u = wid - 2;
      const int ti = b / 7, cj = b % 7;
      const int tg = lane >> 3, cg = lane & 7;
      const int t0 = 64 * ti + 32 * u + 4 * tg, ch0 = 64 * cj + 8 * cg;
      u32x4 kr[7], vr[4], sw[4]; u32x2 o[8];
      u64 seed;
      auto ldkv = [&]() {
        const u32 ko = (u32)(t0 * (D * 2) + ch0 * 2);
#pragma unroll
        for (int i = 0; i < 7; ++i) kr[i] = *(const u32x4*)(kp + (i < 3 && t0 == 0 ? ko : ko + (u32)((i - 3) * (D * 2))));
#pragma unroll
        for (int i = 0; i < 4; ++i) vr[i] = *(const u32x4*)(vp + (ko + (u32)(i * (D * 2))));
        SCHED0();
      };
      auto piece = [&](int p) {
#pragma unroll
        for (int c = 2 * p; c < 2 * p + 2; ++c) {
          const u32 wa = sw[p][(c & 1) * 2], wb = sw[p][(c & 1) * 2 + 1];
          const float w0 = bflo(wa), w1f = bfhi(wa), w2f = bflo(wb), w3f = bfhi(wb);
          float kf[7], ks[4];
#pragma unroll
          for (int i = 0; i < 7; ++i) kf[i] = (c & 1) ? bfhi(kr[i][p]) : bflo(kr[i][p]);
#pragma unroll
          for (int i = 0; i < 4; ++i)
            ks[i] = __builtin_fmaf(kf[i + 3], w0, __builtin_fmaf(kf[i + 2], w1f, __builtin_fmaf(kf[i + 1], w2f, kf[i] * w3f)));
#pragma unroll
          for (int i = 0; i < 2; ++i) {
            const int ta = 3 - 2 * i, tb = 2 - 2 * i;
            const u32 kk = pk2(ks[ta], ks[tb]);
            o[c][i] = pk2(bflo(kk) * ((c & 1) ? bfhi(vr[ta][p]) : bflo(vr[ta][p])), bfhi(kk) * ((c & 1) ? bfhi(vr[tb][p]) : bflo(vr[tb][p])));
          }
          asm volatile("" : "+v"(o[c]));
        }
        SCHED0();
      };
      BAR();
      ldkv();
#pragma unroll
      for (int i = 0; i < 4; ++i) sw[i] = *(const u32x4*)(skp + (u32)(ch0 * 8 + i * 16));
      seed = gload64(ws + WS_E + opq(0));
      if (u && lane < 14) {
        const u32 fo = lane < 8 ? WS_FH + (u32)((2 * panel + (lane & 1)) * 56 + 4 * ft + (lane >> 1)) * 128
                                : (lane < 12 ? WS_FY + (u32)((lane & 1) * 448 + d0 + ((lane >> 1) & 1)) * 128 : WS_FUT + (u32)(cj * 64 + 2 * ti + (lane & 1)) * 128);
        touch(fo, ws, L_DUMMY);
      }
      SCHED0();
      BAR();
      SCHED0();
      BAR();
      SCHED0();
#pragma unroll
      for (int i = 0; i < 3; ++i) if (t0 == 0) kr[i] = u32x4{0, 0, 0, 0};
      piece(0);
      BAR();
      SCHED0();
#pragma unroll
      for (int c = 1; c < 4; ++c) piece(c);
      tag = tag_of(seed);
      const u32 scr = L_USCR + (u32)u * 4096;
#pragma unroll
      for (int c = 0; c < 8; ++c) *(LDSP(u32x2))(scr + (u32)((cg * 8 + c) * 64 + (((7 - tg) ^ (cg & 6)) << 3))) = o[c];
      WAIT_LGKM0();
#pragma unroll
      for (int i = 0; i < 4; ++i) {
        const int dd = 16 * i + (lane >> 2), c4 = lane & 3;
        u32x4 v = *(LDSP(u32x4))(scr + (u32)(dd * 64 + ((c4 ^ (dd >> 4)) << 4)));
        st128_dev(rs, WS_UT + (u32)((64 * cj + dd) * (S * 2) + (2016 - 64 * ti - 32 * u) * 2 + c4 * 16), v);
      }
      gflag_publish(ws + WS_FUT + (cj * 64 + 2 * ti + u) * 128, tag, lane);
    }
    const int j = wid;
    const u32 o2 = w2_voff(lane);
    const char* wbase = w2 + dl * 128;
    u32x4 pk[8][4];
    auto park = [&](int i0, int i1) {
#pragma unroll
      for (int i = i0; i < i1; ++i) {
        const char* src = wbase + (size_t)g2_ks(j, i + 6) * 32 * (D * 2);
        asm volatile("" ::: "memory");
#pragma unroll
        for (int pc = 0; pc < 4; ++pc) pk[i][pc] = *(const u32x4*)(src + (o2 + (u32)(pc * 8 * (D * 2))));
      }
      SCHED0();
    };
    park(0, 6);
    if (wid == 2) {
      gflag_wait<8>(ws + WS_FUT + ((d0 >> 6) * 64 + lane) * 128, tag);
      lflag_wait(L_FREE, 7, 7);
      glds16x4_dev(lane * 16, ws + WS_UT + d0 * (S * 2), CV_U);
      glds16x4_dev(lane * 16, ws + WS_UT + (d0 + 1) * (S * 2), CV_U + CV_USTR);
      if (lane < 32) {
        glds16(lane * 16, seq_tab + 32, CV_U + 4096);
        glds16(lane * 16, seq_tab + 32, CV_U + CV_USTR + 4096);
      }
      dflag_set(L_CONV + 8, 1);
    }
    if (wid == 3) lflag_set(L_CONV + 12, 1);
    park(6, 8);
    if (wid == 1) {
      lflag_wait(L_FREE, 7, 7);
      const u32 vl = lane * 16;
#pragma unroll 1
      for (int n = 0; n < 8; ++n) {
        const int rr = n & 3;
        const char* src = ssmk + (size_t)(d0 + (n >> 2)) * (S * 2) + (rr ? (rr - 4) * 2 : 0);
        glds16x4(rr && !lane ? 16u : vl, vl, vl, vl, src, CV + (u32)n * CV_HSTR + (rr ? 24 : 32));
        if (rr == 3) dflag_set(L_CONV + (n >> 2) * 4, 1);
      }
    }
    lflag_wait(L_FREE, 7, 7);
#pragma unroll 1
    for (int i = 0; i < 6; ++i) {
      if (i == 3) dflag_set(L_FED + j * 4, 3);
      if (i == 0) {
        if (wid == 3) {
          lflag_wait(L_PUBA, 3, 1);
          __builtin_amdgcn_s_sleep(4);
          const int e = lane < 28 ? lane : 27;
          gflag_wait<4>(ws + WS_FH + (tau * 56 + 4 * (e >> 1) + (e & 1)) * 128, tag);
          lflag_set(L_H + 4, 1);
        }
        lflag_wait(L_PUB, 3, 1);
        lflag_wait(L_CONV, 3, 1);
      }
      const char* src = wbase + (size_t)g2_ks(j, i) * 32 * (D * 2);
      glds16x4(o2, o2 + 8 * (D * 2) - 1024, o2 + 16 * (D * 2) - 2048, o2 + 24 * (D * 2) - 3072, src, g2_place(j, i));
    }
    dflag_set(L_FED + j * 4, 6);
    if (wid == 3) {
      lflag_wait(L_PUB, 3, 2);
      const int e = lane < 28 ? lane : 27;
      gflag_wait<4>(ws + WS_FH + (tau * 56 + 4 * (e >> 1) + (e & 1) + 2) * 128, tag);
      dflag_set(L_H, 1);
    }
    __builtin_amdgcn_s_setprio(0);
    WAIT_VM(0);
    const u32 wl = opq(lane * 16 + j * 12288);
#pragma unroll
    for (int i = 0; i < 8; i += 2) {
      lflag_wait(L_PROG + j * 4, 0, i + 2);
#pragma unroll
      for (int n = 0; n < 8; ++n) lds_st128(wl + g2_place(0, (i + (n >> 2)) % 6) + (n & 3) * 1024, pk[i + (n >> 2)][n & 3]);
      lflag_set(L_FED + j * 4, i + 8);
    }
    return;
  }
  const rsrc_t rs = ws_rsrc(ws);

  if (b >= 224) {
    if (wid >= 8 && b != 255) return;
    const int tid = (int)opq(threadIdx.x), lane = tid & 63;
    const int sp = b - 224;
    const u64 seed = gload64(ws + WS_E + opq(0));
    if (tid == 0) touch(WS_FST + sp * 128, ws, L_DUMMY);
    if (wid < 8) {
      const int row = 64 * sp + (tid >> 3), c8 = tid & 7;
      const int cu = sp < 28 ? sp : 27, mat = cu / 14, ct = cu % 14, kg = tid >> 4, c16 = tid & 15;
      const char* wp = (mat ? w3 : w1) + ct * 256;
      const u32 wo = (u32)(kg * (F * 2) + c16 * 16), xo = (u32)(row * (D * 2) + c8 * 16);
      u32x4 xv[7], wv[14];
#pragma unroll
      for (int i = 0; i < 7; ++i) xv[i] = *(const u32x4*)(x + (xo + i * 128));
#pragma unroll
      for (int i = 0; i < 14; ++i) wv[i] = *(const u32x4*)(wp + (wo + i * 32 * (F * 2)));
      SCHED0();
      float s = 0.f;
#pragma unroll
      for (int i = 0; i < 7; ++i)
#pragma unroll
        for (int j = 0; j < 4; ++j) s += bflo(xv[i][j]) + bfhi(xv[i][j]);
      const float m = sum8(s) * (1.0f / D);
      float q = 0.f;
#pragma unroll
      for (int i = 0; i < 7; ++i)
#pragma unroll
        for (int j = 0; j < 4; ++j) {
          const float aa = bflo(xv[i][j]) - m, bb = bfhi(xv[i][j]) - m;
          q = __builtin_fmaf(aa, aa, __builtin_fmaf(bb, bb, q));
        }
      const float sd = rbf(__builtin_amdgcn_sqrtf(rbf(rbf(sum8(q) * (1.0f / D)) + 1e-5f)));
      if (c8 == 0) *(LDSP(f32x2))(u32)(16384 + (tid >> 3) * 8) = f32x2{rbf(m), __builtin_amdgcn_rcpf(sd)};
      SCHED0();
      float a[8] = {};
#pragma unroll
      for (int i = 0; i < 14; ++i)
#pragma unroll
        for (int j = 0; j < 4; ++j) { a[2 * j] += bflo(wv[i][j]); a[2 * j + 1] += bfhi(wv[i][j]); }
      f32x4 t;
#pragma unroll
      for (int j = 0; j < 4; ++j) {
        auto p = __builtin_amdgcn_permlane16_swap(__builtin_bit_cast(u32, a[j]), __builtin_bit_cast(u32, a[j + 4]), false, false);
        t[j] = asf(p[0]) + asf(p[1]);
      }
      *(LDSP(f32x4))(u32)((kg >> 1) * 512 + c16 * 32 + (kg & 1) * 16) = t;
    }
    WAIT_LGKM0(); BAR();
    const u64 tag = tag_of(seed);
    if (wid == 0) {
      const int cu = sp < 28 ? sp : 27;
      f32x2 cs = {};
#pragma unroll
      for (int i = 0; i < 16; ++i) cs += *(LDSP(f32x2))(u32)(i * 512 + lane * 8);
      st64_dev(rs, WS_STAT + (u32)(64 * sp + lane) * 8, *(LDSP(u32x2))(u32)(16384 + lane * 8));
      if (sp < 28) st64_dev(rs, WS_CSUM + (u32)((cu % 14) * 1024 + (cu / 14) * 512 + lane * 8), __builtin_bit_cast(u32x2, cs));
      gflag_publish(ws + WS_FST + sp * 128, tag, lane);
    }
    WAIT_VM(0); BAR();
    if (tid < 392) touch((u32)(sp * 392 + tid) * 128, w2, L_DUMMY);
    if (tid < 448) touch((u32)(sp * 448 + tid) * 128, ssmk, L_DUMMY);
    if (b == 255) {
      __builtin_amdgcn_s_sleep(127);
      gflag_wait<64>(ws + (tid < 480 ? WS_FUT + tid * 128 : WS_FH + ((((tid < 704 ? tid - 480 : 223) / 14) * 2) * 56 + ((tid < 704 ? tid - 480 : 223) % 14) * 4) * 128), tag);
      BAR();
      if (tid == 0) gstore64(ws + WS_E, tag);
    }
    return;
  }

  const int cw = wid - 4, th = cw & 1, fq = cw >> 1;
  {
    const int lane = (int)opq(threadIdx.x) & 63, g = lane >> 4, hh = lane >> 5, l31 = lane & 31;
    const int q4 = (lane >> 2) & 3, p4 = lane & 3;
    const u32 aA = 16384 + (u32)((8 * hh + q4) * 256 + ((fq ^ q4) << 6) + (g & 1) * 32 + p4 * 8);
    const u32 aB = (u32)((64 * th + l31) * 128 + ((hh ^ ((l31 >> 1) & 7)) << 4));
    f32x16 acc[2][2] = {};
    s16x8 fa[2][2][2], fb[2][2][2];
    u32 pa, pb[4];
    auto setslot = [&](u32 slot) {
      pa = opq(aA + slot);
#pragma unroll
      for (int k = 0; k < 4; ++k) pb[k] = opq((aB ^ (u32)(k << 5)) + slot);
    };
    auto rd = [&](int set) {
#pragma unroll
      for (int k = 0; k < 2; ++k) {
        const int ks = 2 * set + k;
        fa[set][k][0] = lds_tr2(pa, ks * 4096, ks * 4096 + 1024);
        fb[set][k][0] = lds_b128(pb[ks], 0);
        fb[set][k][1] = lds_b128(pb[ks], 4096);
        fa[set][k][1] = lds_tr2(pa, 16384 + ks * 4096, 16384 + ks * 4096 + 1024);
      }
    };
    auto mm = [&](int set, int n0, int n1) {
#pragma unroll
      for (int n = n0; n < n1; ++n) acc[(n >> 1) & 1][n & 1] = mfma32(fa[set][n >> 2][(n >> 1) & 1], fb[set][n >> 2][n & 1], acc[(n >> 1) & 1][n & 1]);
    };
    setslot(0);
    const u32 fad = opq(L_FULL + (lane & 1) * 4);
#pragma unroll
    for (int i = 0; i < 4; ++i) asm volatile("" : "+v"(acc[i >> 1][i & 1]));
    __builtin_amdgcn_s_setprio(1);
    lflag_wait(L_FULL, 1, 1);
    rd(0);
    SCHED0();
    u32 slot = 0;
    auto stage = [&](int c, bool bar) {
      rd(1);
      const u32 fl = *(volatile LDSP(u32))fad;
      mm(0, 0, 6);
      SGB(0x008, 1); SGB(0x100, 3); SGB(0x008, 1); SGB(0x100, 3); SGB(0x008, 1); SGB(0x100, 3); SGB(0x008, 1); SGB(0x100, 4); SGB(0x008, 2);
      SCHED0();
      slot = slot == 2 * SLOT ? 0 : slot + SLOT;
      setslot(slot);
      SCHED0();
      mm(0, 6, 7);
      SCHED0();
      __builtin_amdgcn_s_waitcnt(0xC07F);
      const bool miss = c < 6 && __builtin_amdgcn_ballot_w64(fl < (u32)(c + 2));
      SCHED0();
      mm(0, 7, 8);
      SCHED0();
      if (bar) BAR();
      SCHED0();
      mm(1, 0, 2);
      SCHED0();
      if (!bar && c == 6) lflag_set(L_FREE + cw * 4, 7);
      if (miss) lflag_wait(L_FULL, 1, c + 2);
      SCHED0();
      rd(0); mm(1, 2, 8);
#pragma unroll
      for (int i = 0; i < 4; ++i) { SGB(0x008, 1); SGB(0x100, 3); }
      SGB(0x008, 2);
      SCHED0();
    };
#pragma unroll 1
    for (int c = 0; c < 4; ++c) stage(c, true);
#pragma unroll 1
    for (int c = 4; c < 7; ++c) stage(c, false);
    const u32 hdst = WS_H + (u32)((2 * panel + th) * 56 + 4 * ft + fq) * 4096 + lane * 16;
    const u32 sa = opq(L_STAT + (u32)(64 * th + l31) * 8), ca = opq(L_CSUM + (u32)(32 * fq + 4 * hh) * 4);
    f32x2 st[2]; f32x4 csa[2][2][2];
    auto rdc = [&]() {
#pragma unroll
      for (int j = 0; j < 2; ++j) st[j] = *(volatile LDSP(f32x2))((LDSP(char))sa + j * 256);
#pragma unroll
      for (int n = 0; n < 8; ++n) csa[n >> 2][(n >> 1) & 1][n & 1] = *(volatile LDSP(f32x4))((LDSP(char))ca + ((n >> 1) & 1) * 512 + (n >> 2) * 64 + (n & 1) * 32);
    };
    const u32 ef = *(volatile LDSP(u32))L_EPI;
    rdc();
    if (__builtin_amdgcn_readfirstlane(ef) == 0) { lflag_wait(L_EPI, 0, 1); rdc(); }
    float nm[2], c1[2], irs2[2];
#pragma unroll
    for (int j = 0; j < 2; ++j) { nm[j] = -st[j][0]; c1[j] = -1.44269504f * st[j][1]; irs2[j] = __builtin_amdgcn_rcpf(st[j][1] * st[j][1]); }
#pragma unroll
    for (int s = 0; s < 2; ++s) {
      const f32x4 (&cs)[2][2] = csa[s];
#pragma unroll
      for (int j = 0; j < 2; ++j) {
        float a[8], ex[8], hv[8];
#pragma unroll
        for (int e = 0; e < 8; ++e) a[e] = __builtin_fmaf(nm[j], cs[0][e >> 2][e & 3], acc[0][j][8 * s + e]);
#pragma unroll
        for (int e = 0; e < 8; ++e) ex[e] = a[e] * c1[j];
        SCHED0();
#pragma unroll
        for (int e = 0; e < 8; ++e) { ex[e] = __builtin_amdgcn_exp2f(ex[e]); hv[e] = __builtin_fmaf(nm[j], cs[1][e >> 2][e & 3], acc[1][j][8 * s + e]); }
        SCHED0();
#pragma unroll
        for (int e = 0; e < 8; ++e) ex[e] = __builtin_fmaf(ex[e], irs2[j], irs2[j]);
        SCHED0();
#pragma unroll
        for (int e = 0; e < 8; ++e) { ex[e] = __builtin_amdgcn_rcpf(ex[e]); hv[e] *= a[e]; }
        SCHED0();
#pragma unroll
        for (int e = 0; e < 8; ++e) hv[e] *= ex[e];
        st128_dev(rs, hdst + (s * 2 + j) * 1024, u32x4{pk2(hv[0], hv[1]), pk2(hv[2], hv[3]), pk2(hv[4], hv[5]), pk2(hv[6], hv[7])});
      }
    }
  }
  char* hflag = ws + WS_FH + ((2 * panel + th) * 56 + 4 * ft + fq) * 128;
  u32x4 xv, sv;
  if (cw < 4) {
    const int ht = (int)opq(threadIdx.x) & 255, lane = ht & 63, l15 = ht & 15;
    const u64 tag = tag_of(*(volatile LDSP(u64))L_TAG);
    const int ch = cw >> 1, var = cw & 1;
    gflag_publish(hflag, tag, lane);
    lflag_set(L_PUBA + cw * 4, 1);
    u32x4 xr[2]; u32x2 st[2];
#pragma unroll
    for (int i = 0; i < 2; ++i) {
      const int n = ht + 256 * i, row = n >> 3, c = n & 7;
      xr[i] = *(const u32x4*)(x + (u32)((64 * tau + row) * (D * 2) + (64 * dl + 8 * c) * 2));
      st[i] = ld64_dev(rs, WS_STAT + (u32)(64 * tau + row) * 8);
    }
    SCHED0();
    conv_compute(lane, cw, d0 + ch, rs);
    u32x4 xn[2];
#pragma unroll
    for (int i = 0; i < 2; ++i) {
      const float mb = asf(st[i][0]), rsd = asf(st[i][1]);
#pragma unroll
      for (int e = 0; e < 4; ++e) {
        const u32 dd = pk2(bflo(xr[i][e]) - mb, bfhi(xr[i][e]) - mb);
        xn[i][e] = pk2(bflo(dd) * rsd, bfhi(dd) * rsd);
      }
    }
    asm volatile("" : "+v"(xn[0]), "+v"(xn[1]));
    SCHED0();
    gflag_publish(ws + WS_FY + (var * 448 + d0 + ch) * 128, tag, lane);
    xv = xr[0]; sv = xn[0];
    lflag_wait(L_XCH, 1, 1);
    lds_st128(E_S + (u32)ht * 16, xn[1]); lds_st128(E_X + (u32)ht * 16, xr[1]);
    gflag_wait<4>(ws + WS_FY + ((tau >> 4) * 448 + 64 * dl + 16 * cw + l15) * 128, tag);
#pragma unroll
    for (int i = 0; i < 2; ++i)
      glds16_dev((u32)((lane >> 3) * (S * 2) + (((lane & 7) ^ (2 * cw + i)) << 4)),
                 ws + WS_YT + (64 * dl + 16 * cw + 8 * i) * (S * 2) + 128 * tau, xbuf(cw ^ 1) + (u32)i * 1024);
    WAIT_VM(0);
  } else {
    const int j = cw - 4;
    const int lane = (int)opq(threadIdx.x) & 63;
    lflag_set(L_PUB + j * 4, 1);
    const u64 btag = tag_of(*(volatile LDSP(u64))L_TAG);
    const bool hready = __builtin_amdgcn_readfirstlane(*(volatile LDSP(u32))(L_H + 4)) != 0;
    const u32 hl = lane * 16;
    const char* hbase = ws + WS_H + (size_t)tau * 56 * 4096;
    const int q4 = (lane >> 2) & 3;
    const u32 ta = (u32)((4 * (lane >> 5) + q4) * 128 + ((q4 >> 1) << 6) + ((lane >> 4) & 1) * 32 + (lane & 3) * 8);
    s16x8 fa[2][4], fb[3][4];
    f32x16 acc[2][2] = {};
    auto ldh = [&](int i) {
      const char* p = hbase + (size_t)g2_ks(j, i) * 4096;
      asm volatile("global_load_dwordx4 %0, %4, %5 sc1\n\tglobal_load_dwordx4 %1, %4, %5 offset:1024 sc1\n\t"
                   "global_load_dwordx4 %2, %4, %5 offset:2048 sc1\n\tglobal_load_dwordx4 %3, %4, %5 offset:3072 sc1"
                   : "=&v"(fb[i % 3][0]), "=&v"(fb[i % 3][1]), "=&v"(fb[i % 3][2]), "=&v"(fb[i % 3][3])
                   : "v"(hl), "s"(p) : "memory");
    };
    u32 tb[2];
#pragma unroll
    for (int a = 0; a < 2; ++a) tb[a] = opq((ta ^ (u32)(a << 6)) + g2_place(j, 0));
    auto rda = [&](int i) {
#pragma unroll
      for (int a = 0; a < 2; ++a)
        if (i > 0 && i % 3 == 0) tb[a] = opq(i % 6 ? tb[a] - 2 * SLOT : tb[a] + 2 * SLOT);
#pragma unroll
      for (int n = 0; n < 4; ++n) {
        const int o = (i % 3) * 4096 + (n >> 1) * 2048;
        fa[i & 1][n] = lds_tr2(tb[n & 1], o, o + 1024);
      }
    };
    const u32 fed = opq(L_FED + j * 4), prog = opq(L_PROG + j * 4);
    if (!hready) { gflag_publish(hflag, btag, lane); lflag_set(L_PUB + j * 4, 2); lflag_wait(L_H + 4, 0, 1); }
#pragma unroll
    for (int i = 0; i < 3; ++i) ldh(i);
    if (hready) {
      WAIT_VM(12);
      if (lane == 0) gstore64(hflag, btag);
      lflag_set(L_PUB + j * 4, 2);
    }
    lflag_wait(L_FED + j * 4, 0, 3);
    rda(0);
    SCHED0();
    u32 fl = 0;
#pragma unroll
    for (int i = 0; i < 14; ++i) {
      if (i == 2 || (i >= 5 && i < 13 && (i & 1))) {
        if (__builtin_amdgcn_readfirstlane(fl) < i + 2) lflag_wait(L_FED + j * 4, 0, i + 2);
      }
      asm volatile("s_waitcnt vmcnt(%4)" : "+v"(fb[i % 3][0]), "+v"(fb[i % 3][1]), "+v"(fb[i % 3][2]), "+v"(fb[i % 3][3])
                   : "i"(i < 12 ? 8 : (13 - i) * 4) : "memory");
      SCHED0();
      if (i < 13) rda(i + 1);
      if (i == 1 || (i >= 4 && i < 12 && !(i & 1))) fl = *(volatile LDSP(u32))fed;
      if (i < 13) {
#pragma unroll
        for (int s = 0; s < 2; ++s)
#pragma unroll
          for (int a = 0; a < 2; ++a)
#pragma unroll
            for (int t = 0; t < 2; ++t) acc[a][t] = mfma32(fa[i & 1][2 * s + a], fb[i % 3][2 * s + t], acc[a][t]);
#pragma unroll
        for (int n = 0; n < 5; ++n) { SGB(0x008, 1); SGB(0x100, 2); }
        SGB(0x008, 3);
      } else {
        __builtin_amdgcn_s_waitcnt(0xC07F);
        const int ln = (int)opq(threadIdx.x), lt = ln & 31, lh = (ln >> 5) & 1;
#pragma unroll
        for (int n = 0; n < 5; ++n) {
          if (n < 4) {
#pragma unroll
            for (int s = 0; s < 2; ++s) acc[n & 1][n >> 1] = mfma32(fa[i & 1][2 * s + (n & 1)], fb[i % 3][2 * s + (n >> 1)], acc[n & 1][n >> 1]);
          }
          SCHED0();
          if (n > 0) {
            const int a = (n - 1) & 1, t = (n - 1) >> 1;
#pragma unroll
            for (int G = 0; G < 4; ++G)
              *(LDSP(f32x4))(g2_place(j, t ? 3 : 0) + (u32)(lt * 256 + (((8 * a + 2 * G + lh) ^ (lt & 7)) << 4))) =
                  f32x4{acc[a][t][4 * G], acc[a][t][4 * G + 1], acc[a][t][4 * G + 2], acc[a][t][4 * G + 3]};
          }
          SCHED0();
        }
      }
      SCHED0();
      if (i < 8 && (i & 1)) *(volatile LDSP(u32))prog = i + 1;
      if (i == 4) lflag_wait(L_H, 0, 1);
      if (i + 3 < 14) ldh(i + 3);
      SCHED0();
    }
  }
  {
    const int ht = (int)opq(threadIdx.x) & 255;
    const int half = cw >> 2, tk = ht >> 3, dg = ht & 7, tok = 32 * half + tk;
    const u32 pa0 = opq((half ? 0u : 2 * SLOT) + (u32)(tk * 256 + (((2 * dg) ^ (tk & 7)) << 4))), pa1 = opq(pa0 ^ 16u);
    const u32 ya = opq(xbuf((dg >> 1) ^ 1) + (u32)(8 * (dg & 1) * 128 + (((tok >> 3) ^ dg) << 4) + (tok & 7) * 2));
    const u32 ea = opq(E_S + (u32)ht * 16);
    const u32 oo = opq((u32)((64 * tau + tok) * (D * 2) + (64 * dl + 8 * dg) * 2));
    WAIT_LGKM0();
    BAR();
    f32x4 p0[4], p1[4];
    u32 yv[8];
#pragma unroll
    for (int w = 0; w < 4; ++w) {
      p0[w] = *(LDSP(f32x4))((LDSP(char))pa0 + w * 12288);
      p1[w] = *(LDSP(f32x4))((LDSP(char))pa1 + w * 12288);
    }
#pragma unroll
    for (int e = 0; e < 8; ++e) yv[e] = *(LDSP(u16))((LDSP(char))ya + e * 128);
    SCHED0();
    if (half) { sv = *(LDSP(u32x4))ea; xv = *(LDSP(u32x4))((LDSP(char))ea + (E_X - E_S)); }
    const f32x4 s0 = (p0[0] + p0[1]) + (p0[2] + p0[3]), s1 = (p1[0] + p1[1]) + (p1[2] + p1[3]);
    u32 f[4];
    float ex[8];
#pragma unroll
    for (int e = 0; e < 4; ++e) f[e] = e < 2 ? pk2(s0[2 * e], s0[2 * e + 1]) : pk2(s1[2 * e - 4], s1[2 * e - 3]);
#pragma unroll
    for (int e = 0; e < 8; ++e) ex[e] = __builtin_amdgcn_exp2f(-1.44269504f * ((e & 1) ? bfhi(f[e >> 1]) : bflo(f[e >> 1])));
    SCHED0();
    u32 se[4];
#pragma unroll
    for (int e = 0; e < 4; ++e) se[e] = pk2(bflo(sv[e]) * bflo(yv[2 * e]), bfhi(sv[e]) * bflo(yv[2 * e + 1]));
#pragma unroll
    for (int e = 0; e < 8; ++e) ex[e] = __builtin_amdgcn_rcpf(1.0f + ex[e]);
    SCHED0();
    u32x4 o;
#pragma unroll
    for (int e = 0; e < 4; ++e) {
      const u32 gt = pk2(bflo(f[e]) * ex[2 * e], bfhi(f[e]) * ex[2 * e + 1]);
      const u32 p = pk2(bflo(se[e]) * bflo(gt), bfhi(se[e]) * bfhi(gt));
      o[e] = pk2(bflo(xv[e]) + bflo(p), bfhi(xv[e]) + bfhi(p));
    }
    __builtin_amdgcn_raw_buffer_store_b128(o, __builtin_amdgcn_make_buffer_rsrc(out, 0, S * D * 2, 0x00020000), oo, 0, 16);
  }
}
}
#if defined(__HIP_DEVICE_COMPILE__) && __HIP_DEVICE_COMPILE__
#pragma clang attribute pop
#endif

extern "C" void kernel_launch(void* const* d_in, const int* in_sizes, int n_in, void* d_out, int out_size, void* d_ws,
                              size_t ws_size, hipStream_t stream) {
  (void)hipFuncSetAttribute((const void*)fused_kernel, hipFuncAttributeMaxDynamicSharedMemorySize, 163840);
  fused_kernel<<<256, 768, 163840, stream>>>((const char*)d_in[0], (const char*)d_in[1], (const char*)d_in[2], (const char*)d_in[3],
                                             (const char*)d_in[4], (const char*)d_in[5], (const char*)d_in[7], (const char*)d_in[8],
                                             (const char*)d_in[9], (char*)d_ws, (char*)d_out);
}
```

```cpp
#include <hip/hip_runtime.h>
#include <hip/hip_bf16.h>
#include <stdint.h>
#pragma clang diagnostic ignored "-Wint-to-pointer-cast"

#if defined(__HIP_DEVICE_COMPILE__) && __HIP_DEVICE_COMPILE__
#pragma clang attribute push(__attribute__((target("no-packed-fp32-ops"))), apply_to = function)
#endif
namespace {
typedef unsigned short u16;
typedef unsigned int u32;
typedef unsigned long long u64;
typedef __attribute__((ext_vector_type(4))) short s16x4;
typedef __attribute__((ext_vector_type(8))) short s16x8;
typedef __attribute__((ext_vector_type(8))) __bf16 bf16x8;
typedef __attribute__((ext_vector_type(2))) __bf16 bf16x2;
typedef __attribute__((ext_vector_type(16))) float f32x16;
typedef __attribute__((ext_vector_type(4))) float f32x4;
typedef __attribute__((ext_vector_type(2))) float f32x2;
typedef __attribute__((ext_vector_type(4))) u32 u32x4;
typedef __attribute__((ext_vector_type(2))) u32 u32x2;
#define LDSP(T) __attribute__((address_space(3))) T*
#define GLBP(T) __attribute__((address_space(1))) T*
#define DEV __device__ __forceinline__

constexpr int S = 2048, D = 448, F = 1792;
constexpr u32 WS_H = 0;
constexpr u32 WS_UT = WS_H + 7340032;
constexpr u32 WS_YT = WS_UT + 1835008;
constexpr u32 WS_STAT = WS_YT + 1835008;
constexpr u32 WS_CSUM = WS_STAT + 16384;
constexpr u32 WS_E = WS_CSUM + 16384;
constexpr u32 WS_FUT = WS_E + 128;
constexpr u32 WS_FST = WS_FUT + 448 * 128;
constexpr u32 WS_FY = WS_FST + 32 * 128;
constexpr u32 WS_FH = WS_FY + 896 * 128;
constexpr u32 WS_END = WS_FH + 1792 * 128;
constexpr u32 SLOT = 49152;
constexpr u32 MISC = 3 * SLOT;
constexpr u32 L_FULL = MISC, L_EPI = MISC + 8, L_H = MISC + 16, L_FREE = MISC + 32, L_CONV = MISC + 64, L_FED = MISC + 80,
              L_TAG = MISC + 96, L_DD = MISC + 104, L_PROG = MISC + 128, L_XCH = MISC + 144, L_PUB = MISC + 160, L_PUBA = MISC + 176, L_STAT = MISC + 256,
              L_CSUM = MISC + 1280, L_USCR = MISC + 4096, L_DUMMY = MISC + 2304, L_XB2 = MISC + 12288;
constexpr u32 CV = SLOT, CV_HSTR = 4160, CV_U = CV + 8 * CV_HSTR, CV_USTR = 4608;
constexpr u32 E_S = SLOT, E_X = SLOT + 4096;
constexpr int SPIN_CAP = 1 << 18;

__device__ const u32 seq_tab[160] __attribute__((aligned(128))) = {0, 1, 2, 3, 4, 5, 6, 7};
DEV float asf(u32 v) { return __builtin_bit_cast(float, v); }
DEV float bflo(u32 v) { return asf(v << 16); }
DEV float bfhi(u32 v) { return asf(v & 0xffff0000u); }
template <int CTRL> DEV float dpp(float v) { return asf(__builtin_amdgcn_update_dpp(0, __builtin_bit_cast(u32, v), CTRL, 0xf, 0xf, true)); }
DEV float sum8(float v) { v += dpp<0xB1>(v); v += dpp<0x4E>(v); return v + dpp<0x141>(v); }
DEV u32 pk2(float lo, float hi) {
  asm("" : "+v"(lo)); asm("" : "+v"(hi));
  bf16x2 r = {(__bf16)lo, (__bf16)hi};
  u32 v = __builtin_bit_cast(u32, r);
  asm("" : "+v"(v));
  return v;
}
DEV float rbf(float f) { bf16x2 r = {(__bf16)f, (__bf16)f}; return bflo(__builtin_bit_cast(u32, r)); }
DEV f32x4 mfma16(s16x8 a, s16x8 b, f32x4 c) {
  return __builtin_amdgcn_mfma_f32_16x16x32_bf16(__builtin_bit_cast(bf16x8, a), __builtin_bit_cast(bf16x8, b), c, 0, 0, 0);
}
DEV f32x16 mfma32(s16x8 a, s16x8 b, f32x16 c) {
  return __builtin_amdgcn_mfma_f32_32x32x16_bf16(__builtin_bit_cast(bf16x8, a), __builtin_bit_cast(bf16x8, b), c, 0, 0, 0);
}
DEV u32 opq(u32 v) { asm volatile("" : "+v"(v)); return v; }
DEV s16x8 lds_tr2(u32 base, int o0, int o1) {
  s16x4 lo = __builtin_amdgcn_ds_read_tr16_b64_v4i16((LDSP(s16x4))((LDSP(char))base + o0));
  s16x4 hi = __builtin_amdgcn_ds_read_tr16_b64_v4i16((LDSP(s16x4))((LDSP(char))base + o1));
  return __builtin_shufflevector(lo, hi, 0, 1, 2, 3, 4, 5, 6, 7);
}
DEV s16x8 lds_b128(u32 base, int o) { return *(LDSP(s16x8))((LDSP(char))base + o); }
DEV void lds_st128(u32 a, u32x4 v) { *(LDSP(u32x4))a = v; }
DEV void glds16x4(u32 v0, u32 v1, u32 v2, u32 v3, const void* sbase, u32 ldst) {
  asm volatile("s_mov_b32 m0, %5\n\ts_nop 0\n\tglobal_load_lds_dwordx4 %0, %4\n\tglobal_load_lds_dwordx4 %1, %4 offset:1024\n\t"
               "global_load_lds_dwordx4 %2, %4 offset:2048\n\tglobal_load_lds_dwordx4 %3, %4 offset:3072"
               ::"v"(v0), "v"(v1), "v"(v2), "v"(v3), "s"(sbase), "s"(ldst) : "memory");
}
DEV void glds16(u32 voff, const void* sbase, u32 ldst) {
  asm volatile("s_mov_b32 m0, %2\n\ts_nop 0\n\tglobal_load_lds_dwordx4 %0, %1" ::"v"(voff), "s"(sbase), "s"(ldst) : "memory");
}
DEV void glds16x4_dev(u32 v, const void* sbase, u32 ldst) {
  asm volatile("s_mov_b32 m0, %2\n\ts_nop 0\n\tglobal_load_lds_dwordx4 %0, %1 sc1\n\tglobal_load_lds_dwordx4 %0, %1 offset:1024 sc1\n\t"
               "global_load_lds_dwordx4 %0, %1 offset:2048 sc1\n\tglobal_load_lds_dwordx4 %0, %1 offset:3072 sc1"
               ::"v"(v), "s"(sbase), "s"(ldst) : "memory");
}
DEV void glds16_dev(u32 voff, const void* sbase, u32 ldst) {
  asm volatile("s_mov_b32 m0, %2\n\ts_nop 0\n\tglobal_load_lds_dwordx4 %0, %1 sc1" ::"v"(voff), "s"(sbase), "s"(ldst) : "memory");
}
DEV void dflag_set(u32 addr, u32 val) {
  asm volatile("s_waitcnt lgkmcnt(0)\n\ts_mov_b32 m0, %2\n\ts_mov_b64 exec, 1\n\tglobal_load_lds_dword %0, %1\n\ts_mov_b64 exec, -1"
               ::"v"(0), "s"(seq_tab + val), "s"(addr) : "memory");
}
#define WAIT_VM(n) asm volatile("s_waitcnt vmcnt(%0)" ::"i"(n) : "memory")
#define WAIT_LGKM0() asm volatile("s_waitcnt lgkmcnt(0)" ::: "memory")
#define BAR() do { asm volatile("" ::: "memory"); __builtin_amdgcn_s_barrier(); asm volatile("" ::: "memory"); } while (0)
#define SCHED0() __builtin_amdgcn_sched_barrier(0)
#define SGB(mask, n) __builtin_amdgcn_sched_group_barrier(mask, n, 0)

DEV void lflag_set(u32 addr, u32 val) {
  asm volatile("s_waitcnt lgkmcnt(0)\n\tds_write_b32 %0, %1" ::"v"(addr), "v"(val) : "memory");
}
DEV void lflag_wait(u32 addr, u32 mask, u32 val) {
  const u32 a = addr + 4 * (threadIdx.x & mask);
#pragma unroll 1
  for (int i = 0; i < SPIN_CAP; ++i) {
    u32 v;
    asm volatile("ds_read_b32 %0, %1\n\ts_waitcnt lgkmcnt(0)" : "=v"(v) : "v"(a) : "memory");
    if (__builtin_amdgcn_ballot_w64(v < val) == 0) break;
    __builtin_amdgcn_s_sleep(1);
  }
}
DEV u64 gload64(const char* p) { return __hip_atomic_load((GLBP(u64))p, __ATOMIC_RELAXED, __HIP_MEMORY_SCOPE_AGENT); }
DEV void gstore64(char* p, u64 v) { __hip_atomic_store((GLBP(u64))p, v, __ATOMIC_RELAXED, __HIP_MEMORY_SCOPE_AGENT); }
template <int SLEEP>
DEV void gflag_wait(const char* lane_ptr, u64 tag) {
  bool ok = false;
#pragma unroll 1
  for (int i = 0; i < SPIN_CAP; ++i) {
    asm volatile("" ::: "memory");
    if (!ok) ok = gload64(lane_ptr) == tag;
    if (__builtin_amdgcn_ballot_w64(!ok) == 0) break;
    __builtin_amdgcn_s_sleep(SLEEP);
  }
  asm volatile("" ::: "memory");
}
DEV void gflag_publish(char* p, u64 tag, int lane) {
  WAIT_VM(0);
  if (lane == 0) gstore64(p, tag);
}
DEV u64 tag_of(u64 seed) { return seed * 0x9E3779B97F4A7C15ull + 0xD1B54A32D192ED03ull; }
DEV u64 make_tag(const char* ws) { return tag_of(gload64(ws + WS_E)); }
typedef __amdgpu_buffer_rsrc_t rsrc_t;
DEV rsrc_t ws_rsrc(char* ws) { return __builtin_amdgcn_make_buffer_rsrc(ws, 0, WS_END, 0x00020000); }
DEV void st128_dev(rsrc_t r, u32 off, u32x4 v) { __builtin_amdgcn_raw_buffer_store_b128(v, r, off, 0, 16); }
DEV void st64_dev(rsrc_t r, u32 off, u32x2 v) { __builtin_amdgcn_raw_buffer_store_b64(v, r, off, 0, 16); }
DEV u32x2 ld64_dev(rsrc_t r, u32 off) { return __builtin_amdgcn_raw_buffer_load_b64(r, off, 0, 16); }
DEV void touch(u32 voff, const void* sbase, u32 ldst) {
  asm volatile("s_mov_b32 m0, %2\n\ts_nop 0\n\tglobal_load_lds_dword %0, %1" ::"v"(voff), "s"(sbase), "s"(ldst) : "memory");
}

DEV void glds4_dev(u32 voff, const void* sbase, u32 ldst) {
  asm volatile("s_mov_b32 m0, %2\n\ts_nop 0\n\tglobal_load_lds_dword %0, %1 sc1" ::"v"(voff), "s"(sbase), "s"(ldst) : "memory");
}

DEV u32 xbuf(int w) { return w < 2 ? L_USCR + (u32)w * 4096 : (w == 2 ? L_XB2 : CV + 45056); }
DEV void conv_compute(int lane, int cw, int d, rsrc_t rs) {
  const int ch = cw >> 1, var = cw & 1;
  const int m = lane & 15, g = lane >> 4, q4 = m >> 2, p4 = lane & 3;
  u32 aa = opq(CV + (u32)(ch * 4 + ((q4 + 1) & 3)) * CV_HSTR + (u32)(((q4 + 1) & 4) + 8 * g + 4 * p4) * 2 + var * 256);
  u32 ba = opq(CV_U + (u32)ch * CV_USTR + (u32)(240 - 16 * m + 8 * g) * 2 + var * 256);
  f32x4 acc[8] = {};
  s16x8 A[8], B[8];
  const u32 a0 = opq(CV + (u32)ch * 4 * CV_HSTR + (u32)(m + 1 + 8 * g) * 2);
  u32x4 msk;
#pragma unroll
  for (int i = 0; i < 4; ++i) { const int e = m + 8 * g + 2 * i - 15; msk[i] = e >= 0 ? ~0u : (e == -1 ? 0xffff0000u : 0u); }
#pragma unroll
  for (int i = 0; i < 8; ++i) asm volatile("" : "+v"(acc[i]));
  asm volatile("" : "+v"(msk));
  lflag_wait(L_CONV, 3, 1);
  u32 h0[8];
#pragma unroll
  for (int j = 0; j < 8; ++j) h0[j] = *(LDSP(u16))((LDSP(char))a0 + j * 2);
  SCHED0();
#pragma unroll
  for (int p = 0; p < 8; ++p) { A[7 - p] = lds_tr2(aa, (7 - p) * 512, (7 - p) * 512 + 8); B[p] = lds_b128(ba, (7 - p) * 512); }
  SCHED0();
  if (var == 0) {
    u32x4 f;
#pragma unroll
    for (int i = 0; i < 4; ++i) f[i] = (h0[2 * i] | (h0[2 * i + 1] << 16)) & msk[i];
    A[0] = __builtin_bit_cast(s16x8, f);
  }
  SCHED0();
#pragma unroll 1
  for (int it = 0; it < 4; ++it) {
#pragma unroll
    for (int p = 7; p >= 0; --p) {
#pragma unroll
      for (int dd = 0; dd <= 7 - p; ++dd) {
        acc[p + dd] = mfma16(A[p], B[dd], acc[p + dd]);
        if (p == 0) { SCHED0(); B[dd] = lds_b128(ba, 64 + (7 - dd) * 512); SCHED0(); }
      }
      SCHED0();
      A[p] = lds_tr2(aa, 64 + p * 512, 64 + p * 512 + 8);
      SCHED0();
    }
    aa = opq(aa + 64); ba = opq(ba + 64);
  }
  f32x4 y[4];
#pragma unroll
  for (int i = 0; i < 4; ++i) {
    *(LDSP(f32x4))(xbuf(cw) + (u32)(i * 1024 + lane * 16)) = var ? acc[i] : acc[4 + i];
    y[i] = var ? acc[4 + i] : acc[i];
  }
  lflag_set(L_XCH + cw * 4, 1);
  const u32 dv = *(LDSP(u32))L_DD;
  const float Dd = ch ? bfhi(dv) : bflo(dv);
  u32x2 uu[4];
#pragma unroll
  for (int i = 0; i < 4; ++i) uu[i] = *(LDSP(u32x2))(CV_U + (u32)ch * CV_USTR + (u32)(2044 - (1024 * var + 256 * i + 16 * m + 4 * g)) * 2);
  u32x2 sk[4];
#pragma unroll
  for (int i = 0; i < 4; ++i) sk[i] = u32x2{pk2(bfhi(uu[i][1]) * Dd, bflo(uu[i][1]) * Dd), pk2(bfhi(uu[i][0]) * Dd, bflo(uu[i][0]) * Dd)};
  const u32 xa = opq(xbuf(cw ^ 1) + (u32)lane * 16), yo = opq(WS_YT + (u32)(d * (S * 2) + (1024 * var + 16 * m + 4 * g) * 2));
  SCHED0();
  lflag_wait(L_XCH + (cw ^ 1) * 4, 0, 1);
  f32x4 pv[4];
#pragma unroll
  for (int i = 0; i < 4; ++i) pv[i] = *(LDSP(f32x4))((LDSP(char))xa + i * 1024);
  SCHED0();
#pragma unroll
  for (int i = 0; i < 4; ++i) {
    const f32x4 s = y[i] + pv[i];
    const u32 c0 = pk2(s[0], s[1]), c1 = pk2(s[2], s[3]);
    st64_dev(rs, yo + i * 512, u32x2{pk2(bflo(c0) + bflo(sk[i][0]), bfhi(c0) + bfhi(sk[i][0])), pk2(bflo(c1) + bflo(sk[i][1]), bfhi(c1) + bfhi(sk[i][1]))});
  }
}

DEV int g2_ks(int j, int i) { const int e = 7 * j + (i < 7 ? i : i - 7); return 4 * (e >> 1) + (e & 1) + (i < 7 ? 0 : 2); }
DEV u32 g2_place(int j, int p) { return (p < 3 ? 2 * SLOT + (u32)p * 4096 : (u32)(p - 3) * 4096) + (u32)j * 12288; }
DEV u32 w2_voff(int lane) { return (u32)((lane >> 3) * (D * 2) + (((lane & 7) ^ (((lane >> 4) & 1) << 2)) << 4)); }

DEV void share(int b, int n, const char* base) {
  const int lane = (int)opq(threadIdx.x) & 63;
  if (lane < n) touch((u32)(b * n + lane) * 128, base, L_DUMMY);
}

__global__ __launch_bounds__(768, 3) void fused_kernel(const char* x, const char* kp, const char* vp, const char* skp,
                                                       const char* ssmk, const char* ssmD, const char* w1, const char* w3,
                                                       const char* w2, char* ws, char* out) {
  const int b = blockIdx.x, xcd = b & 7, r = b >> 3;
  const int wid = __builtin_amdgcn_readfirstlane(threadIdx.x >> 6);
  const int panel = (xcd >> 1) * 4 + r / 7, ft = (xcd & 1) * 7 + r % 7;
  const int tau = xcd * 4 + r / 7, dl = r % 7;
  const int d0 = 2 * b;

  if (b < 224) {
    if (wid >= 4 && wid < 7) share(b, wid == 4 ? 64 : 56, wid == 4 ? x : (wid == 5 ? w1 : w3));
    if (wid == 7) { *(LDSP(u32))(MISC + (threadIdx.x & 63) * 4) = 0; WAIT_LGKM0(); }
    BAR();
    if (wid < 2) {
      const int lane = (int)opq(threadIdx.x) & 63;
      const int g = lane >> 4, l15 = lane & 15;
      u32 vx[8], vw[8];
      const u32 ox = (u32)((lane >> 3) * (D * 2) + (((lane & 7) ^ g) << 4));
      const u32 ow = (u32)(g * (F * 2) + (((l15 >> 2) ^ g) << 6) + ((lane & 3) << 4));
#pragma unroll
      for (int i = 0; i < 8; ++i) {
        vx[i] = (ox ^ (u32)((i & 1) * 64)) + i * 8 * (D * 2) - (i & 3) * 1024;
        vw[i] = ow + i * 4 * (F * 2) - (i & 3) * 1024;
      }
#pragma unroll 1
      for (int c = 0; c < 7; ++c) {
        if (c >= 3) BAR();
        const u32 slot = (u32)(c % 3) * SLOT + wid * 8192;
        const char* xb = x + (size_t)(panel * 128 + wid * 64) * (D * 2) + c * 128;
        const char* w1b = w1 + (size_t)(c * 64 + wid * 32) * (F * 2) + ft * 256;
        const char* w3b = w3 + (size_t)(c * 64 + wid * 32) * (F * 2) + ft * 256;
#pragma unroll
        for (int i = 0; i < 8; i += 4) {
          glds16x4(vx[i], vx[i + 1], vx[i + 2], vx[i + 3], xb, slot + i * 1024);
          glds16x4(vw[i], vw[i + 1], vw[i + 2], vw[i + 3], w1b, slot + 16384 + i * 1024);
          glds16x4(vw[i], vw[i + 1], vw[i + 2], vw[i + 3], w3b, slot + 32768 + i * 1024);
        }
        dflag_set(L_FULL + wid * 4, c + 1);
      }
    }
    if (wid == 7 && (threadIdx.x & 63) < 5) touch((threadIdx.x & 63) * 128, seq_tab, L_DUMMY);
  }

  if (__builtin_expect(wid < 4 && b < 224, 1)) {
    const int lane = (int)opq(threadIdx.x) & 63;
    u64 tag = 0;
    if (wid < 2) {
      if (wid == 0) {
        tag = make_tag(ws);
        const int k = lane & 7;
        gflag_wait<8>(ws + WS_FST + (k < 2 ? 2 * panel + k : (k == 2 ? tau : (k & 1) * 14 + ft)) * 128, tag);
        glds16_dev(lane * 16, ws + WS_STAT + panel * 1024, L_STAT);
        glds16_dev(lane * 16, ws + WS_CSUM + ft * 1024, L_CSUM);
        if (lane < 2) glds4_dev(lane * 4, ws + WS_E, L_TAG);
        if (lane < 1) glds4_dev(0, ssmD + d0 * 2, L_DD);
        dflag_set(L_EPI, 1);
      }
    } else {
      __builtin_amdgcn_s_setprio(3);
      const rsrc_t rs = ws_rsrc(ws);
      const int u = wid - 2;
      const int ti = b / 7, cj = b % 7;
      const int tg = lane >> 3, cg = lane & 7;
      const int t0 = 64 * ti + 32 * u + 4 * tg, ch0 = 64 * cj + 8 * cg;
      u32x4 kr[7], vr[4], sw[4]; u32x2 o[8];
      u64 seed;
      auto ldkv = [&]() {
        const u32 ko = (u32)(t0 * (D * 2) + ch0 * 2);
#pragma unroll
        for (int i = 0; i < 7; ++i) kr[i] = *(const u32x4*)(kp + (i < 3 && t0 == 0 ? ko : ko + (u32)((i - 3) * (D * 2))));
#pragma unroll
        for (int i = 0; i < 4; ++i) vr[i] = *(const u32x4*)(vp + (ko + (u32)(i * (D * 2))));
        SCHED0();
      };
      auto piece = [&](int p) {
#pragma unroll
        for (int c = 2 * p; c < 2 * p + 2; ++c) {
          const u32 wa = sw[p][(c & 1) * 2], wb = sw[p][(c & 1) * 2 + 1];
          const float w0 = bflo(wa), w1f = bfhi(wa), w2f = bflo(wb), w3f = bfhi(wb);
          float kf[7], ks[4];
#pragma unroll
          for (int i = 0; i < 7; ++i) kf[i] = (c & 1) ? bfhi(kr[i][p]) : bflo(kr[i][p]);
#pragma unroll
          for (int i = 0; i < 4; ++i)
            ks[i] = __builtin_fmaf(kf[i + 3], w0, __builtin_fmaf(kf[i + 2], w1f, __builtin_fmaf(kf[i + 1], w2f, kf[i] * w3f)));
#pragma unroll
          for (int i = 0; i < 2; ++i) {
            const int ta = 3 - 2 * i, tb = 2 - 2 * i;
            const u32 kk = pk2(ks[ta], ks[tb]);
            o[c][i] = pk2(bflo(kk) * ((c & 1) ? bfhi(vr[ta][p]) : bflo(vr[ta][p])), bfhi(kk) * ((c & 1) ? bfhi(vr[tb][p]) : bflo(vr[tb][p])));
          }
          asm volatile("" : "+v"(o[c]));
        }
        SCHED0();
      };
      BAR();
      ldkv();
#pragma unroll
      for (int i = 0; i < 4; ++i) sw[i] = *(const u32x4*)(skp + (u32)(ch0 * 8 + i * 16));
      seed = gload64(ws + WS_E + opq(0));
      if (u && lane < 14) {
        const u32 fo = lane < 8 ? WS_FH + (u32)((2 * panel + (lane & 1)) * 56 + 4 * ft + (lane >> 1)) * 128
                                : (lane < 12 ? WS_FY + (u32)((lane & 1) * 448 + d0 + ((lane >> 1) & 1)) * 128 : WS_FUT + (u32)(cj * 64 + 2 * ti + (lane & 1)) * 128);
        touch(fo, ws, L_DUMMY);
      }
      SCHED0();
      BAR();
      SCHED0();
      BAR();
      SCHED0();
#pragma unroll
      for (int i = 0; i < 3; ++i) if (t0 == 0) kr[i] = u32x4{0, 0, 0, 0};
      piece(0);
      BAR();
      SCHED0();
#pragma unroll
      for (int c = 1; c < 4; ++c) piece(c);
      tag = tag_of(seed);
      const u32 scr = L_USCR + (u32)u * 4096;
#pragma unroll
      for (int c = 0; c < 8; ++c) *(LDSP(u32x2))(scr + (u32)((cg * 8 + c) * 64 + (((7 - tg) ^ (cg & 6)) << 3))) = o[c];
      WAIT_LGKM0();
#pragma unroll
      for (int i = 0; i < 4; ++i) {
        const int dd = 16 * i + (lane >> 2), c4 = lane & 3;
        u32x4 v = *(LDSP(u32x4))(scr + (u32)(dd * 64 + ((c4 ^ (dd >> 4)) << 4)));
        st128_dev(rs, WS_UT + (u32)((64 * cj + dd) * (S * 2) + (2016 - 64 * ti - 32 * u) * 2 + c4 * 16), v);
      }
      gflag_publish(ws + WS_FUT + (cj * 64 + 2 * ti + u) * 128, tag, lane);
    }
    const int j = wid;
    const u32 o2 = w2_voff(lane);
    const char* wbase = w2 + dl * 128;
    u32x4 pk[8][4];
    auto park = [&](int i0, int i1) {
#pragma unroll
      for (int i = i0; i < i1; ++i) {
        const char* src = wbase + (size_t)g2_ks(j, i) * 32 * (D * 2);
        asm volatile("" ::: "memory");
#pragma unroll
        for (int pc = 0; pc < 4; ++pc) pk[i & 7][pc] = *(const u32x4*)(src + (o2 + (u32)(pc * 8 * (D * 2))));
      }
      SCHED0();
    };
    park(0, 6);
    if (wid == 2) {
      gflag_wait<8>(ws + WS_FUT + ((d0 >> 6) * 64 + lane) * 128, tag);
      lflag_wait(L_FREE, 7, 7);
      glds16x4_dev(lane * 16, ws + WS_UT + d0 * (S * 2), CV_U);
      glds16x4_dev(lane * 16, ws + WS_UT + (d0 + 1) * (S * 2), CV_U + CV_USTR);
      if (lane < 32) {
        glds16(lane * 16, seq_tab + 32, CV_U + 4096);
        glds16(lane * 16, seq_tab + 32, CV_U + CV_USTR + 4096);
      }
      dflag_set(L_CONV + 8, 1);
    }
    if (wid == 3) lflag_set(L_CONV + 12, 1);
    park(6, 8);
    if (wid == 1) {
      lflag_wait(L_FREE, 7, 7);
      const u32 vl = lane * 16;
#pragma unroll 1
      for (int n = 0; n < 8; ++n) {
        const int rr = n & 3;
        const char* src = ssmk + (size_t)(d0 + (n >> 2)) * (S * 2) + (rr ? (rr - 4) * 2 : 0);
        glds16x4(rr && !lane ? 16u : vl, vl, vl, vl, src, CV + (u32)n * CV_HSTR + (rr ? 24 : 32));
        if (rr == 3) dflag_set(L_CONV + (n >> 2) * 4, 1);
      }
    }
    lflag_wait(L_FREE, 7, 7);
    const u32 wl = opq(lane * 16 + j * 12288);
    auto feed = [&](int i0, int i1) {
#pragma unroll
      for (int i = i0; i < i1; ++i)
#pragma unroll
        for (int n = 0; n < 4; ++n) lds_st128(wl + g2_place(0, i % 6) + n * 1024, pk[i & 7][n]);
      lflag_set(L_FED + j * 4, i1);
    };
    feed(0, 3);
    if (wid == 3) {
      lflag_wait(L_PUBA, 3, 1);
      const int e = lane < 28 ? lane : 27;
      gflag_wait<4>(ws + WS_FH + (tau * 56 + 4 * (e >> 1) + (e & 1)) * 128, tag);
      lflag_set(L_H + 4, 1);
    }
    feed(3, 6);
    lflag_wait(L_PUB, 3, 1);
    lflag_wait(L_CONV, 3, 1);
    park(8, 14);
    if (wid == 3) {
      lflag_wait(L_PUB, 3, 2);
      const int e = lane < 28 ? lane : 27;
      gflag_wait<4>(ws + WS_FH + (tau * 56 + 4 * (e >> 1) + (e & 1) + 2) * 128, tag);
      lflag_set(L_H, 1);
    }
    __builtin_amdgcn_s_setprio(0);
#pragma unroll
    for (int i = 6; i < 14; i += 2) {
      lflag_wait(L_PROG + j * 4, 0, i - 4);
      feed(i, i + 2);
    }
    return;
  }
  const rsrc_t rs = ws_rsrc(ws);

  if (b >= 224) {
    if (wid >= 8 && b != 255) return;
    const int tid = (int)opq(threadIdx.x), lane = tid & 63;
    const int sp = b - 224;
    const u64 seed = gload64(ws + WS_E + opq(0));
    if (tid == 0) touch(WS_FST + sp * 128, ws, L_DUMMY);
    if (wid < 8) {
      const int row = 64 * sp + (tid >> 3), c8 = tid & 7;
      const int cu = sp < 28 ? sp : 27, mat = cu / 14, ct = cu % 14, kg = tid >> 4, c16 = tid & 15;
      const char* wp = (mat ? w3 : w1) + ct * 256;
      const u32 wo = (u32)(kg * (F * 2) + c16 * 16), xo = (u32)(row * (D * 2) + c8 * 16);
      u32x4 xv[7], wv[14];
#pragma unroll
      for (int i = 0; i < 7; ++i) xv[i] = *(const u32x4*)(x + (xo + i * 128));
#pragma unroll
      for (int i = 0; i < 14; ++i) wv[i] = *(const u32x4*)(wp + (wo + i * 32 * (F * 2)));
      SCHED0();
      float s = 0.f;
#pragma unroll
      for (int i = 0; i < 7; ++i)
#pragma unroll
        for (int j = 0; j < 4; ++j) s += bflo(xv[i][j]) + bfhi(xv[i][j]);
      const float m = sum8(s) * (1.0f / D);
      float q = 0.f;
#pragma unroll
      for (int i = 0; i < 7; ++i)
#pragma unroll
        for (int j = 0; j < 4; ++j) {
          const float aa = bflo(xv[i][j]) - m, bb = bfhi(xv[i][j]) - m;
          q = __builtin_fmaf(aa, aa, __builtin_fmaf(bb, bb, q));
        }
      const float sd = rbf(__builtin_amdgcn_sqrtf(rbf(rbf(sum8(q) * (1.0f / D)) + 1e-5f)));
      if (c8 == 0) *(LDSP(f32x2))(u32)(16384 + (tid >> 3) * 8) = f32x2{rbf(m), __builtin_amdgcn_rcpf(sd)};
      SCHED0();
      float a[8] = {};
#pragma unroll
      for (int i = 0; i < 14; ++i)
#pragma unroll
        for (int j = 0; j < 4; ++j) { a[2 * j] += bflo(wv[i][j]); a[2 * j + 1] += bfhi(wv[i][j]); }
      f32x4 t;
#pragma unroll
      for (int j = 0; j < 4; ++j) {
        auto p = __builtin_amdgcn_permlane16_swap(__builtin_bit_cast(u32, a[j]), __builtin_bit_cast(u32, a[j + 4]), false, false);
        t[j] = asf(p[0]) + asf(p[1]);
      }
      *(LDSP(f32x4))(u32)((kg >> 1) * 512 + c16 * 32 + (kg & 1) * 16) = t;
    }
    WAIT_LGKM0(); BAR();
    const u64 tag = tag_of(seed);
    if (wid == 0) {
      const int cu = sp < 28 ? sp : 27;
      f32x2 cs = {};
#pragma unroll
      for (int i = 0; i < 16; ++i) cs += *(LDSP(f32x2))(u32)(i * 512 + lane * 8);
      st64_dev(rs, WS_STAT + (u32)(64 * sp + lane) * 8, *(LDSP(u32x2))(u32)(16384 + lane * 8));
      if (sp < 28) st64_dev(rs, WS_CSUM + (u32)((cu % 14) * 1024 + (cu / 14) * 512 + lane * 8), __builtin_bit_cast(u32x2, cs));
      gflag_publish(ws + WS_FST + sp * 128, tag, lane);
    }
    WAIT_VM(0); BAR();
    if (tid < 392) touch((u32)(sp * 392 + tid) * 128, w2, L_DUMMY);
    if (tid < 448) touch((u32)(sp * 448 + tid) * 128, ssmk, L_DUMMY);
    if (b == 255) {
      __builtin_amdgcn_s_sleep(127);
      gflag_wait<64>(ws + (tid < 480 ? WS_FUT + tid * 128 : WS_FH + ((((tid < 704 ? tid - 480 : 223) / 14) * 2) * 56 + ((tid < 704 ? tid - 480 : 223) % 14) * 4) * 128), tag);
      BAR();
      if (tid == 0) gstore64(ws + WS_E, tag);
    }
    return;
  }

  const int cw = wid - 4, th = cw & 1, fq = cw >> 1;
  {
    const int lane = (int)opq(threadIdx.x) & 63, g = lane >> 4, hh = lane >> 5, l31 = lane & 31;
    const int q4 = (lane >> 2) & 3, p4 = lane & 3;
    const u32 aA = 16384 + (u32)((8 * hh + q4) * 256 + ((fq ^ q4) << 6) + (g & 1) * 32 + p4 * 8);
    const u32 aB = (u32)((64 * th + l31) * 128 + ((hh ^ ((l31 >> 1) & 7)) << 4));
    f32x16 acc[2][2] = {};
    s16x8 fa[2][2][2], fb[2][2][2];
    u32 pa, pb[4];
    auto setslot = [&](u32 slot) {
      pa = opq(aA + slot);
#pragma unroll
      for (int k = 0; k < 4; ++k) pb[k] = opq((aB ^ (u32)(k << 5)) + slot);
    };
    auto rd = [&](int set) {
#pragma unroll
      for (int k = 0; k < 2; ++k) {
        const int ks = 2 * set + k;
        fa[set][k][0] = lds_tr2(pa, ks * 4096, ks * 4096 + 1024);
        fb[set][k][0] = lds_b128(pb[ks], 0);
        fb[set][k][1] = lds_b128(pb[ks], 4096);
        fa[set][k][1] = lds_tr2(pa, 16384 + ks * 4096, 16384 + ks * 4096 + 1024);
      }
    };
    auto mm = [&](int set, int n0, int n1) {
#pragma unroll
      for (int n = n0; n < n1; ++n) acc[(n >> 1) & 1][n & 1] = mfma32(fa[set][n >> 2][(n >> 1) & 1], fb[set][n >> 2][n & 1], acc[(n >> 1) & 1][n & 1]);
    };
    setslot(0);
    const u32 fad = opq(L_FULL + (lane & 1) * 4);
#pragma unroll
    for (int i = 0; i < 4; ++i) asm volatile("" : "+v"(acc[i >> 1][i & 1]));
    __builtin_amdgcn_s_setprio(1);
    lflag_wait(L_FULL, 1, 1);
    rd(0);
    SCHED0();
    u32 slot = 0;
    auto stage = [&](int c, bool bar) {
      rd(1);
      const u32 fl = *(volatile LDSP(u32))fad;
      mm(0, 0, 6);
      SGB(0x008, 1); SGB(0x100, 3); SGB(0x008, 1); SGB(0x100, 3); SGB(0x008, 1); SGB(0x100, 3); SGB(0x008, 1); SGB(0x100, 4); SGB(0x008, 2);
      SCHED0();
      slot = slot == 2 * SLOT ? 0 : slot + SLOT;
      setslot(slot);
      SCHED0();
      mm(0, 6, 7);
      SCHED0();
      __builtin_amdgcn_s_waitcnt(0xC07F);
      const bool miss = c < 6 && __builtin_amdgcn_ballot_w64(fl < (u32)(c + 2));
      SCHED0();
      mm(0, 7, 8);
      SCHED0();
      if (bar) BAR();
      SCHED0();
      mm(1, 0, 2);
      SCHED0();
      if (!bar && c == 6) lflag_set(L_FREE + cw * 4, 7);
      if (miss) lflag_wait(L_FULL, 1, c + 2);
      SCHED0();
      rd(0); mm(1, 2, 8);
#pragma unroll
      for (int i = 0; i < 4; ++i) { SGB(0x008, 1); SGB(0x100, 3); }
      SGB(0x008, 2);
      SCHED0();
    };
#pragma unroll 1
    for (int c = 0; c < 4; ++c) stage(c, true);
#pragma unroll 1
    for (int c = 4; c < 7; ++c) stage(c, false);
    const u32 hdst = WS_H + (u32)((2 * panel + th) * 56 + 4 * ft + fq) * 4096 + lane * 16;
    const u32 sa = opq(L_STAT + (u32)(64 * th + l31) * 8), ca = opq(L_CSUM + (u32)(32 * fq + 4 * hh) * 4);
    f32x2 st[2]; f32x4 csa[2][2][2];
    auto rdc = [&]() {
#pragma unroll
      for (int j = 0; j < 2; ++j) st[j] = *(volatile LDSP(f32x2))((LDSP(char))sa + j * 256);
#pragma unroll
      for (int n = 0; n < 8; ++n) csa[n >> 2][(n >> 1) & 1][n & 1] = *(volatile LDSP(f32x4))((LDSP(char))ca + ((n >> 1) & 1) * 512 + (n >> 2) * 64 + (n & 1) * 32);
    };
    const u32 ef = *(volatile LDSP(u32))L_EPI;
    rdc();
    if (__builtin_amdgcn_readfirstlane(ef) == 0) { lflag_wait(L_EPI, 0, 1); rdc(); }
    float nm[2], c1[2], irs2[2];
#pragma unroll
    for (int j = 0; j < 2; ++j) { nm[j] = -st[j][0]; c1[j] = -1.44269504f * st[j][1]; irs2[j] = __builtin_amdgcn_rcpf(st[j][1] * st[j][1]); }
#pragma unroll
    for (int s = 0; s < 2; ++s) {
      const f32x4 (&cs)[2][2] = csa[s];
#pragma unroll
      for (int j = 0; j < 2; ++j) {
        float a[8], ex[8], hv[8];
#pragma unroll
        for (int e = 0; e < 8; ++e) a[e] = __builtin_fmaf(nm[j], cs[0][e >> 2][e & 3], acc[0][j][8 * s + e]);
#pragma unroll
        for (int e = 0; e < 8; ++e) ex[e] = a[e] * c1[j];
        SCHED0();
#pragma unroll
        for (int e = 0; e < 8; ++e) { ex[e] = __builtin_amdgcn_exp2f(ex[e]); hv[e] = __builtin_fmaf(nm[j], cs[1][e >> 2][e & 3], acc[1][j][8 * s + e]); }
        SCHED0();
#pragma unroll
        for (int e = 0; e < 8; ++e) ex[e] = __builtin_fmaf(ex[e], irs2[j], irs2[j]);
        SCHED0();
#pragma unroll
        for (int e = 0; e < 8; ++e) { ex[e] = __builtin_amdgcn_rcpf(ex[e]); hv[e] *= a[e]; }
        SCHED0();
#pragma unroll
        for (int e = 0; e < 8; ++e) hv[e] *= ex[e];
        st128_dev(rs, hdst + (s * 2 + j) * 1024, u32x4{pk2(hv[0], hv[1]), pk2(hv[2], hv[3]), pk2(hv[4], hv[5]), pk2(hv[6], hv[7])});
      }
    }
  }
  char* hflag = ws + WS_FH + ((2 * panel + th) * 56 + 4 * ft + fq) * 128;
  u32x4 xv, sv;
  if (cw < 4) {
    const int ht = (int)opq(threadIdx.x) & 255, lane = ht & 63, l15 = ht & 15;
    const u64 tag = tag_of(*(volatile LDSP(u64))L_TAG);
    const int ch = cw >> 1, var = cw & 1;
    gflag_publish(hflag, tag, lane);
    lflag_set(L_PUBA + cw * 4, 1);
    u32x4 xr[2]; u32x2 st[2];
#pragma unroll
    for (int i = 0; i < 2; ++i) {
      const int n = ht + 256 * i, row = n >> 3, c = n & 7;
      xr[i] = *(const u32x4*)(x + (u32)((64 * tau + row) * (D * 2) + (64 * dl + 8 * c) * 2));
      st[i] = ld64_dev(rs, WS_STAT + (u32)(64 * tau + row) * 8);
    }
    SCHED0();
    conv_compute(lane, cw, d0 + ch, rs);
    u32x4 xn[2];
#pragma unroll
    for (int i = 0; i < 2; ++i) {
      const float mb = asf(st[i][0]), rsd = asf(st[i][1]);
#pragma unroll
      for (int e = 0; e < 4; ++e) {
        const u32 dd = pk2(bflo(xr[i][e]) - mb, bfhi(xr[i][e]) - mb);
        xn[i][e] = pk2(bflo(dd) * rsd, bfhi(dd) * rsd);
      }
    }
    asm volatile("" : "+v"(xn[0]), "+v"(xn[1]));
    SCHED0();
    gflag_publish(ws + WS_FY + (var * 448 + d0 + ch) * 128, tag, lane);
    xv = xr[0]; sv = xn[0];
    lflag_wait(L_XCH, 1, 1);
    lds_st128(E_S + (u32)ht * 16, xn[1]); lds_st128(E_X + (u32)ht * 16, xr[1]);
    gflag_wait<4>(ws + WS_FY + ((tau >> 4) * 448 + 64 * dl + 16 * cw + l15) * 128, tag);
#pragma unroll
    for (int i = 0; i < 2; ++i)
      glds16_dev((u32)((lane >> 3) * (S * 2) + (((lane & 7) ^ (2 * cw + i)) << 4)),
                 ws + WS_YT + (64 * dl + 16 * cw + 8 * i) * (S * 2) + 128 * tau, xbuf(cw ^ 1) + (u32)i * 1024);
    WAIT_VM(0);
  } else {
    const int j = cw - 4;
    const int lane = (int)opq(threadIdx.x) & 63;
    lflag_set(L_PUB + j * 4, 1);
    const u64 btag = tag_of(*(volatile LDSP(u64))L_TAG);
    const bool hready = __builtin_amdgcn_readfirstlane(*(volatile LDSP(u32))(L_H + 4)) != 0;
    const u32 hl = lane * 16;
    const char* hbase = ws + WS_H + (size_t)tau * 56 * 4096;
    const int q4 = (lane >> 2) & 3;
    const u32 ta = (u32)((4 * (lane >> 5) + q4) * 128 + ((q4 >> 1) << 6) + ((lane >> 4) & 1) * 32 + (lane & 3) * 8);
    s16x8 fa[2][4], fb[3][4];
    f32x16 acc[2][2] = {};
    auto ldh = [&](int i) {
      const char* p = hbase + (size_t)g2_ks(j, i) * 4096;
      asm volatile("global_load_dwordx4 %0, %4, %5 sc1\n\tglobal_load_dwordx4 %1, %4, %5 offset:1024 sc1\n\t"
                   "global_load_dwordx4 %2, %4, %5 offset:2048 sc1\n\tglobal_load_dwordx4 %3, %4, %5 offset:3072 sc1"
                   : "=&v"(fb[i % 3][0]), "=&v"(fb[i % 3][1]), "=&v"(fb[i % 3][2]), "=&v"(fb[i % 3][3])
                   : "v"(hl), "s"(p) : "memory");
    };
    u32 tb[2];
#pragma unroll
    for (int a = 0; a < 2; ++a) tb[a] = opq((ta ^ (u32)(a << 6)) + g2_place(j, 0));
    auto rda = [&](int i) {
#pragma unroll
      for (int a = 0; a < 2; ++a)
        if (i > 0 && i % 3 == 0) tb[a] = opq(i % 6 ? tb[a] - 2 * SLOT : tb[a] + 2 * SLOT);
#pragma unroll
      for (int n = 0; n < 4; ++n) {
        const int o = (i % 3) * 4096 + (n >> 1) * 2048;
        fa[i & 1][n] = lds_tr2(tb[n & 1], o, o + 1024);
      }
    };
    const u32 fed = opq(L_FED + j * 4), prog = opq(L_PROG + j * 4);
    if (!hready) { gflag_publish(hflag, btag, lane); lflag_set(L_PUB + j * 4, 2); lflag_wait(L_H + 4, 0, 1); }
#pragma unroll
    for (int i = 0; i < 3; ++i) ldh(i);
    if (hready) {
      WAIT_VM(12);
      if (lane == 0) gstore64(hflag, btag);
      lflag_set(L_PUB + j * 4, 2);
    }
    lflag_wait(L_FED + j * 4, 0, 3);
    rda(0);
    SCHED0();
    u32 fl = 0;
#pragma unroll
    for (int i = 0; i < 14; ++i) {
      if (i == 2 || (i >= 5 && i < 13 && (i & 1))) {
        if (__builtin_amdgcn_readfirstlane(fl) < i + 2) lflag_wait(L_FED + j * 4, 0, i + 2);
      }
      asm volatile("s_waitcnt vmcnt(%4)" : "+v"(fb[i % 3][0]), "+v"(fb[i % 3][1]), "+v"(fb[i % 3][2]), "+v"(fb[i % 3][3])
                   : "i"(i < 12 ? 8 : (13 - i) * 4) : "memory");
      SCHED0();
      if (i < 13) rda(i + 1);
      if (i == 1 || (i >= 4 && i < 12 && !(i & 1))) fl = *(volatile LDSP(u32))fed;
      if (i < 13) {
#pragma unroll
        for (int s = 0; s < 2; ++s)
#pragma unroll
          for (int a = 0; a < 2; ++a)
#pragma unroll
            for (int t = 0; t < 2; ++t) acc[a][t] = mfma32(fa[i & 1][2 * s + a], fb[i % 3][2 * s + t], acc[a][t]);
#pragma unroll
        for (int n = 0; n < 5; ++n) { SGB(0x008, 1); SGB(0x100, 2); }
        SGB(0x008, 3);
      } else {
        __builtin_amdgcn_s_waitcnt(0xC07F);
        const int ln = (int)opq(threadIdx.x), lt = ln & 31, lh = (ln >> 5) & 1;
#pragma unroll
        for (int n = 0; n < 5; ++n) {
          if (n < 4) {
#pragma unroll
            for (int s = 0; s < 2; ++s) acc[n & 1][n >> 1] = mfma32(fa[i & 1][2 * s + (n & 1)], fb[i % 3][2 * s + (n >> 1)], acc[n & 1][n >> 1]);
          }
          SCHED0();
          if (n > 0) {
            const int a = (n - 1) & 1, t = (n - 1) >> 1;
#pragma unroll
            for (int G = 0; G < 4; ++G)
              *(LDSP(f32x4))(g2_place(j, t ? 3 : 0) + (u32)(lt * 256 + (((8 * a + 2 * G + lh) ^ (lt & 7)) << 4))) =
                  f32x4{acc[a][t][4 * G], acc[a][t][4 * G + 1], acc[a][t][4 * G + 2], acc[a][t][4 * G + 3]};
          }
          SCHED0();
        }
      }
      SCHED0();
      if (i < 8 && (i & 1)) *(volatile LDSP(u32))prog = i + 1;
      if (i == 4) lflag_wait(L_H, 0, 1);
      if (i + 3 < 14) ldh(i + 3);
      SCHED0();
    }
  }
  {
    const int ht = (int)opq(threadIdx.x) & 255;
    const int half = cw >> 2, tk = ht >> 3, dg = ht & 7, tok = 32 * half + tk;
    const u32 pa0 = opq((half ? 0u : 2 * SLOT) + (u32)(tk * 256 + (((2 * dg) ^ (tk & 7)) << 4))), pa1 = opq(pa0 ^ 16u);
    const u32 ya = opq(xbuf((dg >> 1) ^ 1) + (u32)(8 * (dg & 1) * 128 + (((tok >> 3) ^ dg) << 4) + (tok & 7) * 2));
    const u32 ea = opq(E_S + (u32)ht * 16);
    const u32 oo = opq((u32)((64 * tau + tok) * (D * 2) + (64 * dl + 8 * dg) * 2));
    WAIT_LGKM0();
    BAR();
    f32x4 p0[4], p1[4];
    u32 yv[8];
#pragma unroll
    for (int w = 0; w < 4; ++w) {
      p0[w] = *(LDSP(f32x4))((LDSP(char))pa0 + w * 12288);
      p1[w] = *(LDSP(f32x4))((LDSP(char))pa1 + w * 12288);
    }
#pragma unroll
    for (int e = 0; e < 8; ++e) yv[e] = *(LDSP(u16))((LDSP(char))ya + e * 128);
    SCHED0();
    if (half) { sv = *(LDSP(u32x4))ea; xv = *(LDSP(u32x4))((LDSP(char))ea + (E_X - E_S)); }
    const f32x4 s0 = (p0[0] + p0[1]) + (p0[2] + p0[3]), s1 = (p1[0] + p1[1]) + (p1[2] + p1[3]);
    u32 f[4];
    float ex[8];
#pragma unroll
    for (int e = 0; e < 4; ++e) f[e] = e < 2 ? pk2(s0[2 * e], s0[2 * e + 1]) : pk2(s1[2 * e - 4], s1[2 * e - 3]);
#pragma unroll
    for (int e = 0; e < 8; ++e) ex[e] = __builtin_amdgcn_exp2f(-1.44269504f * ((e & 1) ? bfhi(f[e >> 1]) : bflo(f[e >> 1])));
    SCHED0();
    u32 se[4];
#pragma unroll
    for (int e = 0; e < 4; ++e) se[e] = pk2(bflo(sv[e]) * bflo(yv[2 * e]), bfhi(sv[e]) * bflo(yv[2 * e + 1]));
#pragma unroll
    for (int e = 0; e < 8; ++e) ex[e] = __builtin_amdgcn_rcpf(1.0f + ex[e]);
    SCHED0();
    u32x4 o;
#pragma unroll
    for (int e = 0; e < 4; ++e) {
      const u32 gt = pk2(bflo(f[e]) * ex[2 * e], bfhi(f[e]) * ex[2 * e + 1]);
      const u32 p = pk2(bflo(se[e]) * bflo(gt), bfhi(se[e]) * bfhi(gt));
      o[e] = pk2(bflo(xv[e]) + bflo(p), bfhi(xv[e]) + bfhi(p));
    }
    __builtin_amdgcn_raw_buffer_store_b128(o, __builtin_amdgcn_make_buffer_rsrc(out, 0, S * D * 2, 0x00020000), oo, 0, 16);
  }
}
}
#if defined(__HIP_DEVICE_COMPILE__) && __HIP_DEVICE_COMPILE__
#pragma clang attribute pop
#endif

extern "C" void kernel_launch(void* const* d_in, const int* in_sizes, int n_in, void* d_out, int out_size, void* d_ws,
                              size_t ws_size, hipStream_t stream) {
  (void)hipFuncSetAttribute((const void*)fused_kernel, hipFuncAttributeMaxDynamicSharedMemorySize, 163840);
  fused_kernel<<<256, 768, 163840, stream>>>((const char*)d_in[0], (const char*)d_in[1], (const char*)d_in[2], (const char*)d_in[3],
                                             (const char*)d_in[4], (const char*)d_in[5], (const char*)d_in[7], (const char*)d_in[8],
                                             (const char*)d_in[9], (char*)d_ws, (char*)d_out);
}
```

```cpp
#include <hip/hip_runtime.h>
#include <hip/hip_bf16.h>
#include <stdint.h>
#pragma clang diagnostic ignored "-Wint-to-pointer-cast"

#if defined(__HIP_DEVICE_COMPILE__) && __HIP_DEVICE_COMPILE__
#pragma clang attribute push(__attribute__((target("no-packed-fp32-ops"))), apply_to = function)
#endif
namespace {
typedef unsigned short u16;
typedef unsigned int u32;
typedef unsigned long long u64;
typedef __attribute__((ext_vector_type(4))) short s16x4;
typedef __attribute__((ext_vector_type(8))) short s16x8;
typedef __attribute__((ext_vector_type(8))) __bf16 bf16x8;
typedef __attribute__((ext_vector_type(2))) __bf16 bf16x2;
typedef __attribute__((ext_vector_type(16))) float f32x16;
typedef __attribute__((ext_vector_type(4))) float f32x4;
typedef __attribute__((ext_vector_type(2))) float f32x2;
typedef __attribute__((ext_vector_type(4))) u32 u32x4;
typedef __attribute__((ext_vector_type(2))) u32 u32x2;
#define LDSP(T) __attribute__((address_space(3))) T*
#define GLBP(T) __attribute__((address_space(1))) T*
#define DEV __device__ __forceinline__

constexpr int S = 2048, D = 448, F = 1792;
constexpr u32 WS_H = 0;
constexpr u32 WS_UT = WS_H + 7340032;
constexpr u32 WS_YT = WS_UT + 1835008;
constexpr u32 WS_STAT = WS_YT + 1835008;
constexpr u32 WS_CSUM = WS_STAT + 16384;
constexpr u32 WS_E = WS_CSUM + 16384;
constexpr u32 WS_FUT = WS_E + 128;
constexpr u32 WS_FST = WS_FUT + 448 * 128;
constexpr u32 WS_FY = WS_FST + 32 * 128;
constexpr u32 WS_FH = WS_FY + 896 * 128;
constexpr u32 WS_END = WS_FH + 1792 * 128;
constexpr u32 SLOT = 49152;
constexpr u32 MISC = 3 * SLOT;
constexpr u32 L_FULL = MISC, L_EPI = MISC + 8, L_H = MISC + 16, L_FREE = MISC + 32, L_CONV = MISC + 64, L_FED = MISC + 80,
              L_TAG = MISC + 96, L_DD = MISC + 104, L_PROG = MISC + 128, L_XCH = MISC + 144, L_PUB = MISC + 160, L_PUBA = MISC + 176, L_HE = MISC + 192, L_STAT = MISC + 256,
              L_CSUM = MISC + 1280, L_USCR = MISC + 4096, L_DUMMY = MISC + 2304, L_XB2 = MISC + 12288;
constexpr u32 CV = SLOT, CV_HSTR = 4160, CV_U = CV + 8 * CV_HSTR, CV_USTR = 4608;
constexpr u32 E_S = SLOT, E_X = SLOT + 4096;
constexpr int SPIN_CAP = 1 << 18;

__device__ const u32 seq_tab[160] __attribute__((aligned(128))) = {0, 1, 2, 3, 4, 5, 6, 7};
DEV float asf(u32 v) { return __builtin_bit_cast(float, v); }
DEV float bflo(u32 v) { return asf(v << 16); }
DEV float bfhi(u32 v) { return asf(v & 0xffff0000u); }
template <int CTRL> DEV float dpp(float v) { return asf(__builtin_amdgcn_update_dpp(0, __builtin_bit_cast(u32, v), CTRL, 0xf, 0xf, true)); }
DEV float sum8(float v) { v += dpp<0xB1>(v); v += dpp<0x4E>(v); return v + dpp<0x141>(v); }
DEV u32 pk2(float lo, float hi) {
  asm("" : "+v"(lo)); asm("" : "+v"(hi));
  bf16x2 r = {(__bf16)lo, (__bf16)hi};
  u32 v = __builtin_bit_cast(u32, r);
  asm("" : "+v"(v));
  return v;
}
DEV float rbf(float f) { bf16x2 r = {(__bf16)f, (__bf16)f}; return bflo(__builtin_bit_cast(u32, r)); }
DEV f32x4 mfma16(s16x8 a, s16x8 b, f32x4 c) {
  return __builtin_amdgcn_mfma_f32_16x16x32_bf16(__builtin_bit_cast(bf16x8, a), __builtin_bit_cast(bf16x8, b), c, 0, 0, 0);
}
DEV f32x16 mfma32(s16x8 a, s16x8 b, f32x16 c) {
  return __builtin_amdgcn_mfma_f32_32x32x16_bf16(__builtin_bit_cast(bf16x8, a), __builtin_bit_cast(bf16x8, b), c, 0, 0, 0);
}
DEV u32 opq(u32 v) { asm volatile("" : "+v"(v)); return v; }
DEV s16x8 lds_tr2(u32 base, int o0, int o1) {
  s16x4 lo = __builtin_amdgcn_ds_read_tr16_b64_v4i16((LDSP(s16x4))((LDSP(char))base + o0));
  s16x4 hi = __builtin_amdgcn_ds_read_tr16_b64_v4i16((LDSP(s16x4))((LDSP(char))base + o1));
  return __builtin_shufflevector(lo, hi, 0, 1, 2, 3, 4, 5, 6, 7);
}
DEV s16x8 lds_b128(u32 base, int o) { return *(LDSP(s16x8))((LDSP(char))base + o); }
DEV void lds_st128(u32 a, u32x4 v) { *(LDSP(u32x4))a = v; }
DEV void glds16x4(u32 v0, u32 v1, u32 v2, u32 v3, const void* sbase, u32 ldst) {
  asm volatile("s_mov_b32 m0, %5\n\ts_nop 0\n\tglobal_load_lds_dwordx4 %0, %4\n\tglobal_load_lds_dwordx4 %1, %4 offset:1024\n\t"
               "global_load_lds_dwordx4 %2, %4 offset:2048\n\tglobal_load_lds_dwordx4 %3, %4 offset:3072"
               ::"v"(v0), "v"(v1), "v"(v2), "v"(v3), "s"(sbase), "s"(ldst) : "memory");
}
DEV void glds16(u32 voff, const void* sbase, u32 ldst) {
  asm volatile("s_mov_b32 m0, %2\n\ts_nop 0\n\tglobal_load_lds_dwordx4 %0, %1" ::"v"(voff), "s"(sbase), "s"(ldst) : "memory");
}
DEV void glds16x4_dev(u32 v, const void* sbase, u32 ldst) {
  asm volatile("s_mov_b32 m0, %2\n\ts_nop 0\n\tglobal_load_lds_dwordx4 %0, %1 sc1\n\tglobal_load_lds_dwordx4 %0, %1 offset:1024 sc1\n\t"
               "global_load_lds_dwordx4 %0, %1 offset:2048 sc1\n\tglobal_load_lds_dwordx4 %0, %1 offset:3072 sc1"
               ::"v"(v), "s"(sbase), "s"(ldst) : "memory");
}
DEV void glds16_dev(u32 voff, const void* sbase, u32 ldst) {
  asm volatile("s_mov_b32 m0, %2\n\ts_nop 0\n\tglobal_load_lds_dwordx4 %0, %1 sc1" ::"v"(voff), "s"(sbase), "s"(ldst) : "memory");
}
DEV void dflag_set(u32 addr, u32 val) {
  asm volatile("s_waitcnt lgkmcnt(0)\n\ts_mov_b32 m0, %2\n\ts_mov_b64 exec, 1\n\tglobal_load_lds_dword %0, %1\n\ts_mov_b64 exec, -1"
               ::"v"(0), "s"(seq_tab + val), "s"(addr) : "memory");
}
#define WAIT_VM(n) asm volatile("s_waitcnt vmcnt(%0)" ::"i"(n) : "memory")
#define WAIT_LGKM0() asm volatile("s_waitcnt lgkmcnt(0)" ::: "memory")
#define BAR() do { asm volatile("" ::: "memory"); __builtin_amdgcn_s_barrier(); asm volatile("" ::: "memory"); } while (0)
#define SCHED0() __builtin_amdgcn_sched_barrier(0)
#define SGB(mask, n) __builtin_amdgcn_sched_group_barrier(mask, n, 0)

DEV void lflag_set(u32 addr, u32 val) {
  asm volatile("s_waitcnt lgkmcnt(0)\n\tds_write_b32 %0, %1" ::"v"(addr), "v"(val) : "memory");
}
DEV void lflag_wait(u32 addr, u32 mask, u32 val) {
  const u32 a = addr + 4 * (threadIdx.x & mask);
#pragma unroll 1
  for (int i = 0; i < SPIN_CAP; ++i) {
    u32 v;
    asm volatile("ds_read_b32 %0, %1\n\ts_waitcnt lgkmcnt(0)" : "=v"(v) : "v"(a) : "memory");
    if (__builtin_amdgcn_ballot_w64(v < val) == 0) break;
    __builtin_amdgcn_s_sleep(1);
  }
}
DEV u64 gload64(const char* p) { return __hip_atomic_load((GLBP(u64))p, __ATOMIC_RELAXED, __HIP_MEMORY_SCOPE_AGENT); }
DEV void gstore64(char* p, u64 v) { __hip_atomic_store((GLBP(u64))p, v, __ATOMIC_RELAXED, __HIP_MEMORY_SCOPE_AGENT); }
template <int SLEEP>
DEV void gflag_wait(const char* lane_ptr, u64 tag) {
  bool ok = false;
#pragma unroll 1
  for (int i = 0; i < SPIN_CAP; ++i) {
    asm volatile("" ::: "memory");
    if (!ok) ok = gload64(lane_ptr) == tag;
    if (__builtin_amdgcn_ballot_w64(!ok) == 0) break;
    __builtin_amdgcn_s_sleep(SLEEP);
  }
  asm volatile("" ::: "memory");
}
DEV void gflag_publish(char* p, u64 tag, int lane) {
  WAIT_VM(0);
  if (lane == 0) gstore64(p, tag);
}
DEV u64 tag_of(u64 seed) { return seed * 0x9E3779B97F4A7C15ull + 0xD1B54A32D192ED03ull; }
DEV u64 make_tag(const char* ws) { return tag_of(gload64(ws + WS_E)); }
typedef __amdgpu_buffer_rsrc_t rsrc_t;
DEV rsrc_t ws_rsrc(char* ws) { return __builtin_amdgcn_make_buffer_rsrc(ws, 0, WS_END, 0x00020000); }
DEV void st128_dev(rsrc_t r, u32 off, u32x4 v) { __builtin_amdgcn_raw_buffer_store_b128(v, r, off, 0, 16); }
DEV void st64_dev(rsrc_t r, u32 off, u32x2 v) { __builtin_amdgcn_raw_buffer_store_b64(v, r, off, 0, 16); }
DEV u32x2 ld64_dev(rsrc_t r, u32 off) { return __builtin_amdgcn_raw_buffer_load_b64(r, off, 0, 16); }
DEV void touch(u32 voff, const void* sbase, u32 ldst) {
  asm volatile("s_mov_b32 m0, %2\n\ts_nop 0\n\tglobal_load_lds_dword %0, %1" ::"v"(voff), "s"(sbase), "s"(ldst) : "memory");
}

DEV void glds4_dev(u32 voff, const void* sbase, u32 ldst) {
  asm volatile("s_mov_b32 m0, %2\n\ts_nop 0\n\tglobal_load_lds_dword %0, %1 sc1" ::"v"(voff), "s"(sbase), "s"(ldst) : "memory");
}

DEV u32 shr16(u32 hi, u32 lo) { return __builtin_amdgcn_alignbit(hi, lo, 16); }
DEV u32 xbuf(int w) { return w < 2 ? L_USCR + (u32)w * 4096 : (w == 2 ? L_XB2 : CV + 45056); }
DEV void conv_compute(int lane, int cw, int d, rsrc_t rs) {
  const int ch = cw >> 1, var = cw & 1;
  const int m = lane & 15, g = lane >> 4, q4 = m >> 2, p4 = lane & 3;
  u32 aa = opq(CV + (u32)(ch * 4 + ((q4 + 1) & 3)) * CV_HSTR + (u32)(((q4 + 1) & 4) + 8 * g + 4 * p4) * 2 + var * 256);
  u32 ba = opq(CV_U + (u32)ch * CV_USTR + (u32)(240 - 16 * m + 8 * g) * 2 + var * 256);
  f32x4 acc[8] = {};
  s16x8 A[8], B[8];
  const u32 a0 = opq(CV + (u32)ch * 4 * CV_HSTR + (u32)(m + 1 + 8 * g) * 2);
  u32x4 msk;
#pragma unroll
  for (int i = 0; i < 4; ++i) { const int e = m + 8 * g + 2 * i - 15; msk[i] = e >= 0 ? ~0u : (e == -1 ? 0xffff0000u : 0u); }
#pragma unroll
  for (int i = 0; i < 8; ++i) asm volatile("" : "+v"(acc[i]));
  asm volatile("" : "+v"(msk));
  const u32 xw = opq(xbuf(cw) + (u32)lane * 16);
  const u32 xa = opq(xbuf(cw ^ 1) + (u32)lane * 16), yo = opq(WS_YT + (u32)(d * (S * 2) + (1024 * var + 16 * m + 4 * g) * 2));
  const u32 ua = opq(CV_U + (u32)ch * CV_USTR + (u32)(2044 - 768 - (1024 * var + 16 * m + 4 * g)) * 2);
  const u32 dv = *(LDSP(u32))L_DD;
  const float Dd = ch ? bfhi(dv) : bflo(dv);
  lflag_wait(L_CONV, 3, 1);
  u32 h0[8];
#pragma unroll
  for (int j = 0; j < 8; ++j) h0[j] = *(LDSP(u16))((LDSP(char))a0 + j * 2);
  SCHED0();
#pragma unroll
  for (int p = 0; p < 8; ++p) { A[7 - p] = lds_tr2(aa, (7 - p) * 512, (7 - p) * 512 + 8); B[p] = lds_b128(ba, (7 - p) * 512); }
  u32x2 uu[4];
#pragma unroll
  for (int i = 0; i < 4; ++i) uu[i] = *(LDSP(u32x2))((LDSP(char))ua + (3 - i) * 512);
  SCHED0();
  if (var == 0) {
    u32x4 f;
#pragma unroll
    for (int i = 0; i < 4; ++i) f[i] = (h0[2 * i] | (h0[2 * i + 1] << 16)) & msk[i];
    A[0] = __builtin_bit_cast(s16x8, f);
  }
  SCHED0();
#pragma unroll 1
  for (int it = 0; it < 3; ++it) {
#pragma unroll
    for (int p = 7; p >= 0; --p) {
#pragma unroll
      for (int dd = 0; dd <= 7 - p; ++dd) {
        acc[p + dd] = mfma16(A[p], B[dd], acc[p + dd]);
        if (p == 0) { SCHED0(); B[dd] = lds_b128(ba, 64 + (7 - dd) * 512); SCHED0(); }
      }
      SCHED0();
      A[p] = lds_tr2(aa, 64 + p * 512, 64 + p * 512 + 8);
      SCHED0();
    }
    aa = opq(aa + 64); ba = opq(ba + 64);
  }
  auto half = [&](int hi) {
#pragma unroll
    for (int p = 7; p >= 0; --p)
#pragma unroll
      for (int dd = 0; dd <= 7 - p; ++dd)
        if (((p + dd) >> 2) == hi) acc[p + dd] = mfma16(A[p], B[dd], acc[p + dd]);
    SCHED0();
  };
  auto send = [&](int hi) {
#pragma unroll
    for (int i = 0; i < 4; ++i) *(LDSP(f32x4))((LDSP(char))xw + i * 1024) = acc[4 * hi + i];
    asm volatile("" ::: "memory");
    *(volatile LDSP(u32))(L_XCH + cw * 4) = 1;
    SCHED0();
  };
  f32x4 y[4];
  if (var) {
    half(0); send(0); half(1);
#pragma unroll
    for (int i = 0; i < 4; ++i) y[i] = acc[4 + i];
  } else {
    half(1); send(1); half(0);
#pragma unroll
    for (int i = 0; i < 4; ++i) y[i] = acc[i];
  }
  u32x2 sk[4];
#pragma unroll
  for (int i = 0; i < 4; ++i) sk[i] = u32x2{pk2(bfhi(uu[i][1]) * Dd, bflo(uu[i][1]) * Dd), pk2(bfhi(uu[i][0]) * Dd, bflo(uu[i][0]) * Dd)};
  SCHED0();
  lflag_wait(L_XCH + (cw ^ 1) * 4, 0, 1);
  f32x4 pv[4];
#pragma unroll
  for (int i = 0; i < 4; ++i) pv[i] = *(LDSP(f32x4))((LDSP(char))xa + i * 1024);
  SCHED0();
#pragma unroll
  for (int i = 0; i < 4; ++i) {
    const f32x4 s = y[i] + pv[i];
    const u32 c0 = pk2(s[0], s[1]), c1 = pk2(s[2], s[3]);
    st64_dev(rs, yo + i * 512, u32x2{pk2(bflo(c0) + bflo(sk[i][0]), bfhi(c0) + bfhi(sk[i][0])), pk2(bflo(c1) + bflo(sk[i][1]), bfhi(c1) + bfhi(sk[i][1]))});
  }
}

DEV int g2_ks(int j, int i) { const int e = 7 * j + (i < 7 ? i : i - 7); return 4 * (e >> 1) + (e & 1) + (i < 7 ? 0 : 2); }
DEV u32 g2_place(int j, int p) { return (p < 3 ? 2 * SLOT + (u32)p * 4096 : (u32)(p - 3) * 4096) + (u32)j * 12288; }
DEV u32 w2_voff(int lane) { return (u32)((lane >> 3) * (D * 2) + (((lane & 7) ^ (((lane >> 4) & 1) << 2)) << 4)); }

DEV void share(int b, int n, const char* base) {
  const int lane = (int)opq(threadIdx.x) & 63;
  if (lane < n) touch((u32)(b * n + lane) * 128, base, L_DUMMY);
}

__global__ __launch_bounds__(768, 3) void fused_kernel(const char* x, const char* kp, const char* vp, const char* skp,
                                                       const char* ssmk, const char* ssmD, const char* w1, const char* w3,
                                                       const char* w2, char* ws, char* out) {
  const int b = blockIdx.x, xcd = b & 7, r = b >> 3;
  const int wid = __builtin_amdgcn_readfirstlane(threadIdx.x >> 6);
  const int panel = (xcd >> 1) * 4 + r / 7, ft = (xcd & 1) * 7 + r % 7;
  const int tau = xcd * 4 + r / 7, dl = r % 7;
  const int d0 = 2 * b;

  if (b < 224) {
    if (wid >= 4 && wid < 7) share(b, wid == 4 ? 64 : 56, wid == 4 ? x : (wid == 5 ? w1 : w3));
    if (wid == 7) { *(LDSP(u32))(MISC + (threadIdx.x & 63) * 4) = 0; WAIT_LGKM0(); }
    BAR();
    if (wid < 2) {
      const int lane = (int)opq(threadIdx.x) & 63;
      const int g = lane >> 4, l15 = lane & 15;
      u32 vx[8], vw[8];
      const u32 ox = (u32)((lane >> 3) * (D * 2) + (((lane & 7) ^ g) << 4));
      const u32 ow = (u32)(g * (F * 2) + (((l15 >> 2) ^ g) << 6) + ((lane & 3) << 4));
#pragma unroll
      for (int i = 0; i < 8; ++i) {
        vx[i] = (ox ^ (u32)((i & 1) * 64)) + i * 8 * (D * 2) - (i & 3) * 1024;
        vw[i] = ow + i * 4 * (F * 2) - (i & 3) * 1024;
      }
#pragma unroll 1
      for (int c = 0; c < 7; ++c) {
        if (c >= 3) BAR();
        const u32 slot = (u32)(c % 3) * SLOT + wid * 8192;
        const char* xb = x + (size_t)(panel * 128 + wid * 64) * (D * 2) + c * 128;
        const char* w1b = w1 + (size_t)(c * 64 + wid * 32) * (F * 2) + ft * 256;
        const char* w3b = w3 + (size_t)(c * 64 + wid * 32) * (F * 2) + ft * 256;
#pragma unroll
        for (int i = 0; i < 8; i += 4) {
          glds16x4(vx[i], vx[i + 1], vx[i + 2], vx[i + 3], xb, slot + i * 1024);
          glds16x4(vw[i], vw[i + 1], vw[i + 2], vw[i + 3], w1b, slot + 16384 + i * 1024);
          glds16x4(vw[i], vw[i + 1], vw[i + 2], vw[i + 3], w3b, slot + 32768 + i * 1024);
        }
        dflag_set(L_FULL + wid * 4, c + 1);
      }
    }
    if (wid == 7 && (threadIdx.x & 63) < 5) touch((threadIdx.x & 63) * 128, seq_tab, L_DUMMY);
  }

  if (__builtin_expect(wid < 4 && b < 224, 1)) {
    const int lane = (int)opq(threadIdx.x) & 63;
    u64 tag = 0;
    if (wid < 2) {
      if (wid == 0) {
        tag = make_tag(ws);
        const int k = lane & 7;
        gflag_wait<8>(ws + WS_FST + (k < 2 ? 2 * panel + k : (k == 2 ? tau : (k & 1) * 14 + ft)) * 128, tag);
        glds16_dev(lane * 16, ws + WS_STAT + panel * 1024, L_STAT);
        glds16_dev(lane * 16, ws + WS_CSUM + ft * 1024, L_CSUM);
        if (lane < 2) glds4_dev(lane * 4, ws + WS_E, L_TAG);
        if (lane < 1) glds4_dev(0, ssmD + d0 * 2, L_DD);
        dflag_set(L_EPI, 1);
      }
    } else {
      __builtin_amdgcn_s_setprio(3);
      const rsrc_t rs = ws_rsrc(ws);
      const int u = wid - 2;
      const int ti = b / 7, cj = b % 7;
      const int tg = lane >> 3, cg = lane & 7;
      const int t0 = 64 * ti + 32 * u + 4 * tg, ch0 = 64 * cj + 8 * cg;
      u32x4 kr[7], vr[4], sw[4]; u32x2 o[8];
      u64 seed;
      auto ldkv = [&]() {
        const u32 ko = (u32)(t0 * (D * 2) + ch0 * 2);
#pragma unroll
        for (int i = 0; i < 7; ++i) kr[i] = *(const u32x4*)(kp + (i < 3 && t0 == 0 ? ko : ko + (u32)((i - 3) * (D * 2))));
#pragma unroll
        for (int i = 0; i < 4; ++i) vr[i] = *(const u32x4*)(vp + (ko + (u32)(i * (D * 2))));
        SCHED0();
      };
      auto piece = [&](int p) {
#pragma unroll
        for (int c = 2 * p; c < 2 * p + 2; ++c) {
          const u32 wa = sw[p][(c & 1) * 2], wb = sw[p][(c & 1) * 2 + 1];
          const float w0 = bflo(wa), w1f = bfhi(wa), w2f = bflo(wb), w3f = bfhi(wb);
          float kf[7], ks[4];
#pragma unroll
          for (int i = 0; i < 7; ++i) kf[i] = (c & 1) ? bfhi(kr[i][p]) : bflo(kr[i][p]);
#pragma unroll
          for (int i = 0; i < 4; ++i)
            ks[i] = __builtin_fmaf(kf[i + 3], w0, __builtin_fmaf(kf[i + 2], w1f, __builtin_fmaf(kf[i + 1], w2f, kf[i] * w3f)));
#pragma unroll
          for (int i = 0; i < 2; ++i) {
            const int ta = 3 - 2 * i, tb = 2 - 2 * i;
            const u32 kk = pk2(ks[ta], ks[tb]);
            o[c][i] = pk2(bflo(kk) * ((c & 1) ? bfhi(vr[ta][p]) : bflo(vr[ta][p])), bfhi(kk) * ((c & 1) ? bfhi(vr[tb][p]) : bflo(vr[tb][p])));
          }
          asm volatile("" : "+v"(o[c]));
        }
        SCHED0();
      };
      BAR();
      ldkv();
#pragma unroll
      for (int i = 0; i < 4; ++i) sw[i] = *(const u32x4*)(skp + (u32)(ch0 * 8 + i * 16));
      seed = gload64(ws + WS_E + opq(0));
      if (u && lane < 14) {
        const u32 fo = lane < 8 ? WS_FH + (u32)((2 * panel + (lane & 1)) * 56 + 4 * ft + (lane >> 1)) * 128
                                : (lane < 12 ? WS_FY + (u32)((lane & 1) * 448 + d0 + ((lane >> 1) & 1)) * 128 : WS_FUT + (u32)(cj * 64 + 2 * ti + (lane & 1)) * 128);
        touch(fo, ws, L_DUMMY);
      }
      SCHED0();
      BAR();
      SCHED0();
      BAR();
      SCHED0();
#pragma unroll
      for (int i = 0; i < 3; ++i) if (t0 == 0) kr[i] = u32x4{0, 0, 0, 0};
      piece(0);
      BAR();
      SCHED0();
#pragma unroll
      for (int c = 1; c < 4; ++c) piece(c);
      tag = tag_of(seed);
      const u32 scr = L_USCR + (u32)u * 4096;
#pragma unroll
      for (int c = 0; c < 8; ++c) *(LDSP(u32x2))(scr + (u32)((cg * 8 + c) * 64 + (((7 - tg) ^ (cg & 6)) << 3))) = o[c];
      WAIT_LGKM0();
#pragma unroll
      for (int i = 0; i < 4; ++i) {
        const int dd = 16 * i + (lane >> 2), c4 = lane & 3;
        u32x4 v = *(LDSP(u32x4))(scr + (u32)(dd * 64 + ((c4 ^ (dd >> 4)) << 4)));
        st128_dev(rs, WS_UT + (u32)((64 * cj + dd) * (S * 2) + (2016 - 64 * ti - 32 * u) * 2 + c4 * 16), v);
      }
      gflag_publish(ws + WS_FUT + (cj * 64 + 2 * ti + u) * 128, tag, lane);
    }
    const int j = wid;
    const u32 o2 = w2_voff(lane);
    const char* wbase = w2 + dl * 128;
    u32x4 pk[8][4];
    auto park = [&](int i0, int i1) {
#pragma unroll
      for (int i = i0; i < i1; ++i) {
        const char* src = wbase + (size_t)g2_ks(j, i) * 32 * (D * 2);
        asm volatile("" ::: "memory");
#pragma unroll
        for (int pc = 0; pc < 4; ++pc) pk[i & 7][pc] = *(const u32x4*)(src + (o2 + (u32)(pc * 8 * (D * 2))));
      }
      SCHED0();
    };
    park(0, 5);
    if (wid != 1) park(5, 6);
    if (wid == 2) {
      gflag_wait<8>(ws + WS_FUT + ((d0 >> 6) * 64 + lane) * 128, tag);
      lflag_wait(L_FREE, 7, 7);
      glds16x4_dev(lane * 16, ws + WS_UT + d0 * (S * 2), CV_U);
      glds16x4_dev(lane * 16, ws + WS_UT + (d0 + 1) * (S * 2), CV_U + CV_USTR);
      if (lane < 32) {
        glds16(lane * 16, seq_tab + 32, CV_U + 4096);
        glds16(lane * 16, seq_tab + 32, CV_U + CV_USTR + 4096);
      }
      dflag_set(L_CONV + 8, 1);
    }
    if (wid == 3) lflag_set(L_CONV + 12, 1);
    if (wid == 1) {
      u32x4 cv[8]; u32x2 cn[8];
#pragma unroll
      for (int i = 0; i < 8; ++i) {
        const u32 o = (u32)(d0 * (S * 2) + (lane + 64 * i) * 16);
        cv[i] = *(const u32x4*)(ssmk + o);
        cn[i] = *(const u32x2*)(ssmk + (o + (lane + 64 * i < 511 ? 16u : 0u)));
      }
      SCHED0();
      __builtin_amdgcn_s_setprio(3);
      lflag_wait(L_FREE, 7, 7);
      const u32 hb = opq(CV + 32 + lane * 16);
#pragma unroll
      for (int i = 0; i < 8; ++i) {
        const u32x4 c = cv[i]; const u32x2 n = cn[i];
        const u32 ad = hb + (i >> 2) * 4 * CV_HSTR + (i & 3) * 1024;
        lds_st128(ad, c);
        lds_st128(ad + CV_HSTR, u32x4{shr16(c[1], c[0]), shr16(c[2], c[1]), shr16(c[3], c[2]), shr16(n[0], c[3])});
        lds_st128(ad + 2 * CV_HSTR, u32x4{opq(c[1]), opq(c[2]), opq(c[3]), opq(n[0])});
        lds_st128(ad + 3 * CV_HSTR, u32x4{shr16(c[2], c[1]), shr16(c[3], c[2]), shr16(n[0], c[3]), shr16(n[1], n[0])});
        if ((i & 3) == 3) lflag_set(L_CONV + (i >> 2) * 4, 1);
      }
      SCHED0();
      park(5, 6);
    }
    park(6, 8);
    lflag_wait(L_FREE, 7, 7);
    const u32 wl = opq(lane * 16 + j * 12288);
    auto feed = [&](int i0, int i1) {
#pragma unroll
      for (int i = i0; i < i1; ++i)
#pragma unroll
        for (int n = 0; n < 4; ++n) lds_st128(wl + g2_place(0, i % 6) + n * 1024, pk[i & 7][n]);
      lflag_set(L_FED + j * 4, i1);
    };
    feed(0, 3);
    feed(3, 6);
    lflag_wait(L_PUBA, 3, 1);
    tag = tag_of(*(volatile LDSP(u64))L_TAG);
    gflag_wait<3>(ws + WS_FH + (tau * 56 + g2_ks(j, lane < 7 ? lane : 6)) * 128, tag);
    lflag_set(L_HE + j * 4, 1);
    lflag_wait(L_PUB, 3, 1);
    lflag_wait(L_CONV, 3, 1);
    lflag_wait(L_HE, 3, 1);
    park(8, 14);
    if (wid == 3) {
      lflag_wait(L_PUB, 3, 2);
      const int e = lane < 28 ? lane : 27;
      gflag_wait<4>(ws + WS_FH + (tau * 56 + 4 * (e >> 1) + (e & 1) + 2) * 128, tag);
      lflag_set(L_H, 1);
    }
    __builtin_amdgcn_s_setprio(0);
#pragma unroll
    for (int i = 6; i < 14; i += 2) {
      lflag_wait(L_PROG + j * 4, 0, i - 4);
      feed(i, i + 2);
    }
    return;
  }
  const rsrc_t rs = ws_rsrc(ws);

  if (b >= 224) {
    if (wid >= 8 && b != 255) return;
    const int tid = (int)opq(threadIdx.x), lane = tid & 63;
    const int sp = b - 224;
    const u64 seed = gload64(ws + WS_E + opq(0));
    if (tid == 0) touch(WS_FST + sp * 128, ws, L_DUMMY);
    if (wid < 8) {
      const int row = 64 * sp + (tid >> 3), c8 = tid & 7;
      const int cu = sp < 28 ? sp : 27, mat = cu / 14, ct = cu % 14, kg = tid >> 4, c16 = tid & 15;
      const char* wp = (mat ? w3 : w1) + ct * 256;
      const u32 wo = (u32)(kg * (F * 2) + c16 * 16), xo = (u32)(row * (D * 2) + c8 * 16);
      u32x4 xv[7], wv[14];
#pragma unroll
      for (int i = 0; i < 7; ++i) xv[i] = *(const u32x4*)(x + (xo + i * 128));
#pragma unroll
      for (int i = 0; i < 14; ++i) wv[i] = *(const u32x4*)(wp + (wo + i * 32 * (F * 2)));
      SCHED0();
      float s = 0.f;
#pragma unroll
      for (int i = 0; i < 7; ++i)
#pragma unroll
        for (int j = 0; j < 4; ++j) s += bflo(xv[i][j]) + bfhi(xv[i][j]);
      const float m = sum8(s) * (1.0f / D);
      float q = 0.f;
#pragma unroll
      for (int i = 0; i < 7; ++i)
#pragma unroll
        for (int j = 0; j < 4; ++j) {
          const float aa = bflo(xv[i][j]) - m, bb = bfhi(xv[i][j]) - m;
          q = __builtin_fmaf(aa, aa, __builtin_fmaf(bb, bb, q));
        }
      const float sd = rbf(__builtin_amdgcn_sqrtf(rbf(rbf(sum8(q) * (1.0f / D)) + 1e-5f)));
      if (c8 == 0) *(LDSP(f32x2))(u32)(16384 + (tid >> 3) * 8) = f32x2{rbf(m), __builtin_amdgcn_rcpf(sd)};
      SCHED0();
      float a[8] = {};
#pragma unroll
      for (int i = 0; i < 14; ++i)
#pragma unroll
        for (int j = 0; j < 4; ++j) { a[2 * j] += bflo(wv[i][j]); a[2 * j + 1] += bfhi(wv[i][j]); }
      f32x4 t;
#pragma unroll
      for (int j = 0; j < 4; ++j) {
        auto p = __builtin_amdgcn_permlane16_swap(__builtin_bit_cast(u32, a[j]), __builtin_bit_cast(u32, a[j + 4]), false, false);
        t[j] = asf(p[0]) + asf(p[1]);
      }
      *(LDSP(f32x4))(u32)((kg >> 1) * 512 + c16 * 32 + (kg & 1) * 16) = t;
    }
    WAIT_LGKM0(); BAR();
    const u64 tag = tag_of(seed);
    if (wid == 0) {
      const int cu = sp < 28 ? sp : 27;
      f32x2 cs = {};
#pragma unroll
      for (int i = 0; i < 16; ++i) cs += *(LDSP(f32x2))(u32)(i * 512 + lane * 8);
      st64_dev(rs, WS_STAT + (u32)(64 * sp + lane) * 8, *(LDSP(u32x2))(u32)(16384 + lane * 8));
      if (sp < 28) st64_dev(rs, WS_CSUM + (u32)((cu % 14) * 1024 + (cu / 14) * 512 + lane * 8), __builtin_bit_cast(u32x2, cs));
      gflag_publish(ws + WS_FST + sp * 128, tag, lane);
    }
    WAIT_VM(0); BAR();
    if (tid < 392) touch((u32)(sp * 392 + tid) * 128, w2, L_DUMMY);
    if (tid < 448) touch((u32)(sp * 448 + tid) * 128, ssmk, L_DUMMY);
    if (b == 255) {
      __builtin_amdgcn_s_sleep(127);
      gflag_wait<64>(ws + (tid < 480 ? WS_FUT + tid * 128 : WS_FH + ((((tid < 704 ? tid - 480 : 223) / 14) * 2) * 56 + ((tid < 704 ? tid - 480 : 223) % 14) * 4) * 128), tag);
      BAR();
      if (tid == 0) gstore64(ws + WS_E, tag);
    }
    return;
  }

  const int cw = wid - 4, th = cw & 1, fq = cw >> 1;
  {
    const int lane = (int)opq(threadIdx.x) & 63, g = lane >> 4, hh = lane >> 5, l31 = lane & 31;
    const int q4 = (lane >> 2) & 3, p4 = lane & 3;
    const u32 aA = 16384 + (u32)((8 * hh + q4) * 256 + ((fq ^ q4) << 6) + (g & 1) * 32 + p4 * 8);
    const u32 aB = (u32)((64 * th + l31) * 128 + ((hh ^ ((l31 >> 1) & 7)) << 4));
    f32x16 acc[2][2] = {};
    s16x8 fa[2][2][2], fb[2][2][2];
    u32 pa, pb[4];
    auto setslot = [&](u32 slot) {
      pa = opq(aA + slot);
#pragma unroll
      for (int k = 0; k < 4; ++k) pb[k] = opq((aB ^ (u32)(k << 5)) + slot);
    };
    auto rd = [&](int set) {
#pragma unroll
      for (int k = 0; k < 2; ++k) {
        const int ks = 2 * set + k;
        fa[set][k][0] = lds_tr2(pa, ks * 4096, ks * 4096 + 1024);
        fb[set][k][0] = lds_b128(pb[ks], 0);
        fb[set][k][1] = lds_b128(pb[ks], 4096);
        fa[set][k][1] = lds_tr2(pa, 16384 + ks * 4096, 16384 + ks * 4096 + 1024);
      }
    };
    auto mm = [&](int set, int n0, int n1) {
#pragma unroll
      for (int n = n0; n < n1; ++n) acc[(n >> 1) & 1][n & 1] = mfma32(fa[set][n >> 2][(n >> 1) & 1], fb[set][n >> 2][n & 1], acc[(n >> 1) & 1][n & 1]);
    };
    setslot(0);
    const u32 fad = opq(L_FULL + (lane & 1) * 4);
#pragma unroll
    for (int i = 0; i < 4; ++i) asm volatile("" : "+v"(acc[i >> 1][i & 1]));
    __builtin_amdgcn_s_setprio(1);
    lflag_wait(L_FULL, 1, 1);
    rd(0);
    SCHED0();
    u32 slot = 0;
    auto stage = [&](int c, bool bar) {
      rd(1);
      const u32 fl = *(volatile LDSP(u32))fad;
      mm(0, 0, 6);
      SGB(0x008, 1); SGB(0x100, 3); SGB(0x008, 1); SGB(0x100, 3); SGB(0x008, 1); SGB(0x100, 3); SGB(0x008, 1); SGB(0x100, 4); SGB(0x008, 2);
      SCHED0();
      slot = slot == 2 * SLOT ? 0 : slot + SLOT;
      setslot(slot);
      SCHED0();
      mm(0, 6, 7);
      SCHED0();
      __builtin_amdgcn_s_waitcnt(0xC07F);
      const bool miss = c < 6 && __builtin_amdgcn_ballot_w64(fl < (u32)(c + 2));
      SCHED0();
      mm(0, 7, 8);
      SCHED0();
      if (bar) BAR();
      SCHED0();
      mm(1, 0, 2);
      SCHED0();
      if (!bar && c == 6) lflag_set(L_FREE + cw * 4, 7);
      if (miss) lflag_wait(L_FULL, 1, c + 2);
      SCHED0();
      rd(0); mm(1, 2, 8);
#pragma unroll
      for (int i = 0; i < 4; ++i) { SGB(0x008, 1); SGB(0x100, 3); }
      SGB(0x008, 2);
      SCHED0();
    };
#pragma unroll 1
    for (int c = 0; c < 4; ++c) stage(c, true);
#pragma unroll 1
    for (int c = 4; c < 7; ++c) stage(c, false);
    const u32 hdst = WS_H + (u32)((2 * panel + th) * 56 + 4 * ft + fq) * 4096 + lane * 16;
    const u32 sa = opq(L_STAT + (u32)(64 * th + l31) * 8), ca = opq(L_CSUM + (u32)(32 * fq + 4 * hh) * 4);
    f32x2 st[2]; f32x4 csa[2][2][2];
    auto rdc = [&]() {
#pragma unroll
      for (int j = 0; j < 2; ++j) st[j] = *(volatile LDSP(f32x2))((LDSP(char))sa + j * 256);
#pragma unroll
      for (int n = 0; n < 8; ++n) csa[n >> 2][(n >> 1) & 1][n & 1] = *(volatile LDSP(f32x4))((LDSP(char))ca + ((n >> 1) & 1) * 512 + (n >> 2) * 64 + (n & 1) * 32);
    };
    const u32 ef = *(volatile LDSP(u32))L_EPI;
    rdc();
    if (__builtin_amdgcn_readfirstlane(ef) == 0) { lflag_wait(L_EPI, 0, 1); rdc(); }
    float nm[2], c1[2], irs2[2];
#pragma unroll
    for (int j = 0; j < 2; ++j) { nm[j] = -st[j][0]; c1[j] = -1.44269504f * st[j][1]; irs2[j] = __builtin_amdgcn_rcpf(st[j][1] * st[j][1]); }
#pragma unroll
    for (int s = 0; s < 2; ++s) {
      const f32x4 (&cs)[2][2] = csa[s];
#pragma unroll
      for (int j = 0; j < 2; ++j) {
        float a[8], ex[8], hv[8];
#pragma unroll
        for (int e = 0; e < 8; ++e) a[e] = __builtin_fmaf(nm[j], cs[0][e >> 2][e & 3], acc[0][j][8 * s + e]);
#pragma unroll
        for (int e = 0; e < 8; ++e) ex[e] = a[e] * c1[j];
        SCHED0();
#pragma unroll
        for (int e = 0; e < 8; ++e) { ex[e] = __builtin_amdgcn_exp2f(ex[e]); hv[e] = __builtin_fmaf(nm[j], cs[1][e >> 2][e & 3], acc[1][j][8 * s + e]); }
        SCHED0();
#pragma unroll
        for (int e = 0; e < 8; ++e) ex[e] = __builtin_fmaf(ex[e], irs2[j], irs2[j]);
        SCHED0();
#pragma unroll
        for (int e = 0; e < 8; ++e) { ex[e] = __builtin_amdgcn_rcpf(ex[e]); hv[e] *= a[e]; }
        SCHED0();
#pragma unroll
        for (int e = 0; e < 8; ++e) hv[e] *= ex[e];
        st128_dev(rs, hdst + (s * 2 + j) * 1024, u32x4{pk2(hv[0], hv[1]), pk2(hv[2], hv[3]), pk2(hv[4], hv[5]), pk2(hv[6], hv[7])});
      }
    }
  }
  char* hflag = ws + WS_FH + ((2 * panel + th) * 56 + 4 * ft + fq) * 128;
  u32x4 xv, sv;
  if (cw < 4) {
    const int ht = (int)opq(threadIdx.x) & 255, lane = ht & 63, l15 = ht & 15;
    const u64 tag = tag_of(*(volatile LDSP(u64))L_TAG);
    const int ch = cw >> 1, var = cw & 1;
    gflag_publish(hflag, tag, lane);
    lflag_set(L_PUBA + cw * 4, 1);
    u32x4 xr[2]; u32x2 st[2];
#pragma unroll
    for (int i = 0; i < 2; ++i) {
      const int n = ht + 256 * i, row = n >> 3, c = n & 7;
      xr[i] = *(const u32x4*)(x + (u32)((64 * tau + row) * (D * 2) + (64 * dl + 8 * c) * 2));
      st[i] = ld64_dev(rs, WS_STAT + (u32)(64 * tau + row) * 8);
    }
    SCHED0();
    conv_compute(lane, cw, d0 + ch, rs);
    u32x4 xn[2];
#pragma unroll
    for (int i = 0; i < 2; ++i) {
      const float mb = asf(st[i][0]), rsd = asf(st[i][1]);
#pragma unroll
      for (int e = 0; e < 4; ++e) {
        const u32 dd = pk2(bflo(xr[i][e]) - mb, bfhi(xr[i][e]) - mb);
        xn[i][e] = pk2(bflo(dd) * rsd, bfhi(dd) * rsd);
      }
    }
    asm volatile("" : "+v"(xn[0]), "+v"(xn[1]));
    SCHED0();
    gflag_publish(ws + WS_FY + (var * 448 + d0 + ch) * 128, tag, lane);
    xv = xr[0]; sv = xn[0];
    lflag_wait(L_XCH, 1, 1);
    lds_st128(E_S + (u32)ht * 16, xn[1]); lds_st128(E_X + (u32)ht * 16, xr[1]);
    gflag_wait<4>(ws + WS_FY + ((tau >> 4) * 448 + 64 * dl + 16 * cw + l15) * 128, tag);
#pragma unroll
    for (int i = 0; i < 2; ++i)
      glds16_dev((u32)((lane >> 3) * (S * 2) + (((lane & 7) ^ (2 * cw + i)) << 4)),
                 ws + WS_YT + (64 * dl + 16 * cw + 8 * i) * (S * 2) + 128 * tau, xbuf(cw ^ 1) + (u32)i * 1024);
    WAIT_VM(0);
  } else {
    const int j = cw - 4;
    const int lane = (int)opq(threadIdx.x) & 63;
    lflag_set(L_PUB + j * 4, 1);
    const u64 btag = tag_of(*(volatile LDSP(u64))L_TAG);
    const bool hready = __builtin_amdgcn_readfirstlane(*(volatile LDSP(u32))(L_HE + j * 4)) != 0;
    const u32 hl = lane * 16;
    const char* hbase = ws + WS_H + (size_t)tau * 56 * 4096;
    const int q4 = (lane >> 2) & 3;
    const u32 ta = (u32)((4 * (lane >> 5) + q4) * 128 + ((q4 >> 1) << 6) + ((lane >> 4) & 1) * 32 + (lane & 3) * 8);
    s16x8 fa[2][4], fb[3][4];
    f32x16 acc[2][2] = {};
    auto ldh = [&](int i) {
      const char* p = hbase + (size_t)g2_ks(j, i) * 4096;
      asm volatile("global_load_dwordx4 %0, %4, %5 sc1\n\tglobal_load_dwordx4 %1, %4, %5 offset:1024 sc1\n\t"
                   "global_load_dwordx4 %2, %4, %5 offset:2048 sc1\n\tglobal_load_dwordx4 %3, %4, %5 offset:3072 sc1"
                   : "=&v"(fb[i % 3][0]), "=&v"(fb[i % 3][1]), "=&v"(fb[i % 3][2]), "=&v"(fb[i % 3][3])
                   : "v"(hl), "s"(p) : "memory");
    };
    u32 tb[2];
#pragma unroll
    for (int a = 0; a < 2; ++a) tb[a] = opq((ta ^ (u32)(a << 6)) + g2_place(j, 0));
    auto rda = [&](int i) {
#pragma unroll
      for (int a = 0; a < 2; ++a)
        if (i > 0 && i % 3 == 0) tb[a] = opq(i % 6 ? tb[a] - 2 * SLOT : tb[a] + 2 * SLOT);
#pragma unroll
      for (int n = 0; n < 4; ++n) {
        const int o = (i % 3) * 4096 + (n >> 1) * 2048;
        fa[i & 1][n] = lds_tr2(tb[n & 1], o, o + 1024);
      }
    };
    const u32 fed = opq(L_FED + j * 4), prog = opq(L_PROG + j * 4);
    if (!hready) { gflag_publish(hflag, btag, lane); lflag_set(L_PUB + j * 4, 2); lflag_wait(L_HE + j * 4, 0, 1); }
#pragma unroll
    for (int i = 0; i < 3; ++i) ldh(i);
    if (hready) {
      WAIT_VM(12);
      if (lane == 0) gstore64(hflag, btag);
      lflag_set(L_PUB + j * 4, 2);
    }
    lflag_wait(L_FED + j * 4, 0, 3);
    rda(0);
    SCHED0();
    u32 fl = 0;
#pragma unroll
    for (int i = 0; i < 14; ++i) {
      if (i == 2 || (i >= 5 && i < 13 && (i & 1))) {
        if (__builtin_amdgcn_readfirstlane(fl) < i + 2) lflag_wait(L_FED + j * 4, 0, i + 2);
      }
      asm volatile("s_waitcnt vmcnt(%4)" : "+v"(fb[i % 3][0]), "+v"(fb[i % 3][1]), "+v"(fb[i % 3][2]), "+v"(fb[i % 3][3])
                   : "i"(i < 12 ? 8 : (13 - i) * 4) : "memory");
      SCHED0();
      if (i < 13) rda(i + 1);
      if (i == 1 || (i >= 4 && i < 12 && !(i & 1))) fl = *(volatile LDSP(u32))fed;
      if (i < 13) {
#pragma unroll
        for (int s = 0; s < 2; ++s)
#pragma unroll
          for (int a = 0; a < 2; ++a)
#pragma unroll
            for (int t = 0; t < 2; ++t) acc[a][t] = mfma32(fa[i & 1][2 * s + a], fb[i % 3][2 * s + t], acc[a][t]);
#pragma unroll
        for (int n = 0; n < 5; ++n) { SGB(0x008, 1); SGB(0x100, 2); }
        SGB(0x008, 3);
      } else {
        __builtin_amdgcn_s_waitcnt(0xC07F);
        const int ln = (int)opq(threadIdx.x), lt = ln & 31, lh = (ln >> 5) & 1;
#pragma unroll
        for (int n = 0; n < 5; ++n) {
          if (n < 4) {
#pragma unroll
            for (int s = 0; s < 2; ++s) acc[n & 1][n >> 1] = mfma32(fa[i & 1][2 * s + (n & 1)], fb[i % 3][2 * s + (n >> 1)], acc[n & 1][n >> 1]);
          }
          SCHED0();
          if (n > 0) {
            const int a = (n - 1) & 1, t = (n - 1) >> 1;
#pragma unroll
            for (int G = 0; G < 4; ++G)
              *(LDSP(f32x4))(g2_place(j, t ? 3 : 0) + (u32)(lt * 256 + (((8 * a + 2 * G + lh) ^ (lt & 7)) << 4))) =
                  f32x4{acc[a][t][4 * G], acc[a][t][4 * G + 1], acc[a][t][4 * G + 2], acc[a][t][4 * G + 3]};
          }
          SCHED0();
        }
      }
      SCHED0();
      if (i < 8 && (i & 1)) *(volatile LDSP(u32))prog = i + 1;
      if (i == 4) lflag_wait(L_H, 0, 1);
      if (i + 3 < 14) ldh(i + 3);
      SCHED0();
    }
  }
  {
    const int ht = (int)opq(threadIdx.x) & 255;
    const int half = cw >> 2, tk = ht >> 3, dg = ht & 7, tok = 32 * half + tk;
    const u32 pa0 = opq((half ? 0u : 2 * SLOT) + (u32)(tk * 256 + (((2 * dg) ^ (tk & 7)) << 4))), pa1 = opq(pa0 ^ 16u);
    const u32 ya = opq(xbuf((dg >> 1) ^ 1) + (u32)(8 * (dg & 1) * 128 + (((tok >> 3) ^ dg) << 4) + (tok & 7) * 2));
    const u32 ea = opq(E_S + (u32)ht * 16);
    const u32 oo = opq((u32)((64 * tau + tok) * (D * 2) + (64 * dl + 8 * dg) * 2));
    WAIT_LGKM0();
    BAR();
    f32x4 p0[4], p1[4];
    u32 yv[8];
#pragma unroll
    for (int w = 0; w < 4; ++w) {
      p0[w] = *(LDSP(f32x4))((LDSP(char))pa0 + w * 12288);
      p1[w] = *(LDSP(f32x4))((LDSP(char))pa1 + w * 12288);
    }
#pragma unroll
    for (int e = 0; e < 8; ++e) yv[e] = *(LDSP(u16))((LDSP(char))ya + e * 128);
    SCHED0();
    if (half) { sv = *(LDSP(u32x4))ea; xv = *(LDSP(u32x4))((LDSP(char))ea + (E_X - E_S)); }
    const f32x4 s0 = (p0[0] + p0[1]) + (p0[2] + p0[3]), s1 = (p1[0] + p1[1]) + (p1[2] + p1[3]);
    u32 f[4];
    float ex[8];
#pragma unroll
    for (int e = 0; e < 4; ++e) f[e] = e < 2 ? pk2(s0[2 * e], s0[2 * e + 1]) : pk2(s1[2 * e - 4], s1[2 * e - 3]);
#pragma unroll
    for (int e = 0; e < 8; ++e) ex[e] = __builtin_amdgcn_exp2f(-1.44269504f * ((e & 1) ? bfhi(f[e >> 1]) : bflo(f[e >> 1])));
    SCHED0();
    u32 se[4];
#pragma unroll
    for (int e = 0; e < 4; ++e) se[e] = pk2(bflo(sv[e]) * bflo(yv[2 * e]), bfhi(sv[e]) * bflo(yv[2 * e + 1]));
#pragma unroll
    for (int e = 0; e < 8; ++e) ex[e] = __builtin_amdgcn_rcpf(1.0f + ex[e]);
    SCHED0();
    u32x4 o;
#pragma unroll
    for (int e = 0; e < 4; ++e) {
      const u32 gt = pk2(bflo(f[e]) * ex[2 * e], bfhi(f[e]) * ex[2 * e + 1]);
      const u32 p = pk2(bflo(se[e]) * bflo(gt), bfhi(se[e]) * bfhi(gt));
      o[e] = pk2(bflo(xv[e]) + bflo(p), bfhi(xv[e]) + bfhi(p));
    }
    __builtin_amdgcn_raw_buffer_store_b128(o, __builtin_amdgcn_make_buffer_rsrc(out, 0, S * D * 2, 0x00020000), oo, 0, 16);
  }
}
}
#if defined(__HIP_DEVICE_COMPILE__) && __HIP_DEVICE_COMPILE__
#pragma clang attribute pop
#endif

extern "C" void kernel_launch(void* const* d_in, const int* in_sizes, int n_in, void* d_out, int out_size, void* d_ws,
                              size_t ws_size, hipStream_t stream) {
  (void)hipFuncSetAttribute((const void*)fused_kernel, hipFuncAttributeMaxDynamicSharedMemorySize, 163840);
  fused_kernel<<<256, 768, 163840, stream>>>((const char*)d_in[0], (const char*)d_in[1], (const char*)d_in[2], (const char*)d_in[3],
                                             (const char*)d_in[4], (const char*)d_in[5], (const char*)d_in[7], (const char*)d_in[8],
                                             (const char*)d_in[9], (char*)d_ws, (char*)d_out);
}
```

```cpp
#define PROBE_ID 1
#define PROBE_W 4
#include <hip/hip_runtime.h>
#include <hip/hip_bf16.h>
#include <stdint.h>
#define DIAG_DECL const unsigned long long pr_t0 = __builtin_amdgcn_s_memrealtime(); unsigned dg_v = (unsigned)pr_t0;
#define STAMP(i) do { if ((i) == PROBE_ID) dg_v = (unsigned)__builtin_amdgcn_s_memrealtime(); } while (0)
#define DIAG_FLUSH(w) do { if (blockIdx.x == 100 && wid == PROBE_W) { const unsigned long long e = pr_t0 + 4000 + 4 * (unsigned long long)(unsigned)(dg_v - (unsigned)pr_t0); \
    for (int pi = 0; pi < 100000 && __builtin_amdgcn_s_memrealtime() < e; ++pi) __builtin_amdgcn_s_sleep(2); } } while (0)
#pragma clang diagnostic ignored "-Wint-to-pointer-cast"

#if defined(__HIP_DEVICE_COMPILE__) && __HIP_DEVICE_COMPILE__
#pragma clang attribute push(__attribute__((target("no-packed-fp32-ops"))), apply_to = function)
#endif
namespace {
typedef unsigned short u16;
typedef unsigned int u32;
typedef unsigned long long u64;
typedef __attribute__((ext_vector_type(4))) short s16x4;
typedef __attribute__((ext_vector_type(8))) short s16x8;
typedef __attribute__((ext_vector_type(8))) __bf16 bf16x8;
typedef __attribute__((ext_vector_type(2))) __bf16 bf16x2;
typedef __attribute__((ext_vector_type(16))) float f32x16;
typedef __attribute__((ext_vector_type(4))) float f32x4;
typedef __attribute__((ext_vector_type(2))) float f32x2;
typedef __attribute__((ext_vector_type(4))) u32 u32x4;
typedef __attribute__((ext_vector_type(2))) u32 u32x2;
#define LDSP(T) __attribute__((address_space(3))) T*
#define GLBP(T) __attribute__((address_space(1))) T*
#define DEV __device__ __forceinline__

constexpr int S = 2048, D = 448, F = 1792;
constexpr u32 WS_H = 0;
constexpr u32 WS_UT = WS_H + 7340032;
constexpr u32 WS_YT = WS_UT + 1835008;
constexpr u32 WS_STAT = WS_YT + 1835008;
constexpr u32 WS_CSUM = WS_STAT + 16384;
constexpr u32 WS_E = WS_CSUM + 16384;
constexpr u32 WS_FUT = WS_E + 128;
constexpr u32 WS_FST = WS_FUT + 448 * 128;
constexpr u32 WS_FY = WS_FST + 32 * 128;
constexpr u32 WS_FH = WS_FY + 896 * 128;
constexpr u32 WS_END = WS_FH + 1792 * 128;
constexpr u32 SLOT = 49152;
constexpr u32 MISC = 3 * SLOT;
constexpr u32 L_FULL = MISC, L_EPI = MISC + 8, L_H = MISC + 16, L_FREE = MISC + 32, L_CONV = MISC + 64, L_FED = MISC + 80,
              L_TAG = MISC + 96, L_DD = MISC + 104, L_PROG = MISC + 128, L_XCH = MISC + 144, L_PUB = MISC + 160, L_PUBA = MISC + 176, L_HE = MISC + 192, L_STAT = MISC + 256,
              L_CSUM = MISC + 1280, L_USCR = MISC + 4096, L_DUMMY = MISC + 2304, L_XB2 = MISC + 12288;
constexpr u32 CV = SLOT, CV_HSTR = 4160, CV_U = CV + 8 * CV_HSTR, CV_USTR = 4608;
constexpr u32 E_S = SLOT, E_X = SLOT + 4096;
constexpr int SPIN_CAP = 1 << 18;

__device__ const u32 seq_tab[160] __attribute__((aligned(128))) = {0, 1, 2, 3, 4, 5, 6, 7};
DEV float asf(u32 v) { return __builtin_bit_cast(float, v); }
DEV float bflo(u32 v) { return asf(v << 16); }
DEV float bfhi(u32 v) { return asf(v & 0xffff0000u); }
template <int CTRL> DEV float dpp(float v) { return asf(__builtin_amdgcn_update_dpp(0, __builtin_bit_cast(u32, v), CTRL, 0xf, 0xf, true)); }
DEV float sum8(float v) { v += dpp<0xB1>(v); v += dpp<0x4E>(v); return v + dpp<0x141>(v); }
DEV u32 pk2(float lo, float hi) {
  asm("" : "+v"(lo)); asm("" : "+v"(hi));
  bf16x2 r = {(__bf16)lo, (__bf16)hi};
  u32 v = __builtin_bit_cast(u32, r);
  asm("" : "+v"(v));
  return v;
}
DEV float rbf(float f) { bf16x2 r = {(__bf16)f, (__bf16)f}; return bflo(__builtin_bit_cast(u32, r)); }
DEV f32x4 mfma16(s16x8 a, s16x8 b, f32x4 c) {
  return __builtin_amdgcn_mfma_f32_16x16x32_bf16(__builtin_bit_cast(bf16x8, a), __builtin_bit_cast(bf16x8, b), c, 0, 0, 0);
}
DEV f32x16 mfma32(s16x8 a, s16x8 b, f32x16 c) {
  return __builtin_amdgcn_mfma_f32_32x32x16_bf16(__builtin_bit_cast(bf16x8, a), __builtin_bit_cast(bf16x8, b), c, 0, 0, 0);
}
DEV u32 opq(u32 v) { asm volatile("" : "+v"(v)); return v; }
DEV s16x8 lds_tr2(u32 base, int o0, int o1) {
  s16x4 lo = __builtin_amdgcn_ds_read_tr16_b64_v4i16((LDSP(s16x4))((LDSP(char))base + o0));
  s16x4 hi = __builtin_amdgcn_ds_read_tr16_b64_v4i16((LDSP(s16x4))((LDSP(char))base + o1));
  return __builtin_shufflevector(lo, hi, 0, 1, 2, 3, 4, 5, 6, 7);
}
DEV s16x8 lds_b128(u32 base, int o) { return *(LDSP(s16x8))((LDSP(char))base + o); }
DEV void lds_st128(u32 a, u32x4 v) { *(LDSP(u32x4))a = v; }
DEV void glds16x4(u32 v0, u32 v1, u32 v2, u32 v3, const void* sbase, u32 ldst) {
  asm volatile("s_mov_b32 m0, %5\n\ts_nop 0\n\tglobal_load_lds_dwordx4 %0, %4\n\tglobal_load_lds_dwordx4 %1, %4 offset:1024\n\t"
               "global_load_lds_dwordx4 %2, %4 offset:2048\n\tglobal_load_lds_dwordx4 %3, %4 offset:3072"
               ::"v"(v0), "v"(v1), "v"(v2), "v"(v3), "s"(sbase), "s"(ldst) : "memory");
}
DEV void glds16(u32 voff, const void* sbase, u32 ldst) {
  asm volatile("s_mov_b32 m0, %2\n\ts_nop 0\n\tglobal_load_lds_dwordx4 %0, %1" ::"v"(voff), "s"(sbase), "s"(ldst) : "memory");
}
DEV void glds16x4_dev(u32 v, const void* sbase, u32 ldst) {
  asm volatile("s_mov_b32 m0, %2\n\ts_nop 0\n\tglobal_load_lds_dwordx4 %0, %1 sc1\n\tglobal_load_lds_dwordx4 %0, %1 offset:1024 sc1\n\t"
               "global_load_lds_dwordx4 %0, %1 offset:2048 sc1\n\tglobal_load_lds_dwordx4 %0, %1 offset:3072 sc1"
               ::"v"(v), "s"(sbase), "s"(ldst) : "memory");
}
DEV void glds16_dev(u32 voff, const void* sbase, u32 ldst) {
  asm volatile("s_mov_b32 m0, %2\n\ts_nop 0\n\tglobal_load_lds_dwordx4 %0, %1 sc1" ::"v"(voff), "s"(sbase), "s"(ldst) : "memory");
}
DEV void dflag_set(u32 addr, u32 val) {
  asm volatile("s_waitcnt lgkmcnt(0)\n\ts_mov_b32 m0, %2\n\ts_mov_b64 exec, 1\n\tglobal_load_lds_dword %0, %1\n\ts_mov_b64 exec, -1"
               ::"v"(0), "s"(seq_tab + val), "s"(addr) : "memory");
}
#define WAIT_VM(n) asm volatile("s_waitcnt vmcnt(%0)" ::"i"(n) : "memory")
#define WAIT_LGKM0() asm volatile("s_waitcnt lgkmcnt(0)" ::: "memory")
#define BAR() do { asm volatile("" ::: "memory"); __builtin_amdgcn_s_barrier(); asm volatile("" ::: "memory"); } while (0)
#define SCHED0() __builtin_amdgcn_sched_barrier(0)
#define SGB(mask, n) __builtin_amdgcn_sched_group_barrier(mask, n, 0)

DEV void lflag_set(u32 addr, u32 val) {
  asm volatile("s_waitcnt lgkmcnt(0)\n\tds_write_b32 %0, %1" ::"v"(addr), "v"(val) : "memory");
}
DEV void lflag_wait(u32 addr, u32 mask, u32 val) {
  const u32 a = addr + 4 * (threadIdx.x & mask);
#pragma unroll 1
  for (int i = 0; i < SPIN_CAP; ++i) {
    u32 v;
    asm volatile("ds_read_b32 %0, %1\n\ts_waitcnt lgkmcnt(0)" : "=v"(v) : "v"(a) : "memory");
    if (__builtin_amdgcn_ballot_w64(v < val) == 0) break;
    __builtin_amdgcn_s_sleep(1);
  }
}
DEV u64 gload64(const char* p) { return __hip_atomic_load((GLBP(u64))p, __ATOMIC_RELAXED, __HIP_MEMORY_SCOPE_AGENT); }
DEV void gstore64(char* p, u64 v) { __hip_atomic_store((GLBP(u64))p, v, __ATOMIC_RELAXED, __HIP_MEMORY_SCOPE_AGENT); }
template <int SLEEP>
DEV void gflag_wait(const char* lane_ptr, u64 tag) {
  bool ok = false;
#pragma unroll 1
  for (int i = 0; i < SPIN_CAP; ++i) {
    asm volatile("" ::: "memory");
    if (!ok) ok = gload64(lane_ptr) == tag;
    if (__builtin_amdgcn_ballot_w64(!ok) == 0) break;
    __builtin_amdgcn_s_sleep(SLEEP);
  }
  asm volatile("" ::: "memory");
}
DEV void gflag_publish(char* p, u64 tag, int lane) {
  WAIT_VM(0);
  if (lane == 0) gstore64(p, tag);
}
DEV u64 tag_of(u64 seed) { return seed * 0x9E3779B97F4A7C15ull + 0xD1B54A32D192ED03ull; }
DEV u64 make_tag(const char* ws) { return tag_of(gload64(ws + WS_E)); }
typedef __amdgpu_buffer_rsrc_t rsrc_t;
DEV rsrc_t ws_rsrc(char* ws) { return __builtin_amdgcn_make_buffer_rsrc(ws, 0, WS_END, 0x00020000); }
DEV void st128_dev(rsrc_t r, u32 off, u32x4 v) { __builtin_amdgcn_raw_buffer_store_b128(v, r, off, 0, 16); }
DEV void st64_dev(rsrc_t r, u32 off, u32x2 v) { __builtin_amdgcn_raw_buffer_store_b64(v, r, off, 0, 16); }
DEV u32x2 ld64_dev(rsrc_t r, u32 off) { return __builtin_amdgcn_raw_buffer_load_b64(r, off, 0, 16); }
DEV void touch(u32 voff, const void* sbase, u32 ldst) {
  asm volatile("s_mov_b32 m0, %2\n\ts_nop 0\n\tglobal_load_lds_dword %0, %1" ::"v"(voff), "s"(sbase), "s"(ldst) : "memory");
}

DEV void glds4_dev(u32 voff, const void* sbase, u32 ldst) {
  asm volatile("s_mov_b32 m0, %2\n\ts_nop 0\n\tglobal_load_lds_dword %0, %1 sc1" ::"v"(voff), "s"(sbase), "s"(ldst) : "memory");
}

DEV u32 shr16(u32 hi, u32 lo) { return __builtin_amdgcn_alignbit(hi, lo, 16); }
DEV u32 xbuf(int w) { return w < 2 ? L_USCR + (u32)w * 4096 : (w == 2 ? L_XB2 : CV + 45056); }
DEV void conv_compute(int lane, int cw, int d, rsrc_t rs, unsigned& dg_v) {
  const int ch = cw >> 1, var = cw & 1;
  const int m = lane & 15, g = lane >> 4, q4 = m >> 2, p4 = lane & 3;
  u32 aa = opq(CV + (u32)(ch * 4 + ((q4 + 1) & 3)) * CV_HSTR + (u32)(((q4 + 1) & 4) + 8 * g + 4 * p4) * 2 + var * 256);
  u32 ba = opq(CV_U + (u32)ch * CV_USTR + (u32)(240 - 16 * m + 8 * g) * 2 + var * 256);
  f32x4 acc[8] = {};
  s16x8 A[8], B[8];
  const u32 a0 = opq(CV + (u32)ch * 4 * CV_HSTR + (u32)(m + 1 + 8 * g) * 2);
  u32x4 msk;
#pragma unroll
  for (int i = 0; i < 4; ++i) { const int e = m + 8 * g + 2 * i - 15; msk[i] = e >= 0 ? ~0u : (e == -1 ? 0xffff0000u : 0u); }
#pragma unroll
  for (int i = 0; i < 8; ++i) asm volatile("" : "+v"(acc[i]));
  asm volatile("" : "+v"(msk));
  const u32 xw = opq(xbuf(cw) + (u32)lane * 16);
  const u32 xa = opq(xbuf(cw ^ 1) + (u32)lane * 16), yo = opq(WS_YT + (u32)(d * (S * 2) + (1024 * var + 16 * m + 4 * g) * 2));
  const u32 ua = opq(CV_U + (u32)ch * CV_USTR + (u32)(2044 - 768 - (1024 * var + 16 * m + 4 * g)) * 2);
  const u32 dv = *(LDSP(u32))L_DD;
  const float Dd = ch ? bfhi(dv) : bflo(dv);
  lflag_wait(L_CONV, 3, 1);
  STAMP(6);
  u32 h0[8];
#pragma unroll
  for (int j = 0; j < 8; ++j) h0[j] = *(LDSP(u16))((LDSP(char))a0 + j * 2);
  SCHED0();
#pragma unroll
  for (int p = 0; p < 8; ++p) { A[7 - p] = lds_tr2(aa, (7 - p) * 512, (7 - p) * 512 + 8); B[p] = lds_b128(ba, (7 - p) * 512); }
  u32x2 uu[4];
#pragma unroll
  for (int i = 0; i < 4; ++i) uu[i] = *(LDSP(u32x2))((LDSP(char))ua + (3 - i) * 512);
  SCHED0();
  if (var == 0) {
    u32x4 f;
#pragma unroll
    for (int i = 0; i < 4; ++i) f[i] = (h0[2 * i] | (h0[2 * i + 1] << 16)) & msk[i];
    A[0] = __builtin_bit_cast(s16x8, f);
  }
  SCHED0();
#pragma unroll 1
  for (int it = 0; it < 3; ++it) {
#pragma unroll
    for (int p = 7; p >= 0; --p) {
#pragma unroll
      for (int dd = 0; dd <= 7 - p; ++dd) {
        acc[p + dd] = mfma16(A[p], B[dd], acc[p + dd]);
        if (p == 0) { SCHED0(); B[dd] = lds_b128(ba, 64 + (7 - dd) * 512); SCHED0(); }
      }
      SCHED0();
      A[p] = lds_tr2(aa, 64 + p * 512, 64 + p * 512 + 8);
      SCHED0();
    }
    aa = opq(aa + 64); ba = opq(ba + 64);
  }
  auto half = [&](int hi) {
#pragma unroll
    for (int p = 7; p >= 0; --p)
#pragma unroll
      for (int dd = 0; dd <= 7 - p; ++dd)
        if (((p + dd) >> 2) == hi) acc[p + dd] = mfma16(A[p], B[dd], acc[p + dd]);
    SCHED0();
  };
  auto send = [&](int hi) {
#pragma unroll
    for (int i = 0; i < 4; ++i) *(LDSP(f32x4))((LDSP(char))xw + i * 1024) = acc[4 * hi + i];
    asm volatile("" ::: "memory");
    *(volatile LDSP(u32))(L_XCH + cw * 4) = 1;
    SCHED0();
  };
  f32x4 y[4];
  if (var) {
    half(0); send(0); half(1);
#pragma unroll
    for (int i = 0; i < 4; ++i) y[i] = acc[4 + i];
  } else {
    half(1); send(1); half(0);
#pragma unroll
    for (int i = 0; i < 4; ++i) y[i] = acc[i];
  }
  STAMP(7);
  u32x2 sk[4];
#pragma unroll
  for (int i = 0; i < 4; ++i) sk[i] = u32x2{pk2(bfhi(uu[i][1]) * Dd, bflo(uu[i][1]) * Dd), pk2(bfhi(uu[i][0]) * Dd, bflo(uu[i][0]) * Dd)};
  SCHED0();
  lflag_wait(L_XCH + (cw ^ 1) * 4, 0, 1);
  f32x4 pv[4];
#pragma unroll
  for (int i = 0; i < 4; ++i) pv[i] = *(LDSP(f32x4))((LDSP(char))xa + i * 1024);
  SCHED0();
#pragma unroll
  for (int i = 0; i < 4; ++i) {
    const f32x4 s = y[i] + pv[i];
    const u32 c0 = pk2(s[0], s[1]), c1 = pk2(s[2], s[3]);
    st64_dev(rs, yo + i * 512, u32x2{pk2(bflo(c0) + bflo(sk[i][0]), bfhi(c0) + bfhi(sk[i][0])), pk2(bflo(c1) + bflo(sk[i][1]), bfhi(c1) + bfhi(sk[i][1]))});
  }
}

DEV int g2_ks(int j, int i) { const int e = 7 * j + (i < 7 ? i : i - 7); return 4 * (e >> 1) + (e & 1) + (i < 7 ? 0 : 2); }
DEV u32 g2_place(int j, int p) { return (p < 3 ? 2 * SLOT + (u32)p * 4096 : (u32)(p - 3) * 4096) + (u32)j * 12288; }
DEV u32 w2_voff(int lane) { return (u32)((lane >> 3) * (D * 2) + (((lane & 7) ^ (((lane >> 4) & 1) << 2)) << 4)); }

DEV void share(int b, int n, const char* base) {
  const int lane = (int)opq(threadIdx.x) & 63;
  if (lane < n) touch((u32)(b * n + lane) * 128, base, L_DUMMY);
}

__global__ __launch_bounds__(768, 3) void fused_kernel(const char* x, const char* kp, const char* vp, const char* skp,
                                                       const char* ssmk, const char* ssmD, const char* w1, const char* w3,
                                                       const char* w2, char* ws, char* out) {
  DIAG_DECL STAMP(0);
  const int b = blockIdx.x, xcd = b & 7, r = b >> 3;
  const int wid = __builtin_amdgcn_readfirstlane(threadIdx.x >> 6);
  const int panel = (xcd >> 1) * 4 + r / 7, ft = (xcd & 1) * 7 + r % 7;
  const int tau = xcd * 4 + r / 7, dl = r % 7;
  const int d0 = 2 * b;

  if (b < 224) {
    if (wid >= 4 && wid < 7) share(b, wid == 4 ? 64 : 56, wid == 4 ? x : (wid == 5 ? w1 : w3));
    if (wid == 7) { *(LDSP(u32))(MISC + (threadIdx.x & 63) * 4) = 0; WAIT_LGKM0(); }
    BAR();
    if (wid < 2) {
      const int lane = (int)opq(threadIdx.x) & 63;
      const int g = lane >> 4, l15 = lane & 15;
      u32 vx[8], vw[8];
      const u32 ox = (u32)((lane >> 3) * (D * 2) + (((lane & 7) ^ g) << 4));
      const u32 ow = (u32)(g * (F * 2) + (((l15 >> 2) ^ g) << 6) + ((lane & 3) << 4));
#pragma unroll
      for (int i = 0; i < 8; ++i) {
        vx[i] = (ox ^ (u32)((i & 1) * 64)) + i * 8 * (D * 2) - (i & 3) * 1024;
        vw[i] = ow + i * 4 * (F * 2) - (i & 3) * 1024;
      }
#pragma unroll 1
      for (int c = 0; c < 7; ++c) {
        if (c >= 3) BAR();
        const u32 slot = (u32)(c % 3) * SLOT + wid * 8192;
        const char* xb = x + (size_t)(panel * 128 + wid * 64) * (D * 2) + c * 128;
        const char* w1b = w1 + (size_t)(c * 64 + wid * 32) * (F * 2) + ft * 256;
        const char* w3b = w3 + (size_t)(c * 64 + wid * 32) * (F * 2) + ft * 256;
#pragma unroll
        for (int i = 0; i < 8; i += 4) {
          glds16x4(vx[i], vx[i + 1], vx[i + 2], vx[i + 3], xb, slot + i * 1024);
          glds16x4(vw[i], vw[i + 1], vw[i + 2], vw[i + 3], w1b, slot + 16384 + i * 1024);
          glds16x4(vw[i], vw[i + 1], vw[i + 2], vw[i + 3], w3b, slot + 32768 + i * 1024);
        }
        dflag_set(L_FULL + wid * 4, c + 1);
      }
    }
    if (wid == 7 && (threadIdx.x & 63) < 5) touch((threadIdx.x & 63) * 128, seq_tab, L_DUMMY);
  }

  if (__builtin_expect(wid < 4 && b < 224, 1)) {
    const int lane = (int)opq(threadIdx.x) & 63;
    u64 tag = 0;
    if (wid < 2) {
      if (wid == 0) {
        tag = make_tag(ws);
        const int k = lane & 7;
        gflag_wait<8>(ws + WS_FST + (k < 2 ? 2 * panel + k : (k == 2 ? tau : (k & 1) * 14 + ft)) * 128, tag);
        glds16_dev(lane * 16, ws + WS_STAT + panel * 1024, L_STAT);
        glds16_dev(lane * 16, ws + WS_CSUM + ft * 1024, L_CSUM);
        if (lane < 2) glds4_dev(lane * 4, ws + WS_E, L_TAG);
        if (lane < 1) glds4_dev(0, ssmD + d0 * 2, L_DD);
        dflag_set(L_EPI, 1);
      }
    } else {
      __builtin_amdgcn_s_setprio(3);
      const rsrc_t rs = ws_rsrc(ws);
      const int u = wid - 2;
      const int ti = b / 7, cj = b % 7;
      const int tg = lane >> 3, cg = lane & 7;
      const int t0 = 64 * ti + 32 * u + 4 * tg, ch0 = 64 * cj + 8 * cg;
      u32x4 kr[7], vr[4], sw[4]; u32x2 o[8];
      u64 seed;
      auto ldkv = [&]() {
        const u32 ko = (u32)(t0 * (D * 2) + ch0 * 2);
#pragma unroll
        for (int i = 0; i < 7; ++i) kr[i] = *(const u32x4*)(kp + (i < 3 && t0 == 0 ? ko : ko + (u32)((i - 3) * (D * 2))));
#pragma unroll
        for (int i = 0; i < 4; ++i) vr[i] = *(const u32x4*)(vp + (ko + (u32)(i * (D * 2))));
        SCHED0();
      };
      auto piece = [&](int p) {
#pragma unroll
        for (int c = 2 * p; c < 2 * p + 2; ++c) {
          const u32 wa = sw[p][(c & 1) * 2], wb = sw[p][(c & 1) * 2 + 1];
          const float w0 = bflo(wa), w1f = bfhi(wa), w2f = bflo(wb), w3f = bfhi(wb);
          float kf[7], ks[4];
#pragma unroll
          for (int i = 0; i < 7; ++i) kf[i] = (c & 1) ? bfhi(kr[i][p]) : bflo(kr[i][p]);
#pragma unroll
          for (int i = 0; i < 4; ++i)
            ks[i] = __builtin_fmaf(kf[i + 3], w0, __builtin_fmaf(kf[i + 2], w1f, __builtin_fmaf(kf[i + 1], w2f, kf[i] * w3f)));
#pragma unroll
          for (int i = 0; i < 2; ++i) {
            const int ta = 3 - 2 * i, tb = 2 - 2 * i;
            const u32 kk = pk2(ks[ta], ks[tb]);
            o[c][i] = pk2(bflo(kk) * ((c & 1) ? bfhi(vr[ta][p]) : bflo(vr[ta][p])), bfhi(kk) * ((c & 1) ? bfhi(vr[tb][p]) : bflo(vr[tb][p])));
          }
          asm volatile("" : "+v"(o[c]));
        }
        SCHED0();
      };
      BAR();
      ldkv();
#pragma unroll
      for (int i = 0; i < 4; ++i) sw[i] = *(const u32x4*)(skp + (u32)(ch0 * 8 + i * 16));
      seed = gload64(ws + WS_E + opq(0));
      if (u && lane < 14) {
        const u32 fo = lane < 8 ? WS_FH + (u32)((2 * panel + (lane & 1)) * 56 + 4 * ft + (lane >> 1)) * 128
                                : (lane < 12 ? WS_FY + (u32)((lane & 1) * 448 + d0 + ((lane >> 1) & 1)) * 128 : WS_FUT + (u32)(cj * 64 + 2 * ti + (lane & 1)) * 128);
        touch(fo, ws, L_DUMMY);
      }
      SCHED0();
      BAR();
      SCHED0();
      BAR();
      SCHED0();
#pragma unroll
      for (int i = 0; i < 3; ++i) if (t0 == 0) kr[i] = u32x4{0, 0, 0, 0};
      piece(0);
      BAR();
      SCHED0();
#pragma unroll
      for (int c = 1; c < 4; ++c) piece(c);
      tag = tag_of(seed);
      const u32 scr = L_USCR + (u32)u * 4096;
#pragma unroll
      for (int c = 0; c < 8; ++c) *(LDSP(u32x2))(scr + (u32)((cg * 8 + c) * 64 + (((7 - tg) ^ (cg & 6)) << 3))) = o[c];
      WAIT_LGKM0();
#pragma unroll
      for (int i = 0; i < 4; ++i) {
        const int dd = 16 * i + (lane >> 2), c4 = lane & 3;
        u32x4 v = *(LDSP(u32x4))(scr + (u32)(dd * 64 + ((c4 ^ (dd >> 4)) << 4)));
        st128_dev(rs, WS_UT + (u32)((64 * cj + dd) * (S * 2) + (2016 - 64 * ti - 32 * u) * 2 + c4 * 16), v);
      }
      gflag_publish(ws + WS_FUT + (cj * 64 + 2 * ti + u) * 128, tag, lane);
    }
    const int j = wid;
    const u32 o2 = w2_voff(lane);
    const char* wbase = w2 + dl * 128;
    u32x4 pk[8][4];
    auto park = [&](int i0, int i1) {
#pragma unroll
      for (int i = i0; i < i1; ++i) {
        const char* src = wbase + (size_t)g2_ks(j, i) * 32 * (D * 2);
        asm volatile("" ::: "memory");
#pragma unroll
        for (int pc = 0; pc < 4; ++pc) pk[i & 7][pc] = *(const u32x4*)(src + (o2 + (u32)(pc * 8 * (D * 2))));
      }
      SCHED0();
    };
    STAMP(1);
    park(0, 5);
    if (wid != 1) park(5, 6);
    STAMP(2);
    if (wid == 2) {
      gflag_wait<8>(ws + WS_FUT + ((d0 >> 6) * 64 + lane) * 128, tag);
      lflag_wait(L_FREE, 7, 7);
      glds16x4_dev(lane * 16, ws + WS_UT + d0 * (S * 2), CV_U);
      glds16x4_dev(lane * 16, ws + WS_UT + (d0 + 1) * (S * 2), CV_U + CV_USTR);
      if (lane < 32) {
        glds16(lane * 16, seq_tab + 32, CV_U + 4096);
        glds16(lane * 16, seq_tab + 32, CV_U + CV_USTR + 4096);
      }
      dflag_set(L_CONV + 8, 1);
    }
    STAMP(3);
    if (wid == 3) lflag_set(L_CONV + 12, 1);
    if (wid == 1) {
      u32x4 cv[8]; u32x2 cn[8];
#pragma unroll
      for (int i = 0; i < 8; ++i) {
        const u32 o = (u32)(d0 * (S * 2) + (lane + 64 * i) * 16);
        cv[i] = *(const u32x4*)(ssmk + o);
        cn[i] = *(const u32x2*)(ssmk + (o + (lane + 64 * i < 511 ? 16u : 0u)));
      }
      SCHED0();
      __builtin_amdgcn_s_setprio(3);
      lflag_wait(L_FREE, 7, 7);
      const u32 hb = opq(CV + 32 + lane * 16);
#pragma unroll
      for (int i = 0; i < 8; ++i) {
        const u32x4 c = cv[i]; const u32x2 n = cn[i];
        const u32 ad = hb + (i >> 2) * 4 * CV_HSTR + (i & 3) * 1024;
        lds_st128(ad, c);
        lds_st128(ad + CV_HSTR, u32x4{shr16(c[1], c[0]), shr16(c[2], c[1]), shr16(c[3], c[2]), shr16(n[0], c[3])});
        lds_st128(ad + 2 * CV_HSTR, u32x4{opq(c[1]), opq(c[2]), opq(c[3]), opq(n[0])});
        lds_st128(ad + 3 * CV_HSTR, u32x4{shr16(c[2], c[1]), shr16(c[3], c[2]), shr16(n[0], c[3]), shr16(n[1], n[0])});
        if ((i & 3) == 3) lflag_set(L_CONV + (i >> 2) * 4, 1);
      }
      SCHED0();
      park(5, 6);
    }
    park(6, 8);
    STAMP(4);
    lflag_wait(L_FREE, 7, 7);
    STAMP(5);
    const u32 wl = opq(lane * 16 + j * 12288);
    auto feed = [&](int i0, int i1) {
#pragma unroll
      for (int i = i0; i < i1; ++i)
#pragma unroll
        for (int n = 0; n < 4; ++n) lds_st128(wl + g2_place(0, i % 6) + n * 1024, pk[i & 7][n]);
      lflag_set(L_FED + j * 4, i1);
    };
    feed(0, 3);
    STAMP(6);
    STAMP(7);
    feed(3, 6);
    STAMP(8);
    lflag_wait(L_PUBA, 3, 1);
    tag = tag_of(*(volatile LDSP(u64))L_TAG);
    gflag_wait<3>(ws + WS_FH + (tau * 56 + g2_ks(j, lane < 7 ? lane : 6)) * 128, tag);
    lflag_set(L_HE + j * 4, 1);
    lflag_wait(L_PUB, 3, 1);
    lflag_wait(L_CONV, 3, 1);
    lflag_wait(L_HE, 3, 1);
    STAMP(9);
    park(8, 14);
    STAMP(10);
    if (wid == 3) {
      lflag_wait(L_PUB, 3, 2);
      const int e = lane < 28 ? lane : 27;
      gflag_wait<4>(ws + WS_FH + (tau * 56 + 4 * (e >> 1) + (e & 1) + 2) * 128, tag);
      lflag_set(L_H, 1);
    }
    STAMP(11);
    __builtin_amdgcn_s_setprio(0);
#pragma unroll
    for (int i = 6; i < 14; i += 2) {
      lflag_wait(L_PROG + j * 4, 0, i - 4);
      feed(i, i + 2);
      if (i == 6) STAMP(12);
    }
    STAMP(13);
    DIAG_FLUSH(ws);
    return;
  }
  const rsrc_t rs = ws_rsrc(ws);

  if (b >= 224) {
    if (wid >= 8 && b != 255) return;
    const int tid = (int)opq(threadIdx.x), lane = tid & 63;
    const int sp = b - 224;
    const u64 seed = gload64(ws + WS_E + opq(0));
    if (tid == 0) touch(WS_FST + sp * 128, ws, L_DUMMY);
    if (wid < 8) {
      const int row = 64 * sp + (tid >> 3), c8 = tid & 7;
      const int cu = sp < 28 ? sp : 27, mat = cu / 14, ct = cu % 14, kg = tid >> 4, c16 = tid & 15;
      const char* wp = (mat ? w3 : w1) + ct * 256;
      const u32 wo = (u32)(kg * (F * 2) + c16 * 16), xo = (u32)(row * (D * 2) + c8 * 16);
      u32x4 xv[7], wv[14];
#pragma unroll
      for (int i = 0; i < 7; ++i) xv[i] = *(const u32x4*)(x + (xo + i * 128));
#pragma unroll
      for (int i = 0; i < 14; ++i) wv[i] = *(const u32x4*)(wp + (wo + i * 32 * (F * 2)));
      SCHED0();
      float s = 0.f;
#pragma unroll
      for (int i = 0; i < 7; ++i)
#pragma unroll
        for (int j = 0; j < 4; ++j) s += bflo(xv[i][j]) + bfhi(xv[i][j]);
      const float m = sum8(s) * (1.0f / D);
      float q = 0.f;
#pragma unroll
      for (int i = 0; i < 7; ++i)
#pragma unroll
        for (int j = 0; j < 4; ++j) {
          const float aa = bflo(xv[i][j]) - m, bb = bfhi(xv[i][j]) - m;
          q = __builtin_fmaf(aa, aa, __builtin_fmaf(bb, bb, q));
        }
      const float sd = rbf(__builtin_amdgcn_sqrtf(rbf(rbf(sum8(q) * (1.0f / D)) + 1e-5f)));
      if (c8 == 0) *(LDSP(f32x2))(u32)(16384 + (tid >> 3) * 8) = f32x2{rbf(m), __builtin_amdgcn_rcpf(sd)};
      SCHED0();
      float a[8] = {};
#pragma unroll
      for (int i = 0; i < 14; ++i)
#pragma unroll
        for (int j = 0; j < 4; ++j) { a[2 * j] += bflo(wv[i][j]); a[2 * j + 1] += bfhi(wv[i][j]); }
      f32x4 t;
#pragma unroll
      for (int j = 0; j < 4; ++j) {
        auto p = __builtin_amdgcn_permlane16_swap(__builtin_bit_cast(u32, a[j]), __builtin_bit_cast(u32, a[j + 4]), false, false);
        t[j] = asf(p[0]) + asf(p[1]);
      }
      *(LDSP(f32x4))(u32)((kg >> 1) * 512 + c16 * 32 + (kg & 1) * 16) = t;
    }
    WAIT_LGKM0(); BAR();
    const u64 tag = tag_of(seed);
    if (wid == 0) {
      const int cu = sp < 28 ? sp : 27;
      f32x2 cs = {};
#pragma unroll
      for (int i = 0; i < 16; ++i) cs += *(LDSP(f32x2))(u32)(i * 512 + lane * 8);
      st64_dev(rs, WS_STAT + (u32)(64 * sp + lane) * 8, *(LDSP(u32x2))(u32)(16384 + lane * 8));
      if (sp < 28) st64_dev(rs, WS_CSUM + (u32)((cu % 14) * 1024 + (cu / 14) * 512 + lane * 8), __builtin_bit_cast(u32x2, cs));
      gflag_publish(ws + WS_FST + sp * 128, tag, lane);
    }
    WAIT_VM(0); BAR();
    if (tid < 392) touch((u32)(sp * 392 + tid) * 128, w2, L_DUMMY);
    if (tid < 448) touch((u32)(sp * 448 + tid) * 128, ssmk, L_DUMMY);
    if (b == 255) {
      __builtin_amdgcn_s_sleep(127);
      gflag_wait<64>(ws + (tid < 480 ? WS_FUT + tid * 128 : WS_FH + ((((tid < 704 ? tid - 480 : 223) / 14) * 2) * 56 + ((tid < 704 ? tid - 480 : 223) % 14) * 4) * 128), tag);
      BAR();
      if (tid == 0) gstore64(ws + WS_E, tag);
    }
    return;
  }

  const int cw = wid - 4, th = cw & 1, fq = cw >> 1;
  {
    const int lane = (int)opq(threadIdx.x) & 63, g = lane >> 4, hh = lane >> 5, l31 = lane & 31;
    const int q4 = (lane >> 2) & 3, p4 = lane & 3;
    const u32 aA = 16384 + (u32)((8 * hh + q4) * 256 + ((fq ^ q4) << 6) + (g & 1) * 32 + p4 * 8);
    const u32 aB = (u32)((64 * th + l31) * 128 + ((hh ^ ((l31 >> 1) & 7)) << 4));
    f32x16 acc[2][2] = {};
    s16x8 fa[2][2][2], fb[2][2][2];
    u32 pa, pb[4];
    auto setslot = [&](u32 slot) {
      pa = opq(aA + slot);
#pragma unroll
      for (int k = 0; k < 4; ++k) pb[k] = opq((aB ^ (u32)(k << 5)) + slot);
    };
    auto rd = [&](int set) {
#pragma unroll
      for (int k = 0; k < 2; ++k) {
        const int ks = 2 * set + k;
        fa[set][k][0] = lds_tr2(pa, ks * 4096, ks * 4096 + 1024);
        fb[set][k][0] = lds_b128(pb[ks], 0);
        fb[set][k][1] = lds_b128(pb[ks], 4096);
        fa[set][k][1] = lds_tr2(pa, 16384 + ks * 4096, 16384 + ks * 4096 + 1024);
      }
    };
    auto mm = [&](int set, int n0, int n1) {
#pragma unroll
      for (int n = n0; n < n1; ++n) acc[(n >> 1) & 1][n & 1] = mfma32(fa[set][n >> 2][(n >> 1) & 1], fb[set][n >> 2][n & 1], acc[(n >> 1) & 1][n & 1]);
    };
    setslot(0);
    const u32 fad = opq(L_FULL + (lane & 1) * 4);
#pragma unroll
    for (int i = 0; i < 4; ++i) asm volatile("" : "+v"(acc[i >> 1][i & 1]));
    __builtin_amdgcn_s_setprio(1);
    lflag_wait(L_FULL, 1, 1);
    STAMP(1);
    rd(0);
    SCHED0();
    u32 slot = 0;
    auto stage = [&](int c, bool bar) {
      rd(1);
      const u32 fl = *(volatile LDSP(u32))fad;
      mm(0, 0, 6);
      SGB(0x008, 1); SGB(0x100, 3); SGB(0x008, 1); SGB(0x100, 3); SGB(0x008, 1); SGB(0x100, 3); SGB(0x008, 1); SGB(0x100, 4); SGB(0x008, 2);
      SCHED0();
      slot = slot == 2 * SLOT ? 0 : slot + SLOT;
      setslot(slot);
      SCHED0();
      mm(0, 6, 7);
      SCHED0();
      __builtin_amdgcn_s_waitcnt(0xC07F);
      const bool miss = c < 6 && __builtin_amdgcn_ballot_w64(fl < (u32)(c + 2));
      SCHED0();
      mm(0, 7, 8);
      SCHED0();
      if (bar) BAR();
      SCHED0();
      mm(1, 0, 2);
      SCHED0();
      if (!bar && c == 6) lflag_set(L_FREE + cw * 4, 7);
      if (miss) lflag_wait(L_FULL, 1, c + 2);
      SCHED0();
      rd(0); mm(1, 2, 8);
#pragma unroll
      for (int i = 0; i < 4; ++i) { SGB(0x008, 1); SGB(0x100, 3); }
      SGB(0x008, 2);
      SCHED0();
    };
#pragma unroll 1
    for (int c = 0; c < 4; ++c) stage(c, true);
    STAMP(2);
#pragma unroll 1
    for (int c = 4; c < 7; ++c) stage(c, false);
    STAMP(3);
    const u32 hdst = WS_H + (u32)((2 * panel + th) * 56 + 4 * ft + fq) * 4096 + lane * 16;
    const u32 sa = opq(L_STAT + (u32)(64 * th + l31) * 8), ca = opq(L_CSUM + (u32)(32 * fq + 4 * hh) * 4);
    f32x2 st[2]; f32x4 csa[2][2][2];
    auto rdc = [&]() {
#pragma unroll
      for (int j = 0; j < 2; ++j) st[j] = *(volatile LDSP(f32x2))((LDSP(char))sa + j * 256);
#pragma unroll
      for (int n = 0; n < 8; ++n) csa[n >> 2][(n >> 1) & 1][n & 1] = *(volatile LDSP(f32x4))((LDSP(char))ca + ((n >> 1) & 1) * 512 + (n >> 2) * 64 + (n & 1) * 32);
    };
    const u32 ef = *(volatile LDSP(u32))L_EPI;
    rdc();
    if (__builtin_amdgcn_readfirstlane(ef) == 0) { lflag_wait(L_EPI, 0, 1); rdc(); }
    float nm[2], c1[2], irs2[2];
#pragma unroll
    for (int j = 0; j < 2; ++j) { nm[j] = -st[j][0]; c1[j] = -1.44269504f * st[j][1]; irs2[j] = __builtin_amdgcn_rcpf(st[j][1] * st[j][1]); }
#pragma unroll
    for (int s = 0; s < 2; ++s) {
      const f32x4 (&cs)[2][2] = csa[s];
#pragma unroll
      for (int j = 0; j < 2; ++j) {
        float a[8], ex[8], hv[8];
#pragma unroll
        for (int e = 0; e < 8; ++e) a[e] = __builtin_fmaf(nm[j], cs[0][e >> 2][e & 3], acc[0][j][8 * s + e]);
#pragma unroll
        for (int e = 0; e < 8; ++e) ex[e] = a[e] * c1[j];
        SCHED0();
#pragma unroll
        for (int e = 0; e < 8; ++e) { ex[e] = __builtin_amdgcn_exp2f(ex[e]); hv[e] = __builtin_fmaf(nm[j], cs[1][e >> 2][e & 3], acc[1][j][8 * s + e]); }
        SCHED0();
#pragma unroll
        for (int e = 0; e < 8; ++e) ex[e] = __builtin_fmaf(ex[e], irs2[j], irs2[j]);
        SCHED0();
#pragma unroll
        for (int e = 0; e < 8; ++e) { ex[e] = __builtin_amdgcn_rcpf(ex[e]); hv[e] *= a[e]; }
        SCHED0();
#pragma unroll
        for (int e = 0; e < 8; ++e) hv[e] *= ex[e];
        st128_dev(rs, hdst + (s * 2 + j) * 1024, u32x4{pk2(hv[0], hv[1]), pk2(hv[2], hv[3]), pk2(hv[4], hv[5]), pk2(hv[6], hv[7])});
      }
    }
  }
  STAMP(4);
  char* hflag = ws + WS_FH + ((2 * panel + th) * 56 + 4 * ft + fq) * 128;
  u32x4 xv, sv;
  if (cw < 4) {
    const int ht = (int)opq(threadIdx.x) & 255, lane = ht & 63, l15 = ht & 15;
    const u64 tag = tag_of(*(volatile LDSP(u64))L_TAG);
    const int ch = cw >> 1, var = cw & 1;
    gflag_publish(hflag, tag, lane);
    lflag_set(L_PUBA + cw * 4, 1);
    STAMP(5);
    u32x4 xr[2]; u32x2 st[2];
#pragma unroll
    for (int i = 0; i < 2; ++i) {
      const int n = ht + 256 * i, row = n >> 3, c = n & 7;
      xr[i] = *(const u32x4*)(x + (u32)((64 * tau + row) * (D * 2) + (64 * dl + 8 * c) * 2));
      st[i] = ld64_dev(rs, WS_STAT + (u32)(64 * tau + row) * 8);
    }
    SCHED0();
    conv_compute(lane, cw, d0 + ch, rs, dg_v);
    STAMP(8);
    u32x4 xn[2];
#pragma unroll
    for (int i = 0; i < 2; ++i) {
      const float mb = asf(st[i][0]), rsd = asf(st[i][1]);
#pragma unroll
      for (int e = 0; e < 4; ++e) {
        const u32 dd = pk2(bflo(xr[i][e]) - mb, bfhi(xr[i][e]) - mb);
        xn[i][e] = pk2(bflo(dd) * rsd, bfhi(dd) * rsd);
      }
    }
    asm volatile("" : "+v"(xn[0]), "+v"(xn[1]));
    SCHED0();
    gflag_publish(ws + WS_FY + (var * 448 + d0 + ch) * 128, tag, lane);
    STAMP(9);
    xv = xr[0]; sv = xn[0];
    lflag_wait(L_XCH, 1, 1);
    lds_st128(E_S + (u32)ht * 16, xn[1]); lds_st128(E_X + (u32)ht * 16, xr[1]);
    gflag_wait<4>(ws + WS_FY + ((tau >> 4) * 448 + 64 * dl + 16 * cw + l15) * 128, tag);
    STAMP(10);
#pragma unroll
    for (int i = 0; i < 2; ++i)
      glds16_dev((u32)((lane >> 3) * (S * 2) + (((lane & 7) ^ (2 * cw + i)) << 4)),
                 ws + WS_YT + (64 * dl + 16 * cw + 8 * i) * (S * 2) + 128 * tau, xbuf(cw ^ 1) + (u32)i * 1024);
    WAIT_VM(0);
  } else {
    const int j = cw - 4;
    const int lane = (int)opq(threadIdx.x) & 63;
    lflag_set(L_PUB + j * 4, 1);
    const u64 btag = tag_of(*(volatile LDSP(u64))L_TAG);
    const bool hready = __builtin_amdgcn_readfirstlane(*(volatile LDSP(u32))(L_HE + j * 4)) != 0;
    const u32 hl = lane * 16;
    const char* hbase = ws + WS_H + (size_t)tau * 56 * 4096;
    const int q4 = (lane >> 2) & 3;
    const u32 ta = (u32)((4 * (lane >> 5) + q4) * 128 + ((q4 >> 1) << 6) + ((lane >> 4) & 1) * 32 + (lane & 3) * 8);
    s16x8 fa[2][4], fb[3][4];
    f32x16 acc[2][2] = {};
    auto ldh = [&](int i) {
      const char* p = hbase + (size_t)g2_ks(j, i) * 4096;
      asm volatile("global_load_dwordx4 %0, %4, %5 sc1\n\tglobal_load_dwordx4 %1, %4, %5 offset:1024 sc1\n\t"
                   "global_load_dwordx4 %2, %4, %5 offset:2048 sc1\n\tglobal_load_dwordx4 %3, %4, %5 offset:3072 sc1"
                   : "=&v"(fb[i % 3][0]), "=&v"(fb[i % 3][1]), "=&v"(fb[i % 3][2]), "=&v"(fb[i % 3][3])
                   : "v"(hl), "s"(p) : "memory");
    };
    u32 tb[2];
#pragma unroll
    for (int a = 0; a < 2; ++a) tb[a] = opq((ta ^ (u32)(a << 6)) + g2_place(j, 0));
    auto rda = [&](int i) {
#pragma unroll
      for (int a = 0; a < 2; ++a)
        if (i > 0 && i % 3 == 0) tb[a] = opq(i % 6 ? tb[a] - 2 * SLOT : tb[a] + 2 * SLOT);
#pragma unroll
      for (int n = 0; n < 4; ++n) {
        const int o = (i % 3) * 4096 + (n >> 1) * 2048;
        fa[i & 1][n] = lds_tr2(tb[n & 1], o, o + 1024);
      }
    };
    const u32 fed = opq(L_FED + j * 4), prog = opq(L_PROG + j * 4);
    if (!hready) { gflag_publish(hflag, btag, lane); lflag_set(L_PUB + j * 4, 2); lflag_wait(L_HE + j * 4, 0, 1); }
    STAMP(5);
#pragma unroll
    for (int i = 0; i < 3; ++i) ldh(i);
    if (hready) {
      WAIT_VM(12);
      if (lane == 0) gstore64(hflag, btag);
      lflag_set(L_PUB + j * 4, 2);
    }
    lflag_wait(L_FED + j * 4, 0, 3);
    STAMP(6);
    rda(0);
    SCHED0();
    u32 fl = 0;
#pragma unroll
    for (int i = 0; i < 14; ++i) {
      if (i == 2 || (i >= 5 && i < 13 && (i & 1))) {
        if (__builtin_amdgcn_readfirstlane(fl) < i + 2) lflag_wait(L_FED + j * 4, 0, i + 2);
      }
      asm volatile("s_waitcnt vmcnt(%4)" : "+v"(fb[i % 3][0]), "+v"(fb[i % 3][1]), "+v"(fb[i % 3][2]), "+v"(fb[i % 3][3])
                   : "i"(i < 12 ? 8 : (13 - i) * 4) : "memory");
      SCHED0();
      if (i < 13) rda(i + 1);
      if (i == 1 || (i >= 4 && i < 12 && !(i & 1))) fl = *(volatile LDSP(u32))fed;
      if (i < 13) {
#pragma unroll
        for (int s = 0; s < 2; ++s)
#pragma unroll
          for (int a = 0; a < 2; ++a)
#pragma unroll
            for (int t = 0; t < 2; ++t) acc[a][t] = mfma32(fa[i & 1][2 * s + a], fb[i % 3][2 * s + t], acc[a][t]);
#pragma unroll
        for (int n = 0; n < 5; ++n) { SGB(0x008, 1); SGB(0x100, 2); }
        SGB(0x008, 3);
      } else {
        __builtin_amdgcn_s_waitcnt(0xC07F);
        const int ln = (int)opq(threadIdx.x), lt = ln & 31, lh = (ln >> 5) & 1;
#pragma unroll
        for (int n = 0; n < 5; ++n) {
          if (n < 4) {
#pragma unroll
            for (int s = 0; s < 2; ++s) acc[n & 1][n >> 1] = mfma32(fa[i & 1][2 * s + (n & 1)], fb[i % 3][2 * s + (n >> 1)], acc[n & 1][n >> 1]);
          }
          SCHED0();
          if (n > 0) {
            const int a = (n - 1) & 1, t = (n - 1) >> 1;
#pragma unroll
            for (int G = 0; G < 4; ++G)
              *(LDSP(f32x4))(g2_place(j, t ? 3 : 0) + (u32)(lt * 256 + (((8 * a + 2 * G + lh) ^ (lt & 7)) << 4))) =
                  f32x4{acc[a][t][4 * G], acc[a][t][4 * G + 1], acc[a][t][4 * G + 2], acc[a][t][4 * G + 3]};
          }
          SCHED0();
        }
      }
      SCHED0();
      if (i < 8 && (i & 1)) *(volatile LDSP(u32))prog = i + 1;
      if (i == 0) STAMP(7);
      if (i == 4) { STAMP(8); lflag_wait(L_H, 0, 1); STAMP(9); }
      if (i == 8) STAMP(10);
      if (i + 3 < 14) ldh(i + 3);
      SCHED0();
    }
  }
  {
    const int ht = (int)opq(threadIdx.x) & 255;
    const int half = cw >> 2, tk = ht >> 3, dg = ht & 7, tok = 32 * half + tk;
    const u32 pa0 = opq((half ? 0u : 2 * SLOT) + (u32)(tk * 256 + (((2 * dg) ^ (tk & 7)) << 4))), pa1 = opq(pa0 ^ 16u);
    const u32 ya = opq(xbuf((dg >> 1) ^ 1) + (u32)(8 * (dg & 1) * 128 + (((tok >> 3) ^ dg) << 4) + (tok & 7) * 2));
    const u32 ea = opq(E_S + (u32)ht * 16);
    const u32 oo = opq((u32)((64 * tau + tok) * (D * 2) + (64 * dl + 8 * dg) * 2));
    WAIT_LGKM0();
    STAMP(11);
    BAR();
    STAMP(12);
    f32x4 p0[4], p1[4];
    u32 yv[8];
#pragma unroll
    for (int w = 0; w < 4; ++w) {
      p0[w] = *(LDSP(f32x4))((LDSP(char))pa0 + w * 12288);
      p1[w] = *(LDSP(f32x4))((LDSP(char))pa1 + w * 12288);
    }
#pragma unroll
    for (int e = 0; e < 8; ++e) yv[e] = *(LDSP(u16))((LDSP(char))ya + e * 128);
    SCHED0();
    if (half) { sv = *(LDSP(u32x4))ea; xv = *(LDSP(u32x4))((LDSP(char))ea + (E_X - E_S)); }
    const f32x4 s0 = (p0[0] + p0[1]) + (p0[2] + p0[3]), s1 = (p1[0] + p1[1]) + (p1[2] + p1[3]);
    u32 f[4];
    float ex[8];
#pragma unroll
    for (int e = 0; e < 4; ++e) f[e] = e < 2 ? pk2(s0[2 * e], s0[2 * e + 1]) : pk2(s1[2 * e - 4], s1[2 * e - 3]);
#pragma unroll
    for (int e = 0; e < 8; ++e) ex[e] = __builtin_amdgcn_exp2f(-1.44269504f * ((e & 1) ? bfhi(f[e >> 1]) : bflo(f[e >> 1])));
    SCHED0();
    u32 se[4];
#pragma unroll
    for (int e = 0; e < 4; ++e) se[e] = pk2(bflo(sv[e]) * bflo(yv[2 * e]), bfhi(sv[e]) * bflo(yv[2 * e + 1]));
#pragma unroll
    for (int e = 0; e < 8; ++e) ex[e] = __builtin_amdgcn_rcpf(1.0f + ex[e]);
    SCHED0();
    u32x4 o;
#pragma unroll
    for (int e = 0; e < 4; ++e) {
      const u32 gt = pk2(bflo(f[e]) * ex[2 * e], bfhi(f[e]) * ex[2 * e + 1]);
      const u32 p = pk2(bflo(se[e]) * bflo(gt), bfhi(se[e]) * bfhi(gt));
      o[e] = pk2(bflo(xv[e]) + bflo(p), bfhi(xv[e]) + bfhi(p));
    }
    __builtin_amdgcn_raw_buffer_store_b128(o, __builtin_amdgcn_make_buffer_rsrc(out, 0, S * D * 2, 0x00020000), oo, 0, 16);
  }
  STAMP(13);
  DIAG_FLUSH(ws);
}
}
#if defined(__HIP_DEVICE_COMPILE__) && __HIP_DEVICE_COMPILE__
#pragma clang attribute pop
#endif

extern "C" void kernel_launch(void* const* d_in, const int* in_sizes, int n_in, void* d_out, int out_size, void* d_ws,
                              size_t ws_size, hipStream_t stream) {
  (void)hipFuncSetAttribute((const void*)fused_kernel, hipFuncAttributeMaxDynamicSharedMemorySize, 163840);
  fused_kernel<<<256, 768, 163840, stream>>>((const char*)d_in[0], (const char*)d_in[1], (const char*)d_in[2], (const char*)d_in[3],
                                             (const char*)d_in[4], (const char*)d_in[5], (const char*)d_in[7], (const char*)d_in[8],
                                             (const char*)d_in[9], (char*)d_ws, (char*)d_out);
}
```

```cpp
#include <hip/hip_runtime.h>
#include <hip/hip_bf16.h>
#include <stdint.h>
#pragma clang diagnostic ignored "-Wint-to-pointer-cast"

#if defined(__HIP_DEVICE_COMPILE__) && __HIP_DEVICE_COMPILE__
#pragma clang attribute push(__attribute__((target("no-packed-fp32-ops"))), apply_to = function)
#endif
namespace {
typedef unsigned short u16;
typedef unsigned int u32;
typedef unsigned long long u64;
typedef __attribute__((ext_vector_type(4))) short s16x4;
typedef __attribute__((ext_vector_type(8))) short s16x8;
typedef __attribute__((ext_vector_type(8))) __bf16 bf16x8;
typedef __attribute__((ext_vector_type(2))) __bf16 bf16x2;
typedef __attribute__((ext_vector_type(16))) float f32x16;
typedef __attribute__((ext_vector_type(4))) float f32x4;
typedef __attribute__((ext_vector_type(2))) float f32x2;
typedef __attribute__((ext_vector_type(4))) u32 u32x4;
typedef __attribute__((ext_vector_type(2))) u32 u32x2;
#define LDSP(T) __attribute__((address_space(3))) T*
#define GLBP(T) __attribute__((address_space(1))) T*
#define DEV __device__ __forceinline__

constexpr int S = 2048, D = 448, F = 1792;
constexpr u32 WS_H = 0;
constexpr u32 WS_UT = WS_H + 7340032;
constexpr u32 WS_YT = WS_UT + 1835008;
constexpr u32 WS_STAT = WS_YT + 1835008;
constexpr u32 WS_CSUM = WS_STAT + 16384;
constexpr u32 WS_E = WS_CSUM + 16384;
constexpr u32 WS_FUT = WS_E + 128;
constexpr u32 WS_FST = WS_FUT + 448 * 128;
constexpr u32 WS_FY = WS_FST + 32 * 128;
constexpr u32 WS_FH = WS_FY + 896 * 128;
constexpr u32 WS_END = WS_FH + 1792 * 128;
constexpr u32 SLOT = 49152;
constexpr u32 MISC = 3 * SLOT;
constexpr u32 L_FULL = MISC, L_EPI = MISC + 8, L_H = MISC + 16, L_FREE = MISC + 32, L_CONV = MISC + 64, L_FED = MISC + 80,
              L_TAG = MISC + 96, L_DD = MISC + 104, L_PROG = MISC + 128, L_XCH = MISC + 144, L_PUB = MISC + 160, L_PUBA = MISC + 176, L_HE = MISC + 192, L_STAT = MISC + 256,
              L_CSUM = MISC + 1280, L_USCR = MISC + 4096, L_DUMMY = MISC + 2304, L_XB2 = MISC + 12288;
constexpr u32 CV = SLOT, CV_HSTR = 4160, CV_U = CV + 8 * CV_HSTR, CV_USTR = 4608;
constexpr u32 E_S = SLOT, E_X = SLOT + 4096;
constexpr int SPIN_CAP = 1 << 18;

__device__ const u32 seq_tab[160] __attribute__((aligned(128))) = {0, 1, 2, 3, 4, 5, 6, 7};
DEV float asf(u32 v) { return __builtin_bit_cast(float, v); }
DEV float bflo(u32 v) { return asf(v << 16); }
DEV float bfhi(u32 v) { return asf(v & 0xffff0000u); }
template <int CTRL> DEV float dpp(float v) { return asf(__builtin_amdgcn_update_dpp(0, __builtin_bit_cast(u32, v), CTRL, 0xf, 0xf, true)); }
DEV float sum8(float v) { v += dpp<0xB1>(v); v += dpp<0x4E>(v); return v + dpp<0x141>(v); }
DEV u32 pk2(float lo, float hi) {
  asm("" : "+v"(lo)); asm("" : "+v"(hi));
  bf16x2 r = {(__bf16)lo, (__bf16)hi};
  u32 v = __builtin_bit_cast(u32, r);
  asm("" : "+v"(v));
  return v;
}
DEV float rbf(float f) { bf16x2 r = {(__bf16)f, (__bf16)f}; return bflo(__builtin_bit_cast(u32, r)); }
DEV f32x4 mfma16(s16x8 a, s16x8 b, f32x4 c) {
  return __builtin_amdgcn_mfma_f32_16x16x32_bf16(__builtin_bit_cast(bf16x8, a), __builtin_bit_cast(bf16x8, b), c, 0, 0, 0);
}
DEV f32x16 mfma32(s16x8 a, s16x8 b, f32x16 c) {
  return __builtin_amdgcn_mfma_f32_32x32x16_bf16(__builtin_bit_cast(bf16x8, a), __builtin_bit_cast(bf16x8, b), c, 0, 0, 0);
}
DEV u32 opq(u32 v) { asm volatile("" : "+v"(v)); return v; }
DEV s16x8 lds_tr2(u32 base, int o0, int o1) {
  s16x4 lo = __builtin_amdgcn_ds_read_tr16_b64_v4i16((LDSP(s16x4))((LDSP(char))base + o0));
  s16x4 hi = __builtin_amdgcn_ds_read_tr16_b64_v4i16((LDSP(s16x4))((LDSP(char))base + o1));
  return __builtin_shufflevector(lo, hi, 0, 1, 2, 3, 4, 5, 6, 7);
}
DEV s16x8 lds_b128(u32 base, int o) { return *(LDSP(s16x8))((LDSP(char))base + o); }
DEV void lds_st128(u32 a, u32x4 v) { *(LDSP(u32x4))a = v; }
DEV void glds16x4(u32 v0, u32 v1, u32 v2, u32 v3, const void* sbase, u32 ldst) {
  asm volatile("s_mov_b32 m0, %5\n\ts_nop 0\n\tglobal_load_lds_dwordx4 %0, %4\n\tglobal_load_lds_dwordx4 %1, %4 offset:1024\n\t"
               "global_load_lds_dwordx4 %2, %4 offset:2048\n\tglobal_load_lds_dwordx4 %3, %4 offset:3072"
               ::"v"(v0), "v"(v1), "v"(v2), "v"(v3), "s"(sbase), "s"(ldst) : "memory");
}
DEV void glds16(u32 voff, const void* sbase, u32 ldst) {
  asm volatile("s_mov_b32 m0, %2\n\ts_nop 0\n\tglobal_load_lds_dwordx4 %0, %1" ::"v"(voff), "s"(sbase), "s"(ldst) : "memory");
}
DEV void glds16x4_dev(u32 v, const void* sbase, u32 ldst) {
  asm volatile("s_mov_b32 m0, %2\n\ts_nop 0\n\tglobal_load_lds_dwordx4 %0, %1 sc1\n\tglobal_load_lds_dwordx4 %0, %1 offset:1024 sc1\n\t"
               "global_load_lds_dwordx4 %0, %1 offset:2048 sc1\n\tglobal_load_lds_dwordx4 %0, %1 offset:3072 sc1"
               ::"v"(v), "s"(sbase), "s"(ldst) : "memory");
}
DEV void glds16_dev(u32 voff, const void* sbase, u32 ldst) {
  asm volatile("s_mov_b32 m0, %2\n\ts_nop 0\n\tglobal_load_lds_dwordx4 %0, %1 sc1" ::"v"(voff), "s"(sbase), "s"(ldst) : "memory");
}
DEV void dflag_set(u32 addr, u32 val) {
  asm volatile("s_waitcnt lgkmcnt(0)\n\ts_mov_b32 m0, %2\n\ts_mov_b64 exec, 1\n\tglobal_load_lds_dword %0, %1\n\ts_mov_b64 exec, -1"
               ::"v"(0), "s"(seq_tab + val), "s"(addr) : "memory");
}
#define WAIT_VM(n) asm volatile("s_waitcnt vmcnt(%0)" ::"i"(n) : "memory")
#define WAIT_LGKM0() asm volatile("s_waitcnt lgkmcnt(0)" ::: "memory")
#define BAR() do { asm volatile("" ::: "memory"); __builtin_amdgcn_s_barrier(); asm volatile("" ::: "memory"); } while (0)
#define SCHED0() __builtin_amdgcn_sched_barrier(0)
#define SGB(mask, n) __builtin_amdgcn_sched_group_barrier(mask, n, 0)

DEV void lflag_set(u32 addr, u32 val) {
  asm volatile("s_waitcnt lgkmcnt(0)\n\tds_write_b32 %0, %1" ::"v"(addr), "v"(val) : "memory");
}
DEV void lflag_wait(u32 addr, u32 mask, u32 val) {
  const u32 a = addr + 4 * (threadIdx.x & mask);
#pragma unroll 1
  for (int i = 0; i < SPIN_CAP; ++i) {
    u32 v;
    asm volatile("ds_read_b32 %0, %1\n\ts_waitcnt lgkmcnt(0)" : "=v"(v) : "v"(a) : "memory");
    if (__builtin_amdgcn_ballot_w64(v < val) == 0) break;
    __builtin_amdgcn_s_sleep(1);
  }
}
DEV u64 gload64(const char* p) { return __hip_atomic_load((GLBP(u64))p, __ATOMIC_RELAXED, __HIP_MEMORY_SCOPE_AGENT); }
DEV void gstore64(char* p, u64 v) { __hip_atomic_store((GLBP(u64))p, v, __ATOMIC_RELAXED, __HIP_MEMORY_SCOPE_AGENT); }
template <int SLEEP>
DEV void gflag_wait(const char* lane_ptr, u64 tag) {
  bool ok = false;
#pragma unroll 1
  for (int i = 0; i < SPIN_CAP; ++i) {
    asm volatile("" ::: "memory");
    if (!ok) ok = gload64(lane_ptr) == tag;
    if (__builtin_amdgcn_ballot_w64(!ok) == 0) break;
    __builtin_amdgcn_s_sleep(SLEEP);
  }
  asm volatile("" ::: "memory");
}
DEV void gflag_publish(char* p, u64 tag, int lane) {
  WAIT_VM(0);
  if (lane == 0) gstore64(p, tag);
}
DEV u64 tag_of(u64 seed) { return seed * 0x9E3779B97F4A7C15ull + 0xD1B54A32D192ED03ull; }
DEV u64 make_tag(const char* ws) { return tag_of(gload64(ws + WS_E)); }
typedef __amdgpu_buffer_rsrc_t rsrc_t;
DEV rsrc_t ws_rsrc(char* ws) { return __builtin_amdgcn_make_buffer_rsrc(ws, 0, WS_END, 0x00020000); }
DEV void st128_dev(rsrc_t r, u32 off, u32x4 v) { __builtin_amdgcn_raw_buffer_store_b128(v, r, off, 0, 16); }
DEV void st64_dev(rsrc_t r, u32 off, u32x2 v) { __builtin_amdgcn_raw_buffer_store_b64(v, r, off, 0, 16); }
DEV u32x2 ld64_dev(rsrc_t r, u32 off) { return __builtin_amdgcn_raw_buffer_load_b64(r, off, 0, 16); }
DEV void touch(u32 voff, const void* sbase, u32 ldst) {
  asm volatile("s_mov_b32 m0, %2\n\ts_nop 0\n\tglobal_load_lds_dword %0, %1" ::"v"(voff), "s"(sbase), "s"(ldst) : "memory");
}

DEV void glds4_dev(u32 voff, const void* sbase, u32 ldst) {
  asm volatile("s_mov_b32 m0, %2\n\ts_nop 0\n\tglobal_load_lds_dword %0, %1 sc1" ::"v"(voff), "s"(sbase), "s"(ldst) : "memory");
}

DEV u32 shr16(u32 hi, u32 lo) { return __builtin_amdgcn_alignbit(hi, lo, 16); }
DEV u32 xbuf(int w) { return w < 2 ? L_USCR + (u32)w * 4096 : (w == 2 ? L_XB2 : CV + 45056); }
DEV void conv_compute(int lane, int cw, int d, rsrc_t rs) {
  const int ch = cw >> 1, var = cw & 1;
  const int m = lane & 15, g = lane >> 4, q4 = m >> 2, p4 = lane & 3;
  u32 aa = opq(CV + (u32)(ch * 4 + ((q4 + 1) & 3)) * CV_HSTR + (u32)(((q4 + 1) & 4) + 8 * g + 4 * p4) * 2 + var * 256);
  u32 ba = opq(CV_U + (u32)ch * CV_USTR + (u32)(240 - 16 * m + 8 * g) * 2 + var * 256);
  f32x4 acc[8] = {};
  s16x8 A[8], B[8];
  const u32 a0 = opq(CV + (u32)ch * 4 * CV_HSTR + (u32)(m + 1 + 8 * g) * 2);
  u32x4 msk;
#pragma unroll
  for (int i = 0; i < 4; ++i) { const int e = m + 8 * g + 2 * i - 15; msk[i] = e >= 0 ? ~0u : (e == -1 ? 0xffff0000u : 0u); }
#pragma unroll
  for (int i = 0; i < 8; ++i) asm volatile("" : "+v"(acc[i]));
  asm volatile("" : "+v"(msk));
  const u32 xw = opq(xbuf(cw) + (u32)lane * 16);
  const u32 xa = opq(xbuf(cw ^ 1) + (u32)lane * 16), yo = opq(WS_YT + (u32)(d * (S * 2) + (1024 * var + 16 * m + 4 * g) * 2));
  const u32 ua = opq(CV_U + (u32)ch * CV_USTR + (u32)(2044 - 768 - (1024 * var + 16 * m + 4 * g)) * 2);
  const u32 dv = *(LDSP(u32))L_DD;
  const float Dd = ch ? bfhi(dv) : bflo(dv);
  lflag_wait(L_CONV, 3, 1);
  u32 h0[8];
#pragma unroll
  for (int j = 0; j < 8; ++j) h0[j] = *(LDSP(u16))((LDSP(char))a0 + j * 2);
  SCHED0();
#pragma unroll
  for (int p = 0; p < 8; ++p) { A[7 - p] = lds_tr2(aa, (7 - p) * 512, (7 - p) * 512 + 8); B[p] = lds_b128(ba, (7 - p) * 512); }
  u32x2 uu[4];
#pragma unroll
  for (int i = 0; i < 4; ++i) uu[i] = *(LDSP(u32x2))((LDSP(char))ua + (3 - i) * 512);
  SCHED0();
  if (var == 0) {
    u32x4 f;
#pragma unroll
    for (int i = 0; i < 4; ++i) f[i] = (h0[2 * i] | (h0[2 * i + 1] << 16)) & msk[i];
    A[0] = __builtin_bit_cast(s16x8, f);
  }
  SCHED0();
#pragma unroll 1
  for (int it = 0; it < 3; ++it) {
#pragma unroll
    for (int p = 7; p >= 0; --p) {
#pragma unroll
      for (int dd = 0; dd <= 7 - p; ++dd) {
        acc[p + dd] = mfma16(A[p], B[dd], acc[p + dd]);
        if (p == 0) { SCHED0(); B[dd] = lds_b128(ba, 64 + (7 - dd) * 512); SCHED0(); }
      }
      SCHED0();
      A[p] = lds_tr2(aa, 64 + p * 512, 64 + p * 512 + 8);
      SCHED0();
    }
    aa = opq(aa + 64); ba = opq(ba + 64);
  }
  auto half = [&](int hi) {
#pragma unroll
    for (int p = 7; p >= 0; --p)
#pragma unroll
      for (int dd = 0; dd <= 7 - p; ++dd)
        if (((p + dd) >> 2) == hi) acc[p + dd] = mfma16(A[p], B[dd], acc[p + dd]);
    SCHED0();
  };
  auto send = [&](int hi) {
#pragma unroll
    for (int i = 0; i < 4; ++i) *(LDSP(f32x4))((LDSP(char))xw + i * 1024) = acc[4 * hi + i];
    asm volatile("" ::: "memory");
    *(volatile LDSP(u32))(L_XCH + cw * 4) = 1;
    SCHED0();
  };
  f32x4 y[4];
  if (var) {
    half(0); send(0); half(1);
#pragma unroll
    for (int i = 0; i < 4; ++i) y[i] = acc[4 + i];
  } else {
    half(1); send(1); half(0);
#pragma unroll
    for (int i = 0; i < 4; ++i) y[i] = acc[i];
  }
  u32x2 sk[4];
#pragma unroll
  for (int i = 0; i < 4; ++i) sk[i] = u32x2{pk2(bfhi(uu[i][1]) * Dd, bflo(uu[i][1]) * Dd), pk2(bfhi(uu[i][0]) * Dd, bflo(uu[i][0]) * Dd)};
  SCHED0();
  lflag_wait(L_XCH + (cw ^ 1) * 4, 0, 1);
  f32x4 pv[4];
#pragma unroll
  for (int i = 0; i < 4; ++i) pv[i] = *(LDSP(f32x4))((LDSP(char))xa + i * 1024);
  SCHED0();
#pragma unroll
  for (int i = 0; i < 4; ++i) {
    const f32x4 s = y[i] + pv[i];
    const u32 c0 = pk2(s[0], s[1]), c1 = pk2(s[2], s[3]);
    st64_dev(rs, yo + i * 512, u32x2{pk2(bflo(c0) + bflo(sk[i][0]), bfhi(c0) + bfhi(sk[i][0])), pk2(bflo(c1) + bflo(sk[i][1]), bfhi(c1) + bfhi(sk[i][1]))});
  }
}

DEV int g2_ks(int j, int i) { const int e = 7 * j + (i < 7 ? i : i - 7); return 4 * (e >> 1) + (e & 1) + (i < 7 ? 0 : 2); }
DEV u32 g2_place(int j, int p) { return (p < 3 ? 2 * SLOT + (u32)p * 4096 : (u32)(p - 3) * 4096) + (u32)j * 12288; }
DEV u32 w2_voff(int lane) { return (u32)((lane >> 3) * (D * 2) + (((lane & 7) ^ (((lane >> 4) & 1) << 2)) << 4)); }

DEV void share(int b, int n, const char* base) {
  const int lane = (int)opq(threadIdx.x) & 63;
  if (lane < n) touch((u32)(b * n + lane) * 128, base, L_DUMMY);
}

__global__ __launch_bounds__(768, 3) void fused_kernel(const char* x, const char* kp, const char* vp, const char* skp,
                                                       const char* ssmk, const char* ssmD, const char* w1, const char* w3,
                                                       const char* w2, char* ws, char* out) {
  const int b = blockIdx.x, xcd = b & 7, r = b >> 3;
  const int wid = __builtin_amdgcn_readfirstlane(threadIdx.x >> 6);
  const int panel = (xcd >> 1) * 4 + r / 7, ft = (xcd & 1) * 7 + r % 7;
  const int tau = xcd * 4 + r / 7, dl = r % 7;
  const int d0 = 2 * b;

  if (b < 224) {
    if (wid >= 4 && wid < 7) share(b, wid == 4 ? 64 : 56, wid == 4 ? x : (wid == 5 ? w1 : w3));
    if (wid == 7) { *(LDSP(u32))(MISC + (threadIdx.x & 63) * 4) = 0; WAIT_LGKM0(); }
    BAR();
    if (wid < 2) {
      const int lane = (int)opq(threadIdx.x) & 63;
      const int g = lane >> 4, l15 = lane & 15;
      u32 vx[8], vw[8];
      const u32 ox = (u32)((lane >> 3) * (D * 2) + (((lane & 7) ^ g) << 4));
      const u32 ow = (u32)(g * (F * 2) + (((l15 >> 2) ^ g) << 6) + ((lane & 3) << 4));
#pragma unroll
      for (int i = 0; i < 8; ++i) {
        vx[i] = (ox ^ (u32)((i & 1) * 64)) + i * 8 * (D * 2) - (i & 3) * 1024;
        vw[i] = ow + i * 4 * (F * 2) - (i & 3) * 1024;
      }
#pragma unroll 1
      for (int c = 0; c < 7; ++c) {
        if (c >= 3) BAR();
        const u32 slot = (u32)(c % 3) * SLOT + wid * 8192;
        const char* xb = x + (size_t)(panel * 128 + wid * 64) * (D * 2) + c * 128;
        const char* w1b = w1 + (size_t)(c * 64 + wid * 32) * (F * 2) + ft * 256;
        const char* w3b = w3 + (size_t)(c * 64 + wid * 32) * (F * 2) + ft * 256;
#pragma unroll
        for (int i = 0; i < 8; i += 4) {
          glds16x4(vx[i], vx[i + 1], vx[i + 2], vx[i + 3], xb, slot + i * 1024);
          glds16x4(vw[i], vw[i + 1], vw[i + 2], vw[i + 3], w1b, slot + 16384 + i * 1024);
          glds16x4(vw[i], vw[i + 1], vw[i + 2], vw[i + 3], w3b, slot + 32768 + i * 1024);
        }
        dflag_set(L_FULL + wid * 4, c + 1);
      }
    }
    if (wid == 7 && (threadIdx.x & 63) < 5) touch((threadIdx.x & 63) * 128, seq_tab, L_DUMMY);
  }

  if (__builtin_expect(wid < 4 && b < 224, 1)) {
    const int lane = (int)opq(threadIdx.x) & 63;
    u64 tag = 0;
    if (wid < 2) {
      if (wid == 0) {
        tag = make_tag(ws);
        const int k = lane & 7;
        gflag_wait<8>(ws + WS_FST + (k < 2 ? 2 * panel + k : (k == 2 ? tau : (k & 1) * 14 + ft)) * 128, tag);
        glds16_dev(lane * 16, ws + WS_STAT + panel * 1024, L_STAT);
        glds16_dev(lane * 16, ws + WS_CSUM + ft * 1024, L_CSUM);
        if (lane < 2) glds4_dev(lane * 4, ws + WS_E, L_TAG);
        if (lane < 1) glds4_dev(0, ssmD + d0 * 2, L_DD);
        dflag_set(L_EPI, 1);
      }
    } else {
      __builtin_amdgcn_s_setprio(3);
      const rsrc_t rs = ws_rsrc(ws);
      const int u = wid - 2;
      const int ti = b / 7, cj = b % 7;
      const int tg = lane >> 3, cg = lane & 7;
      const int t0 = 64 * ti + 32 * u + 4 * tg, ch0 = 64 * cj + 8 * cg;
      u32x4 kr[7], vr[4], sw[4]; u32x2 o[8];
      u64 seed;
      auto ldkv = [&]() {
        const u32 ko = (u32)(t0 * (D * 2) + ch0 * 2);
#pragma unroll
        for (int i = 0; i < 7; ++i) kr[i] = *(const u32x4*)(kp + (i < 3 && t0 == 0 ? ko : ko + (u32)((i - 3) * (D * 2))));
#pragma unroll
        for (int i = 0; i < 4; ++i) vr[i] = *(const u32x4*)(vp + (ko + (u32)(i * (D * 2))));
        SCHED0();
      };
      auto piece = [&](int p) {
#pragma unroll
        for (int c = 2 * p; c < 2 * p + 2; ++c) {
          const u32 wa = sw[p][(c & 1) * 2], wb = sw[p][(c & 1) * 2 + 1];
          const float w0 = bflo(wa), w1f = bfhi(wa), w2f = bflo(wb), w3f = bfhi(wb);
          float kf[7], ks[4];
#pragma unroll
          for (int i = 0; i < 7; ++i) kf[i] = (c & 1) ? bfhi(kr[i][p]) : bflo(kr[i][p]);
#pragma unroll
          for (int i = 0; i < 4; ++i)
            ks[i] = __builtin_fmaf(kf[i + 3], w0, __builtin_fmaf(kf[i + 2], w1f, __builtin_fmaf(kf[i + 1], w2f, kf[i] * w3f)));
#pragma unroll
          for (int i = 0; i < 2; ++i) {
            const int ta = 3 - 2 * i, tb = 2 - 2 * i;
            const u32 kk = pk2(ks[ta], ks[tb]);
            o[c][i] = pk2(bflo(kk) * ((c & 1) ? bfhi(vr[ta][p]) : bflo(vr[ta][p])), bfhi(kk) * ((c & 1) ? bfhi(vr[tb][p]) : bflo(vr[tb][p])));
          }
          asm volatile("" : "+v"(o[c]));
        }
        SCHED0();
      };
      lflag_wait(L_FULL, 1, 1);
      share(b, 64, u ? vp : kp);
      BAR();
      ldkv();
#pragma unroll
      for (int i = 0; i < 4; ++i) sw[i] = *(const u32x4*)(skp + (u32)(ch0 * 8 + i * 16));
      seed = gload64(ws + WS_E + opq(0));
      if (u && lane < 14) {
        const u32 fo = lane < 8 ? WS_FH + (u32)((2 * panel + (lane & 1)) * 56 + 4 * ft + (lane >> 1)) * 128
                                : (lane < 12 ? WS_FY + (u32)((lane & 1) * 448 + d0 + ((lane >> 1) & 1)) * 128 : WS_FUT + (u32)(cj * 64 + 2 * ti + (lane & 1)) * 128);
        touch(fo, ws, L_DUMMY);
      }
      SCHED0();
      BAR();
      SCHED0();
      BAR();
      SCHED0();
#pragma unroll
      for (int i = 0; i < 3; ++i) if (t0 == 0) kr[i] = u32x4{0, 0, 0, 0};
      piece(0);
      BAR();
      SCHED0();
#pragma unroll
      for (int c = 1; c < 4; ++c) piece(c);
      tag = tag_of(seed);
      const u32 scr = L_USCR + (u32)u * 4096;
#pragma unroll
      for (int c = 0; c < 8; ++c) *(LDSP(u32x2))(scr + (u32)((cg * 8 + c) * 64 + (((7 - tg) ^ (cg & 6)) << 3))) = o[c];
      WAIT_LGKM0();
#pragma unroll
      for (int i = 0; i < 4; ++i) {
        const int dd = 16 * i + (lane >> 2), c4 = lane & 3;
        u32x4 v = *(LDSP(u32x4))(scr + (u32)(dd * 64 + ((c4 ^ (dd >> 4)) << 4)));
        st128_dev(rs, WS_UT + (u32)((64 * cj + dd) * (S * 2) + (2016 - 64 * ti - 32 * u) * 2 + c4 * 16), v);
      }
      gflag_publish(ws + WS_FUT + (cj * 64 + 2 * ti + u) * 128, tag, lane);
    }
    const int j = wid;
    const u32 o2 = w2_voff(lane);
    const char* wbase = w2 + dl * 128;
    u32x4 pk[8][4];
    auto park = [&](int i0, int i1) {
#pragma unroll
      for (int i = i0; i < i1; ++i) {
        const char* src = wbase + (size_t)g2_ks(j, i) * 32 * (D * 2);
        asm volatile("" ::: "memory");
#pragma unroll
        for (int pc = 0; pc < 4; ++pc) pk[i & 7][pc] = *(const u32x4*)(src + (o2 + (u32)(pc * 8 * (D * 2))));
      }
      SCHED0();
    };
    park(0, 5);
    if (wid != 1) park(5, 6);
    if (wid == 2) {
      gflag_wait<8>(ws + WS_FUT + ((d0 >> 6) * 64 + lane) * 128, tag);
      lflag_wait(L_FREE, 7, 7);
      glds16x4_dev(lane * 16, ws + WS_UT + d0 * (S * 2), CV_U);
      glds16x4_dev(lane * 16, ws + WS_UT + (d0 + 1) * (S * 2), CV_U + CV_USTR);
      if (lane < 32) {
        glds16(lane * 16, seq_tab + 32, CV_U + 4096);
        glds16(lane * 16, seq_tab + 32, CV_U + CV_USTR + 4096);
      }
      dflag_set(L_CONV + 8, 1);
    }
    if (wid == 3) lflag_set(L_CONV + 12, 1);
    if (wid == 1) {
      u32x4 cv[8]; u32x2 cn[8];
#pragma unroll
      for (int i = 0; i < 8; ++i) {
        const u32 o = (u32)(d0 * (S * 2) + (lane + 64 * i) * 16);
        cv[i] = *(const u32x4*)(ssmk + o);
        cn[i] = *(const u32x2*)(ssmk + (o + (lane + 64 * i < 511 ? 16u : 0u)));
      }
      SCHED0();
      __builtin_amdgcn_s_setprio(3);
      lflag_wait(L_FREE, 7, 7);
      const u32 hb = opq(CV + 32 + lane * 16);
#pragma unroll
      for (int i = 0; i < 8; ++i) {
        const u32x4 c = cv[i]; const u32x2 n = cn[i];
        const u32 ad = hb + (i >> 2) * 4 * CV_HSTR + (i & 3) * 1024;
        lds_st128(ad, c);
        lds_st128(ad + CV_HSTR, u32x4{shr16(c[1], c[0]), shr16(c[2], c[1]), shr16(c[3], c[2]), shr16(n[0], c[3])});
        lds_st128(ad + 2 * CV_HSTR, u32x4{opq(c[1]), opq(c[2]), opq(c[3]), opq(n[0])});
        lds_st128(ad + 3 * CV_HSTR, u32x4{shr16(c[2], c[1]), shr16(c[3], c[2]), shr16(n[0], c[3]), shr16(n[1], n[0])});
        if ((i & 3) == 3) lflag_set(L_CONV + (i >> 2) * 4, 1);
      }
      SCHED0();
      park(5, 6);
    }
    park(6, 8);
    lflag_wait(L_FREE, 7, 7);
    const u32 wl = opq(lane * 16 + j * 12288);
    auto feed = [&](int i0, int i1) {
#pragma unroll
      for (int i = i0; i < i1; ++i)
#pragma unroll
        for (int n = 0; n < 4; ++n) lds_st128(wl + g2_place(0, i % 6) + n * 1024, pk[i & 7][n]);
      lflag_set(L_FED + j * 4, i1);
    };
    feed(0, 3);
    feed(3, 6);
    lflag_wait(L_PUBA, 3, 1);
    tag = tag_of(*(volatile LDSP(u64))L_TAG);
    gflag_wait<3>(ws + WS_FH + (tau * 56 + g2_ks(j, lane < 7 ? lane : 6)) * 128, tag);
    lflag_set(L_HE + j * 4, 1);
    lflag_wait(L_PUB, 3, 1);
    lflag_wait(L_CONV, 3, 1);
    lflag_wait(L_HE, 3, 1);
    park(8, 14);
    if (wid == 3) {
      lflag_wait(L_PUB, 3, 2);
      const int e = lane < 28 ? lane : 27;
      gflag_wait<4>(ws + WS_FH + (tau * 56 + 4 * (e >> 1) + (e & 1) + 2) * 128, tag);
      lflag_set(L_H, 1);
    }
    __builtin_amdgcn_s_setprio(0);
#pragma unroll
    for (int i = 6; i < 14; i += 2) {
      lflag_wait(L_PROG + j * 4, 0, i - 4);
      feed(i, i + 2);
    }
    return;
  }
  const rsrc_t rs = ws_rsrc(ws);

  if (b >= 224) {
    if (wid >= 8 && b != 255) return;
    const int tid = (int)opq(threadIdx.x), lane = tid & 63;
    const int sp = b - 224;
    const u64 seed = gload64(ws + WS_E + opq(0));
    if (tid == 0) touch(WS_FST + sp * 128, ws, L_DUMMY);
    if (wid < 8) {
      const int row = 64 * sp + (tid >> 3), c8 = tid & 7;
      const int cu = sp < 28 ? sp : 27, mat = cu / 14, ct = cu % 14, kg = tid >> 4, c16 = tid & 15;
      const char* wp = (mat ? w3 : w1) + ct * 256;
      const u32 wo = (u32)(kg * (F * 2) + c16 * 16), xo = (u32)(row * (D * 2) + c8 * 16);
      u32x4 xv[7], wv[14];
#pragma unroll
      for (int i = 0; i < 7; ++i) xv[i] = *(const u32x4*)(x + (xo + i * 128));
#pragma unroll
      for (int i = 0; i < 14; ++i) wv[i] = *(const u32x4*)(wp + (wo + i * 32 * (F * 2)));
      SCHED0();
      float s = 0.f;
#pragma unroll
      for (int i = 0; i < 7; ++i)
#pragma unroll
        for (int j = 0; j < 4; ++j) s += bflo(xv[i][j]) + bfhi(xv[i][j]);
      const float m = sum8(s) * (1.0f / D);
      float q = 0.f;
#pragma unroll
      for (int i = 0; i < 7; ++i)
#pragma unroll
        for (int j = 0; j < 4; ++j) {
          const float aa = bflo(xv[i][j]) - m, bb = bfhi(xv[i][j]) - m;
          q = __builtin_fmaf(aa, aa, __builtin_fmaf(bb, bb, q));
        }
      const float sd = rbf(__builtin_amdgcn_sqrtf(rbf(rbf(sum8(q) * (1.0f / D)) + 1e-5f)));
      if (c8 == 0) *(LDSP(f32x2))(u32)(16384 + (tid >> 3) * 8) = f32x2{rbf(m), __builtin_amdgcn_rcpf(sd)};
      SCHED0();
      float a[8] = {};
#pragma unroll
      for (int i = 0; i < 14; ++i)
#pragma unroll
        for (int j = 0; j < 4; ++j) { a[2 * j] += bflo(wv[i][j]); a[2 * j + 1] += bfhi(wv[i][j]); }
      f32x4 t;
#pragma unroll
      for (int j = 0; j < 4; ++j) {
        auto p = __builtin_amdgcn_permlane16_swap(__builtin_bit_cast(u32, a[j]), __builtin_bit_cast(u32, a[j + 4]), false, false);
        t[j] = asf(p[0]) + asf(p[1]);
      }
      *(LDSP(f32x4))(u32)((kg >> 1) * 512 + c16 * 32 + (kg & 1) * 16) = t;
    }
    WAIT_LGKM0(); BAR();
    const u64 tag = tag_of(seed);
    if (wid == 0) {
      const int cu = sp < 28 ? sp : 27;
      f32x2 cs = {};
#pragma unroll
      for (int i = 0; i < 16; ++i) cs += *(LDSP(f32x2))(u32)(i * 512 + lane * 8);
      st64_dev(rs, WS_STAT + (u32)(64 * sp + lane) * 8, *(LDSP(u32x2))(u32)(16384 + lane * 8));
      if (sp < 28) st64_dev(rs, WS_CSUM + (u32)((cu % 14) * 1024 + (cu / 14) * 512 + lane * 8), __builtin_bit_cast(u32x2, cs));
      gflag_publish(ws + WS_FST + sp * 128, tag, lane);
    }
    WAIT_VM(0); BAR();
    if (tid < 392) touch((u32)(sp * 392 + tid) * 128, w2, L_DUMMY);
    if (tid < 448) touch((u32)(sp * 448 + tid) * 128, ssmk, L_DUMMY);
    if (b == 255) {
      __builtin_amdgcn_s_sleep(127);
      gflag_wait<64>(ws + (tid < 480 ? WS_FUT + tid * 128 : WS_FH + ((((tid < 704 ? tid - 480 : 223) / 14) * 2) * 56 + ((tid < 704 ? tid - 480 : 223) % 14) * 4) * 128), tag);
      BAR();
      if (tid == 0) gstore64(ws + WS_E, tag);
    }
    return;
  }

  const int cw = wid - 4, th = cw & 1, fq = cw >> 1;
  {
    const int lane = (int)opq(threadIdx.x) & 63, g = lane >> 4, hh = lane >> 5, l31 = lane & 31;
    const int q4 = (lane >> 2) & 3, p4 = lane & 3;
    const u32 aA = 16384 + (u32)((8 * hh + q4) * 256 + ((fq ^ q4) << 6) + (g & 1) * 32 + p4 * 8);
    const u32 aB = (u32)((64 * th + l31) * 128 + ((hh ^ ((l31 >> 1) & 7)) << 4));
    f32x16 acc[2][2] = {};
    s16x8 fa[2][2][2], fb[2][2][2];
    u32 pa, pb[4];
    auto setslot = [&](u32 slot) {
      pa = opq(aA + slot);
#pragma unroll
      for (int k = 0; k < 4; ++k) pb[k] = opq((aB ^ (u32)(k << 5)) + slot);
    };
    auto rd = [&](int set) {
#pragma unroll
      for (int k = 0; k < 2; ++k) {
        const int ks = 2 * set + k;
        fa[set][k][0] = lds_tr2(pa, ks * 4096, ks * 4096 + 1024);
        fb[set][k][0] = lds_b128(pb[ks], 0);
        fb[set][k][1] = lds_b128(pb[ks], 4096);
        fa[set][k][1] = lds_tr2(pa, 16384 + ks * 4096, 16384 + ks * 4096 + 1024);
      }
    };
    auto mm = [&](int set, int n0, int n1) {
#pragma unroll
      for (int n = n0; n < n1; ++n) acc[(n >> 1) & 1][n & 1] = mfma32(fa[set][n >> 2][(n >> 1) & 1], fb[set][n >> 2][n & 1], acc[(n >> 1) & 1][n & 1]);
    };
    setslot(0);
    const u32 fad = opq(L_FULL + (lane & 1) * 4);
#pragma unroll
    for (int i = 0; i < 4; ++i) asm volatile("" : "+v"(acc[i >> 1][i & 1]));
    __builtin_amdgcn_s_setprio(1);
    lflag_wait(L_FULL, 1, 1);
    rd(0);
    SCHED0();
    u32 slot = 0;
    auto stage = [&](int c, bool bar) {
      rd(1);
      const u32 fl = *(volatile LDSP(u32))fad;
      mm(0, 0, 6);
      SGB(0x008, 1); SGB(0x100, 3); SGB(0x008, 1); SGB(0x100, 3); SGB(0x008, 1); SGB(0x100, 3); SGB(0x008, 1); SGB(0x100, 4); SGB(0x008, 2);
      SCHED0();
      slot = slot == 2 * SLOT ? 0 : slot + SLOT;
      setslot(slot);
      SCHED0();
      mm(0, 6, 7);
      SCHED0();
      __builtin_amdgcn_s_waitcnt(0xC07F);
      const bool miss = c < 6 && __builtin_amdgcn_ballot_w64(fl < (u32)(c + 2));
      SCHED0();
      mm(0, 7, 8);
      SCHED0();
      if (bar) BAR();
      SCHED0();
      mm(1, 0, 2);
      SCHED0();
      if (!bar && c == 6) lflag_set(L_FREE + cw * 4, 7);
      if (miss) lflag_wait(L_FULL, 1, c + 2);
      SCHED0();
      rd(0); mm(1, 2, 8);
#pragma unroll
      for (int i = 0; i < 4; ++i) { SGB(0x008, 1); SGB(0x100, 3); }
      SGB(0x008, 2);
      SCHED0();
    };
#pragma unroll 1
    for (int c = 0; c < 4; ++c) stage(c, true);
#pragma unroll 1
    for (int c = 4; c < 7; ++c) stage(c, false);
    const u32 hdst = WS_H + (u32)((2 * panel + th) * 56 + 4 * ft + fq) * 4096 + lane * 16;
    const u32 sa = opq(L_STAT + (u32)(64 * th + l31) * 8), ca = opq(L_CSUM + (u32)(32 * fq + 4 * hh) * 4);
    f32x2 st[2]; f32x4 csa[2][2][2];
    auto rdc = [&]() {
#pragma unroll
      for (int j = 0; j < 2; ++j) st[j] = *(volatile LDSP(f32x2))((LDSP(char))sa + j * 256);
#pragma unroll
      for (int n = 0; n < 8; ++n) csa[n >> 2][(n >> 1) & 1][n & 1] = *(volatile LDSP(f32x4))((LDSP(char))ca + ((n >> 1) & 1) * 512 + (n >> 2) * 64 + (n & 1) * 32);
    };
    const u32 ef = *(volatile LDSP(u32))L_EPI;
    rdc();
    if (__builtin_amdgcn_readfirstlane(ef) == 0) { lflag_wait(L_EPI, 0, 1); rdc(); }
    float nm[2], c1[2], irs2[2];
#pragma unroll
    for (int j = 0; j < 2; ++j) { nm[j] = -st[j][0]; c1[j] = -1.44269504f * st[j][1]; irs2[j] = __builtin_amdgcn_rcpf(st[j][1] * st[j][1]); }
#pragma unroll
    for (int s = 0; s < 2; ++s) {
      const f32x4 (&cs)[2][2] = csa[s];
#pragma unroll
      for (int j = 0; j < 2; ++j) {
        float a[8], ex[8], hv[8];
#pragma unroll
        for (int e = 0; e < 8; ++e) a[e] = __builtin_fmaf(nm[j], cs[0][e >> 2][e & 3], acc[0][j][8 * s + e]);
#pragma unroll
        for (int e = 0; e < 8; ++e) ex[e] = a[e] * c1[j];
        SCHED0();
#pragma unroll
        for (int e = 0; e < 8; ++e) { ex[e] = __builtin_amdgcn_exp2f(ex[e]); hv[e] = __builtin_fmaf(nm[j], cs[1][e >> 2][e & 3], acc[1][j][8 * s + e]); }
        SCHED0();
#pragma unroll
        for (int e = 0; e < 8; ++e) ex[e] = __builtin_fmaf(ex[e], irs2[j], irs2[j]);
        SCHED0();
#pragma unroll
        for (int e = 0; e < 8; ++e) { ex[e] = __builtin_amdgcn_rcpf(ex[e]); hv[e] *= a[e]; }
        SCHED0();
#pragma unroll
        for (int e = 0; e < 8; ++e) hv[e] *= ex[e];
        st128_dev(rs, hdst + (s * 2 + j) * 1024, u32x4{pk2(hv[0], hv[1]), pk2(hv[2], hv[3]), pk2(hv[4], hv[5]), pk2(hv[6], hv[7])});
      }
    }
  }
  char* hflag = ws + WS_FH + ((2 * panel + th) * 56 + 4 * ft + fq) * 128;
  u32x4 xv, sv;
  if (cw < 4) {
    const int ht = (int)opq(threadIdx.x) & 255, lane = ht & 63, l15 = ht & 15;
    const u64 tag = tag_of(*(volatile LDSP(u64))L_TAG);
    const int ch = cw >> 1, var = cw & 1;
    gflag_publish(hflag, tag, lane);
    lflag_set(L_PUBA + cw * 4, 1);
    u32x4 xr[2]; u32x2 st[2];
#pragma unroll
    for (int i = 0; i < 2; ++i) {
      const int n = ht + 256 * i, row = n >> 3, c = n & 7;
      xr[i] = *(const u32x4*)(x + (u32)((64 * tau + row) * (D * 2) + (64 * dl + 8 * c) * 2));
      st[i] = ld64_dev(rs, WS_STAT + (u32)(64 * tau + row) * 8);
    }
    SCHED0();
    conv_compute(lane, cw, d0 + ch, rs);
    u32x4 xn[2];
#pragma unroll
    for (int i = 0; i < 2; ++i) {
      const float mb = asf(st[i][0]), rsd = asf(st[i][1]);
#pragma unroll
      for (int e = 0; e < 4; ++e) {
        const u32 dd = pk2(bflo(xr[i][e]) - mb, bfhi(xr[i][e]) - mb);
        xn[i][e] = pk2(bflo(dd) * rsd, bfhi(dd) * rsd);
      }
    }
    asm volatile("" : "+v"(xn[0]), "+v"(xn[1]));
    SCHED0();
    gflag_publish(ws + WS_FY + (var * 448 + d0 + ch) * 128, tag, lane);
    xv = xr[0]; sv = xn[0];
    lflag_wait(L_XCH, 1, 1);
    lds_st128(E_S + (u32)ht * 16, xn[1]); lds_st128(E_X + (u32)ht * 16, xr[1]);
    gflag_wait<4>(ws + WS_FY + ((tau >> 4) * 448 + 64 * dl + 16 * cw + l15) * 128, tag);
#pragma unroll
    for (int i = 0; i < 2; ++i)
      glds16_dev((u32)((lane >> 3) * (S * 2) + (((lane & 7) ^ (2 * cw + i)) << 4)),
                 ws + WS_YT + (64 * dl + 16 * cw + 8 * i) * (S * 2) + 128 * tau, xbuf(cw ^ 1) + (u32)i * 1024);
    WAIT_VM(0);
  } else {
    const int j = cw - 4;
    const int lane = (int)opq(threadIdx.x) & 63;
    lflag_set(L_PUB + j * 4, 1);
    const u64 btag = tag_of(*(volatile LDSP(u64))L_TAG);
    const bool hready = __builtin_amdgcn_readfirstlane(*(volatile LDSP(u32))(L_HE + j * 4)) != 0;
    const u32 hl = lane * 16;
    const char* hbase = ws + WS_H + (size_t)tau * 56 * 4096;
    const int q4 = (lane >> 2) & 3;
    const u32 ta = (u32)((4 * (lane >> 5) + q4) * 128 + ((q4 >> 1) << 6) + ((lane >> 4) & 1) * 32 + (lane & 3) * 8);
    s16x8 fa[2][4], fb[3][4];
    f32x16 acc[2][2] = {};
    auto ldh = [&](int i) {
      const char* p = hbase + (size_t)g2_ks(j, i) * 4096;
      asm volatile("global_load_dwordx4 %0, %4, %5 sc1\n\tglobal_load_dwordx4 %1, %4, %5 offset:1024 sc1\n\t"
                   "global_load_dwordx4 %2, %4, %5 offset:2048 sc1\n\tglobal_load_dwordx4 %3, %4, %5 offset:3072 sc1"
                   : "=&v"(fb[i % 3][0]), "=&v"(fb[i % 3][1]), "=&v"(fb[i % 3][2]), "=&v"(fb[i % 3][3])
                   : "v"(hl), "s"(p) : "memory");
    };
    u32 tb[2];
#pragma unroll
    for (int a = 0; a < 2; ++a) tb[a] = opq((ta ^ (u32)(a << 6)) + g2_place(j, 0));
    auto rda = [&](int i) {
#pragma unroll
      for (int a = 0; a < 2; ++a)
        if (i > 0 && i % 3 == 0) tb[a] = opq(i % 6 ? tb[a] - 2 * SLOT : tb[a] + 2 * SLOT);
#pragma unroll
      for (int n = 0; n < 4; ++n) {
        const int o = (i % 3) * 4096 + (n >> 1) * 2048;
        fa[i & 1][n] = lds_tr2(tb[n & 1], o, o + 1024);
      }
    };
    const u32 fed = opq(L_FED + j * 4), prog = opq(L_PROG + j * 4);
    if (!hready) { gflag_publish(hflag, btag, lane); lflag_set(L_PUB + j * 4, 2); lflag_wait(L_HE + j * 4, 0, 1); }
#pragma unroll
    for (int i = 0; i < 3; ++i) ldh(i);
    if (hready) {
      WAIT_VM(12);
      if (lane == 0) gstore64(hflag, btag);
      lflag_set(L_PUB + j * 4, 2);
    }
    lflag_wait(L_FED + j * 4, 0, 3);
    rda(0);
    SCHED0();
    u32 fl = 0;
#pragma unroll
    for (int i = 0; i < 14; ++i) {
      if (i == 2 || (i >= 5 && i < 13 && (i & 1))) {
        if (__builtin_amdgcn_readfirstlane(fl) < i + 2) lflag_wait(L_FED + j * 4, 0, i + 2);
      }
      asm volatile("s_waitcnt vmcnt(%4)" : "+v"(fb[i % 3][0]), "+v"(fb[i % 3][1]), "+v"(fb[i % 3][2]), "+v"(fb[i % 3][3])
                   : "i"(i < 12 ? 8 : (13 - i) * 4) : "memory");
      SCHED0();
      if (i < 13) rda(i + 1);
      if (i == 1 || (i >= 4 && i < 12 && !(i & 1))) fl = *(volatile LDSP(u32))fed;
      if (i < 13) {
#pragma unroll
        for (int s = 0; s < 2; ++s)
#pragma unroll
          for (int a = 0; a < 2; ++a)
#pragma unroll
            for (int t = 0; t < 2; ++t) acc[a][t] = mfma32(fa[i & 1][2 * s + a], fb[i % 3][2 * s + t], acc[a][t]);
#pragma unroll
        for (int n = 0; n < 5; ++n) { SGB(0x008, 1); SGB(0x100, 2); }
        SGB(0x008, 3);
      } else {
        __builtin_amdgcn_s_waitcnt(0xC07F);
        const int ln = (int)opq(threadIdx.x), lt = ln & 31, lh = (ln >> 5) & 1;
#pragma unroll
        for (int n = 0; n < 5; ++n) {
          if (n < 4) {
#pragma unroll
            for (int s = 0; s < 2; ++s) acc[n & 1][n >> 1] = mfma32(fa[i & 1][2 * s + (n & 1)], fb[i % 3][2 * s + (n >> 1)], acc[n & 1][n >> 1]);
          }
          SCHED0();
          if (n > 0) {
            const int a = (n - 1) & 1, t = (n - 1) >> 1;
#pragma unroll
            for (int G = 0; G < 4; ++G)
              *(LDSP(f32x4))(g2_place(j, t ? 3 : 0) + (u32)(lt * 256 + (((8 * a + 2 * G + lh) ^ (lt & 7)) << 4))) =
                  f32x4{acc[a][t][4 * G], acc[a][t][4 * G + 1], acc[a][t][4 * G + 2], acc[a][t][4 * G + 3]};
          }
          SCHED0();
        }
      }
      SCHED0();
      if (i < 8 && (i & 1)) *(volatile LDSP(u32))prog = i + 1;
      if (i == 4) lflag_wait(L_H, 0, 1);
      if (i + 3 < 14) ldh(i + 3);
      SCHED0();
    }
  }
  {
    const int ht = (int)opq(threadIdx.x) & 255;
    const int half = cw >> 2, tk = ht >> 3, dg = ht & 7, tok = 32 * half + tk;
    const u32 pa0 = opq((half ? 0u : 2 * SLOT) + (u32)(tk * 256 + (((2 * dg) ^ (tk & 7)) << 4))), pa1 = opq(pa0 ^ 16u);
    const u32 ya = opq(xbuf((dg >> 1) ^ 1) + (u32)(8 * (dg & 1) * 128 + (((tok >> 3) ^ dg) << 4) + (tok & 7) * 2));
    const u32 ea = opq(E_S + (u32)ht * 16);
    const u32 oo = opq((u32)((64 * tau + tok) * (D * 2) + (64 * dl + 8 * dg) * 2));
    WAIT_LGKM0();
    BAR();
    f32x4 p0[4], p1[4];
    u32 yv[8];
#pragma unroll
    for (int w = 0; w < 4; ++w) {
      p0[w] = *(LDSP(f32x4))((LDSP(char))pa0 + w * 12288);
      p1[w] = *(LDSP(f32x4))((LDSP(char))pa1 + w * 12288);
    }
#pragma unroll
    for (int e = 0; e < 8; ++e) yv[e] = *(LDSP(u16))((LDSP(char))ya + e * 128);
    SCHED0();
    if (half) { sv = *(LDSP(u32x4))ea; xv = *(LDSP(u32x4))((LDSP(char))ea + (E_X - E_S)); }
    const f32x4 s0 = (p0[0] + p0[1]) + (p0[2] + p0[3]), s1 = (p1[0] + p1[1]) + (p1[2] + p1[3]);
    u32 f[4];
    float ex[8];
#pragma unroll
    for (int e = 0; e < 4; ++e) f[e] = e < 2 ? pk2(s0[2 * e], s0[2 * e + 1]) : pk2(s1[2 * e - 4], s1[2 * e - 3]);
#pragma unroll
    for (int e = 0; e < 8; ++e) ex[e] = __builtin_amdgcn_exp2f(-1.44269504f * ((e & 1) ? bfhi(f[e >> 1]) : bflo(f[e >> 1])));
    SCHED0();
    u32 se[4];
#pragma unroll
    for (int e = 0; e < 4; ++e) se[e] = pk2(bflo(sv[e]) * bflo(yv[2 * e]), bfhi(sv[e]) * bflo(yv[2 * e + 1]));
#pragma unroll
    for (int e = 0; e < 8; ++e) ex[e] = __builtin_amdgcn_rcpf(1.0f + ex[e]);
    SCHED0();
    u32x4 o;
#pragma unroll
    for (int e = 0; e < 4; ++e) {
      const u32 gt = pk2(bflo(f[e]) * ex[2 * e], bfhi(f[e]) * ex[2 * e + 1]);
      const u32 p = pk2(bflo(se[e]) * bflo(gt), bfhi(se[e]) * bfhi(gt));
      o[e] = pk2(bflo(xv[e]) + bflo(p), bfhi(xv[e]) + bfhi(p));
    }
    __builtin_amdgcn_raw_buffer_store_b128(o, __builtin_amdgcn_make_buffer_rsrc(out, 0, S * D * 2, 0x00020000), oo, 0, 16);
  }
}
}
#if defined(__HIP_DEVICE_COMPILE__) && __HIP_DEVICE_COMPILE__
#pragma clang attribute pop
#endif

extern "C" void kernel_launch(void* const* d_in, const int* in_sizes, int n_in, void* d_out, int out_size, void* d_ws,
                              size_t ws_size, hipStream_t stream) {
  (void)hipFuncSetAttribute((const void*)fused_kernel, hipFuncAttributeMaxDynamicSharedMemorySize, 163840);
  fused_kernel<<<256, 768, 163840, stream>>>((const char*)d_in[0], (const char*)d_in[1], (const char*)d_in[2], (const char*)d_in[3],
                                             (const char*)d_in[4], (const char*)d_in[5], (const char*)d_in[7], (const char*)d_in[8],
                                             (const char*)d_in[9], (char*)d_ws, (char*)d_out);
}
```

```cpp
#include <hip/hip_runtime.h>
#include <hip/hip_bf16.h>
#include <stdint.h>
#pragma clang diagnostic ignored "-Wint-to-pointer-cast"

#if defined(__HIP_DEVICE_COMPILE__) && __HIP_DEVICE_COMPILE__
#pragma clang attribute push(__attribute__((target("no-packed-fp32-ops"))), apply_to = function)
#endif
namespace {
typedef unsigned short u16;
typedef unsigned int u32;
typedef unsigned long long u64;
typedef __attribute__((ext_vector_type(4))) short s16x4;
typedef __attribute__((ext_vector_type(8))) short s16x8;
typedef __attribute__((ext_vector_type(8))) __bf16 bf16x8;
typedef __attribute__((ext_vector_type(2))) __bf16 bf16x2;
typedef __attribute__((ext_vector_type(16))) float f32x16;
typedef __attribute__((ext_vector_type(4))) float f32x4;
typedef __attribute__((ext_vector_type(2))) float f32x2;
typedef __attribute__((ext_vector_type(4))) u32 u32x4;
typedef __attribute__((ext_vector_type(2))) u32 u32x2;
#define LDSP(T) __attribute__((address_space(3))) T*
#define GLBP(T) __attribute__((address_space(1))) T*
#define DEV __device__ __forceinline__

constexpr int S = 2048, D = 448, F = 1792;
constexpr u32 WS_H = 0;
constexpr u32 WS_UT = WS_H + 7340032;
constexpr u32 WS_YT = WS_UT + 1835008;
constexpr u32 WS_STAT = WS_YT + 1835008;
constexpr u32 WS_CSUM = WS_STAT + 16384;
constexpr u32 WS_E = WS_CSUM + 16384;
constexpr u32 WS_FUT = WS_E + 128;
constexpr u32 WS_FST = WS_FUT + 448 * 128;
constexpr u32 WS_FY = WS_FST + 32 * 128;
constexpr u32 WS_FH = WS_FY + 896 * 128;
constexpr u32 WS_END = WS_FH + 1792 * 128;
constexpr u32 SLOT = 49152;
constexpr u32 MISC = 3 * SLOT;
constexpr u32 L_FULL = MISC, L_EPI = MISC + 8, L_H = MISC + 16, L_FREE = MISC + 32, L_CONV = MISC + 64, L_FED = MISC + 80,
              L_TAG = MISC + 96, L_DD = MISC + 104, L_PROG = MISC + 128, L_XCH = MISC + 144, L_PUB = MISC + 160, L_PUBA = MISC + 176, L_HE = MISC + 192, L_STAT = MISC + 256,
              L_CSUM = MISC + 1280, L_USCR = MISC + 4096, L_DUMMY = MISC + 2304, L_XB2 = MISC + 12288;
constexpr u32 CV = SLOT, CV_HSTR = 4160, CV_U = CV + 8 * CV_HSTR, CV_USTR = 4608;
constexpr u32 E_S = SLOT, E_X = SLOT + 4096;
constexpr int SPIN_CAP = 1 << 18;

__device__ const u32 seq_tab[160] __attribute__((aligned(128))) = {0, 1, 2, 3, 4, 5, 6, 7};
DEV float asf(u32 v) { return __builtin_bit_cast(float, v); }
DEV float bflo(u32 v) { return asf(v << 16); }
DEV float bfhi(u32 v) { return asf(v & 0xffff0000u); }
template <int CTRL> DEV float dpp(float v) { return asf(__builtin_amdgcn_update_dpp(0, __builtin_bit_cast(u32, v), CTRL, 0xf, 0xf, true)); }
DEV float sum8(float v) { v += dpp<0xB1>(v); v += dpp<0x4E>(v); return v + dpp<0x141>(v); }
DEV u32 pk2(float lo, float hi) {
  asm("" : "+v"(lo)); asm("" : "+v"(hi));
  bf16x2 r = {(__bf16)lo, (__bf16)hi};
  u32 v = __builtin_bit_cast(u32, r);
  asm("" : "+v"(v));
  return v;
}
DEV float rbf(float f) { bf16x2 r = {(__bf16)f, (__bf16)f}; return bflo(__builtin_bit_cast(u32, r)); }
DEV f32x4 mfma16(s16x8 a, s16x8 b, f32x4 c) {
  return __builtin_amdgcn_mfma_f32_16x16x32_bf16(__builtin_bit_cast(bf16x8, a), __builtin_bit_cast(bf16x8, b), c, 0, 0, 0);
}
DEV f32x16 mfma32(s16x8 a, s16x8 b, f32x16 c) {
  return __builtin_amdgcn_mfma_f32_32x32x16_bf16(__builtin_bit_cast(bf16x8, a), __builtin_bit_cast(bf16x8, b), c, 0, 0, 0);
}
DEV u32 opq(u32 v) { asm volatile("" : "+v"(v)); return v; }
DEV s16x8 lds_tr2(u32 base, int o0, int o1) {
  s16x4 lo = __builtin_amdgcn_ds_read_tr16_b64_v4i16((LDSP(s16x4))((LDSP(char))base + o0));
  s16x4 hi = __builtin_amdgcn_ds_read_tr16_b64_v4i16((LDSP(s16x4))((LDSP(char))base + o1));
  return __builtin_shufflevector(lo, hi, 0, 1, 2, 3, 4, 5, 6, 7);
}
DEV s16x8 lds_b128(u32 base, int o) { return *(LDSP(s16x8))((LDSP(char))base + o); }
DEV void lds_st128(u32 a, u32x4 v) { *(LDSP(u32x4))a = v; }
DEV void glds16x4(u32 v0, u32 v1, u32 v2, u32 v3, const void* sbase, u32 ldst) {
  asm volatile("s_mov_b32 m0, %5\n\ts_nop 0\n\tglobal_load_lds_dwordx4 %0, %4\n\tglobal_load_lds_dwordx4 %1, %4 offset:1024\n\t"
               "global_load_lds_dwordx4 %2, %4 offset:2048\n\tglobal_load_lds_dwordx4 %3, %4 offset:3072"
               ::"v"(v0), "v"(v1), "v"(v2), "v"(v3), "s"(sbase), "s"(ldst) : "memory");
}
DEV void glds16(u32 voff, const void* sbase, u32 ldst) {
  asm volatile("s_mov_b32 m0, %2\n\ts_nop 0\n\tglobal_load_lds_dwordx4 %0, %1" ::"v"(voff), "s"(sbase), "s"(ldst) : "memory");
}
DEV void glds16x4_dev(u32 v, const void* sbase, u32 ldst) {
  asm volatile("s_mov_b32 m0, %2\n\ts_nop 0\n\tglobal_load_lds_dwordx4 %0, %1 sc1\n\tglobal_load_lds_dwordx4 %0, %1 offset:1024 sc1\n\t"
               "global_load_lds_dwordx4 %0, %1 offset:2048 sc1\n\tglobal_load_lds_dwordx4 %0, %1 offset:3072 sc1"
               ::"v"(v), "s"(sbase), "s"(ldst) : "memory");
}
DEV void glds16_dev(u32 voff, const void* sbase, u32 ldst) {
  asm volatile("s_mov_b32 m0, %2\n\ts_nop 0\n\tglobal_load_lds_dwordx4 %0, %1 sc1" ::"v"(voff), "s"(sbase), "s"(ldst) : "memory");
}
DEV void dflag_set(u32 addr, u32 val) {
  asm volatile("s_waitcnt lgkmcnt(0)\n\ts_mov_b32 m0, %2\n\ts_mov_b64 exec, 1\n\tglobal_load_lds_dword %0, %1\n\ts_mov_b64 exec, -1"
               ::"v"(0), "s"(seq_tab + val), "s"(addr) : "memory");
}
#define WAIT_VM(n) asm volatile("s_waitcnt vmcnt(%0)" ::"i"(n) : "memory")
#define WAIT_LGKM0() asm volatile("s_waitcnt lgkmcnt(0)" ::: "memory")
#define BAR() do { asm volatile("" ::: "memory"); __builtin_amdgcn_s_barrier(); asm volatile("" ::: "memory"); } while (0)
#define SCHED0() __builtin_amdgcn_sched_barrier(0)
#define SGB(mask, n) __builtin_amdgcn_sched_group_barrier(mask, n, 0)

DEV void lflag_set(u32 addr, u32 val) {
  asm volatile("s_waitcnt lgkmcnt(0)\n\tds_write_b32 %0, %1" ::"v"(addr), "v"(val) : "memory");
}
DEV void lflag_wait(u32 addr, u32 mask, u32 val) {
  const u32 a = addr + 4 * (threadIdx.x & mask);
#pragma unroll 1
  for (int i = 0; i < SPIN_CAP; ++i) {
    u32 v;
    asm volatile("ds_read_b32 %0, %1\n\ts_waitcnt lgkmcnt(0)" : "=v"(v) : "v"(a) : "memory");
    if (__builtin_amdgcn_ballot_w64(v < val) == 0) break;
    __builtin_amdgcn_s_sleep(1);
  }
}
DEV u64 gload64(const char* p) { return __hip_atomic_load((GLBP(u64))p, __ATOMIC_RELAXED, __HIP_MEMORY_SCOPE_AGENT); }
DEV void gstore64(char* p, u64 v) { __hip_atomic_store((GLBP(u64))p, v, __ATOMIC_RELAXED, __HIP_MEMORY_SCOPE_AGENT); }
template <int SLEEP>
DEV void gflag_wait(const char* lane_ptr, u64 tag) {
  bool ok = false;
#pragma unroll 1
  for (int i = 0; i < SPIN_CAP; ++i) {
    asm volatile("" ::: "memory");
    if (!ok) ok = gload64(lane_ptr) == tag;
    if (__builtin_amdgcn_ballot_w64(!ok) == 0) break;
    __builtin_amdgcn_s_sleep(SLEEP);
  }
  asm volatile("" ::: "memory");
}
DEV void gflag_publish(char* p, u64 tag, int lane) {
  WAIT_VM(0);
  if (lane == 0) gstore64(p, tag);
}
DEV u64 tag_of(u64 seed) { return seed * 0x9E3779B97F4A7C15ull + 0xD1B54A32D192ED03ull; }
DEV u64 make_tag(const char* ws) { return tag_of(gload64(ws + WS_E)); }
typedef __amdgpu_buffer_rsrc_t rsrc_t;
DEV rsrc_t ws_rsrc(char* ws) { return __builtin_amdgcn_make_buffer_rsrc(ws, 0, WS_END, 0x00020000); }
DEV void st128_dev(rsrc_t r, u32 off, u32x4 v) { __builtin_amdgcn_raw_buffer_store_b128(v, r, off, 0, 16); }
DEV void st64_dev(rsrc_t r, u32 off, u32x2 v) { __builtin_amdgcn_raw_buffer_store_b64(v, r, off, 0, 16); }
DEV u32x2 ld64_dev(rsrc_t r, u32 off) { return __builtin_amdgcn_raw_buffer_load_b64(r, off, 0, 16); }
DEV void touch(u32 voff, const void* sbase, u32 ldst) {
  asm volatile("s_mov_b32 m0, %2\n\ts_nop 0\n\tglobal_load_lds_dword %0, %1" ::"v"(voff), "s"(sbase), "s"(ldst) : "memory");
}

DEV void glds4_dev(u32 voff, const void* sbase, u32 ldst) {
  asm volatile("s_mov_b32 m0, %2\n\ts_nop 0\n\tglobal_load_lds_dword %0, %1 sc1" ::"v"(voff), "s"(sbase), "s"(ldst) : "memory");
}

DEV u32 shr16(u32 hi, u32 lo) { return __builtin_amdgcn_alignbit(hi, lo, 16); }
DEV u32 xbuf(int w) { return w < 2 ? L_USCR + (u32)w * 4096 : (w == 2 ? L_XB2 : CV + 45056); }
DEV void conv_compute(int lane, int cw, int d, rsrc_t rs) {
  const int ch = cw >> 1, var = cw & 1;
  const int m = lane & 15, g = lane >> 4, q4 = m >> 2, p4 = lane & 3;
  u32 aa = opq(CV + (u32)(ch * 4 + ((q4 + 1) & 3)) * CV_HSTR + (u32)(((q4 + 1) & 4) + 8 * g + 4 * p4) * 2 + var * 256);
  u32 ba = opq(CV_U + (u32)ch * CV_USTR + (u32)(240 - 16 * m + 8 * g) * 2 + var * 256);
  f32x4 acc[8] = {};
  s16x8 A[8], B[8];
  const u32 a0 = opq(CV + (u32)ch * 4 * CV_HSTR + (u32)(m + 1 + 8 * g) * 2);
  u32x4 msk;
#pragma unroll
  for (int i = 0; i < 4; ++i) { const int e = m + 8 * g + 2 * i - 15; msk[i] = e >= 0 ? ~0u : (e == -1 ? 0xffff0000u : 0u); }
#pragma unroll
  for (int i = 0; i < 8; ++i) asm volatile("" : "+v"(acc[i]));
  asm volatile("" : "+v"(msk));
  const u32 xw = opq(xbuf(cw) + (u32)lane * 16);
  const u32 xa = opq(xbuf(cw ^ 1) + (u32)lane * 16), yo = opq(WS_YT + (u32)(d * (S * 2) + (1024 * var + 16 * m + 4 * g) * 2));
  const u32 ua = opq(CV_U + (u32)ch * CV_USTR + (u32)(2044 - 768 - (1024 * var + 16 * m + 4 * g)) * 2);
  const u32 dv = *(LDSP(u32))L_DD;
  const float Dd = ch ? bfhi(dv) : bflo(dv);
  lflag_wait(L_CONV, 3, 1);
  u32 h0[8];
#pragma unroll
  for (int j = 0; j < 8; ++j) h0[j] = *(LDSP(u16))((LDSP(char))a0 + j * 2);
  SCHED0();
#pragma unroll
  for (int p = 0; p < 8; ++p) { A[7 - p] = lds_tr2(aa, (7 - p) * 512, (7 - p) * 512 + 8); B[p] = lds_b128(ba, (7 - p) * 512); }
  u32x2 uu[4];
#pragma unroll
  for (int i = 0; i < 4; ++i) uu[i] = *(LDSP(u32x2))((LDSP(char))ua + (3 - i) * 512);
  SCHED0();
  if (var == 0) {
    u32x4 f;
#pragma unroll
    for (int i = 0; i < 4; ++i) f[i] = (h0[2 * i] | (h0[2 * i + 1] << 16)) & msk[i];
    A[0] = __builtin_bit_cast(s16x8, f);
  }
  SCHED0();
#pragma unroll 1
  for (int it = 0; it < 3; ++it) {
#pragma unroll
    for (int p = 7; p >= 0; --p) {
#pragma unroll
      for (int dd = 0; dd <= 7 - p; ++dd) {
        acc[p + dd] = mfma16(A[p], B[dd], acc[p + dd]);
        if (p == 0) { SCHED0(); B[dd] = lds_b128(ba, 64 + (7 - dd) * 512); SCHED0(); }
      }
      SCHED0();
      A[p] = lds_tr2(aa, 64 + p * 512, 64 + p * 512 + 8);
      SCHED0();
    }
    aa = opq(aa + 64); ba = opq(ba + 64);
  }
  auto half = [&](int hi) {
#pragma unroll
    for (int p = 7; p >= 0; --p)
#pragma unroll
      for (int dd = 0; dd <= 7 - p; ++dd)
        if (((p + dd) >> 2) == hi) acc[p + dd] = mfma16(A[p], B[dd], acc[p + dd]);
    SCHED0();
  };
  auto send = [&](int hi) {
#pragma unroll
    for (int i = 0; i < 4; ++i) *(LDSP(f32x4))((LDSP(char))xw + i * 1024) = acc[4 * hi + i];
    asm volatile("" ::: "memory");
    *(volatile LDSP(u32))(L_XCH + cw * 4) = 1;
    SCHED0();
  };
  f32x4 y[4];
  if (var) {
    half(0); send(0); half(1);
#pragma unroll
    for (int i = 0; i < 4; ++i) y[i] = acc[4 + i];
  } else {
    half(1); send(1); half(0);
#pragma unroll
    for (int i = 0; i < 4; ++i) y[i] = acc[i];
  }
  u32x2 sk[4];
#pragma unroll
  for (int i = 0; i < 4; ++i) sk[i] = u32x2{pk2(bfhi(uu[i][1]) * Dd, bflo(uu[i][1]) * Dd), pk2(bfhi(uu[i][0]) * Dd, bflo(uu[i][0]) * Dd)};
  SCHED0();
  lflag_wait(L_XCH + (cw ^ 1) * 4, 0, 1);
  f32x4 pv[4];
#pragma unroll
  for (int i = 0; i < 4; ++i) pv[i] = *(LDSP(f32x4))((LDSP(char))xa + i * 1024);
  SCHED0();
#pragma unroll
  for (int i = 0; i < 4; ++i) {
    const f32x4 s = y[i] + pv[i];
    const u32 c0 = pk2(s[0], s[1]), c1 = pk2(s[2], s[3]);
    st64_dev(rs, yo + i * 512, u32x2{pk2(bflo(c0) + bflo(sk[i][0]), bfhi(c0) + bfhi(sk[i][0])), pk2(bflo(c1) + bflo(sk[i][1]), bfhi(c1) + bfhi(sk[i][1]))});
  }
}

DEV int g2_ks(int j, int i) { const int e = 7 * j + (i < 7 ? i : i - 7); return 4 * (e >> 1) + (e & 1) + (i < 7 ? 0 : 2); }
DEV u32 g2_place(int j, int p) { return (p < 3 ? 2 * SLOT + (u32)p * 4096 : (u32)(p - 3) * 4096) + (u32)j * 12288; }
DEV u32 w2_voff(int lane) { return (u32)((lane >> 3) * (D * 2) + (((lane & 7) ^ (((lane >> 4) & 1) << 2)) << 4)); }

DEV void share(int b, int n, const char* base) {
  const int lane = (int)opq(threadIdx.x) & 63;
  if (lane < n) touch((u32)(b * n + lane) * 128, base, L_DUMMY);
}

__global__ __launch_bounds__(768, 3) void fused_kernel(const char* x, const char* kp, const char* vp, const char* skp,
                                                       const char* ssmk, const char* ssmD, const char* w1, const char* w3,
                                                       const char* w2, char* ws, char* out) {
  const int b = blockIdx.x, xcd = b & 7, r = b >> 3;
  const int wid = __builtin_amdgcn_readfirstlane(threadIdx.x >> 6);
  const int panel = (xcd >> 1) * 4 + r / 7, ft = (xcd & 1) * 7 + r % 7;
  const int tau = xcd * 4 + r / 7, dl = r % 7;
  const int d0 = 2 * b;

  if (b < 224) {
    if (wid >= 4 && wid < 7) share(wid == 4 ? xcd * 28 + r : b, wid == 4 ? 64 : 56, wid == 4 ? x : (wid == 5 ? w1 : w3));
    if (wid == 7) { *(LDSP(u32))(MISC + (threadIdx.x & 63) * 4) = 0; WAIT_LGKM0(); }
    BAR();
    if (wid < 2) {
      const int lane = (int)opq(threadIdx.x) & 63;
      const int g = lane >> 4, l15 = lane & 15;
      u32 vx[8], vw[8];
      const u32 ox = (u32)((lane >> 3) * (D * 2) + (((lane & 7) ^ g) << 4));
      const u32 ow = (u32)(g * (F * 2) + (((l15 >> 2) ^ g) << 6) + ((lane & 3) << 4));
#pragma unroll
      for (int i = 0; i < 8; ++i) {
        vx[i] = (ox ^ (u32)((i & 1) * 64)) + i * 8 * (D * 2) - (i & 3) * 1024;
        vw[i] = ow + i * 4 * (F * 2) - (i & 3) * 1024;
      }
#pragma unroll 1
      for (int c = 0; c < 7; ++c) {
        if (c >= 3) BAR();
        const u32 slot = (u32)(c % 3) * SLOT + wid * 8192;
        const char* xb = x + (size_t)(panel * 128 + wid * 64) * (D * 2) + c * 128;
        const char* w1b = w1 + (size_t)(c * 64 + wid * 32) * (F * 2) + ft * 256;
        const char* w3b = w3 + (size_t)(c * 64 + wid * 32) * (F * 2) + ft * 256;
#pragma unroll
        for (int i = 0; i < 8; i += 4) {
          glds16x4(vx[i], vx[i + 1], vx[i + 2], vx[i + 3], xb, slot + i * 1024);
          glds16x4(vw[i], vw[i + 1], vw[i + 2], vw[i + 3], w1b, slot + 16384 + i * 1024);
          glds16x4(vw[i], vw[i + 1], vw[i + 2], vw[i + 3], w3b, slot + 32768 + i * 1024);
        }
        dflag_set(L_FULL + wid * 4, c + 1);
      }
    }
    if (wid == 7 && (threadIdx.x & 63) < 5) touch((threadIdx.x & 63) * 128, seq_tab, L_DUMMY);
  }

  if (__builtin_expect(wid < 4 && b < 224, 1)) {
    const int lane = (int)opq(threadIdx.x) & 63;
    u64 tag = 0;
    if (wid < 2) {
      if (wid == 0) {
        tag = make_tag(ws);
        const int k = lane & 7;
        gflag_wait<8>(ws + WS_FST + (k < 2 ? 2 * panel + k : (k == 2 ? tau : (k & 1) * 14 + ft)) * 128, tag);
        glds16_dev(lane * 16, ws + WS_STAT + panel * 1024, L_STAT);
        glds16_dev(lane * 16, ws + WS_CSUM + ft * 1024, L_CSUM);
        if (lane < 2) glds4_dev(lane * 4, ws + WS_E, L_TAG);
        if (lane < 1) glds4_dev(0, ssmD + d0 * 2, L_DD);
        dflag_set(L_EPI, 1);
      }
    } else {
      __builtin_amdgcn_s_setprio(3);
      const rsrc_t rs = ws_rsrc(ws);
      const int u = wid - 2;
      const int ti = tau, cj = dl;
      const int tg = lane >> 3, cg = lane & 7;
      const int t0 = 64 * ti + 32 * u + 4 * tg, ch0 = 64 * cj + 8 * cg;
      u32x4 kr[7], vr[4], sw[4]; u32x2 o[8];
      u64 seed;
      auto ldkv = [&]() {
        const u32 ko = (u32)(t0 * (D * 2) + ch0 * 2);
#pragma unroll
        for (int i = 0; i < 7; ++i) kr[i] = *(const u32x4*)(kp + (i < 3 && t0 == 0 ? ko : ko + (u32)((i - 3) * (D * 2))));
#pragma unroll
        for (int i = 0; i < 4; ++i) vr[i] = *(const u32x4*)(vp + (ko + (u32)(i * (D * 2))));
        SCHED0();
      };
      auto piece = [&](int p) {
#pragma unroll
        for (int c = 2 * p; c < 2 * p + 2; ++c) {
          const u32 wa = sw[p][(c & 1) * 2], wb = sw[p][(c & 1) * 2 + 1];
          const float w0 = bflo(wa), w1f = bfhi(wa), w2f = bflo(wb), w3f = bfhi(wb);
          float kf[7], ks[4];
#pragma unroll
          for (int i = 0; i < 7; ++i) kf[i] = (c & 1) ? bfhi(kr[i][p]) : bflo(kr[i][p]);
#pragma unroll
          for (int i = 0; i < 4; ++i)
            ks[i] = __builtin_fmaf(kf[i + 3], w0, __builtin_fmaf(kf[i + 2], w1f, __builtin_fmaf(kf[i + 1], w2f, kf[i] * w3f)));
#pragma unroll
          for (int i = 0; i < 2; ++i) {
            const int ta = 3 - 2 * i, tb = 2 - 2 * i;
            const u32 kk = pk2(ks[ta], ks[tb]);
            o[c][i] = pk2(bflo(kk) * ((c & 1) ? bfhi(vr[ta][p]) : bflo(vr[ta][p])), bfhi(kk) * ((c & 1) ? bfhi(vr[tb][p]) : bflo(vr[tb][p])));
          }
          asm volatile("" : "+v"(o[c]));
        }
        SCHED0();
      };
      lflag_wait(L_FULL, 1, 1);
      share(ti * 7 + cj, 64, u ? vp : kp);
      BAR();
      ldkv();
#pragma unroll
      for (int i = 0; i < 4; ++i) sw[i] = *(const u32x4*)(skp + (u32)(ch0 * 8 + i * 16));
      seed = gload64(ws + WS_E + opq(0));
      if (u && lane < 14) {
        const u32 fo = lane < 8 ? WS_FH + (u32)((2 * panel + (lane & 1)) * 56 + 4 * ft + (lane >> 1)) * 128
                                : (lane < 12 ? WS_FY + (u32)((lane & 1) * 448 + d0 + ((lane >> 1) & 1)) * 128 : WS_FUT + (u32)(cj * 64 + 2 * ti + (lane & 1)) * 128);
        touch(fo, ws, L_DUMMY);
      }
      SCHED0();
      BAR();
      SCHED0();
#pragma unroll
      for (int i = 0; i < 3; ++i) if (t0 == 0) kr[i] = u32x4{0, 0, 0, 0};
      piece(0);
      BAR();
      SCHED0();
      piece(1);
      BAR();
      SCHED0();
#pragma unroll
      for (int c = 2; c < 4; ++c) piece(c);
      tag = tag_of(seed);
      const u32 scr = L_USCR + (u32)u * 4096;
#pragma unroll
      for (int c = 0; c < 8; ++c) *(LDSP(u32x2))(scr + (u32)((cg * 8 + c) * 64 + (((7 - tg) ^ (cg & 6)) << 3))) = o[c];
      WAIT_LGKM0();
#pragma unroll
      for (int i = 0; i < 4; ++i) {
        const int dd = 16 * i + (lane >> 2), c4 = lane & 3;
        u32x4 v = *(LDSP(u32x4))(scr + (u32)(dd * 64 + ((c4 ^ (dd >> 4)) << 4)));
        st128_dev(rs, WS_UT + (u32)((64 * cj + dd) * (S * 2) + (2016 - 64 * ti - 32 * u) * 2 + c4 * 16), v);
      }
      gflag_publish(ws + WS_FUT + (cj * 64 + 2 * ti + u) * 128, tag, lane);
    }
    const int j = wid;
    const u32 o2 = w2_voff(lane);
    const char* wbase = w2 + dl * 128;
    u32x4 pk[8][4];
    auto park = [&](int i0, int i1) {
#pragma unroll
      for (int i = i0; i < i1; ++i) {
        const char* src = wbase + (size_t)g2_ks(j, i) * 32 * (D * 2);
        asm volatile("" ::: "memory");
#pragma unroll
        for (int pc = 0; pc < 4; ++pc) pk[i & 7][pc] = *(const u32x4*)(src + (o2 + (u32)(pc * 8 * (D * 2))));
      }
      SCHED0();
    };
    park(0, 5);
    if (wid != 1) park(5, 6);
    if (wid == 2) {
      gflag_wait<8>(ws + WS_FUT + ((d0 >> 6) * 64 + lane) * 128, tag);
      lflag_wait(L_FREE, 7, 7);
      glds16x4_dev(lane * 16, ws + WS_UT + d0 * (S * 2), CV_U);
      glds16x4_dev(lane * 16, ws + WS_UT + (d0 + 1) * (S * 2), CV_U + CV_USTR);
      if (lane < 32) {
        glds16(lane * 16, seq_tab + 32, CV_U + 4096);
        glds16(lane * 16, seq_tab + 32, CV_U + CV_USTR + 4096);
      }
      dflag_set(L_CONV + 8, 1);
    }
    if (wid == 3) lflag_set(L_CONV + 12, 1);
    if (wid == 1) {
      u32x4 cv[8]; u32x2 cn[8];
#pragma unroll
      for (int i = 0; i < 8; ++i) {
        const u32 o = (u32)(d0 * (S * 2) + (lane + 64 * i) * 16);
        cv[i] = *(const u32x4*)(ssmk + o);
        cn[i] = *(const u32x2*)(ssmk + (o + (lane + 64 * i < 511 ? 16u : 0u)));
      }
      SCHED0();
      __builtin_amdgcn_s_setprio(3);
      lflag_wait(L_FREE, 7, 7);
      const u32 hb = opq(CV + 32 + lane * 16);
#pragma unroll
      for (int i = 0; i < 8; ++i) {
        const u32x4 c = cv[i]; const u32x2 n = cn[i];
        const u32 ad = hb + (i >> 2) * 4 * CV_HSTR + (i & 3) * 1024;
        lds_st128(ad, c);
        lds_st128(ad + CV_HSTR, u32x4{shr16(c[1], c[0]), shr16(c[2], c[1]), shr16(c[3], c[2]), shr16(n[0], c[3])});
        lds_st128(ad + 2 * CV_HSTR, u32x4{opq(c[1]), opq(c[2]), opq(c[3]), opq(n[0])});
        lds_st128(ad + 3 * CV_HSTR, u32x4{shr16(c[2], c[1]), shr16(c[3], c[2]), shr16(n[0], c[3]), shr16(n[1], n[0])});
        if ((i & 3) == 3) lflag_set(L_CONV + (i >> 2) * 4, 1);
      }
      SCHED0();
      park(5, 6);
    }
    park(6, 8);
    lflag_wait(L_FREE, 7, 7);
    const u32 wl = opq(lane * 16 + j * 12288);
    auto feed = [&](int i0, int i1) {
#pragma unroll
      for (int i = i0; i < i1; ++i)
#pragma unroll
        for (int n = 0; n < 4; ++n) lds_st128(wl + g2_place(0, i % 6) + n * 1024, pk[i & 7][n]);
      lflag_set(L_FED + j * 4, i1);
    };
    feed(0, 3);
    feed(3, 6);
    lflag_wait(L_PUBA, 3, 1);
    tag = tag_of(*(volatile LDSP(u64))L_TAG);
    gflag_wait<3>(ws + WS_FH + (tau * 56 + g2_ks(j, lane < 7 ? lane : 6)) * 128, tag);
    lflag_set(L_HE + j * 4, 1);
    lflag_wait(L_PUB, 3, 1);
    lflag_wait(L_CONV, 3, 1);
    lflag_wait(L_HE, 3, 1);
    park(8, 14);
    if (wid == 3) {
      lflag_wait(L_PUB, 3, 2);
      const int e = lane < 28 ? lane : 27;
      gflag_wait<4>(ws + WS_FH + (tau * 56 + 4 * (e >> 1) + (e & 1) + 2) * 128, tag);
      lflag_set(L_H, 1);
    }
    __builtin_amdgcn_s_setprio(0);
#pragma unroll
    for (int i = 6; i < 14; i += 2) {
      lflag_wait(L_PROG + j * 4, 0, i - 4);
      feed(i, i + 2);
    }
    return;
  }
  const rsrc_t rs = ws_rsrc(ws);

  if (b >= 224) {
    if (wid >= 8 && b != 255) return;
    const int tid = (int)opq(threadIdx.x), lane = tid & 63;
    const int sp = b - 224;
    const u64 seed = gload64(ws + WS_E + opq(0));
    if (tid == 0) touch(WS_FST + sp * 128, ws, L_DUMMY);
    if (wid < 8) {
      const int row = 64 * sp + (tid >> 3), c8 = tid & 7;
      const int cu = sp < 28 ? sp : 27, mat = cu / 14, ct = cu % 14, kg = tid >> 4, c16 = tid & 15;
      const char* wp = (mat ? w3 : w1) + ct * 256;
      const u32 wo = (u32)(kg * (F * 2) + c16 * 16), xo = (u32)(row * (D * 2) + c8 * 16);
      u32x4 xv[7], wv[14];
#pragma unroll
      for (int i = 0; i < 7; ++i) xv[i] = *(const u32x4*)(x + (xo + i * 128));
#pragma unroll
      for (int i = 0; i < 14; ++i) wv[i] = *(const u32x4*)(wp + (wo + i * 32 * (F * 2)));
      SCHED0();
      float s = 0.f;
#pragma unroll
      for (int i = 0; i < 7; ++i)
#pragma unroll
        for (int j = 0; j < 4; ++j) s += bflo(xv[i][j]) + bfhi(xv[i][j]);
      const float m = sum8(s) * (1.0f / D);
      float q = 0.f;
#pragma unroll
      for (int i = 0; i < 7; ++i)
#pragma unroll
        for (int j = 0; j < 4; ++j) {
          const float aa = bflo(xv[i][j]) - m, bb = bfhi(xv[i][j]) - m;
          q = __builtin_fmaf(aa, aa, __builtin_fmaf(bb, bb, q));
        }
      const float sd = rbf(__builtin_amdgcn_sqrtf(rbf(rbf(sum8(q) * (1.0f / D)) + 1e-5f)));
      if (c8 == 0) *(LDSP(f32x2))(u32)(16384 + (tid >> 3) * 8) = f32x2{rbf(m), __builtin_amdgcn_rcpf(sd)};
      SCHED0();
      float a[8] = {};
#pragma unroll
      for (int i = 0; i < 14; ++i)
#pragma unroll
        for (int j = 0; j < 4; ++j) { a[2 * j] += bflo(wv[i][j]); a[2 * j + 1] += bfhi(wv[i][j]); }
      f32x4 t;
#pragma unroll
      for (int j = 0; j < 4; ++j) {
        auto p = __builtin_amdgcn_permlane16_swap(__builtin_bit_cast(u32, a[j]), __builtin_bit_cast(u32, a[j + 4]), false, false);
        t[j] = asf(p[0]) + asf(p[1]);
      }
      *(LDSP(f32x4))(u32)((kg >> 1) * 512 + c16 * 32 + (kg & 1) * 16) = t;
    }
    WAIT_LGKM0(); BAR();
    const u64 tag = tag_of(seed);
    if (wid == 0) {
      const int cu = sp < 28 ? sp : 27;
      f32x2 cs = {};
#pragma unroll
      for (int i = 0; i < 16; ++i) cs += *(LDSP(f32x2))(u32)(i * 512 + lane * 8);
      st64_dev(rs, WS_STAT + (u32)(64 * sp + lane) * 8, *(LDSP(u32x2))(u32)(16384 + lane * 8));
      if (sp < 28) st64_dev(rs, WS_CSUM + (u32)((cu % 14) * 1024 + (cu / 14) * 512 + lane * 8), __builtin_bit_cast(u32x2, cs));
      gflag_publish(ws + WS_FST + sp * 128, tag, lane);
    }
    WAIT_VM(0); BAR();
    if (tid < 392) touch((u32)(sp * 392 + tid) * 128, w2, L_DUMMY);
    if (tid < 448) touch((u32)(sp * 448 + tid) * 128, ssmk, L_DUMMY);
    if (b == 255) {
      __builtin_amdgcn_s_sleep(127);
      gflag_wait<64>(ws + (tid < 480 ? WS_FUT + tid * 128 : WS_FH + ((((tid < 704 ? tid - 480 : 223) / 14) * 2) * 56 + ((tid < 704 ? tid - 480 : 223) % 14) * 4) * 128), tag);
      BAR();
      if (tid == 0) gstore64(ws + WS_E, tag);
    }
    return;
  }

  const int cw = wid - 4, th = cw & 1, fq = cw >> 1;
  {
    const int lane = (int)opq(threadIdx.x) & 63, g = lane >> 4, hh = lane >> 5, l31 = lane & 31;
    const int q4 = (lane >> 2) & 3, p4 = lane & 3;
    const u32 aA = 16384 + (u32)((8 * hh + q4) * 256 + ((fq ^ q4) << 6) + (g & 1) * 32 + p4 * 8);
    const u32 aB = (u32)((64 * th + l31) * 128 + ((hh ^ ((l31 >> 1) & 7)) << 4));
    f32x16 acc[2][2] = {};
    s16x8 fa[2][2][2], fb[2][2][2];
    u32 pa, pb[4];
    auto setslot = [&](u32 slot) {
      pa = opq(aA + slot);
#pragma unroll
      for (int k = 0; k < 4; ++k) pb[k] = opq((aB ^ (u32)(k << 5)) + slot);
    };
    auto rd = [&](int set) {
#pragma unroll
      for (int k = 0; k < 2; ++k) {
        const int ks = 2 * set + k;
        fa[set][k][0] = lds_tr2(pa, ks * 4096, ks * 4096 + 1024);
        fb[set][k][0] = lds_b128(pb[ks], 0);
        fb[set][k][1] = lds_b128(pb[ks], 4096);
        fa[set][k][1] = lds_tr2(pa, 16384 + ks * 4096, 16384 + ks * 4096 + 1024);
      }
    };
    auto mm = [&](int set, int n0, int n1) {
#pragma unroll
      for (int n = n0; n < n1; ++n) acc[(n >> 1) & 1][n & 1] = mfma32(fa[set][n >> 2][(n >> 1) & 1], fb[set][n >> 2][n & 1], acc[(n >> 1) & 1][n & 1]);
    };
    setslot(0);
    const u32 fad = opq(L_FULL + (lane & 1) * 4);
#pragma unroll
    for (int i = 0; i < 4; ++i) asm volatile("" : "+v"(acc[i >> 1][i & 1]));
    __builtin_amdgcn_s_setprio(1);
    lflag_wait(L_FULL, 1, 1);
    rd(0);
    SCHED0();
    u32 slot = 0;
    auto stage = [&](int c, bool bar) {
      rd(1);
      const u32 fl = *(volatile LDSP(u32))fad;
      mm(0, 0, 6);
      SGB(0x008, 1); SGB(0x100, 3); SGB(0x008, 1); SGB(0x100, 3); SGB(0x008, 1); SGB(0x100, 3); SGB(0x008, 1); SGB(0x100, 4); SGB(0x008, 2);
      SCHED0();
      slot = slot == 2 * SLOT ? 0 : slot + SLOT;
      setslot(slot);
      SCHED0();
      mm(0, 6, 7);
      SCHED0();
      __builtin_amdgcn_s_waitcnt(0xC07F);
      const bool miss = c < 6 && __builtin_amdgcn_ballot_w64(fl < (u32)(c + 2));
      SCHED0();
      mm(0, 7, 8);
      SCHED0();
      if (bar) BAR();
      SCHED0();
      mm(1, 0, 2);
      SCHED0();
      if (!bar && c == 6) lflag_set(L_FREE + cw * 4, 7);
      if (miss) lflag_wait(L_FULL, 1, c + 2);
      SCHED0();
      rd(0); mm(1, 2, 8);
#pragma unroll
      for (int i = 0; i < 4; ++i) { SGB(0x008, 1); SGB(0x100, 3); }
      SGB(0x008, 2);
      SCHED0();
    };
#pragma unroll 1
    for (int c = 0; c < 4; ++c) stage(c, true);
#pragma unroll 1
    for (int c = 4; c < 7; ++c) stage(c, false);
    const u32 hdst = WS_H + (u32)((2 * panel + th) * 56 + 4 * ft + fq) * 4096 + lane * 16;
    const u32 sa = opq(L_STAT + (u32)(64 * th + l31) * 8), ca = opq(L_CSUM + (u32)(32 * fq + 4 * hh) * 4);
    f32x2 st[2]; f32x4 csa[2][2][2];
    auto rdc = [&]() {
#pragma unroll
      for (int j = 0; j < 2; ++j) st[j] = *(volatile LDSP(f32x2))((LDSP(char))sa + j * 256);
#pragma unroll
      for (int n = 0; n < 8; ++n) csa[n >> 2][(n >> 1) & 1][n & 1] = *(volatile LDSP(f32x4))((LDSP(char))ca + ((n >> 1) & 1) * 512 + (n >> 2) * 64 + (n & 1) * 32);
    };
    const u32 ef = *(volatile LDSP(u32))L_EPI;
    rdc();
    if (__builtin_amdgcn_readfirstlane(ef) == 0) { lflag_wait(L_EPI, 0, 1); rdc(); }
    float nm[2], c1[2], irs2[2];
#pragma unroll
    for (int j = 0; j < 2; ++j) { nm[j] = -st[j][0]; c1[j] = -1.44269504f * st[j][1]; irs2[j] = __builtin_amdgcn_rcpf(st[j][1] * st[j][1]); }
#pragma unroll
    for (int s = 0; s < 2; ++s) {
      const f32x4 (&cs)[2][2] = csa[s];
#pragma unroll
      for (int j = 0; j < 2; ++j) {
        float a[8], ex[8], hv[8];
#pragma unroll
        for (int e = 0; e < 8; ++e) a[e] = __builtin_fmaf(nm[j], cs[0][e >> 2][e & 3], acc[0][j][8 * s + e]);
#pragma unroll
        for (int e = 0; e < 8; ++e) ex[e] = a[e] * c1[j];
        SCHED0();
#pragma unroll
        for (int e = 0; e < 8; ++e) { ex[e] = __builtin_amdgcn_exp2f(ex[e]); hv[e] = __builtin_fmaf(nm[j], cs[1][e >> 2][e & 3], acc[1][j][8 * s + e]); }
        SCHED0();
#pragma unroll
        for (int e = 0; e < 8; ++e) ex[e] = __builtin_fmaf(ex[e], irs2[j], irs2[j]);
        SCHED0();
#pragma unroll
        for (int e = 0; e < 8; ++e) { ex[e] = __builtin_amdgcn_rcpf(ex[e]); hv[e] *= a[e]; }
        SCHED0();
#pragma unroll
        for (int e = 0; e < 8; ++e) hv[e] *= ex[e];
        st128_dev(rs, hdst + (s * 2 + j) * 1024, u32x4{pk2(hv[0], hv[1]), pk2(hv[2], hv[3]), pk2(hv[4], hv[5]), pk2(hv[6], hv[7])});
      }
    }
  }
  char* hflag = ws + WS_FH + ((2 * panel + th) * 56 + 4 * ft + fq) * 128;
  u32x4 xv, sv;
  if (cw < 4) {
    const int ht = (int)opq(threadIdx.x) & 255, lane = ht & 63, l15 = ht & 15;
    const u64 tag = tag_of(*(volatile LDSP(u64))L_TAG);
    const int ch = cw >> 1, var = cw & 1;
    gflag_publish(hflag, tag, lane);
    lflag_set(L_PUBA + cw * 4, 1);
    u32x4 xr[2]; u32x2 st[2];
#pragma unroll
    for (int i = 0; i < 2; ++i) {
      const int n = ht + 256 * i, row = n >> 3, c = n & 7;
      xr[i] = *(const u32x4*)(x + (u32)((64 * tau + row) * (D * 2) + (64 * dl + 8 * c) * 2));
      st[i] = ld64_dev(rs, WS_STAT + (u32)(64 * tau + row) * 8);
    }
    SCHED0();
    conv_compute(lane, cw, d0 + ch, rs);
    u32x4 xn[2];
#pragma unroll
    for (int i = 0; i < 2; ++i) {
      const float mb = asf(st[i][0]), rsd = asf(st[i][1]);
#pragma unroll
      for (int e = 0; e < 4; ++e) {
        const u32 dd = pk2(bflo(xr[i][e]) - mb, bfhi(xr[i][e]) - mb);
        xn[i][e] = pk2(bflo(dd) * rsd, bfhi(dd) * rsd);
      }
    }
    asm volatile("" : "+v"(xn[0]), "+v"(xn[1]));
    SCHED0();
    gflag_publish(ws + WS_FY + (var * 448 + d0 + ch) * 128, tag, lane);
    xv = xr[0]; sv = xn[0];
    lflag_wait(L_XCH, 1, 1);
    lds_st128(E_S + (u32)ht * 16, xn[1]); lds_st128(E_X + (u32)ht * 16, xr[1]);
    gflag_wait<4>(ws + WS_FY + ((tau >> 4) * 448 + 64 * dl + 16 * cw + l15) * 128, tag);
#pragma unroll
    for (int i = 0; i < 2; ++i)
      glds16_dev((u32)((lane >> 3) * (S * 2) + (((lane & 7) ^ (2 * cw + i)) << 4)),
                 ws + WS_YT + (64 * dl + 16 * cw + 8 * i) * (S * 2) + 128 * tau, xbuf(cw ^ 1) + (u32)i * 1024);
    WAIT_VM(0);
  } else {
    const int j = cw - 4;
    const int lane = (int)opq(threadIdx.x) & 63;
    lflag_set(L_PUB + j * 4, 1);
    const u64 btag = tag_of(*(volatile LDSP(u64))L_TAG);
    const bool hready = __builtin_amdgcn_readfirstlane(*(volatile LDSP(u32))(L_HE + j * 4)) != 0;
    const u32 hl = lane * 16;
    const char* hbase = ws + WS_H + (size_t)tau * 56 * 4096;
    const int q4 = (lane >> 2) & 3;
    const u32 ta = (u32)((4 * (lane >> 5) + q4) * 128 + ((q4 >> 1) << 6) + ((lane >> 4) & 1) * 32 + (lane & 3) * 8);
    s16x8 fa[2][4], fb[3][4];
    f32x16 acc[2][2] = {};
    auto ldh = [&](int i) {
      const char* p = hbase + (size_t)g2_ks(j, i) * 4096;
      asm volatile("global_load_dwordx4 %0, %4, %5 sc1\n\tglobal_load_dwordx4 %1, %4, %5 offset:1024 sc1\n\t"
                   "global_load_dwordx4 %2, %4, %5 offset:2048 sc1\n\tglobal_load_dwordx4 %3, %4, %5 offset:3072 sc1"
                   : "=&v"(fb[i % 3][0]), "=&v"(fb[i % 3][1]), "=&v"(fb[i % 3][2]), "=&v"(fb[i % 3][3])
                   : "v"(hl), "s"(p) : "memory");
    };
    u32 tb[2];
#pragma unroll
    for (int a = 0; a < 2; ++a) tb[a] = opq((ta ^ (u32)(a << 6)) + g2_place(j, 0));
    auto rda = [&](int i) {
#pragma unroll
      for (int a = 0; a < 2; ++a)
        if (i > 0 && i % 3 == 0) tb[a] = opq(i % 6 ? tb[a] - 2 * SLOT : tb[a] + 2 * SLOT);
#pragma unroll
      for (int n = 0; n < 4; ++n) {
        const int o = (i % 3) * 4096 + (n >> 1) * 2048;
        fa[i & 1][n] = lds_tr2(tb[n & 1], o, o + 1024);
      }
    };
    const u32 fed = opq(L_FED + j * 4), prog = opq(L_PROG + j * 4);
    if (!hready) { gflag_publish(hflag, btag, lane); lflag_set(L_PUB + j * 4, 2); lflag_wait(L_HE + j * 4, 0, 1); }
#pragma unroll
    for (int i = 0; i < 3; ++i) ldh(i);
    if (hready) {
      WAIT_VM(12);
      if (lane == 0) gstore64(hflag, btag);
      lflag_set(L_PUB + j * 4, 2);
    }
    lflag_wait(L_FED + j * 4, 0, 3);
    rda(0);
    SCHED0();
    u32 fl = 0;
#pragma unroll
    for (int i = 0; i < 14; ++i) {
      if (i == 2 || (i >= 5 && i < 13 && (i & 1))) {
        if (__builtin_amdgcn_readfirstlane(fl) < i + 2) lflag_wait(L_FED + j * 4, 0, i + 2);
      }
      asm volatile("s_waitcnt vmcnt(%4)" : "+v"(fb[i % 3][0]), "+v"(fb[i % 3][1]), "+v"(fb[i % 3][2]), "+v"(fb[i % 3][3])
                   : "i"(i < 12 ? 8 : (13 - i) * 4) : "memory");
      SCHED0();
      if (i < 13) rda(i + 1);
      if (i == 1 || (i >= 4 && i < 12 && !(i & 1))) fl = *(volatile LDSP(u32))fed;
      if (i < 13) {
#pragma unroll
        for (int s = 0; s < 2; ++s)
#pragma unroll
          for (int a = 0; a < 2; ++a)
#pragma unroll
            for (int t = 0; t < 2; ++t) acc[a][t] = mfma32(fa[i & 1][2 * s + a], fb[i % 3][2 * s + t], acc[a][t]);
#pragma unroll
        for (int n = 0; n < 5; ++n) { SGB(0x008, 1); SGB(0x100, 2); }
        SGB(0x008, 3);
      } else {
        __builtin_amdgcn_s_waitcnt(0xC07F);
        const int ln = (int)opq(threadIdx.x), lt = ln & 31, lh = (ln >> 5) & 1;
#pragma unroll
        for (int n = 0; n < 5; ++n) {
          if (n < 4) {
#pragma unroll
            for (int s = 0; s < 2; ++s) acc[n & 1][n >> 1] = mfma32(fa[i & 1][2 * s + (n & 1)], fb[i % 3][2 * s + (n >> 1)], acc[n & 1][n >> 1]);
          }
          SCHED0();
          if (n > 0) {
            const int a = (n - 1) & 1, t = (n - 1) >> 1;
#pragma unroll
            for (int G = 0; G < 4; ++G)
              *(LDSP(f32x4))(g2_place(j, t ? 3 : 0) + (u32)(lt * 256 + (((8 * a + 2 * G + lh) ^ (lt & 7)) << 4))) =
                  f32x4{acc[a][t][4 * G], acc[a][t][4 * G + 1], acc[a][t][4 * G + 2], acc[a][t][4 * G + 3]};
          }
          SCHED0();
        }
      }
      SCHED0();
      if (i < 8 && (i & 1)) *(volatile LDSP(u32))prog = i + 1;
      if (i == 4) lflag_wait(L_H, 0, 1);
      if (i + 3 < 14) ldh(i + 3);
      SCHED0();
    }
  }
  {
    const int ht = (int)opq(threadIdx.x) & 255;
    const int half = cw >> 2, tk = ht >> 3, dg = ht & 7, tok = 32 * half + tk;
    const u32 pa0 = opq((half ? 0u : 2 * SLOT) + (u32)(tk * 256 + (((2 * dg) ^ (tk & 7)) << 4))), pa1 = opq(pa0 ^ 16u);
    const u32 ya = opq(xbuf((dg >> 1) ^ 1) + (u32)(8 * (dg & 1) * 128 + (((tok >> 3) ^ dg) << 4) + (tok & 7) * 2));
    const u32 ea = opq(E_S + (u32)ht * 16);
    const u32 oo = opq((u32)((64 * tau + tok) * (D * 2) + (64 * dl + 8 * dg) * 2));
    WAIT_LGKM0();
    BAR();
    f32x4 p0[4], p1[4];
    u32 yv[8];
#pragma unroll
    for (int w = 0; w < 4; ++w) {
      p0[w] = *(LDSP(f32x4))((LDSP(char))pa0 + w * 12288);
      p1[w] = *(LDSP(f32x4))((LDSP(char))pa1 + w * 12288);
    }
#pragma unroll
    for (int e = 0; e < 8; ++e) yv[e] = *(LDSP(u16))((LDSP(char))ya + e * 128);
    SCHED0();
    if (half) { sv = *(LDSP(u32x4))ea; xv = *(LDSP(u32x4))((LDSP(char))ea + (E_X - E_S)); }
    const f32x4 s0 = (p0[0] + p0[1]) + (p0[2] + p0[3]), s1 = (p1[0] + p1[1]) + (p1[2] + p1[3]);
    u32 f[4];
    float ex[8];
#pragma unroll
    for (int e = 0; e < 4; ++e) f[e] = e < 2 ? pk2(s0[2 * e], s0[2 * e + 1]) : pk2(s1[2 * e - 4], s1[2 * e - 3]);
#pragma unroll
    for (int e = 0; e < 8; ++e) ex[e] = __builtin_amdgcn_exp2f(-1.44269504f * ((e & 1) ? bfhi(f[e >> 1]) : bflo(f[e >> 1])));
    SCHED0();
    u32 se[4];
#pragma unroll
    for (int e = 0; e < 4; ++e) se[e] = pk2(bflo(sv[e]) * bflo(yv[2 * e]), bfhi(sv[e]) * bflo(yv[2 * e + 1]));
#pragma unroll
    for (int e = 0; e < 8; ++e) ex[e] = __builtin_amdgcn_rcpf(1.0f + ex[e]);
    SCHED0();
    u32x4 o;
#pragma unroll
    for (int e = 0; e < 4; ++e) {
      const u32 gt = pk2(bflo(f[e]) * ex[2 * e], bfhi(f[e]) * ex[2 * e + 1]);
      const u32 p = pk2(bflo(se[e]) * bflo(gt), bfhi(se[e]) * bfhi(gt));
      o[e] = pk2(bflo(xv[e]) + bflo(p), bfhi(xv[e]) + bfhi(p));
    }
    __builtin_amdgcn_raw_buffer_store_b128(o, __builtin_amdgcn_make_buffer_rsrc(out, 0, S * D * 2, 0x00020000), oo, 0, 16);
  }
}
}
#if defined(__HIP_DEVICE_COMPILE__) && __HIP_DEVICE_COMPILE__
#pragma clang attribute pop
#endif

extern "C" void kernel_launch(void* const* d_in, const int* in_sizes, int n_in, void* d_out, int out_size, void* d_ws,
                              size_t ws_size, hipStream_t stream) {
  (void)hipFuncSetAttribute((const void*)fused_kernel, hipFuncAttributeMaxDynamicSharedMemorySize, 163840);
  fused_kernel<<<256, 768, 163840, stream>>>((const char*)d_in[0], (const char*)d_in[1], (const char*)d_in[2], (const char*)d_in[3],
                                             (const char*)d_in[4], (const char*)d_in[5], (const char*)d_in[7], (const char*)d_in[8],
                                             (const char*)d_in[9], (char*)d_ws, (char*)d_out);
}
```

```cpp
#include <hip/hip_runtime.h>
#include <hip/hip_bf16.h>
#include <stdint.h>
#pragma clang diagnostic ignored "-Wint-to-pointer-cast"

#if defined(__HIP_DEVICE_COMPILE__) && __HIP_DEVICE_COMPILE__
#pragma clang attribute push(__attribute__((target("no-packed-fp32-ops"))), apply_to = function)
#endif
namespace {
typedef unsigned short u16;
typedef unsigned int u32;
typedef unsigned long long u64;
typedef __attribute__((ext_vector_type(4))) short s16x4;
typedef __attribute__((ext_vector_type(8))) short s16x8;
typedef __attribute__((ext_vector_type(8))) __bf16 bf16x8;
typedef __attribute__((ext_vector_type(2))) __bf16 bf16x2;
typedef __attribute__((ext_vector_type(16))) float f32x16;
typedef __attribute__((ext_vector_type(4))) float f32x4;
typedef __attribute__((ext_vector_type(2))) float f32x2;
typedef __attribute__((ext_vector_type(4))) u32 u32x4;
typedef __attribute__((ext_vector_type(2))) u32 u32x2;
#define LDSP(T) __attribute__((address_space(3))) T*
#define GLBP(T) __attribute__((address_space(1))) T*
#define DEV __device__ __forceinline__

constexpr int S = 2048, D = 448, F = 1792;
constexpr u32 WS_H = 0;
constexpr u32 WS_UT = WS_H + 7340032;
constexpr u32 WS_YT = WS_UT + 1835008;
constexpr u32 WS_STAT = WS_YT + 1835008;
constexpr u32 WS_CSUM = WS_STAT + 16384;
constexpr u32 WS_E = WS_CSUM + 16384;
constexpr u32 WS_FUT = WS_E + 128;
constexpr u32 WS_FST = WS_FUT + 448 * 128;
constexpr u32 WS_FY = WS_FST + 32 * 128;
constexpr u32 WS_FH = WS_FY + 896 * 128;
constexpr u32 WS_END = WS_FH + 1792 * 128;
constexpr u32 SLOT = 49152;
constexpr u32 MISC = 3 * SLOT;
constexpr u32 L_FULL = MISC, L_EPI = MISC + 8, L_H = MISC + 16, L_FREE = MISC + 32, L_CONV = MISC + 64, L_FED = MISC + 80,
              L_TAG = MISC + 96, L_DD = MISC + 104, L_PROG = MISC + 128, L_XCH = MISC + 144, L_PUB = MISC + 160,
              L_PUBA = MISC + 176, L_HE = MISC + 192, L_STAT = MISC + 256, L_CSUM = MISC + 1280, L_DUMMY = MISC + 2304,
              L_USCR = MISC + 4096, L_XB2 = MISC + 12288;
constexpr u32 CV = SLOT, CV_HSTR = 4160, CV_U = CV + 8 * CV_HSTR, CV_USTR = 4608;
constexpr u32 E_S = SLOT, E_X = SLOT + 4096;
constexpr int SPIN_CAP = 1 << 18;

__device__ const u32 seq_tab[160] __attribute__((aligned(128))) = {0, 1, 2, 3, 4, 5, 6, 7};
DEV float asf(u32 v) { return __builtin_bit_cast(float, v); }
DEV float bflo(u32 v) { return asf(v << 16); }
DEV float bfhi(u32 v) { return asf(v & 0xffff0000u); }
template <int CTRL> DEV float dpp(float v) { return asf(__builtin_amdgcn_update_dpp(0, __builtin_bit_cast(u32, v), CTRL, 0xf, 0xf, true)); }
DEV float sum8(float v) { v += dpp<0xB1>(v); v += dpp<0x4E>(v); return v + dpp<0x141>(v); }
DEV u32 pk2(float lo, float hi) {
  asm("" : "+v"(lo)); asm("" : "+v"(hi));
  bf16x2 r = {(__bf16)lo, (__bf16)hi};
  u32 v = __builtin_bit_cast(u32, r);
  asm("" : "+v"(v));
  return v;
}
DEV float rbf(float f) { bf16x2 r = {(__bf16)f, (__bf16)f}; return bflo(__builtin_bit_cast(u32, r)); }
DEV f32x4 mfma16(s16x8 a, s16x8 b, f32x4 c) {
  return __builtin_amdgcn_mfma_f32_16x16x32_bf16(__builtin_bit_cast(bf16x8, a), __builtin_bit_cast(bf16x8, b), c, 0, 0, 0);
}
DEV f32x16 mfma32(s16x8 a, s16x8 b, f32x16 c) {
  return __builtin_amdgcn_mfma_f32_32x32x16_bf16(__builtin_bit_cast(bf16x8, a), __builtin_bit_cast(bf16x8, b), c, 0, 0, 0);
}
DEV u32 opq(u32 v) { asm volatile("" : "+v"(v)); return v; }
DEV s16x8 lds_tr2(u32 base, int o0, int o1) {
  s16x4 lo = __builtin_amdgcn_ds_read_tr16_b64_v4i16((LDSP(s16x4))((LDSP(char))base + o0));
  s16x4 hi = __builtin_amdgcn_ds_read_tr16_b64_v4i16((LDSP(s16x4))((LDSP(char))base + o1));
  return __builtin_shufflevector(lo, hi, 0, 1, 2, 3, 4, 5, 6, 7);
}
DEV s16x8 lds_b128(u32 base, int o) { return *(LDSP(s16x8))((LDSP(char))base + o); }
DEV void lds_st128(u32 a, u32x4 v) { *(LDSP(u32x4))a = v; }
DEV void glds16x4(u32 v0, u32 v1, u32 v2, u32 v3, const void* sbase, u32 ldst) {
  asm volatile("s_mov_b32 m0, %5\n\ts_nop 0\n\tglobal_load_lds_dwordx4 %0, %4\n\tglobal_load_lds_dwordx4 %1, %4 offset:1024\n\t"
               "global_load_lds_dwordx4 %2, %4 offset:2048\n\tglobal_load_lds_dwordx4 %3, %4 offset:3072"
               ::"v"(v0), "v"(v1), "v"(v2), "v"(v3), "s"(sbase), "s"(ldst) : "memory");
}
DEV void glds16(u32 voff, const void* sbase, u32 ldst) {
  asm volatile("s_mov_b32 m0, %2\n\ts_nop 0\n\tglobal_load_lds_dwordx4 %0, %1" ::"v"(voff), "s"(sbase), "s"(ldst) : "memory");
}
DEV void glds16x4_dev(u32 v, const void* sbase, u32 ldst) {
  asm volatile("s_mov_b32 m0, %2\n\ts_nop 0\n\tglobal_load_lds_dwordx4 %0, %1 sc1\n\tglobal_load_lds_dwordx4 %0, %1 offset:1024 sc1\n\t"
               "global_load_lds_dwordx4 %0, %1 offset:2048 sc1\n\tglobal_load_lds_dwordx4 %0, %1 offset:3072 sc1"
               ::"v"(v), "s"(sbase), "s"(ldst) : "memory");
}
DEV void glds16_dev(u32 voff, const void* sbase, u32 ldst) {
  asm volatile("s_mov_b32 m0, %2\n\ts_nop 0\n\tglobal_load_lds_dwordx4 %0, %1 sc1" ::"v"(voff), "s"(sbase), "s"(ldst) : "memory");
}
DEV void dflag_set(u32 addr, u32 val) {
  asm volatile("s_waitcnt lgkmcnt(0)\n\ts_mov_b32 m0, %2\n\ts_mov_b64 exec, 1\n\tglobal_load_lds_dword %0, %1\n\ts_mov_b64 exec, -1"
               ::"v"(0), "s"(seq_tab + val), "s"(addr) : "memory");
}
#define WAIT_VM(n) asm volatile("s_waitcnt vmcnt(%0)" ::"i"(n) : "memory")
#define WAIT_LGKM0() asm volatile("s_waitcnt lgkmcnt(0)" ::: "memory")
#define BAR() do { asm volatile("" ::: "memory"); __builtin_amdgcn_s_barrier(); asm volatile("" ::: "memory"); } while (0)
#define SCHED0() __builtin_amdgcn_sched_barrier(0)
#define SGB(mask, n) __builtin_amdgcn_sched_group_barrier(mask, n, 0)

DEV void lflag_set(u32 addr, u32 val) {
  asm volatile("s_waitcnt lgkmcnt(0)\n\tds_write_b32 %0, %1" ::"v"(addr), "v"(val) : "memory");
}
DEV void lflag_wait(u32 addr, u32 mask, u32 val) {
  const u32 a = addr + 4 * (threadIdx.x & mask);
#pragma unroll 1
  for (int i = 0; i < SPIN_CAP; ++i) {
    u32 v;
    asm volatile("ds_read_b32 %0, %1\n\ts_waitcnt lgkmcnt(0)" : "=v"(v) : "v"(a) : "memory");
    if (__builtin_amdgcn_ballot_w64(v < val) == 0) break;
    __builtin_amdgcn_s_sleep(1);
  }
}
DEV u64 gload64(const char* p) { return __hip_atomic_load((GLBP(u64))p, __ATOMIC_RELAXED, __HIP_MEMORY_SCOPE_AGENT); }
DEV void gstore64(char* p, u64 v) { __hip_atomic_store((GLBP(u64))p, v, __ATOMIC_RELAXED, __HIP_MEMORY_SCOPE_AGENT); }
template <int SLEEP>
DEV void gflag_wait(const char* lane_ptr, u64 tag) {
  bool ok = false;
#pragma unroll 1
  for (int i = 0; i < SPIN_CAP; ++i) {
    asm volatile("" ::: "memory");
    if (!ok) ok = gload64(lane_ptr) == tag;
    if (__builtin_amdgcn_ballot_w64(!ok) == 0) break;
    __builtin_amdgcn_s_sleep(SLEEP);
  }
  asm volatile("" ::: "memory");
}
DEV void gflag_publish(char* p, u64 tag, int lane) {
  WAIT_VM(0);
  if (lane == 0) gstore64(p, tag);
}
DEV u64 tag_of(u64 seed) { return seed * 0x9E3779B97F4A7C15ull + 0xD1B54A32D192ED03ull; }
DEV u64 make_tag(const char* ws) { return tag_of(gload64(ws + WS_E)); }
typedef __amdgpu_buffer_rsrc_t rsrc_t;
DEV rsrc_t ws_rsrc(char* ws) { return __builtin_amdgcn_make_buffer_rsrc(ws, 0, WS_END, 0x00020000); }
DEV void st128_dev(rsrc_t r, u32 off, u32x4 v) { __builtin_amdgcn_raw_buffer_store_b128(v, r, off, 0, 16); }
DEV void st64_dev(rsrc_t r, u32 off, u32x2 v) { __builtin_amdgcn_raw_buffer_store_b64(v, r, off, 0, 16); }
DEV u32x2 ld64_dev(rsrc_t r, u32 off) { return __builtin_amdgcn_raw_buffer_load_b64(r, off, 0, 16); }
DEV void touch(u32 voff, const void* sbase, u32 ldst) {
  asm volatile("s_mov_b32 m0, %2\n\ts_nop 0\n\tglobal_load_lds_dword %0, %1" ::"v"(voff), "s"(sbase), "s"(ldst) : "memory");
}

DEV void glds4_dev(u32 voff, const void* sbase, u32 ldst) {
  asm volatile("s_mov_b32 m0, %2\n\ts_nop 0\n\tglobal_load_lds_dword %0, %1 sc1" ::"v"(voff), "s"(sbase), "s"(ldst) : "memory");
}

DEV u32 shr16(u32 hi, u32 lo) { return __builtin_amdgcn_alignbit(hi, lo, 16); }
DEV u32 xbuf(int w) { return w < 2 ? L_USCR + (u32)w * 4096 : (w == 2 ? L_XB2 : CV + 45056); }
DEV void conv_compute(int lane, int cw, int d, rsrc_t rs) {
  const int ch = cw >> 1, var = cw & 1;
  const int m = lane & 15, g = lane >> 4, q4 = m >> 2, p4 = lane & 3;
  u32 aa = opq(CV + (u32)(ch * 4 + ((q4 + 1) & 3)) * CV_HSTR + (u32)(((q4 + 1) & 4) + 8 * g + 4 * p4) * 2 + var * 256);
  u32 ba = opq(CV_U + (u32)ch * CV_USTR + (u32)(240 - 16 * m + 8 * g) * 2 + var * 256);
  f32x4 acc[8] = {};
  s16x8 A[8], B[8];
  const u32 a0 = opq(CV + (u32)ch * 4 * CV_HSTR + (u32)(m + 1 + 8 * g) * 2);
  u32x4 msk;
#pragma unroll
  for (int i = 0; i < 4; ++i) { const int e = m + 8 * g + 2 * i - 15; msk[i] = e >= 0 ? ~0u : (e == -1 ? 0xffff0000u : 0u); }
#pragma unroll
  for (int i = 0; i < 8; ++i) asm volatile("" : "+v"(acc[i]));
  asm volatile("" : "+v"(msk));
  const u32 xw = opq(xbuf(cw) + (u32)lane * 16);
  const u32 xa = opq(xbuf(cw ^ 1) + (u32)lane * 16), yo = opq(WS_YT + (u32)(d * (S * 2) + (1024 * var + 16 * m + 4 * g) * 2));
  const u32 ua = opq(CV_U + (u32)ch * CV_USTR + (u32)(2044 - 768 - (1024 * var + 16 * m + 4 * g)) * 2);
  const u32 dv = *(LDSP(u32))L_DD;
  const float Dd = ch ? bfhi(dv) : bflo(dv);
  lflag_wait(L_CONV, 3, 1);
  u32 h0[8];
#pragma unroll
  for (int j = 0; j < 8; ++j) h0[j] = *(LDSP(u16))((LDSP(char))a0 + j * 2);
  SCHED0();
#pragma unroll
  for (int p = 0; p < 8; ++p) { A[7 - p] = lds_tr2(aa, (7 - p) * 512, (7 - p) * 512 + 8); B[p] = lds_b128(ba, (7 - p) * 512); }
  u32x2 uu[4];
#pragma unroll
  for (int i = 0; i < 4; ++i) uu[i] = *(LDSP(u32x2))((LDSP(char))ua + (3 - i) * 512);
  SCHED0();
  if (var == 0) {
    u32x4 f;
#pragma unroll
    for (int i = 0; i < 4; ++i) f[i] = (h0[2 * i] | (h0[2 * i + 1] << 16)) & msk[i];
    A[0] = __builtin_bit_cast(s16x8, f);
  }
  SCHED0();
#pragma unroll 1
  for (int it = 0; it < 3; ++it) {
#pragma unroll
    for (int p = 7; p >= 0; --p) {
#pragma unroll
      for (int dd = 0; dd <= 7 - p; ++dd) {
        acc[p + dd] = mfma16(A[p], B[dd], acc[p + dd]);
        if (p == 0) { SCHED0(); B[dd] = lds_b128(ba, 64 + (7 - dd) * 512); SCHED0(); }
      }
      SCHED0();
      A[p] = lds_tr2(aa, 64 + p * 512, 64 + p * 512 + 8);
      SCHED0();
    }
    aa = opq(aa + 64); ba = opq(ba + 64);
  }
  auto half = [&](int hi) {
#pragma unroll
    for (int p = 7; p >= 0; --p)
#pragma unroll
      for (int dd = 0; dd <= 7 - p; ++dd)
        if (((p + dd) >> 2) == hi) acc[p + dd] = mfma16(A[p], B[dd], acc[p + dd]);
    SCHED0();
  };
  auto send = [&](int hi) {
#pragma unroll
    for (int i = 0; i < 4; ++i) *(LDSP(f32x4))((LDSP(char))xw + i * 1024) = acc[4 * hi + i];
    asm volatile("" ::: "memory");
    *(volatile LDSP(u32))(L_XCH + cw * 4) = 1;
    SCHED0();
  };
  f32x4 y[4];
  if (var) {
    half(0); send(0); half(1);
#pragma unroll
    for (int i = 0; i < 4; ++i) y[i] = acc[4 + i];
  } else {
    half(1); send(1); half(0);
#pragma unroll
    for (int i = 0; i < 4; ++i) y[i] = acc[i];
  }
  u32x2 sk[4];
#pragma unroll
  for (int i = 0; i < 4; ++i) sk[i] = u32x2{pk2(bfhi(uu[i][1]) * Dd, bflo(uu[i][1]) * Dd), pk2(bfhi(uu[i][0]) * Dd, bflo(uu[i][0]) * Dd)};
  SCHED0();
  lflag_wait(L_XCH + (cw ^ 1) * 4, 0, 1);
  f32x4 pv[4];
#pragma unroll
  for (int i = 0; i < 4; ++i) pv[i] = *(LDSP(f32x4))((LDSP(char))xa + i * 1024);
  SCHED0();
#pragma unroll
  for (int i = 0; i < 4; ++i) {
    const f32x4 s = y[i] + pv[i];
    const u32 c0 = pk2(s[0], s[1]), c1 = pk2(s[2], s[3]);
    st64_dev(rs, yo + i * 512, u32x2{pk2(bflo(c0) + bflo(sk[i][0]), bfhi(c0) + bfhi(sk[i][0])), pk2(bflo(c1) + bflo(sk[i][1]), bfhi(c1) + bfhi(sk[i][1]))});
  }
}

DEV int g2_ks(int j, int i) { const int e = 7 * j + (i < 7 ? i : i - 7); return 4 * (e >> 1) + (e & 1) + (i < 7 ? 0 : 2); }
DEV u32 g2_place(int j, int p) { return (p < 3 ? 2 * SLOT + (u32)p * 4096 : (u32)(p - 3) * 4096) + (u32)j * 12288; }
DEV u32 w2_voff(int lane) { return (u32)((lane >> 3) * (D * 2) + (((lane & 7) ^ (((lane >> 4) & 1) << 2)) << 4)); }

DEV void share(int b, int n, const char* base) {
  const int lane = (int)opq(threadIdx.x) & 63;
  if (lane < n) touch((u32)(b * n + lane) * 128, base, L_DUMMY);
}

__global__ __launch_bounds__(768, 3) void fused_kernel(const char* x, const char* kp, const char* vp, const char* skp,
                                                       const char* ssmk, const char* ssmD, const char* w1, const char* w3,
                                                       const char* w2, char* ws, char* out) {
  const int b = blockIdx.x, xcd = b & 7, r = b >> 3;
  const int wid = __builtin_amdgcn_readfirstlane(threadIdx.x >> 6);
  const int panel = (xcd >> 1) * 4 + r / 7, ft = (xcd & 1) * 7 + r % 7;
  const int tau = xcd * 4 + r / 7, dl = r % 7;
  const int d0 = 2 * b;

  if (b < 224) {
    if (wid >= 4 && wid < 7) share(wid == 4 ? xcd * 28 + r : b, wid == 4 ? 64 : 56, wid == 4 ? x : (wid == 5 ? w1 : w3));
    if (wid == 7) { *(LDSP(u32))(MISC + (threadIdx.x & 63) * 4) = 0; WAIT_LGKM0(); }
    BAR();
    if (wid < 2) {
      const int lane = (int)opq(threadIdx.x) & 63;
      const int g = lane >> 4, l15 = lane & 15;
      u32 vx[8], vw[8];
      const u32 ox = (u32)((lane >> 3) * (D * 2) + (((lane & 7) ^ g) << 4));
      const u32 ow = (u32)(g * (F * 2) + (((l15 >> 2) ^ g) << 6) + ((lane & 3) << 4));
#pragma unroll
      for (int i = 0; i < 8; ++i) {
        vx[i] = (ox ^ (u32)((i & 1) * 64)) + i * 8 * (D * 2) - (i & 3) * 1024;
        vw[i] = ow + i * 4 * (F * 2) - (i & 3) * 1024;
      }
#pragma unroll 1
      for (int c = 0; c < 7; ++c) {
        if (c >= 3) BAR();
        const u32 slot = (u32)(c % 3) * SLOT + wid * 8192;
        const char* xb = x + (size_t)(panel * 128 + wid * 64) * (D * 2) + c * 128;
        const char* w1b = w1 + (size_t)(c * 64 + wid * 32) * (F * 2) + ft * 256;
        const char* w3b = w3 + (size_t)(c * 64 + wid * 32) * (F * 2) + ft * 256;
#pragma unroll
        for (int i = 0; i < 8; i += 4) {
          glds16x4(vx[i], vx[i + 1], vx[i + 2], vx[i + 3], xb, slot + i * 1024);
          glds16x4(vw[i], vw[i + 1], vw[i + 2], vw[i + 3], w1b, slot + 16384 + i * 1024);
          glds16x4(vw[i], vw[i + 1], vw[i + 2], vw[i + 3], w3b, slot + 32768 + i * 1024);
        }
        dflag_set(L_FULL + wid * 4, c + 1);
      }
    }
    if (wid == 7 && (threadIdx.x & 63) < 5) touch((threadIdx.x & 63) * 128, seq_tab, L_DUMMY);
  }

  if (__builtin_expect(wid < 4 && b < 224, 1)) {
    const int lane = (int)opq(threadIdx.x) & 63;
    u64 tag = 0;
    if (wid < 2) {
      if (wid == 0) {
        tag = make_tag(ws);
        const int k = lane & 7;
        gflag_wait<8>(ws + WS_FST + (k < 2 ? 2 * panel + k : (k == 2 ? tau : (k & 1) * 14 + ft)) * 128, tag);
        glds16_dev(lane * 16, ws + WS_STAT + panel * 1024, L_STAT);
        glds16_dev(lane * 16, ws + WS_CSUM + ft * 1024, L_CSUM);
        if (lane < 2) glds4_dev(lane * 4, ws + WS_E, L_TAG);
        if (lane < 1) glds4_dev(0, ssmD + d0 * 2, L_DD);
        dflag_set(L_EPI, 1);
      }
    } else {
      __builtin_amdgcn_s_setprio(3);
      const rsrc_t rs = ws_rsrc(ws);
      const int u = wid - 2;
      const int ti = tau, cj = dl;
      const int tg = lane >> 3, cg = lane & 7;
      const int t0 = 64 * ti + 32 * u + 4 * tg, ch0 = 64 * cj + 8 * cg;
      u32x4 kr[7], vr[4], sw[4]; u32x2 o[8];
      u64 seed;
      auto ldkv = [&]() {
        const u32 ko = (u32)(t0 * (D * 2) + ch0 * 2);
#pragma unroll
        for (int i = 0; i < 7; ++i) kr[i] = *(const u32x4*)(kp + (i < 3 && t0 == 0 ? ko : ko + (u32)((i - 3) * (D * 2))));
#pragma unroll
        for (int i = 0; i < 4; ++i) vr[i] = *(const u32x4*)(vp + (ko + (u32)(i * (D * 2))));
        SCHED0();
      };
      auto piece = [&](int p) {
#pragma unroll
        for (int c = 2 * p; c < 2 * p + 2; ++c) {
          const u32 wa = sw[p][(c & 1) * 2], wb = sw[p][(c & 1) * 2 + 1];
          const float w0 = bflo(wa), w1f = bfhi(wa), w2f = bflo(wb), w3f = bfhi(wb);
          float kf[7], ks[4];
#pragma unroll
          for (int i = 0; i < 7; ++i) kf[i] = (c & 1) ? bfhi(kr[i][p]) : bflo(kr[i][p]);
#pragma unroll
          for (int i = 0; i < 4; ++i)
            ks[i] = __builtin_fmaf(kf[i + 3], w0, __builtin_fmaf(kf[i + 2], w1f, __builtin_fmaf(kf[i + 1], w2f, kf[i] * w3f)));
#pragma unroll
          for (int i = 0; i < 2; ++i) {
            const int ta = 3 - 2 * i, tb = 2 - 2 * i;
            const u32 kk = pk2(ks[ta], ks[tb]);
            o[c][i] = pk2(bflo(kk) * ((c & 1) ? bfhi(vr[ta][p]) : bflo(vr[ta][p])), bfhi(kk) * ((c & 1) ? bfhi(vr[tb][p]) : bflo(vr[tb][p])));
          }
          asm volatile("" : "+v"(o[c]));
        }
        SCHED0();
      };
      lflag_wait(L_FULL, 1, 1);
      share(ti * 7 + cj, 64, u ? vp : kp);
      BAR();
      ldkv();
#pragma unroll
      for (int i = 0; i < 4; ++i) sw[i] = *(const u32x4*)(skp + (u32)(ch0 * 8 + i * 16));
      seed = gload64(ws + WS_E + opq(0));
      if (u && lane < 14) {
        const u32 fo = lane < 8 ? WS_FH + (u32)((2 * panel + (lane & 1)) * 56 + 4 * ft + (lane >> 1)) * 128
                                : (lane < 12 ? WS_FY + (u32)((lane & 1) * 448 + d0 + ((lane >> 1) & 1)) * 128 : WS_FUT + (u32)(cj * 64 + 2 * ti + (lane & 1)) * 128);
        touch(fo, ws, L_DUMMY);
      }
      SCHED0();
      BAR();
      SCHED0();
#pragma unroll
      for (int i = 0; i < 3; ++i) if (t0 == 0) kr[i] = u32x4{0, 0, 0, 0};
      piece(0);
      BAR();
      SCHED0();
      piece(1);
      BAR();
      SCHED0();
#pragma unroll
      for (int c = 2; c < 4; ++c) piece(c);
      tag = tag_of(seed);
      const u32 scr = L_USCR + (u32)u * 4096;
#pragma unroll
      for (int c = 0; c < 8; ++c) *(LDSP(u32x2))(scr + (u32)((cg * 8 + c) * 64 + (((7 - tg) ^ (cg & 6)) << 3))) = o[c];
      WAIT_LGKM0();
#pragma unroll
      for (int i = 0; i < 4; ++i) {
        const int dd = 16 * i + (lane >> 2), c4 = lane & 3;
        u32x4 v = *(LDSP(u32x4))(scr + (u32)(dd * 64 + ((c4 ^ (dd >> 4)) << 4)));
        st128_dev(rs, WS_UT + (u32)((64 * cj + dd) * (S * 2) + (2016 - 64 * ti - 32 * u) * 2 + c4 * 16), v);
      }
      gflag_publish(ws + WS_FUT + (cj * 64 + 2 * ti + u) * 128, tag, lane);
    }
    const int j = wid;
    const u32 o2 = w2_voff(lane);
    const char* wbase = w2 + dl * 128;
    u32x4 pk[8][4];
    auto park = [&](int i0, int i1) {
#pragma unroll
      for (int i = i0; i < i1; ++i) {
        const char* src = wbase + (size_t)g2_ks(j, i) * 32 * (D * 2);
        asm volatile("" ::: "memory");
#pragma unroll
        for (int pc = 0; pc < 4; ++pc) pk[i & 7][pc] = *(const u32x4*)(src + (o2 + (u32)(pc * 8 * (D * 2))));
      }
      SCHED0();
    };
    park(0, 5);
    if (wid != 1) park(5, 6);
    if (wid == 2) {
      gflag_wait<8>(ws + WS_FUT + ((d0 >> 6) * 64 + lane) * 128, tag);
      lflag_wait(L_FREE, 7, 7);
      glds16x4_dev(lane * 16, ws + WS_UT + d0 * (S * 2), CV_U);
      glds16x4_dev(lane * 16, ws + WS_UT + (d0 + 1) * (S * 2), CV_U + CV_USTR);
      if (lane < 32) {
        glds16(lane * 16, seq_tab + 32, CV_U + 4096);
        glds16(lane * 16, seq_tab + 32, CV_U + CV_USTR + 4096);
      }
      dflag_set(L_CONV + 8, 1);
    }
    if (wid == 3) lflag_set(L_CONV + 12, 1);
    if (wid == 1) {
      u32x4 cv[8]; u32x2 cn[8];
#pragma unroll
      for (int i = 0; i < 8; ++i) {
        const u32 o = (u32)(d0 * (S * 2) + (lane + 64 * i) * 16);
        cv[i] = *(const u32x4*)(ssmk + o);
        cn[i] = *(const u32x2*)(ssmk + (o + (lane + 64 * i < 511 ? 16u : 0u)));
      }
      SCHED0();
      __builtin_amdgcn_s_setprio(3);
      lflag_wait(L_FREE, 7, 7);
      const u32 hb = opq(CV + 32 + lane * 16);
#pragma unroll
      for (int i = 0; i < 8; ++i) {
        const u32x4 c = cv[i]; const u32x2 n = cn[i];
        const u32 ad = hb + (i >> 2) * 4 * CV_HSTR + (i & 3) * 1024;
        lds_st128(ad, c);
        lds_st128(ad + CV_HSTR, u32x4{shr16(c[1], c[0]), shr16(c[2], c[1]), shr16(c[3], c[2]), shr16(n[0], c[3])});
        lds_st128(ad + 2 * CV_HSTR, u32x4{opq(c[1]), opq(c[2]), opq(c[3]), opq(n[0])});
        lds_st128(ad + 3 * CV_HSTR, u32x4{shr16(c[2], c[1]), shr16(c[3], c[2]), shr16(n[0], c[3]), shr16(n[1], n[0])});
        if ((i & 3) == 3) lflag_set(L_CONV + (i >> 2) * 4, 1);
      }
      SCHED0();
      park(5, 6);
    }
    park(6, 8);
    lflag_wait(L_FREE, 7, 7);
    const u32 wl = opq(lane * 16 + j * 12288);
    auto feed = [&](int i0, int i1) {
#pragma unroll
      for (int i = i0; i < i1; ++i)
#pragma unroll
        for (int n = 0; n < 4; ++n) lds_st128(wl + g2_place(0, i % 6) + n * 1024, pk[i & 7][n]);
      lflag_set(L_FED + j * 4, i1);
    };
    feed(0, 3);
    feed(3, 6);
    lflag_wait(L_PUBA, 3, 1);
    tag = tag_of(*(volatile LDSP(u64))L_TAG);
    gflag_wait<3>(ws + WS_FH + (tau * 56 + g2_ks(j, lane < 7 ? lane : 6)) * 128, tag);
    lflag_set(L_HE + j * 4, 1);
    lflag_wait(L_PUB, 3, 1);
    lflag_wait(L_CONV, 3, 1);
    lflag_wait(L_HE, 3, 1);
    park(8, 14);
    if (wid == 3) {
      lflag_wait(L_PUB, 3, 2);
      const int e = lane < 28 ? lane : 27;
      gflag_wait<4>(ws + WS_FH + (tau * 56 + 4 * (e >> 1) + (e & 1) + 2) * 128, tag);
      lflag_set(L_H, 1);
    }
    __builtin_amdgcn_s_setprio(0);
#pragma unroll
    for (int i = 6; i < 14; i += 2) {
      lflag_wait(L_PROG + j * 4, 0, i - 4);
      feed(i, i + 2);
    }
    return;
  }
  const rsrc_t rs = ws_rsrc(ws);

  if (b >= 224) {
    if (wid >= 8 && b != 255) return;
    const int tid = (int)opq(threadIdx.x), lane = tid & 63;
    const int sp = b - 224;
    const u64 seed = gload64(ws + WS_E + opq(0));
    if (tid == 0) touch(WS_FST + sp * 128, ws, L_DUMMY);
    if (wid < 8) {
      const int row = 64 * sp + (tid >> 3), c8 = tid & 7;
      const int cu = sp < 28 ? sp : 27, mat = cu / 14, ct = cu % 14, kg = tid >> 4, c16 = tid & 15;
      const char* wp = (mat ? w3 : w1) + ct * 256;
      const u32 wo = (u32)(kg * (F * 2) + c16 * 16), xo = (u32)(row * (D * 2) + c8 * 16);
      u32x4 xv[7], wv[14];
#pragma unroll
      for (int i = 0; i < 7; ++i) xv[i] = *(const u32x4*)(x + (xo + i * 128));
#pragma unroll
      for (int i = 0; i < 14; ++i) wv[i] = *(const u32x4*)(wp + (wo + i * 32 * (F * 2)));
      SCHED0();
      float s = 0.f;
#pragma unroll
      for (int i = 0; i < 7; ++i)
#pragma unroll
        for (int j = 0; j < 4; ++j) s += bflo(xv[i][j]) + bfhi(xv[i][j]);
      const float m = sum8(s) * (1.0f / D);
      float q = 0.f;
#pragma unroll
      for (int i = 0; i < 7; ++i)
#pragma unroll
        for (int j = 0; j < 4; ++j) {
          const float aa = bflo(xv[i][j]) - m, bb = bfhi(xv[i][j]) - m;
          q = __builtin_fmaf(aa, aa, __builtin_fmaf(bb, bb, q));
        }
      const float sd = rbf(__builtin_amdgcn_sqrtf(rbf(rbf(sum8(q) * (1.0f / D)) + 1e-5f)));
      if (c8 == 0) *(LDSP(f32x2))(u32)(16384 + (tid >> 3) * 8) = f32x2{rbf(m), __builtin_amdgcn_rcpf(sd)};
      SCHED0();
      float a[8] = {};
#pragma unroll
      for (int i = 0; i < 14; ++i)
#pragma unroll
        for (int j = 0; j < 4; ++j) { a[2 * j] += bflo(wv[i][j]); a[2 * j + 1] += bfhi(wv[i][j]); }
      f32x4 t;
#pragma unroll
      for (int j = 0; j < 4; ++j) {
        auto p = __builtin_amdgcn_permlane16_swap(__builtin_bit_cast(u32, a[j]), __builtin_bit_cast(u32, a[j + 4]), false, false);
        t[j] = asf(p[0]) + asf(p[1]);
      }
      *(LDSP(f32x4))(u32)((kg >> 1) * 512 + c16 * 32 + (kg & 1) * 16) = t;
    }
    WAIT_LGKM0(); BAR();
    const u64 tag = tag_of(seed);
    if (wid == 0) {
      const int cu = sp < 28 ? sp : 27;
      f32x2 cs = {};
#pragma unroll
      for (int i = 0; i < 16; ++i) cs += *(LDSP(f32x2))(u32)(i * 512 + lane * 8);
      st64_dev(rs, WS_STAT + (u32)(64 * sp + lane) * 8, *(LDSP(u32x2))(u32)(16384 + lane * 8));
      if (sp < 28) st64_dev(rs, WS_CSUM + (u32)((cu % 14) * 1024 + (cu / 14) * 512 + lane * 8), __builtin_bit_cast(u32x2, cs));
      gflag_publish(ws + WS_FST + sp * 128, tag, lane);
    }
    WAIT_VM(0); BAR();
    if (tid < 392) touch((u32)(sp * 392 + tid) * 128, w2, L_DUMMY);
    if (tid < 448) touch((u32)(sp * 448 + tid) * 128, ssmk, L_DUMMY);
    if (b == 255) {
      __builtin_amdgcn_s_sleep(127);
      gflag_wait<64>(ws + (tid < 480 ? WS_FUT + tid * 128 : WS_FH + ((((tid < 704 ? tid - 480 : 223) / 14) * 2) * 56 + ((tid < 704 ? tid - 480 : 223) % 14) * 4) * 128), tag);
      BAR();
      if (tid == 0) gstore64(ws + WS_E, tag);
    }
    return;
  }

  const int cw = wid - 4, th = cw & 1, fq = cw >> 1;
  {
    const int lane = (int)opq(threadIdx.x) & 63, g = lane >> 4, hh = lane >> 5, l31 = lane & 31;
    const int q4 = (lane >> 2) & 3, p4 = lane & 3;
    const u32 aA = 16384 + (u32)((8 * hh + q4) * 256 + ((fq ^ q4) << 6) + (g & 1) * 32 + p4 * 8);
    const u32 aB = (u32)((64 * th + l31) * 128 + ((hh ^ ((l31 >> 1) & 7)) << 4));
    f32x16 acc[2][2] = {};
    s16x8 fa[2][2][2], fb[2][2][2];
    u32 pa, pb[4];
    auto setslot = [&](u32 slot) {
      pa = opq(aA + slot);
#pragma unroll
      for (int k = 0; k < 4; ++k) pb[k] = opq((aB ^ (u32)(k << 5)) + slot);
    };
    auto rd = [&](int set) {
#pragma unroll
      for (int k = 0; k < 2; ++k) {
        const int ks = 2 * set + k;
        fa[set][k][0] = lds_tr2(pa, ks * 4096, ks * 4096 + 1024);
        fb[set][k][0] = lds_b128(pb[ks], 0);
        fb[set][k][1] = lds_b128(pb[ks], 4096);
        fa[set][k][1] = lds_tr2(pa, 16384 + ks * 4096, 16384 + ks * 4096 + 1024);
      }
    };
    auto mm = [&](int set, int n0, int n1) {
#pragma unroll
      for (int n = n0; n < n1; ++n) acc[(n >> 1) & 1][n & 1] = mfma32(fa[set][n >> 2][(n >> 1) & 1], fb[set][n >> 2][n & 1], acc[(n >> 1) & 1][n & 1]);
    };
    setslot(0);
    const u32 fad = opq(L_FULL + (lane & 1) * 4);
#pragma unroll
    for (int i = 0; i < 4; ++i) asm volatile("" : "+v"(acc[i >> 1][i & 1]));
    __builtin_amdgcn_s_setprio(1);
    lflag_wait(L_FULL, 1, 1);
    rd(0);
    SCHED0();
    u32 slot = 0;
    auto stage = [&](int c, bool bar) {
      rd(1);
      const u32 fl = *(volatile LDSP(u32))fad;
      mm(0, 0, 6);
      SGB(0x008, 1); SGB(0x100, 3); SGB(0x008, 1); SGB(0x100, 3); SGB(0x008, 1); SGB(0x100, 3); SGB(0x008, 1); SGB(0x100, 4); SGB(0x008, 2);
      SCHED0();
      slot = slot == 2 * SLOT ? 0 : slot + SLOT;
      setslot(slot);
      SCHED0();
      mm(0, 6, 7);
      SCHED0();
      __builtin_amdgcn_s_waitcnt(0xC07F);
      const bool miss = c < 6 && __builtin_amdgcn_ballot_w64(fl < (u32)(c + 2));
      SCHED0();
      mm(0, 7, 8);
      SCHED0();
      if (bar) BAR();
      SCHED0();
      mm(1, 0, 2);
      SCHED0();
      if (!bar && c == 6) lflag_set(L_FREE + cw * 4, 7);
      if (miss) lflag_wait(L_FULL, 1, c + 2);
      SCHED0();
      rd(0); mm(1, 2, 8);
#pragma unroll
      for (int i = 0; i < 4; ++i) { SGB(0x008, 1); SGB(0x100, 3); }
      SGB(0x008, 2);
      SCHED0();
    };
#pragma unroll 1
    for (int c = 0; c < 4; ++c) stage(c, true);
#pragma unroll 1
    for (int c = 4; c < 7; ++c) stage(c, false);
    const u32 hdst = WS_H + (u32)((2 * panel + th) * 56 + 4 * ft + fq) * 4096 + lane * 16;
    const u32 sa = opq(L_STAT + (u32)(64 * th + l31) * 8), ca = opq(L_CSUM + (u32)(32 * fq + 4 * hh) * 4);
    f32x2 st[2]; f32x4 csa[2][2][2];
    auto rdc = [&]() {
#pragma unroll
      for (int j = 0; j < 2; ++j) st[j] = *(volatile LDSP(f32x2))((LDSP(char))sa + j * 256);
#pragma unroll
      for (int n = 0; n < 8; ++n) csa[n >> 2][(n >> 1) & 1][n & 1] = *(volatile LDSP(f32x4))((LDSP(char))ca + ((n >> 1) & 1) * 512 + (n >> 2) * 64 + (n & 1) * 32);
    };
    const u32 ef = *(volatile LDSP(u32))L_EPI;
    rdc();
    if (__builtin_amdgcn_readfirstlane(ef) == 0) { lflag_wait(L_EPI, 0, 1); rdc(); }
    float nm[2], c1[2], irs2[2];
#pragma unroll
    for (int j = 0; j < 2; ++j) { nm[j] = -st[j][0]; c1[j] = -1.44269504f * st[j][1]; irs2[j] = __builtin_amdgcn_rcpf(st[j][1] * st[j][1]); }
#pragma unroll
    for (int s = 0; s < 2; ++s) {
      const f32x4 (&cs)[2][2] = csa[s];
#pragma unroll
      for (int j = 0; j < 2; ++j) {
        float a[8], ex[8], hv[8];
#pragma unroll
        for (int e = 0; e < 8; ++e) a[e] = __builtin_fmaf(nm[j], cs[0][e >> 2][e & 3], acc[0][j][8 * s + e]);
#pragma unroll
        for (int e = 0; e < 8; ++e) ex[e] = a[e] * c1[j];
        SCHED0();
#pragma unroll
        for (int e = 0; e < 8; ++e) { ex[e] = __builtin_amdgcn_exp2f(ex[e]); hv[e] = __builtin_fmaf(nm[j], cs[1][e >> 2][e & 3], acc[1][j][8 * s + e]); }
        SCHED0();
#pragma unroll
        for (int e = 0; e < 8; ++e) ex[e] = __builtin_fmaf(ex[e], irs2[j], irs2[j]);
        SCHED0();
#pragma unroll
        for (int e = 0; e < 8; ++e) { ex[e] = __builtin_amdgcn_rcpf(ex[e]); hv[e] *= a[e]; }
        SCHED0();
#pragma unroll
        for (int e = 0; e < 8; ++e) hv[e] *= ex[e];
        st128_dev(rs, hdst + (s * 2 + j) * 1024, u32x4{pk2(hv[0], hv[1]), pk2(hv[2], hv[3]), pk2(hv[4], hv[5]), pk2(hv[6], hv[7])});
      }
    }
  }
  char* hflag = ws + WS_FH + ((2 * panel + th) * 56 + 4 * ft + fq) * 128;
  u32x4 xv, sv;
  if (cw < 4) {
    const int ht = (int)opq(threadIdx.x) & 255, lane = ht & 63, l15 = ht & 15;
    const u64 tag = tag_of(*(volatile LDSP(u64))L_TAG);
    const int ch = cw >> 1, var = cw & 1;
    gflag_publish(hflag, tag, lane);
    lflag_set(L_PUBA + cw * 4, 1);
    u32x4 xr[2]; u32x2 st[2];
#pragma unroll
    for (int i = 0; i < 2; ++i) {
      const int n = ht + 256 * i, row = n >> 3, c = n & 7;
      xr[i] = *(const u32x4*)(x + (u32)((64 * tau + row) * (D * 2) + (64 * dl + 8 * c) * 2));
      st[i] = ld64_dev(rs, WS_STAT + (u32)(64 * tau + row) * 8);
    }
    SCHED0();
    conv_compute(lane, cw, d0 + ch, rs);
    u32x4 xn[2];
#pragma unroll
    for (int i = 0; i < 2; ++i) {
      const float mb = asf(st[i][0]), rsd = asf(st[i][1]);
#pragma unroll
      for (int e = 0; e < 4; ++e) {
        const u32 dd = pk2(bflo(xr[i][e]) - mb, bfhi(xr[i][e]) - mb);
        xn[i][e] = pk2(bflo(dd) * rsd, bfhi(dd) * rsd);
      }
    }
    asm volatile("" : "+v"(xn[0]), "+v"(xn[1]));
    SCHED0();
    gflag_publish(ws + WS_FY + (var * 448 + d0 + ch) * 128, tag, lane);
    xv = xr[0]; sv = xn[0];
    lflag_wait(L_XCH, 1, 1);
    lds_st128(E_S + (u32)ht * 16, xn[1]); lds_st128(E_X + (u32)ht * 16, xr[1]);
    gflag_wait<4>(ws + WS_FY + ((tau >> 4) * 448 + 64 * dl + 16 * cw + l15) * 128, tag);
#pragma unroll
    for (int i = 0; i < 2; ++i)
      glds16_dev((u32)((lane >> 3) * (S * 2) + (((lane & 7) ^ (2 * cw + i)) << 4)),
                 ws + WS_YT + (64 * dl + 16 * cw + 8 * i) * (S * 2) + 128 * tau, xbuf(cw ^ 1) + (u32)i * 1024);
    WAIT_VM(0);
  } else {
    const int j = cw - 4;
    const int lane = (int)opq(threadIdx.x) & 63;
    lflag_set(L_PUB + j * 4, 1);
    const u64 btag = tag_of(*(volatile LDSP(u64))L_TAG);
    const bool hready = __builtin_amdgcn_readfirstlane(*(volatile LDSP(u32))(L_HE + j * 4)) != 0;
    const u32 hl = lane * 16;
    const char* hbase = ws + WS_H + (size_t)tau * 56 * 4096;
    const int q4 = (lane >> 2) & 3;
    const u32 ta = (u32)((4 * (lane >> 5) + q4) * 128 + ((q4 >> 1) << 6) + ((lane >> 4) & 1) * 32 + (lane & 3) * 8);
    s16x8 fa[2][4], fb[3][4];
    f32x16 acc[2][2] = {};
    auto ldh = [&](int i) {
      const char* p = hbase + (size_t)g2_ks(j, i) * 4096;
      asm volatile("global_load_dwordx4 %0, %4, %5 sc1\n\tglobal_load_dwordx4 %1, %4, %5 offset:1024 sc1\n\t"
                   "global_load_dwordx4 %2, %4, %5 offset:2048 sc1\n\tglobal_load_dwordx4 %3, %4, %5 offset:3072 sc1"
                   : "=&v"(fb[i % 3][0]), "=&v"(fb[i % 3][1]), "=&v"(fb[i % 3][2]), "=&v"(fb[i % 3][3])
                   : "v"(hl), "s"(p) : "memory");
    };
    u32 tb[2];
#pragma unroll
    for (int a = 0; a < 2; ++a) tb[a] = opq((ta ^ (u32)(a << 6)) + g2_place(j, 0));
    auto rda = [&](int i) {
#pragma unroll
      for (int a = 0; a < 2; ++a)
        if (i > 0 && i % 3 == 0) tb[a] = opq(i % 6 ? tb[a] - 2 * SLOT : tb[a] + 2 * SLOT);
#pragma unroll
      for (int n = 0; n < 4; ++n) {
        const int o = (i % 3) * 4096 + (n >> 1) * 2048;
        fa[i & 1][n] = lds_tr2(tb[n & 1], o, o + 1024);
      }
    };
    const u32 fed = opq(L_FED + j * 4), prog = opq(L_PROG + j * 4);
    if (!hready) { gflag_publish(hflag, btag, lane); lflag_set(L_PUB + j * 4, 2); lflag_wait(L_HE + j * 4, 0, 1); }
#pragma unroll
    for (int i = 0; i < 3; ++i) ldh(i);
    if (hready) {
      WAIT_VM(12);
      if (lane == 0) gstore64(hflag, btag);
      lflag_set(L_PUB + j * 4, 2);
    }
    lflag_wait(L_FED + j * 4, 0, 3);
    rda(0);
    SCHED0();
    u32 fl = 0;
#pragma unroll
    for (int i = 0; i < 14; ++i) {
      if (i == 2 || (i >= 5 && i < 13 && (i & 1))) {
        if (__builtin_amdgcn_readfirstlane(fl) < i + 2) lflag_wait(L_FED + j * 4, 0, i + 2);
      }
      asm volatile("s_waitcnt vmcnt(%4)" : "+v"(fb[i % 3][0]), "+v"(fb[i % 3][1]), "+v"(fb[i % 3][2]), "+v"(fb[i % 3][3])
                   : "i"(i < 12 ? 8 : (13 - i) * 4) : "memory");
      SCHED0();
      if (i < 13) rda(i + 1);
      if (i == 1 || (i >= 4 && i < 12 && !(i & 1))) fl = *(volatile LDSP(u32))fed;
      if (i < 13) {
#pragma unroll
        for (int s = 0; s < 2; ++s)
#pragma unroll
          for (int a = 0; a < 2; ++a)
#pragma unroll
            for (int t = 0; t < 2; ++t) acc[a][t] = mfma32(fa[i & 1][2 * s + a], fb[i % 3][2 * s + t], acc[a][t]);
#pragma unroll
        for (int n = 0; n < 5; ++n) { SGB(0x008, 1); SGB(0x100, 2); }
        SGB(0x008, 3);
      } else {
        __builtin_amdgcn_s_waitcnt(0xC07F);
        const int ln = (int)opq(threadIdx.x), lt = ln & 31, lh = (ln >> 5) & 1;
#pragma unroll
        for (int n = 0; n < 5; ++n) {
          if (n < 4) {
#pragma unroll
            for (int s = 0; s < 2; ++s) acc[n & 1][n >> 1] = mfma32(fa[i & 1][2 * s + (n & 1)], fb[i % 3][2 * s + (n >> 1)], acc[n & 1][n >> 1]);
          }
          SCHED0();
          if (n > 0) {
            const int a = (n - 1) & 1, t = (n - 1) >> 1;
#pragma unroll
            for (int G = 0; G < 4; ++G)
              *(LDSP(f32x4))(g2_place(j, t ? 3 : 0) + (u32)(lt * 256 + (((8 * a + 2 * G + lh) ^ (lt & 7)) << 4))) =
                  f32x4{acc[a][t][4 * G], acc[a][t][4 * G + 1], acc[a][t][4 * G + 2], acc[a][t][4 * G + 3]};
          }
          SCHED0();
        }
      }
      SCHED0();
      if (i < 8 && (i & 1)) *(volatile LDSP(u32))prog = i + 1;
      if (i == 4) lflag_wait(L_H, 0, 1);
      if (i + 3 < 14) ldh(i + 3);
      SCHED0();
    }
  }
  {
    const int ht = (int)opq(threadIdx.x) & 255;
    const int half = cw >> 2, tk = ht >> 3, dg = ht & 7, tok = 32 * half + tk;
    const u32 pa0 = opq((half ? 0u : 2 * SLOT) + (u32)(tk * 256 + (((2 * dg) ^ (tk & 7)) << 4))), pa1 = opq(pa0 ^ 16u);
    const u32 ya = opq(xbuf((dg >> 1) ^ 1) + (u32)(8 * (dg & 1) * 128 + (((tok >> 3) ^ dg) << 4) + (tok & 7) * 2));
    const u32 ea = opq(E_S + (u32)ht * 16);
    const u32 oo = opq((u32)((64 * tau + tok) * (D * 2) + (64 * dl + 8 * dg) * 2));
    WAIT_LGKM0();
    BAR();
    f32x4 p0[4], p1[4];
    u32 yv[8];
#pragma unroll
    for (int w = 0; w < 4; ++w) {
      p0[w] = *(LDSP(f32x4))((LDSP(char))pa0 + w * 12288);
      p1[w] = *(LDSP(f32x4))((LDSP(char))pa1 + w * 12288);
    }
#pragma unroll
    for (int e = 0; e < 8; ++e) yv[e] = *(LDSP(u16))((LDSP(char))ya + e * 128);
    SCHED0();
    if (half) { sv = *(LDSP(u32x4))ea; xv = *(LDSP(u32x4))((LDSP(char))ea + (E_X - E_S)); }
    const f32x4 s0 = (p0[0] + p0[1]) + (p0[2] + p0[3]), s1 = (p1[0] + p1[1]) + (p1[2] + p1[3]);
    u32 f[4];
    float ex[8];
#pragma unroll
    for (int e = 0; e < 4; ++e) f[e] = e < 2 ? pk2(s0[2 * e], s0[2 * e + 1]) : pk2(s1[2 * e - 4], s1[2 * e - 3]);
#pragma unroll
    for (int e = 0; e < 8; ++e) ex[e] = __builtin_amdgcn_exp2f(-1.44269504f * ((e & 1) ? bfhi(f[e >> 1]) : bflo(f[e >> 1])));
    SCHED0();
    u32 se[4];
#pragma unroll
    for (int e = 0; e < 4; ++e) se[e] = pk2(bflo(sv[e]) * bflo(yv[2 * e]), bfhi(sv[e]) * bflo(yv[2 * e + 1]));
#pragma unroll
    for (int e = 0; e < 8; ++e) ex[e] = __builtin_amdgcn_rcpf(1.0f + ex[e]);
    SCHED0();
    u32x4 o;
#pragma unroll
    for (int e = 0; e < 4; ++e) {
      const u32 gt = pk2(bflo(f[e]) * ex[2 * e], bfhi(f[e]) * ex[2 * e + 1]);
      const u32 p = pk2(bflo(se[e]) * bflo(gt), bfhi(se[e]) * bfhi(gt));
      o[e] = pk2(bflo(xv[e]) + bflo(p), bfhi(xv[e]) + bfhi(p));
    }
    __builtin_amdgcn_raw_buffer_store_b128(o, __builtin_amdgcn_make_buffer_rsrc(out, 0, S * D * 2, 0x00020000), oo, 0, 16);
  }
}
}
#if defined(__HIP_DEVICE_COMPILE__) && __HIP_DEVICE_COMPILE__
#pragma clang attribute pop
#endif

extern "C" void kernel_launch(void* const* d_in, const int* in_sizes, int n_in, void* d_out, int out_size, void* d_ws,
                              size_t ws_size, hipStream_t stream) {
  (void)hipFuncSetAttribute((const void*)fused_kernel, hipFuncAttributeMaxDynamicSharedMemorySize, 163840);
  fused_kernel<<<256, 768, 163840, stream>>>((const char*)d_in[0], (const char*)d_in[1], (const char*)d_in[2], (const char*)d_in[3],
                                             (const char*)d_in[4], (const char*)d_in[5], (const char*)d_in[7], (const char*)d_in[8],
                                             (const char*)d_in[9], (char*)d_ws, (char*)d_out);
}
```
